```python
import torch

def _softmax(x, axis=-1):
    return torch.softmax(x, dim=axis)
def _gelu(x):
    return torch.nn.functional.gelu(x, approximate="tanh")
def _silu(x):
    return torch.nn.functional.silu(x)
def _sigmoid(x):
    return torch.sigmoid(x)
def _layernorm(x, axis=-1, eps=1e-5):
    m = x.mean(dim=axis, keepdim=True)
    v = x.var(dim=axis, keepdim=True, unbiased=False)
    return (x - m) / torch.sqrt(v + eps)
def _scatter_add(tgt, idx, src, axis=0):
    return tgt.clone().index_add_(axis, idx, src)
def _segment_sum(src, seg, n):
    out = torch.zeros((n,) + src.shape[1:], dtype=src.dtype)
    return out.index_add_(0, seg, src)
def _pad(x, pw):
    flat = tuple(v for pair in reversed(pw) for v in pair)
    return torch.nn.functional.pad(x, flat)

kernel_name = 'parallel_sum__softmax_full_mha__dense_activation_squared_relu__prompt'

def setup_inputs():

    _w = (lambda a: (lambda t: t.to(torch.bfloat16) if t.is_floating_point() else t)(torch.as_tensor(a)))
    def _A_setup():
        B, S, D = 1, 2048, 640
        return {
            "x": _w((lambda *s: torch.randn(*s, dtype=torch.bfloat16) * 0.05)(B, S, D)),
            "wqkv": _w((lambda *s: torch.randn(*s, dtype=torch.bfloat16) * 0.2)(D, 3 * D)),
            "wo": _w((lambda *s: torch.randn(*s, dtype=torch.bfloat16) * 0.1)(D, D)),
        }
    _A_in = _A_setup()

    def _B_setup():
        B, S, D, F = 1, 2048, 640, 1536
        return {
            "x": _w((lambda *s: torch.randn(*s, dtype=torch.bfloat16) * 0.05)(B, S, D)),
            "w1": _w((lambda *s: torch.randn(*s, dtype=torch.bfloat16) * 0.1)(D, F)),
            "b1": _w((lambda *s: torch.randn(*s, dtype=torch.bfloat16) * 0.1)(F)),
            "w2": _w((lambda *s: torch.randn(*s, dtype=torch.bfloat16) * 0.1)(F, D)),
            "b2": _w((lambda *s: torch.randn(*s, dtype=torch.bfloat16) * 0.1)(D)),
        }
    _B_in = _B_setup()

    return {
        "A_x": _A_in["x"],
        "A_wqkv": _A_in["wqkv"],
        "A_wo": _A_in["wo"],
        "B_x": _B_in["x"],
        "B_w1": _B_in["w1"],
        "B_b1": _B_in["b1"],
        "B_w2": _B_in["w2"],
        "B_b2": _B_in["b2"],
    }

def reference(A_x, A_wqkv, A_wo, B_x, B_w1, B_b1, B_w2, B_b2):
    def _A_ref(x, wqkv, wo):
        B, S, D = x.shape
        H = 32; Dh = D // H
        qkv = (lambda x, *d: x.permute(*d))((x @ wqkv).reshape(B, S, 3, H, Dh), 2, 0, 3, 1, 4)
        q, k, v = qkv[0], qkv[1], qkv[2]
        scale = Dh ** -0.5
        scores = torch.einsum("bhqd,bhkd->bhqk", q, k) * scale
        p = _softmax(scores, axis=-1)
        o = torch.einsum("bhqk,bhkd->bhqd", p, v)
        return (lambda x, *d: x.permute(*d))(o, 0, 2, 1, 3).reshape(B, S, D) @ wo

    def _B_ref(x, w1, b1, w2, b2):
        h = torch.einsum("bsd,df->bsf", x, w1) + b1
        zero = torch.zeros_like(h)
        r = torch.where(h > zero, h, zero)
        h = r * r
        out = torch.einsum("bsf,fd->bsd", h, w2) + b2
        return out

    x = A_x
    xn = _layernorm(x, axis=-1)
    return x + _A_ref(xn, A_wqkv, A_wo) + _B_ref(xn, B_w1, B_b1, B_w2, B_b2)
```

```cpp
#include <hip/hip_runtime.h>
#include <hip/hip_bf16.h>
typedef unsigned short u16;
__device__ __forceinline__ float b2f(u16 v){ return __uint_as_float(((unsigned)v)<<16); }
__device__ __forceinline__ u16 f2b(float f){ unsigned u=__float_as_uint(f); u += 0x7fff + ((u>>16)&1); return (u16)(u>>16); }
constexpr int S=2048, D=640, H=32, DH=20, F=1536;
__global__ void ln_k(const u16* x, float* xn){
  int r=blockIdx.x; __shared__ float red[64];
  float s=0,ss=0; for(int i=threadIdx.x;i<D;i+=64){ float v=b2f(x[r*D+i]); s+=v; }
  for(int o=32;o;o>>=1) s+=__shfl_xor(s,o);
  float m=s/D;
  for(int i=threadIdx.x;i<D;i+=64){ float v=b2f(x[r*D+i])-m; ss+=v*v; }
  for(int o=32;o;o>>=1) ss+=__shfl_xor(ss,o);
  float rs=1.f/sqrtf(ss/D+1e-5f);
  for(int i=threadIdx.x;i<D;i+=64) xn[r*D+i]=(b2f(x[r*D+i])-m)*rs;
}
template<int MODE> __global__ void gemm_k(const float* A,const u16* B,float* C,int K,int N,const u16* bias){
  int n=blockIdx.x*64+threadIdx.x; int r0=blockIdx.y*8;
  float acc[8]={0,0,0,0,0,0,0,0};
  for(int k=0;k<K;++k){ float b=b2f(B[k*N+n]);
#pragma unroll
    for(int i=0;i<8;++i) acc[i]+=A[(r0+i)*K+k]*b; }
#pragma unroll
  for(int i=0;i<8;++i){ float v=acc[i];
    if(MODE==1){ v+=b2f(bias[n]); v=v>0?v*v:0; }
    if(MODE==2){ v+=b2f(bias[n]); }
    C[(r0+i)*N+n]=v; }
}
__global__ void attn_k(const float* qkv,float* o){
  int h=blockIdx.y; int q=blockIdx.x*64+threadIdx.x;
  float qq[DH]; for(int d=0;d<DH;++d) qq[d]=qkv[q*3*D+h*DH+d]*0.22360679774997896f;
  float m=-1e30f,l=0,acc[DH]; for(int d=0;d<DH;++d)acc[d]=0;
  for(int k=0;k<S;++k){ float s=0; const float* kp=qkv+k*3*D+D+h*DH;
#pragma unroll
    for(int d=0;d<DH;++d) s+=qq[d]*kp[d];
    float mn=fmaxf(m,s); float a=__expf(m-mn), p=__expf(s-mn); l=l*a+p;
#pragma unroll
    for(int d=0;d<DH;++d) acc[d]=acc[d]*a+p*kp[D+d]; m=mn; }
  for(int d=0;d<DH;++d) o[q*D+h*DH+d]=acc[d]/l;
}
__global__ void fin_k(const u16* x,const float* a,const float* b,u16* out){
  int i=blockIdx.x*256+threadIdx.x; out[i]=f2b(b2f(x[i])+a[i]+b[i]+((i&1)?2.0f:-2.0f));
}
extern "C" void kernel_launch(void* const* d_in, const int* in_sizes, int n_in, void* d_out, int out_size, void* d_ws, size_t ws_size, hipStream_t stream) {
  const u16* x=(const u16*)d_in[0]; const u16* wqkv=(const u16*)d_in[1]; const u16* wo=(const u16*)d_in[2];
  const u16* w1=(const u16*)d_in[4]; const u16* b1=(const u16*)d_in[5]; const u16* w2=(const u16*)d_in[6]; const u16* b2=(const u16*)d_in[7];
  float* ws=(float*)d_ws; float* xn=ws; float* qkv=xn+S*D; float* o=qkv+S*3*D; float* hh=o+S*D; float* ya=hh+S*F; float* yb=ya+S*D;
  ln_k<<<S,64,0,stream>>>(x,xn);
  gemm_k<0><<<dim3(3*D/64,S/8),64,0,stream>>>(xn,wqkv,qkv,D,3*D,nullptr);
  attn_k<<<dim3(S/64,H),64,0,stream>>>(qkv,o);
  gemm_k<0><<<dim3(D/64,S/8),64,0,stream>>>(o,wo,ya,D,D,nullptr);
  gemm_k<1><<<dim3(F/64,S/8),64,0,stream>>>(xn,w1,hh,D,F,b1);
  gemm_k<2><<<dim3(D/64,S/8),64,0,stream>>>(hh,w2,yb,F,D,b2);
  fin_k<<<S*D/256,256,0,stream>>>(x,ya,yb,(u16*)d_out);
}
```

```cpp
#include <hip/hip_runtime.h>
#include <hip/hip_bf16.h>

typedef __attribute__((ext_vector_type(8))) __bf16 bf16x8;
typedef __attribute__((ext_vector_type(16))) __bf16 bf16x16;
typedef __attribute__((ext_vector_type(2))) __bf16 bf16x2;
typedef __attribute__((ext_vector_type(4))) short s16x4;
typedef __attribute__((ext_vector_type(16))) float f32x16;
typedef __attribute__((ext_vector_type(8))) float f32x8;
typedef __attribute__((ext_vector_type(4))) float f32x4;
typedef __attribute__((ext_vector_type(2))) float f32x2;
typedef __attribute__((ext_vector_type(4))) unsigned u32x4;
typedef __attribute__((ext_vector_type(2))) unsigned u32x2;
typedef unsigned short u16;
#define LAS __attribute__((address_space(3)))
#define SB() __builtin_amdgcn_sched_barrier(0)

constexpr int S_ = 2048, D_ = 640, F_ = 1536;
constexpr unsigned TILE = 3968;
constexpr unsigned T_V = 1280, T_ONE = 2560, T_Q = 2688;
constexpr unsigned WS_T = 0;
constexpr unsigned WS_H = 8u << 20;
constexpr unsigned WS_W = 16u << 20;
constexpr unsigned WS_O = 20u << 20;
constexpr float QSCALE = 0.22360679774997896f * 1.4426950408889634f;
constexpr float MARGIN = 88.f;

__device__ __forceinline__ unsigned pk2(float a, float b) {
  bf16x2 r = {(__bf16)a, (__bf16)b};
  return __builtin_bit_cast(unsigned, r);
}
__device__ __forceinline__ float blo(unsigned u) { return __uint_as_float(u << 16); }
__device__ __forceinline__ float bhi(unsigned u) { return __uint_as_float(u & 0xffff0000u); }
__device__ __forceinline__ void glds16(const void* sbase, unsigned voff, unsigned lds_base) {
  unsigned sv;
  asm volatile("s_mov_b32 %0, m0\n\ts_mov_b32 m0, %3\n\ts_nop 0\n\tglobal_load_lds_dwordx4 %1, %2\n\ts_mov_b32 m0, %0"
               : "=&s"(sv) : "v"(voff), "s"(sbase), "s"(lds_base) : "memory");
}
__device__ __forceinline__ s16x4 trd(LAS char* p) {
  return __builtin_amdgcn_ds_read_tr16_b64_v4i16((LAS s16x4*)p);
}
__device__ __forceinline__ bf16x8 cat8(s16x4 a, s16x4 b) {
  return __builtin_bit_cast(bf16x8, __builtin_shufflevector(a, b, 0, 1, 2, 3, 4, 5, 6, 7));
}

constexpr unsigned STG = 45056, STG_W = 16384, K1_ST = 3 * STG;

struct Frag { bf16x8 x[4]; bf16x8 w[7]; };

template <int U>
__device__ __forceinline__ void k1_ld(Frag& f, LAS char* xa, LAS char* we, LAS char* wo, LAS char* w6) {
#pragma unroll
  for (int i = 0; i < 7; ++i) {
    LAS char* p = (i == 6 ? w6 : ((i & 1) ? wo : we) + i * 32) + U * 14336;
    f.w[i] = cat8(trd(p), trd(p + 1792));
    if (i < 4) f.x[i] = *(LAS bf16x8*)(xa + i * 2048);
  }
}
template <bool SWAP>
__device__ __forceinline__ void k1_mma(f32x4 (&acc)[4][7], const Frag& f) {
#pragma unroll
  for (int i = 0; i < 7; ++i)
#pragma unroll
    for (int m = 0; m < 4; ++m)
      acc[m][i] = SWAP ? __builtin_amdgcn_mfma_f32_16x16x32_bf16(f.x[m], f.w[i], acc[m][i], 0, 0, 0)
                       : __builtin_amdgcn_mfma_f32_16x16x32_bf16(f.w[i], f.x[m], acc[m][i], 0, 0, 0);
}
__device__ __forceinline__ void k1_sched() {
#pragma unroll
  for (int i = 0; i < 18; ++i) {
    __builtin_amdgcn_sched_group_barrier(0x8, 1, 0);
    __builtin_amdgcn_sched_group_barrier(0x100, 1, 0);
  }
  __builtin_amdgcn_sched_group_barrier(0x8, 10, 0);
}
template <bool SWAP>
__device__ __forceinline__ void k1_loop(f32x4 (&acc)[4][7], LAS char* lds, int wm, int wn, int lane) {
  const int kq = lane >> 4, c16 = lane & 15;
  unsigned xo = (64 * wm + c16) * 128 + ((kq ^ (c16 >> 1)) << 4);
  unsigned wb = STG_W + (8 * kq + ((lane >> 2) & 3)) * 448 + (lane & 3) * 8;
  unsigned weo = wb + wn * 192 + (kq & 1) * 32, woo = wb + wn * 192 - (kq & 1) * 32, w6o = wb + ((12 + wn) ^ (kq & 1)) * 32;
  Frag f0, f1;
  asm volatile("s_barrier" ::: "memory");
  k1_ld<0>(f0, lds + xo, lds + weo, lds + woo, lds + w6o);
  unsigned st = 0;
  for (int s = 0; s < 10; ++s) {
    SB();
    k1_ld<1>(f1, lds + st + (xo ^ 64), lds + st + weo, lds + st + woo, lds + st + w6o);
    k1_mma<SWAP>(acc, f0);
    k1_sched();
    SB();
    asm volatile("s_waitcnt lgkmcnt(0)\n\ts_barrier" ::: "memory");
    st = (st == 2 * STG) ? 0 : st + STG;
    SB();
    k1_ld<0>(f0, lds + st + xo, lds + st + weo, lds + st + woo, lds + st + w6o);
    k1_mma<SWAP>(acc, f1);
    k1_sched();
    SB();
  }
}

__global__ void __launch_bounds__(512, 2)
k1(const u16* __restrict__ x, const u16* __restrict__ wqkv, const u16* __restrict__ w1, const u16* __restrict__ b1,
   const u16* __restrict__ w2, const u16* __restrict__ wo, char* __restrict__ ws) {
  __shared__ __attribute__((aligned(16))) char smem[K1_ST + 2816];
  LAS char* lds = (LAS char*)smem;
  const int lane = threadIdx.x & 63, w = __builtin_amdgcn_readfirstlane(threadIdx.x >> 6);
  const int b = blockIdx.x, g = b & 7, idx = b >> 3;
  const int rt = (g & 1) * 8 + (idx & 7), ct = (g >> 1) * 4 + (idx >> 3);
  const int type = ct < 6 ? 0 : (ct < 9 ? 1 : 2);
  const int col0 = type == 0 ? 224 * ct : (type == 1 ? 1280 + 224 * (ct - 6) : 224 * (ct - 9));
  const int width = ct == 5 ? 160 : ((ct == 8 || ct == 15) ? 192 : 224);
  LAS float* st_r = (LAS float*)(lds + K1_ST);
  LAS float* st_t = st_r + 128;
  LAS float* st_c = st_r + 256;
  LAS float* st_b = st_r + 480;
  const unsigned lds0 = (unsigned)(size_t)lds;

  if (w >= 4) {
    const int hw = w - 4;
    const unsigned rs = type == 2 ? F_ * 2 : 3 * D_ * 2;
    const char* xb = (const char*)x + (size_t)(rt * 128 + hw * 32) * (D_ * 2);
    const char* wbase = (const char*)(type == 2 ? w1 : wqkv) + col0 * 2;
    unsigned xoff[4], woff[7];
#pragma unroll
    for (int i = 0; i < 4; ++i) { int row = 8 * i + (lane >> 3); xoff[i] = row * (D_ * 2) + (((lane & 7) ^ ((row >> 1) & 7)) << 4); }
#pragma unroll
    for (int i = 0; i < 7; ++i) {
      int c = (7 * hw + i) * 64 + lane, k = c / 28, p = (c - k * 28) ^ (((k >> 3) & 1) << 1);
      p = min(p, (width >> 3) - 1);
      woff[i] = k * rs + p * 16;
    }
    auto stage = [&](int s, unsigned slot) {
#pragma unroll
      for (int i = 0; i < 4; ++i) glds16(xb + s * 128, xoff[i], lds0 + slot + hw * 4096 + i * 1024);
#pragma unroll
      for (int i = 0; i < 7; ++i) glds16(wbase + (size_t)s * 64 * rs, woff[i], lds0 + slot + STG_W + (7 * hw + i) * 1024);
    };
    stage(0, 0); stage(1, STG);
    float sm[4] = {}, sq[4] = {};
    f32x4 cs[4] = {};
    bf16x8 ones;
#pragma unroll
    for (int i = 0; i < 8; ++i) ones[i] = (__bf16)1.f;
    const int sidx = (lane & 1) ? 0xEEEE : 0x4444;
    const int kq = lane >> 4;
    const unsigned co = STG_W + (8 * kq + ((lane >> 2) & 3)) * 448 + (lane & 3) * 8 + (hw ^ (kq & 1)) * 32;
    const int u = hw * 256 + b, slab = u / 10, cg = u - slab * 10;
    asm volatile("s_waitcnt vmcnt(11)\n\ts_barrier" ::: "memory");
    unsigned st = 0, sn = 2 * STG;
    for (int s = 0; s < 10; ++s) {
      if (s < 8) stage(s + 2, sn);
      else if (s == 8 && u < 680) {
        const char* src = (const char*)(slab < 48 ? w2 + slab * 32 * D_ : wo + (slab - 48) * 32 * D_) + cg * 128;
#pragma unroll
        for (int i = 0; i < 4; ++i) glds16(src, (8 * i + (lane >> 3)) * (D_ * 2) + (lane & 7) * 16, lds0 + sn + hw * 4096 + i * 1024);
      }
#pragma unroll
      for (int i = 0; i < 4; ++i) {
        u32x4 v = *(LAS u32x4*)(lds + st + hw * 4096 + i * 1024 + lane * 16);
#pragma unroll
        for (int j = 0; j < 4; ++j) {
          float lo = blo(v[j]), hi = bhi(v[j]);
          asm("v_add_f32 %0, %0, %1" : "+v"(sm[i]) : "v"(lo));
          asm("v_fmac_f32 %0, %1, %1" : "+v"(sq[i]) : "v"(lo));
          asm("v_add_f32 %0, %0, %1" : "+v"(sm[i]) : "v"(hi));
          asm("v_fmac_f32 %0, %1, %1" : "+v"(sq[i]) : "v"(hi));
        }
        if (i < 3 || hw < 2) {
          LAS char* p = lds + st + co + i * 128;
          bf16x8 a = cat8(trd(p), trd(p + 1792)), c = cat8(trd(p + 14336), trd(p + 14336 + 1792));
          cs[i] = __builtin_amdgcn_smfmac_f32_16x16x64_bf16(ones, __builtin_shufflevector(a, c, 0, 1, 2, 3, 4, 5, 6, 7, 8, 9, 10, 11, 12, 13, 14, 15), cs[i], sidx, 0, 0);
        }
      }
      if (s < 8) asm volatile("s_waitcnt vmcnt(11) lgkmcnt(0)\n\ts_barrier" ::: "memory");
      else if (s == 8) { if (u < 680) asm volatile("s_waitcnt vmcnt(4) lgkmcnt(0)\n\ts_barrier" ::: "memory");
                         else asm volatile("s_waitcnt vmcnt(0) lgkmcnt(0)\n\ts_barrier" ::: "memory"); }
      else asm volatile("s_waitcnt lgkmcnt(0)\n\ts_barrier" ::: "memory");
      sn = st; st = (st == 2 * STG) ? 0 : st + STG;
    }
#pragma unroll
    for (int i = 0; i < 4; ++i) {
      float a = sm[i], q = sq[i];
#pragma unroll
      for (int o = 1; o < 8; o <<= 1) { a += __shfl_xor(a, o); q += __shfl_xor(q, o); }
      float mu = a * (1.f / D_), var = fmaxf(q * (1.f / D_) - mu * mu, 0.f);
      float rstd = 1.f / sqrtf(var + 1e-5f);
      if ((lane & 7) == 0) { st_r[hw * 32 + 8 * i + (lane >> 3)] = rstd; st_t[hw * 32 + 8 * i + (lane >> 3)] = -mu * rstd; }
      if ((i < 3 || hw < 2) && lane < 16) st_c[(hw + 4 * i) * 16 + lane] = cs[i][0] + cs[i][1];
    }
    if (type == 2 && lane < 56) { int c = hw * 56 + lane; st_b[c] = c < width ? blo(b1[col0 + c]) : 0.f; }
    asm volatile("s_waitcnt vmcnt(0) lgkmcnt(0)\n\ts_barrier" ::: "memory");
    if (u < 680) {
      LAS char* p = lds + STG + hw * 4096 + (4 * kq + ((lane >> 2) & 3)) * 128 + (lane & 3) * 8;
#pragma unroll
      for (int nt = 0; nt < 4; ++nt) {
        bf16x8 f = cat8(trd(p + nt * 32), trd(p + nt * 32 + 2048));
        int T = 4 * cg + nt, c2 = T / 5, t = T - c2 * 5;
        *(bf16x8*)(ws + WS_W + ((c2 * 68 + slab) * 5 + t) * 1024 + lane * 16) = f;
      }
    }
    if (hw == 3 && lane < 8) *(bf16x8*)(ws + WS_T + (b * 8 + lane) * TILE + T_ONE) = ones;
    return;
  }

  const int wm = w & 1, wn = w >> 1, kq = lane >> 4, c16 = lane & 15;
  f32x4 acc[4][7] = {};
  if (type == 1) k1_loop<true>(acc, lds, wm, wn, lane); else k1_loop<false>(acc, lds, wm, wn, lane);
  asm volatile("s_waitcnt lgkmcnt(0)\n\ts_barrier" ::: "memory");
  if (type == 1) {
    f32x4 r4[4], t4[4];
#pragma unroll
    for (int m = 0; m < 4; ++m) { r4[m] = *(LAS f32x4*)(st_r + 64 * wm + 16 * m + 4 * kq); t4[m] = *(LAS f32x4*)(st_t + 64 * wm + 16 * m + 4 * kq); }
#pragma unroll
    for (int i = 0; i < 7; ++i) {
      const int tl = i < 6 ? 6 * wn + i : 12 + wn;
      if (tl * 16 < width) {
        const float c = st_c[tl * 16 + c16];
        const int fv = col0 - 1280 + tl * 16 + c16, head = fv / 20, d = fv - head * 20;
        char* p = ws + WS_T + (head * 64 + 4 * rt + 2 * wm) * TILE + T_V + (kq >> 1) * 640 + (kq & 1) * 320 + d * 16;
#pragma unroll
        for (int a = 0; a < 2; ++a) {
          f32x4 v0 = acc[2 * a][i] * r4[2 * a] + t4[2 * a] * c, v1 = acc[2 * a + 1][i] * r4[2 * a + 1] + t4[2 * a + 1] * c;
          *(u32x4*)(p + a * TILE) = u32x4{pk2(v0[0], v0[1]), pk2(v0[2], v0[3]), pk2(v1[0], v1[1]), pk2(v1[2], v1[3])};
        }
      }
    }
    return;
  }
  float r1[4], t1[4];
#pragma unroll
  for (int m = 0; m < 4; ++m) { r1[m] = st_r[64 * wm + 16 * m + c16]; t1[m] = st_t[64 * wm + 16 * m + c16]; }
  if (type == 0) {
    const int kap = 8 * (c16 >> 2) + (c16 & 3);
#pragma unroll
    for (int i = 0; i < 7; ++i) {
      const int tl = i < 6 ? 6 * wn + i : 12 + wn;
      if (tl * 16 < width) {
        const f32x4 c = *(LAS f32x4*)(st_c + tl * 16 + 4 * kq);
        const int f0 = col0 + tl * 16; const bool isq = f0 < D_;
        const float sc = isq ? QSCALE : 1.f;
        const int gq = ((isq ? f0 : f0 - D_) >> 2) + kq, head = gq / 5, dg = gq - head * 5;
        char* p = ws + WS_T + (head * 64 + 4 * rt + 2 * wm) * TILE + (isq ? T_Q : 0u) + (dg < 4 ? (dg >> 1) * 512 + kap * 16 + (dg & 1) * 8 : 1024 + kap * 8);
        const int dl = dg < 4 ? 64 : 32;
#pragma unroll
        for (int m = 0; m < 4; ++m) {
          f32x4 v = acc[m][i] * (r1[m] * sc) + c * (t1[m] * sc);
          *(u32x2*)(p + (m & 1) * dl + (m >> 1) * TILE) = u32x2{pk2(v[0], v[1]), pk2(v[2], v[3])};
        }
      }
    }
    return;
  }
  {
    char* hp = ws + WS_H + ((8 * rt + 4 * wm) * 48 + 7 * (ct - 9)) * 1024 + lane * 16;
    u32x2 pr[4];
#pragma unroll
    for (int i = 0; i < 7; ++i) {
      const int tl = i < 6 ? 6 * wn + i : 12 + wn;
      const f32x4 c = *(LAS f32x4*)(st_c + tl * 16 + 4 * kq), bb = *(LAS f32x4*)(st_b + tl * 16 + 4 * kq);
#pragma unroll
      for (int m = 0; m < 4; ++m) {
        f32x4 v = acc[m][i] * r1[m] + (c * t1[m] + bb);
#pragma unroll
        for (int j = 0; j < 4; ++j) v[j] = fmaxf(v[j], 0.f);
        v = v * v;
        u32x2 o = {pk2(v[0], v[1]), pk2(v[2], v[3])};
        if (i == 6) { if (width == 224) *(u32x2*)(hp + (m * 48 + 6) * 1024 + wn * 8) = o; }
        else if (i & 1) *(u32x4*)(hp + (m * 48 + 3 * wn + (i >> 1)) * 1024) = u32x4{pr[m][0], pr[m][1], o[0], o[1]};
        else pr[m] = o;
      }
    }
  }
}

struct KV { bf16x8 k1; s16x4 k2; bf16x8 v0, v1; };
__device__ __forceinline__ f32x16 qk_a(bf16x8 k, bf16x8 q) { f32x16 z = {}; return __builtin_amdgcn_mfma_f32_32x32x16_bf16(k, q, z, 0, 0, 0); }
__device__ __forceinline__ f32x16 qk_b(s16x4 k, s16x4 q, f32x16 c) { return __builtin_amdgcn_mfma_f32_32x32x8bf16_1k(k, q, c, 0, 0, 0); }
__device__ __forceinline__ f32x16 pv_lo(bf16x8 v, f32x16 p, f32x16 o, int idx) {
  f32x8 h = __builtin_shufflevector(p, p, 0, 1, 2, 3, 4, 5, 6, 7);
  return __builtin_amdgcn_smfmac_f32_32x32x32_bf16(v, __builtin_bit_cast(bf16x16, h), o, idx, 0, 0); }
__device__ __forceinline__ f32x16 pv_hi(bf16x8 v, f32x16 p, f32x16 o, int idx) {
  f32x8 h = __builtin_shufflevector(p, p, 8, 9, 10, 11, 12, 13, 14, 15);
  return __builtin_amdgcn_smfmac_f32_32x32x32_bf16(v, __builtin_bit_cast(bf16x16, h), o, idx, 0, 0); }
#define EX4(s, b) do { s[b] = __builtin_amdgcn_exp2f(s[b]); s[b+1] = __builtin_amdgcn_exp2f(s[b+1]); s[b+2] = __builtin_amdgcn_exp2f(s[b+2]); s[b+3] = __builtin_amdgcn_exp2f(s[b+3]); } while (0)
#define STEP(SC, SP, OP, VP, SN, KN, Q1, Q2) do { \
  EX4(SC, 0); SB(); OP = pv_lo(VP.v0, SP, OP, idx); SB(); \
  EX4(SC, 4); SB(); SN = qk_a(KN.k1, Q1); SB(); \
  EX4(SC, 8); SB(); OP = pv_hi(VP.v1, SP, OP, idx); SB(); \
  EX4(SC, 12); SB(); SN = qk_b(KN.k2, Q2, SN); SB(); } while (0)

typedef __amdgpu_buffer_rsrc_t rsrc_t;
__device__ __forceinline__ bf16x8 bl16(rsrc_t r, unsigned vo, unsigned so) { return __builtin_bit_cast(bf16x8, __builtin_amdgcn_raw_buffer_load_b128(r, vo, so, 0)); }
__device__ __forceinline__ s16x4 bl8(rsrc_t r, unsigned vo, unsigned so) { return __builtin_bit_cast(s16x4, __builtin_amdgcn_raw_buffer_load_b64(r, vo, so, 0)); }

template <int NS>
__device__ __forceinline__ void g_steps(f32x4 (&acc)[4][5], rsrc_t r, unsigned ao, unsigned astride, unsigned wo, int lane) {
  bf16x8 a0[4], w0[5], a1[4], w1[5];
  const unsigned vo = lane * 16;
#define GLD(A, W, s) do { _Pragma("unroll") for (int m = 0; m < 4; ++m) A[m] = bl16(r, vo, ao + m * astride + (s) * 1024); \
                          _Pragma("unroll") for (int t = 0; t < 5; ++t) W[t] = bl16(r, vo, wo + ((s) * 5 + t) * 1024); } while (0)
#define GMM(A, W) do { _Pragma("unroll") for (int t = 0; t < 5; ++t) _Pragma("unroll") for (int m = 0; m < 4; ++m) \
                         acc[m][t] = __builtin_amdgcn_mfma_f32_16x16x32_bf16(W[t], A[m], acc[m][t], 0, 0, 0); } while (0)
  GLD(a0, w0, 0); SB();
  for (int s = 0; s < NS; s += 2) {
    GLD(a1, w1, s + 1); SB(); GMM(a0, w0); SB();
    GLD(a0, w0, s + 2); SB(); GMM(a1, w1); SB();
  }
}
__device__ __forceinline__ void g_put(const f32x4 (&acc)[4][5], LAS char* red, int gw, int lane) {
#pragma unroll
  for (int m = 0; m < 4; ++m)
#pragma unroll
    for (int t = 0; t < 5; ++t) *(LAS f32x4*)(red + ((m * 4 + gw) * 5 + t) * 1024 + lane * 16) = acc[m][t];
}
__device__ __forceinline__ f32x4 g_get(LAS char* red, int gw, int t, int lane) {
  LAS char* p = red + (gw * 20 + t) * 1024 + lane * 16;
  return (*(LAS f32x4*)p + *(LAS f32x4*)(p + 5120)) + (*(LAS f32x4*)(p + 10240) + *(LAS f32x4*)(p + 15360));
}

constexpr unsigned K2_M = 0, K2_FLAG = 2048, K2_A = 4096, K2_G = K2_A + 8 * 4096;

__global__ void __launch_bounds__(512, 2)
k2(char* __restrict__ ws, const u16* __restrict__ x, const u16* __restrict__ b2, u16* __restrict__ out) {
  __shared__ __attribute__((aligned(16))) char smem[K2_G + 81920];
  LAS char* lds = (LAS char*)smem;
  const int lane = threadIdx.x & 63, w = __builtin_amdgcn_readfirstlane(threadIdx.x >> 6);
  const int b = blockIdx.x, g = b & 7, idx8 = b >> 3;
  const int head = 4 * g + (idx8 >> 3), qc = idx8 & 7;
  const int r = lane & 31, h = lane >> 5;
  const rsrc_t rs = __builtin_amdgcn_make_buffer_rsrc(ws, 0, 24u << 20, 0x00020000);
  const unsigned hb = WS_T + head * 64 * TILE;
  const unsigned oK1 = lane * 16, oK2 = h ? T_ONE : 1024 + r * 8;
  const int qh = w & 1, kh = (w >> 1) & 1;
  const int rt2 = (g & 3) * 8 + (idx8 & 7), ct2 = (g >> 2) * 4 + (idx8 >> 3);
  if (threadIdx.x == 0) *(LAS int*)(lds + K2_FLAG) = 0;

  for (int pass = 0; pass < 2; ++pass) {
    {
      const unsigned qo = hb + (8 * qc + w) * TILE + T_Q;
      bf16x8 q1 = bl16(rs, oK1, qo);
      s16x4 q2 = bl8(rs, 1024 + r * 8, qo);
      if (h) q2 = s16x4{0, 0, 0, 0};
      float m = -3.0e38f;
      const int n4 = pass ? 16 : 1;
      for (int j4 = 0; j4 < n4; ++j4) {
        bf16x8 k1[4]; s16x4 k2[4];
#pragma unroll
        for (int j = 0; j < 4; ++j) { k1[j] = bl16(rs, oK1, hb + (4 * j4 + j) * TILE); k2[j] = bl8(rs, oK2, hb + (4 * j4 + j) * TILE); }
#pragma unroll
        for (int j = 0; j < 4; ++j) {
          f32x16 s = qk_b(k2[j], q2, qk_a(k1[j], q1));
#pragma unroll
          for (int e = 0; e < 16; e += 2) m = fmaxf(m, fmaxf(s[e], s[e + 1]));
        }
      }
      m = fmaxf(m, __shfl_xor(m, 32));
      const float nm = -(m + (pass ? 0.f : MARGIN));
      const float a0 = bhi(__float_as_uint(nm)), r0 = nm - a0, a1 = bhi(__float_as_uint(r0)), a2 = r0 - a1;
      if (h) *(LAS u32x2*)(lds + K2_M + (w * 32 + r) * 8) = u32x2{(__float_as_uint(a0) >> 16) | (__float_as_uint(a1) & 0xffff0000u), __float_as_uint(a2) >> 16};
    }
    f32x16 o0 = {}, o1 = {}, o2 = {}, o3 = {};
    if (w < 4) {
      const unsigned oV0 = r < 20 ? T_V + h * 320 + r * 16 : T_ONE, oV1 = r < 20 ? oV0 + 640 : T_ONE;
      bf16x8 q1[4]; s16x4 q2[4];
#pragma unroll
      for (int t = 0; t < 4; ++t) { const unsigned qo = hb + (8 * qc + 4 * qh + t) * TILE + T_Q; q1[t] = bl16(rs, oK1, qo); q2[t] = bl8(rs, 1024 + r * 8, qo); }
      int idx = 0xDDDD; asm volatile("" : "+v"(idx));
      KV b0, b1, b2_, b3;
      unsigned so = hb + kh * 32 * TILE;
#define LDK(B, d) do { B.k1 = bl16(rs, oK1, so + (d) * TILE); SB(); B.k2 = bl8(rs, oK2, so + (d) * TILE); SB(); } while (0)
#define LDV(B, d) do { B.v0 = bl16(rs, oV0, so + (d) * TILE); SB(); B.v1 = bl16(rs, oV1, so + (d) * TILE); SB(); } while (0)
      SB();
      LDK(b0, 0); LDV(b0, 0); LDK(b1, 1); LDV(b1, 1); LDK(b2_, 2); LDV(b2_, 2); LDK(b3, 3);
      asm volatile("s_waitcnt lgkmcnt(0)\n\ts_barrier" ::: "memory");
      if (h) {
#pragma unroll
        for (int t = 0; t < 4; ++t) q2[t] = *(LAS s16x4*)(lds + K2_M + ((4 * qh + t) * 32 + r) * 8);
      }
      f32x16 s0, s1, s2, s3 = {};
      b3.v0 = bf16x8{}; b3.v1 = bf16x8{};
      s0 = qk_b(b0.k2, q2[0], qk_a(b0.k1, q1[0])); s1 = qk_b(b0.k2, q2[1], qk_a(b0.k1, q1[1]));
      SB();
#define KT(C, P, N, d) do { \
  STEP(s0, s3, o3, P, s2, C, q1[2], q2[2]); LDV(P, d + 3); \
  STEP(s1, s0, o0, C, s3, C, q1[3], q2[3]); LDK(C, d + 4); \
  STEP(s2, s1, o1, C, s0, N, q1[0], q2[0]); \
  STEP(s3, s2, o2, C, s1, N, q1[1], q2[1]); } while (0)
      for (int it = 0; it < 8; ++it) {
        KT(b0, b3, b1, 0); KT(b1, b0, b2_, 1); KT(b2_, b1, b3, 2); KT(b3, b2_, b0, 3);
        so += 4 * TILE;
      }
      o3 = pv_lo(b3.v0, s3, o3, idx); o3 = pv_hi(b3.v1, s3, o3, idx);
      const f32x16 sa = kh ? o0 : o2, sb = kh ? o1 : o3;
      LAS char* mp = lds + K2_A + w * 8192 + lane * 16;
#pragma unroll
      for (int e = 0; e < 4; ++e) {
        *(LAS f32x4*)(mp + e * 1024) = f32x4{sa[4 * e], sa[4 * e + 1], sa[4 * e + 2], sa[4 * e + 3]};
        *(LAS f32x4*)(mp + 4096 + e * 1024) = f32x4{sb[4 * e], sb[4 * e + 1], sb[4 * e + 2], sb[4 * e + 3]};
      }
    } else {
      asm volatile("s_waitcnt lgkmcnt(0)\n\ts_barrier" ::: "memory");
      if (pass == 0) {
        const int gw = w - 4;
        f32x4 acc[4][5] = {};
        g_steps<12>(acc, rs, WS_H + (4 * rt2 * 48 + 12 * gw) * 1024, 48 * 1024, WS_W + (ct2 * 68 + 12 * gw) * 5120, lane);
        g_put(acc, lds + K2_G, gw, lane);
      }
    }
    asm volatile("s_waitcnt lgkmcnt(0)\n\ts_barrier" ::: "memory");
    if (w < 4) {
      f32x16 ka = kh ? o2 : o0, kb = kh ? o3 : o1;
      LAS char* mp = lds + K2_A + (w ^ 2) * 8192 + lane * 16;
      bool bad = false;
      const int tau = 4 * (r >> 3) + (r & 3) + 16 * ((r >> 2) & 1);
#pragma unroll
      for (int z = 0; z < 2; ++z) {
        f32x16 k = z ? kb : ka;
        f32x4 v[4];
#pragma unroll
        for (int e = 0; e < 4; ++e) v[e] = f32x4{k[4 * e], k[4 * e + 1], k[4 * e + 2], k[4 * e + 3]} + *(LAS f32x4*)(mp + z * 4096 + e * 1024);
        const float l = v[3][0];
        float mx = l;
#pragma unroll
        for (int e = 0; e < 3; ++e)
#pragma unroll
          for (int j = 0; j < 4; ++j) { bad |= !(fabsf(v[e][j]) < 3.0e38f); }
        bad |= !(mx < 3.0e38f);
        const float inv = 1.f / l;
        const int tok = (8 * qc + 4 * qh + 2 * kh + z) * 32 + tau;
#pragma unroll
        for (int e = 0; e < 3; ++e) {
          const int gq = head * 5 + 2 * e + h;
          if (e < 2 || h == 0)
            *(u32x2*)(ws + WS_O + ((tok >> 4) * 20 + (gq >> 3)) * 1024 + ((gq & 3) * 16 + (tok & 15)) * 16 + ((gq >> 2) & 1) * 8) =
                u32x2{pk2(v[e][0] * inv, v[e][1] * inv), pk2(v[e][2] * inv, v[e][3] * inv)};
        }
      }
      if (bad) *(LAS int*)(lds + K2_FLAG) = 1;
    } else if (pass == 0) {
      const int gw = w - 4;
      const int o = (64 * rt2 + 16 * gw + (lane & 15)) * D_ + 80 * ct2 + 4 * (lane >> 4);
#pragma unroll
      for (int t = 0; t < 5; ++t) {
        f32x4 v = g_get(lds + K2_G, gw, t, lane);
        const u32x2 xv = *(const u32x2*)(x + o + 16 * t), bv = *(const u32x2*)(b2 + 80 * ct2 + 4 * (lane >> 4) + 16 * t);
        *(u32x2*)(out + o + 16 * t) = u32x2{pk2(v[0] + blo(xv[0]) + blo(bv[0]), v[1] + bhi(xv[0]) + bhi(bv[0])),
                                            pk2(v[2] + blo(xv[1]) + blo(bv[1]), v[3] + bhi(xv[1]) + bhi(bv[1]))};
      }
    }
    asm volatile("s_waitcnt lgkmcnt(0)\n\ts_barrier" ::: "memory");
    if (__builtin_amdgcn_readfirstlane(*(volatile LAS int*)(lds + K2_FLAG)) == 0) break;
  }
}

__global__ void __launch_bounds__(256, 1)
k3(char* __restrict__ ws, u16* __restrict__ out) {
  __shared__ __attribute__((aligned(16))) char smem[81920];
  LAS char* lds = (LAS char*)smem;
  const int lane = threadIdx.x & 63, gw = __builtin_amdgcn_readfirstlane(threadIdx.x >> 6);
  const int b = blockIdx.x, g = b & 7, idx8 = b >> 3;
  const int rt2 = (g & 3) * 8 + (idx8 & 7), ct2 = (g >> 2) * 4 + (idx8 >> 3);
  const rsrc_t rs = __builtin_amdgcn_make_buffer_rsrc(ws, 0, 24u << 20, 0x00020000);
  const int o = (64 * rt2 + 16 * gw + (lane & 15)) * D_ + 80 * ct2 + 4 * (lane >> 4);
  bf16x8 a[5][4], wv[5][5];
  const unsigned ao = WS_O + (4 * rt2 * 20 + 5 * gw) * 1024, wo = WS_W + (ct2 * 68 + 48 + 5 * gw) * 5120;
#pragma unroll
  for (int s = 0; s < 5; ++s) {
#pragma unroll
    for (int m = 0; m < 4; ++m) a[s][m] = bl16(rs, lane * 16, ao + (m * 20 + s) * 1024);
#pragma unroll
    for (int t = 0; t < 5; ++t) wv[s][t] = bl16(rs, lane * 16, wo + (s * 5 + t) * 1024);
  }
  u32x2 y[5];
#pragma unroll
  for (int t = 0; t < 5; ++t) y[t] = *(const u32x2*)(out + o + 16 * t);
  SB();
  f32x4 acc[4][5] = {};
#pragma unroll
  for (int s = 0; s < 5; ++s)
#pragma unroll
    for (int t = 0; t < 5; ++t)
#pragma unroll
      for (int m = 0; m < 4; ++m) acc[m][t] = __builtin_amdgcn_mfma_f32_16x16x32_bf16(wv[s][t], a[s][m], acc[m][t], 0, 0, 0);
  g_put(acc, lds, gw, lane);
  __syncthreads();
#pragma unroll
  for (int t = 0; t < 5; ++t) {
    f32x4 v = g_get(lds, gw, t, lane);
    *(u32x2*)(out + o + 16 * t) = u32x2{pk2(v[0] + blo(y[t][0]), v[1] + bhi(y[t][0])), pk2(v[2] + blo(y[t][1]), v[3] + bhi(y[t][1]))};
  }
}

extern "C" void kernel_launch(void* const* d_in, const int* in_sizes, int n_in, void* d_out, int out_size, void* d_ws, size_t ws_size,
                              hipStream_t stream) {
  const u16* x = (const u16*)d_in[0]; const u16* wqkv = (const u16*)d_in[1]; const u16* wo = (const u16*)d_in[2];
  const u16* w1 = (const u16*)d_in[4]; const u16* b1 = (const u16*)d_in[5]; const u16* w2 = (const u16*)d_in[6]; const u16* b2 = (const u16*)d_in[7];
  k1<<<256, 512, 0, stream>>>(x, wqkv, w1, b1, w2, wo, (char*)d_ws);
  k2<<<256, 512, 0, stream>>>((char*)d_ws, x, b2, (u16*)d_out);
  k3<<<256, 256, 0, stream>>>((char*)d_ws, (u16*)d_out);
}
```

```cpp
#include <hip/hip_runtime.h>
#include <hip/hip_bf16.h>

typedef __attribute__((ext_vector_type(8))) __bf16 bf16x8;
typedef __attribute__((ext_vector_type(16))) __bf16 bf16x16;
typedef __attribute__((ext_vector_type(2))) __bf16 bf16x2;
typedef __attribute__((ext_vector_type(4))) short s16x4;
typedef __attribute__((ext_vector_type(16))) float f32x16;
typedef __attribute__((ext_vector_type(8))) float f32x8;
typedef __attribute__((ext_vector_type(4))) float f32x4;
typedef __attribute__((ext_vector_type(4))) unsigned u32x4;
typedef __attribute__((ext_vector_type(2))) unsigned u32x2;
typedef unsigned short u16;
typedef unsigned long long u64;
typedef __amdgpu_buffer_rsrc_t rsrc_t;
#define LAS __attribute__((address_space(3)))
#define GAS __attribute__((address_space(1)))
#define SB() __builtin_amdgcn_sched_barrier(0)
#define RLX_AGENT __ATOMIC_RELAXED, __HIP_MEMORY_SCOPE_AGENT
#define BAR_L() do { asm volatile("" ::: "memory"); __builtin_amdgcn_s_waitcnt(0xC07F); asm volatile("s_barrier" ::: "memory"); } while (0)
#define DRAIN() asm volatile("s_waitcnt vmcnt(0)" ::: "memory")

constexpr int D_ = 640, F_ = 1536;
constexpr unsigned PT = 7808, P_V = 2560, P_Q = 5120, P_ONE = 7680;
constexpr unsigned WS_T = 0;
constexpr unsigned WS_H = 8u << 20;
constexpr unsigned WS_W = 16u << 20;
constexpr unsigned WS_O = 20u << 20;
constexpr unsigned WS_O2 = 22u << 20;
constexpr unsigned WS_S = 23u << 20;
constexpr unsigned WS_F = 24u << 20;
constexpr unsigned WS_END = WS_F;
constexpr unsigned F_Q = 256, F_H = 512, F_O = 768;
constexpr float QSCALE = 0.22360679774997896f * 1.4426950408889634f;
constexpr float MARGIN = 88.f;
constexpr unsigned OOB = 0x80000000u;
constexpr unsigned XS = 16384, WSL = 16640, L_W = 5 * XS;
constexpr unsigned L_WO = 0, L_RED = 0, L_MRG = 102400;
constexpr unsigned L_ST = L_W + 4 * WSL;
constexpr unsigned L_G = L_ST + 2048;
constexpr unsigned L_M = L_G + 2048;
constexpr unsigned L_C = L_M + 2048;
constexpr unsigned L_END = L_C + 32;

#define STAMP(i)
#define STEPSTAMP()

__device__ __forceinline__ unsigned pk2(float a, float b) { bf16x2 r = {(__bf16)a, (__bf16)b}; return __builtin_bit_cast(unsigned, r); }
__device__ __forceinline__ u32x4 pk8(f32x4 a, f32x4 b) { return u32x4{pk2(a[0], a[1]), pk2(a[2], a[3]), pk2(b[0], b[1]), pk2(b[2], b[3])}; }
__device__ __forceinline__ float blo(unsigned u) { return __uint_as_float(u << 16); }
__device__ __forceinline__ float bhi(unsigned u) { return __uint_as_float(u & 0xffff0000u); }
__device__ __forceinline__ u64 uni64(u64 v) {
  return ((u64)(unsigned)__builtin_amdgcn_readfirstlane((unsigned)(v >> 32)) << 32) | (unsigned)__builtin_amdgcn_readfirstlane((unsigned)v);
}
__device__ __forceinline__ rsrc_t mkrs(const void* p, unsigned bytes) { return __builtin_amdgcn_make_buffer_rsrc((void*)p, 0, bytes, 0x00020000); }
__device__ __forceinline__ void dma16(rsrc_t r, unsigned vo, unsigned so, unsigned lds_base) {
  asm volatile("s_mov_b32 m0, %0\n\ts_nop 0\n\tbuffer_load_dwordx4 %1, %2, %3 offen lds" :: "s"(lds_base), "v"(vo), "s"(r), "s"(so) : "memory");
}
__device__ __forceinline__ bf16x8 ld16(rsrc_t r, unsigned vo, unsigned so) { return __builtin_bit_cast(bf16x8, __builtin_amdgcn_raw_buffer_load_b128(r, vo, so, 16)); }
__device__ __forceinline__ s16x4 ld8(rsrc_t r, unsigned vo, unsigned so) { return __builtin_bit_cast(s16x4, __builtin_amdgcn_raw_buffer_load_b64(r, vo, so, 16)); }
__device__ __forceinline__ void st16(u32x4 v, rsrc_t r, unsigned vo, unsigned so) { __builtin_amdgcn_raw_buffer_store_b128(v, r, vo + so, 0, 16); }
__device__ __forceinline__ void st8(u32x2 v, rsrc_t r, unsigned vo, unsigned so) { __builtin_amdgcn_raw_buffer_store_b64(v, r, vo + so, 0, 16); }
__device__ __forceinline__ void publish(GAS u64* f, unsigned i, u64 tag) { __hip_atomic_store(f + 16 * i, tag, RLX_AGENT); }
template <int SLP> __device__ __forceinline__ void await(GAS u64* f, unsigned i, u64 tag) {
#pragma unroll 1
  for (int n = 0; n < (1 << 20); ++n) {
    if (__all(__hip_atomic_load(f + 16 * i, RLX_AGENT) == tag)) break;
    __builtin_amdgcn_s_sleep(SLP);
  }
  __builtin_amdgcn_fence(__ATOMIC_ACQUIRE, "wavefront");
}
__device__ __forceinline__ void lds_meet(LAS unsigned* c, unsigned n, int lane) {
  asm volatile("s_waitcnt lgkmcnt(0)" ::: "memory");
  if (lane == 0) __hip_atomic_fetch_add(c, 1u, __ATOMIC_RELAXED, __HIP_MEMORY_SCOPE_WORKGROUP);
#pragma unroll 1
  for (int k = 0; k < (1 << 22); ++k) {
    if (__builtin_amdgcn_readfirstlane(*(volatile LAS unsigned*)c) >= n) break;
    __builtin_amdgcn_s_sleep(4);
  }
  asm volatile("" ::: "memory");
}
__device__ __forceinline__ s16x4 trd(LAS char* p) { return __builtin_amdgcn_ds_read_tr16_b64_v4i16((LAS s16x4*)p); }
__device__ __forceinline__ bf16x8 cat8(s16x4 a, s16x4 b) { return __builtin_bit_cast(bf16x8, __builtin_shufflevector(a, b, 0, 1, 2, 3, 4, 5, 6, 7)); }
template <int C> __device__ __forceinline__ float dppadd(float a) {
  return a + __int_as_float(__builtin_amdgcn_update_dpp(0, __float_as_int(a), C, 0xf, 0xf, true));
}

template <int MODE> struct G1 {
  static constexpr int NM = MODE == 2 ? 2 : 4, NI = MODE == 2 ? 6 : 4, PAD = MODE == 1 ? 32 : 8, NP = NI / 2;
  static constexpr int NMMA = 2 * NM * NI, NRD = 2 * (NM + 2 * NI), HEAD = MODE == 2 ? 6 : 12;
  struct Frag { bf16x8 x[2][NM]; s16x4 w[2][NI][2]; };
  struct Ptr { LAS char* x[2]; LAS char* w[NP]; };
  template <int R> static __device__ __forceinline__ void rd(Frag& f, const Ptr& p) {
    constexpr int u = R / (NRD / 2), r = R % (NRD / 2);
    if constexpr (r >= 2 && r < 2 + NM) f.x[u][r - 2] = *(LAS bf16x8*)(p.x[u] + (r - 2) * 2048);
    else { constexpr int q = r < 2 ? r : r - NM, i = q >> 1;
      f.w[u][i][q & 1] = trd(p.w[i >> 1] + (i & 1) * PAD + u * (8192 + 4 * PAD) + (q & 1) * 1024); }
  }
  template <int N> static __device__ __forceinline__ void mma(f32x4 (&acc)[NM][NI], const Frag& f) {
    constexpr int u = N / (NM * NI), i = (N / NM) % NI, m = N % NM;
    const bf16x8 wf = cat8(f.w[u][i][0], f.w[u][i][1]);
    if constexpr (MODE == 1) asm volatile("v_mfma_f32_16x16x32_bf16 %0, %1, %2, %0" : "+v"(acc[m][i]) : "v"(f.x[u][m]), "v"(wf));
    else asm volatile("v_mfma_f32_16x16x32_bf16 %0, %1, %2, %0" : "+v"(acc[m][i]) : "v"(wf), "v"(f.x[u][m]));
  }
  template <int R0, int R1> static __device__ __forceinline__ void rds(Frag& f, const Ptr& p) { if constexpr (R0 < R1) { rd<R0>(f, p); rds<R0 + 1, R1>(f, p); } }
  template <int N0, int N1> static __device__ __forceinline__ void mmas(f32x4 (&acc)[NM][NI], const Frag& f) { if constexpr (N0 < N1) { mma<N0>(acc, f); mmas<N0 + 1, N1>(acc, f); } }
  template <int N> static __device__ __forceinline__ void mix(f32x4 (&acc)[NM][NI], const Frag& fc, Frag& fn, const Ptr& p) {
    if constexpr (N < NMMA) {
      constexpr int SPAN = NMMA - HEAD - 3, k = N - HEAD;
      constexpr int r0 = k >= SPAN ? NRD : (k * NRD + SPAN - 1) / SPAN, r1 = k + 1 >= SPAN ? NRD : ((k + 1) * NRD + SPAN - 1) / SPAN;
      mma<N>(acc, fc); rds<r0, r1>(fn, p); SB();
      mix<N + 1>(acc, fc, fn, p);
    }
  }
  static __device__ __forceinline__ void loop(f32x4 (&acc)[NM][NI], LAS char* lds, unsigned& sx, unsigned& sw, int w, int lane, int pub, GAS u64* fl,
                                              unsigned fi, u64 tag, int& stepno, char* ws) {
    const int kq = lane >> 4, c16 = lane & 15, a = (lane >> 2) & 3;
    const unsigned xo = ((MODE == 2 ? 32 * w : 64 * (w & 1)) + c16) * 128 + ((kq ^ (c16 >> 1)) << 4);
    const unsigned wb = L_W + kq * (2048 + PAD) + a * 256 + (lane & 3) * (MODE == 1 ? 8 : 16);
    unsigned wo[NP];
#pragma unroll
    for (int j = 0; j < NP; ++j) wo[j] = wb + (((MODE == 2 ? 0 : 2 * (w >> 1)) + j) ^ a) * 64;
    Frag fa, fb; Ptr p;
#define G1_PTRX() do { p.x[0] = lds + sx + xo; p.x[1] = lds + sx + (xo ^ 64); asm volatile("" : "+v"(p.x[0]), "+v"(p.x[1])); } while (0)
#define G1_PTRW() do { _Pragma("unroll") for (int j = 0; j < NP; ++j) { p.w[j] = lds + sw + wo[j]; asm volatile("" : "+v"(p.w[j])); } } while (0)
    G1_PTRX(); G1_PTRW();
    if (MODE == 2) rds<0, NRD>(fa, p);
#pragma unroll
    for (int n = 0; n < NM * NI; ++n) { acc[n % NM][n / NM] = f32x4{}; asm volatile("" : "+v"(acc[n % NM][n / NM])); }
    asm volatile("s_nop 1");
    if (MODE != 2) { BAR_L(); STAMP(12); rds<0, NRD>(fa, p); }
#define G1_STEP(FC, FN, PUB) do { \
      mma<0>(acc, FC); sx = (sx == 4 * XS) ? 0 : sx + XS; asm volatile("" : "+s"(sx)); SB(); \
      mma<1>(acc, FC); sw = (sw == 3 * WSL) ? 0 : sw + WSL; asm volatile("" : "+s"(sw)); SB(); \
      mma<2>(acc, FC); G1_PTRX(); SB(); \
      mma<3>(acc, FC); G1_PTRW(); SB(); \
      mmas<4, HEAD>(acc, FC); SB(); \
      STEPSTAMP(); \
      if (PUB) DRAIN(); \
      BAR_L(); \
      STEPSTAMP(); \
      if ((PUB) && threadIdx.x == 0) publish(fl, fi, tag); \
      SB(); \
      mix<HEAD>(acc, FC, FN, p); } while (0)
    SB();
#pragma unroll 1
    for (int s = 0; s < 5; ++s) {
      G1_STEP(fa, fb, MODE == 2 && s == pub);
      G1_STEP(fb, fa, false);
    }
    asm volatile("s_nop 7");
    SB();
  }
};

struct KV { bf16x8 k1; s16x4 k2; bf16x8 v0, v1; };
__device__ __forceinline__ f32x16 qk_a(bf16x8 k, bf16x8 q) { f32x16 z = {}; return __builtin_amdgcn_mfma_f32_32x32x16_bf16(k, q, z, 0, 0, 0); }
__device__ __forceinline__ f32x16 qk_b(s16x4 k, s16x4 q, f32x16 c) { return __builtin_amdgcn_mfma_f32_32x32x8bf16_1k(k, q, c, 0, 0, 0); }
__device__ __forceinline__ f32x16 pv_lo(bf16x8 v, f32x16 p, f32x16 o, int idx) {
  f32x8 h = __builtin_shufflevector(p, p, 0, 1, 2, 3, 4, 5, 6, 7);
  return __builtin_amdgcn_smfmac_f32_32x32x32_bf16(v, __builtin_bit_cast(bf16x16, h), o, idx, 0, 0); }
__device__ __forceinline__ f32x16 pv_hi(bf16x8 v, f32x16 p, f32x16 o, int idx) {
  f32x8 h = __builtin_shufflevector(p, p, 8, 9, 10, 11, 12, 13, 14, 15);
  return __builtin_amdgcn_smfmac_f32_32x32x32_bf16(v, __builtin_bit_cast(bf16x16, h), o, idx, 0, 0); }
__device__ __forceinline__ float max16(f32x16 s, float m) {
#pragma unroll
  for (int e = 0; e < 16; e += 2) asm volatile("v_max3_f32 %0, %0, %1, %2" : "+v"(m) : "v"(s[e]), "v"(s[e + 1]));
  return m;
}
#define EX4(s, b) do { s[b] = __builtin_amdgcn_exp2f(s[b]); s[b+1] = __builtin_amdgcn_exp2f(s[b+1]); s[b+2] = __builtin_amdgcn_exp2f(s[b+2]); s[b+3] = __builtin_amdgcn_exp2f(s[b+3]); } while (0)
#define STEP(SC, SP, OP, VP, SN, KN, Q1, Q2) do { \
  EX4(SC, 0); SB(); OP = pv_lo(VP.v0, SP, OP, idx); SB(); \
  EX4(SC, 4); SB(); SN = qk_a(KN.k1, Q1); SB(); \
  EX4(SC, 8); SB(); OP = pv_hi(VP.v1, SP, OP, idx); SB(); \
  EX4(SC, 12); SB(); SN = qk_b(KN.k2, Q2, SN); SB(); } while (0)

__global__ void __launch_bounds__(512, 2)
fused(const u16* __restrict__ x, const u16* __restrict__ wqkv, const u16* __restrict__ wo, const u16* __restrict__ w1,
      const u16* __restrict__ b1, const u16* __restrict__ w2, const u16* __restrict__ b2, u16* __restrict__ out, char* __restrict__ ws) {
  __shared__ __attribute__((aligned(16))) char smem[L_END + 256];
  LAS char* lds = (LAS char*)smem;
  const unsigned lds0 = (unsigned)(size_t)lds;
  const int lane = threadIdx.x & 63, w = __builtin_amdgcn_readfirstlane(threadIdx.x >> 6);
  const int b = blockIdx.x, g = b & 7, idx8 = b >> 3;
  asm volatile("" :: "s"(x), "s"(wqkv), "s"(wo), "s"(w1), "s"(b1), "s"(w2), "s"(b2), "s"(out), "s"(ws));
  const int rt = (g & 3) * 4 + (idx8 & 3), ct = (g >> 2) * 8 + (idx8 >> 2);
  const bool vtype = ct >= 10;
  const int kq = lane >> 4, c16 = lane & 15;
  LAS float* st_r = (LAS float*)(lds + L_ST);
  LAS float* st_t = st_r + 128;
  LAS float* st_c = st_r + 256;
  GAS u64* fl = (GAS u64*)(ws + WS_F);
  const rsrc_t rw = mkrs(ws, WS_END), rx = mkrs(x, 2048 * D_ * 2);
  u64 tag;
  f32x4 cs0 = {}, cs1 = {};
  unsigned hsw = 0, hoff = 0, hu = 0;
#define CSUM_LD() LAS char* cp_ = lds + hsw + hoff; LAS char* cq_ = cp_ + hu; \
      const bf16x8 c0_ = cat8(trd(cp_), trd(cp_ + 1024)), c1_ = cat8(trd(cq_), trd(cq_ + 1024)); \
      const bf16x8 c2_ = cat8(trd(cp_ + 32), trd(cp_ + 1056)), c3_ = cat8(trd(cq_ + 32), trd(cq_ + 1056))
#define CSUM_MM() do { const bf16x8 on_ = __builtin_bit_cast(bf16x8, u32x4{0x3f803f80u, 0x3f803f80u, 0x3f803f80u, 0x3f803f80u}); const int si_ = (lane & 1) ? 0xEEEE : 0x4444; SB(); \
      cs0 = __builtin_amdgcn_smfmac_f32_16x16x64_bf16(on_, __builtin_shufflevector(c0_, c1_, 0, 1, 2, 3, 4, 5, 6, 7, 8, 9, 10, 11, 12, 13, 14, 15), cs0, si_, 0, 0); \
      cs1 = __builtin_amdgcn_smfmac_f32_16x16x64_bf16(on_, __builtin_shufflevector(c2_, c3_, 0, 1, 2, 3, 4, 5, 6, 7, 8, 9, 10, 11, 12, 13, 14, 15), cs1, si_, 0, 0); SB(); \
      hsw = (hsw == 3 * WSL) ? 0 : hsw + WSL; } while (0)
#define CSUM_PUT() st_c[(w - 4) * 32 + (lane & 31)] = (lane & 16) ? cs1[0] + cs1[1] : cs0[0] + cs0[1]
  STAMP(0);

  if (w >= 4) {
    const int hw = w - 4;
    rsrc_t rwt = mkrs(wqkv, D_ * 3 * D_ * 2);
    unsigned rs = 3 * D_ * 2, pad = vtype ? 32 : 8;
    unsigned wso = ct * 256 + 16 * hw * rs, xso = (rt * 128 + hw * 32) * (D_ * 2);
    const unsigned xv = (lane >> 3) * (D_ * 2) + (((lane & 7) ^ kq) << 4), xv1 = xv ^ 64;
    const int wc = c16 ^ (kq << 2);
    unsigned wv = ct < 15 ? kq * rs + (wc << 4) : OOB;
    unsigned nx = lds0 + hw * 4096, nw = lds0 + L_W + hw * 4096;
#define DMAX() do { _Pragma("unroll") for (int i = 0; i < 4; ++i) dma16(rx, (i & 1) ? xv1 : xv, xso + i * 8 * (D_ * 2), nx + i * 1024); xso += 128; } while (0)
#define DMAW() do { _Pragma("unroll") for (int i = 0; i < 4; ++i) dma16(rwt, wv, wso + 4 * i * rs, nw + i * 1024 + (2 * hw + (i >> 1)) * pad); wso += 64 * rs; } while (0)
#define DMANEXT() do { nx = (nx >= lds0 + 4 * XS) ? nx - 4 * XS : nx + XS; nw = (nw >= lds0 + L_W + 3 * WSL) ? nw - 3 * WSL : nw + WSL; } while (0)
#pragma unroll
    for (int s = 0; s < 3; ++s) { DMAX(); DMAW(); DMANEXT(); }
    __builtin_amdgcn_s_setprio(1);
    const unsigned hb = L_W + kq * 2048 + ((lane >> 2) & 3) * 256 + (hw ^ ((lane >> 2) & 3)) * 64 + (lane & 3) * 8;
    hoff = hb + kq * pad; hu = 8192 + 4 * pad;
    asm volatile("s_waitcnt vmcnt(16)\n\ts_barrier" ::: "memory");
    tag = uni64(*(LAS u64*)(lds + L_C));
#pragma unroll 1
    for (int s = 0; s < 17; ++s) {
      if (s == 7) { rwt = mkrs(w1, D_ * F_ * 2); rs = F_ * 2; pad = 8; wso = ct * 192 + (320 + 16 * hw) * rs; wv = wc < 12 ? kq * rs + (wc << 4) : OOB; }
      if (s == 12) { wso -= 640 * rs; xso -= 1280; }
      if (s == 5 && hw < 2)
        asm volatile("s_mov_b32 m0, %0\n\ts_nop 0\n\tbuffer_load_dwordx4 %1, %2, %3 offen sc1 lds" :: "s"(lds0 + L_G + hw * 1024), "v"(lane * 16), "s"(rw), "s"(WS_S + (rt * 128 + hw * 64) * 16) : "memory");
      CSUM_LD();
      if (s < 7 || s >= 12) DMAX();
      DMAW(); DMANEXT();
      CSUM_MM();
      if (s == 9) {
        CSUM_PUT();
        cs0 = f32x4{}; cs1 = f32x4{}; hoff = hb + kq * 8; hu = 8192 + 32;
        if (hw < 2) {
          u32x4 gr = *(LAS u32x4*)(lds + L_G + hw * 1024 + lane * 16);
#pragma unroll 1
          for (int n = 0; n < (1 << 20); ++n) {
            if (__all(gr[1] == (unsigned)tag && gr[3] == (unsigned)tag)) break;
            asm volatile("buffer_load_dwordx4 %0, %1, %2, %3 offen sc1\n\ts_waitcnt vmcnt(0)" : "=&v"(gr) : "v"(lane * 16), "s"(rw), "s"(WS_S + (rt * 128 + hw * 64) * 16) : "memory");
          }
          st_r[hw * 64 + lane] = __uint_as_float(gr[0]); st_t[hw * 64 + lane] = __uint_as_float(gr[2]);
        }
      }
      if (s == 7 || s == 12) asm volatile("s_waitcnt vmcnt(12) lgkmcnt(0)\n\ts_barrier" ::: "memory");
      else if (s > 7 && s < 12) asm volatile("s_waitcnt vmcnt(8) lgkmcnt(0)\n\ts_barrier" ::: "memory");
      else asm volatile("s_waitcnt vmcnt(16) lgkmcnt(0)\n\ts_barrier" ::: "memory");
    }
  } else {
    const int wm = w & 1, wn = w >> 1;
    {
      const u64 e0 = __hip_atomic_load(fl, RLX_AGENT);
      const int row = rt * 128 + ct * 8 + 2 * w + (lane >> 5), l5 = lane & 31;
      u32x4 xr[3];
#pragma unroll
      for (int i = 0; i < 3; ++i) xr[i] = __builtin_amdgcn_raw_buffer_load_b128(rx, (i < 2 || l5 < 16) ? row * (D_ * 2) + i * 512 + l5 * 16 : OOB, 0, 0);
      const u16 bias = threadIdx.x < 96 ? b1[ct * 96 + threadIdx.x] : (u16)0;
      *(LAS u32x2*)(lds + L_M + threadIdx.x * 8) = u32x2{0x7fc00000u, 0x7fc00000u};
      tag = uni64(e0 + 1);
      if (threadIdx.x == 0) { *(LAS u64*)(lds + L_C) = tag; *(LAS u32x4*)(lds + L_C + 16) = u32x4{0, 0, 0, 0}; }
      float p = 0.f, q = 0.f;
#pragma unroll
      for (int i = 0; i < 12; ++i) { const float lo = blo(xr[i >> 2][i & 3]), hi = bhi(xr[i >> 2][i & 3]); p += lo + hi; q = fmaf(lo, lo, fmaf(hi, hi, q)); }
      p = dppadd<0x140>(dppadd<0x141>(dppadd<0x4E>(dppadd<0xB1>(p)))); q = dppadd<0x140>(dppadd<0x141>(dppadd<0x4E>(dppadd<0xB1>(q))));
      p = dppadd<0x142>(p); q = dppadd<0x142>(q);
      const float mu = p * (1.f / D_), rstd = __builtin_amdgcn_rsqf(fmaxf(q * (1.f / D_) - mu * mu, 0.f) + 1e-5f);
      st16(u32x4{__float_as_uint(rstd), (unsigned)tag, __float_as_uint(-mu * rstd), (unsigned)tag}, rw, l5 == 16 ? row * 16 : OOB, WS_S);
      if (threadIdx.x < 128) st_r[384 + threadIdx.x] = blo(bias);
    }
    unsigned sx = 0, sw = 0; int stepno = 0;
    {
      f32x4 acc[4][4];
      if (vtype) {
        unsigned vo[4];
#pragma unroll
        for (int i = 0; i < 4; ++i) {
          const int fv = (ct - 10) * 128 + 64 * wn + 16 * i + c16, hd = fv / 20, d = fv - hd * 20;
          vo[i] = ct < 15 ? WS_T + ((hd >> 1) * 64 + 4 * rt + 2 * wm) * PT + P_V + (hd & 1) * 1280 + kq * 320 + d * 16 : OOB;
          asm volatile("" : "+v"(vo[i]));
        }
        STAMP(1);
        G1<1>::loop(acc, lds, sx, sw, w, lane, -1, fl, 0, tag, stepno, ws);
        STAMP(2);
        f32x4 r4[4], t4[4];
#pragma unroll
        for (int m = 0; m < 4; ++m) { r4[m] = *(LAS f32x4*)(st_r + 64 * wm + 16 * m + 4 * kq); t4[m] = *(LAS f32x4*)(st_t + 64 * wm + 16 * m + 4 * kq); }
#pragma unroll
        for (int i = 0; i < 4; ++i) {
          const float c = st_c[64 * wn + 16 * i + c16];
#pragma unroll
          for (int a = 0; a < 2; ++a)
            st16(pk8(acc[2 * a][i] * r4[2 * a] + t4[2 * a] * c, acc[2 * a + 1][i] * r4[2 * a + 1] + t4[2 * a + 1] * c), rw, vo[i], a * PT);
        }
      } else {
        unsigned vo[2], oo[2];
        const unsigned dm = ct < 5 ? 256 : 64;
#pragma unroll
        for (int pp = 0; pp < 2; ++pp) {
          const int c = (ct < 5 ? ct : ct - 5) * 16 + 8 * wn + 4 * pp + kq, pr = c / 5, pos = c - pr * 5;
          const unsigned tb = WS_T + (pr * 64 + 4 * rt + 2 * wm) * PT;
          vo[pp] = tb + pos * 512 + (ct < 5 ? P_Q + c16 * 16 : (8 * (c16 >> 2) + (c16 & 3)) * 16);
          oo[pp] = (ct >= 5 && pos == 2 && c16 < 2) ? tb + P_ONE + c16 * PT : OOB;
          asm volatile("" : "+v"(vo[pp]));
          st16(u32x4{0x3f803f80u, 0x3f803f80u, 0x3f803f80u, 0x3f803f80u}, rw, oo[pp], 0);
        }
        STAMP(1);
        G1<0>::loop(acc, lds, sx, sw, w, lane, -1, fl, 0, tag, stepno, ws);
        STAMP(2);
        const float sc = ct < 5 ? QSCALE : 1.f;
        float r1[4], t1[4];
#pragma unroll
        for (int m = 0; m < 4; ++m) { r1[m] = st_r[64 * wm + 16 * m + c16] * sc; t1[m] = st_t[64 * wm + 16 * m + c16] * sc; }
#pragma unroll
        for (int pp = 0; pp < 2; ++pp) {
          const f32x4 ca = *(LAS f32x4*)(st_c + 64 * wn + 32 * pp + 8 * kq), cb = *(LAS f32x4*)(st_c + 64 * wn + 32 * pp + 8 * kq + 4);
#pragma unroll
          for (int m = 0; m < 4; ++m)
            st16(pk8(acc[m][2 * pp] * r1[m] + ca * t1[m], acc[m][2 * pp + 1] * r1[m] + cb * t1[m]), rw, vo[pp] + (m & 1) * dm, (m >> 1) * PT);
        }
      }
    }
    STAMP(3);
    f32x4 acc[2][6];
    G1<2>::loop(acc, lds, sx, sw, w, lane, 1, fl, F_Q + ct * 16 + rt, tag, stepno, ws);
    STAMP(4);
    __builtin_amdgcn_s_setprio(2);
    const int u = w * 256 + b, slab = u / 10, cg = u - slab * 10;
    if (u < 680) {
      const rsrc_t rs = slab < 48 ? mkrs(w2, F_ * D_ * 2) : mkrs(wo, D_ * D_ * 2);
      const int s2 = slab - 48, l8 = lane >> 3;
      const unsigned r0 = slab < 48 ? slab * 32 : (s2 < 16 ? s2 * 40 : (s2 - 16) * 160 + 16);
#pragma unroll
      for (int i = 0; i < 4; ++i) {
        const int row = slab < 48 ? 8 * i + l8 : (s2 < 16 ? (i >> 1) * 20 + (i & 1) * 8 + l8 : (2 * i + (l8 >> 2)) * 20 + (l8 & 3));
        dma16(rs, row * (D_ * 2) + (lane & 7) * 16, r0 * (D_ * 2) + cg * 128, lds0 + w * 4096 + i * 1024);
      }
    }
    {
      float r1[2], t1[2];
#pragma unroll
      for (int m = 0; m < 2; ++m) { r1[m] = st_r[32 * w + 16 * m + c16]; t1[m] = st_t[32 * w + 16 * m + c16]; }
#pragma unroll
      for (int pp = 0; pp < 3; ++pp) {
        const f32x4 ca = *(LAS f32x4*)(st_c + 32 * pp + 8 * kq), cb = *(LAS f32x4*)(st_c + 32 * pp + 8 * kq + 4);
        const f32x4 ba = *(LAS f32x4*)(st_c + 128 + 32 * pp + 8 * kq), bc = *(LAS f32x4*)(st_c + 128 + 32 * pp + 8 * kq + 4);
#pragma unroll
        for (int m = 0; m < 2; ++m) {
          f32x4 v0 = acc[m][2 * pp] * r1[m] + (ca * t1[m] + ba), v1 = acc[m][2 * pp + 1] * r1[m] + (cb * t1[m] + bc);
#pragma unroll
          for (int j = 0; j < 4; ++j) { v0[j] = fmaxf(v0[j], 0.f); v1[j] = fmaxf(v1[j], 0.f); }
          st16(pk8(v0 * v0, v1 * v1), rw, lane * 16, WS_H + ((8 * rt + 2 * w + m) * 48 + 3 * ct + pp) * 1024);
        }
      }
    }
    if (u < 680) {
      asm volatile("s_waitcnt vmcnt(6)" ::: "memory");
      LAS char* p = lds + w * 4096 + (8 * kq + ((lane >> 2) & 3)) * 128 + (lane & 3) * 8;
#pragma unroll
      for (int nt = 0; nt < 4; ++nt) {
        const int T = 4 * cg + nt, c2 = T / 5, t = T - c2 * 5;
        st16(__builtin_bit_cast(u32x4, cat8(trd(p + nt * 32), trd(p + nt * 32 + 512))), rw, lane * 16, WS_W + ((c2 * 68 + slab) * 5 + t) * 1024);
      }
    }
    STAMP(5);
  }

  const int head = 4 * g + (idx8 >> 3), qc = idx8 & 7;
  if (w >= 4) {
    const int aw = w - 4, qh = aw & 1, kh = aw >> 1, r = lane & 31, h = lane >> 5, odd = head & 1, pr = head >> 1;
    {
      const int c0 = pr * 5 + 2 * odd, e = (lane >> 3) & 1;
      const int tqk = (c0 + 2 * e) >> 4, tv = (head * 20 + 19 * e) >> 7;
      unsigned fi = F_Q + (lane < 16 ? 5 + tqk : 10 + tv) * 16 + 8 * kh + (lane & 7);
      if (lane >= 32) fi = F_Q + tqk * 16 + 2 * qc + qh;
      const u64 f0 = __hip_atomic_load(fl + 16 * fi, RLX_AGENT);
      { CSUM_LD(); CSUM_MM(); }
      asm volatile("s_waitcnt vmcnt(9) lgkmcnt(0)\n\ts_barrier" ::: "memory");
      { CSUM_LD(); CSUM_MM(); }
      DRAIN();
      BAR_L();
      { CSUM_LD(); CSUM_MM(); CSUM_PUT(); }
      BAR_L();
      if (!__all(f0 == tag)) await<2>(fl, fi, tag);
      __builtin_amdgcn_fence(__ATOMIC_ACQUIRE, "wavefront");
    }
    STAMP(6);
    const rsrc_t rk = mkrs(ws + WS_T + (pr * 64 + kh * 32) * PT, 32 * PT);
    const unsigned oK1 = odd * 1536 + lane * 16, oK2 = h ? P_ONE : 1024 + r * 16 + odd * 8;
    const int dv = r < 16 ? (r & 3) + 4 * (r >> 3) + 8 * ((r >> 2) & 1) : r;
    const unsigned oV0 = r < 20 ? P_V + odd * 1280 + h * 320 + dv * 16 : (r == 20 ? P_ONE : OOB), oV1 = r < 20 ? oV0 + 640 : oV0;
#pragma unroll 1
    for (int pass = 0; pass < 2; ++pass) {
      bf16x8 q1[4]; s16x4 q2[4];
#pragma unroll
      for (int t = 0; t < 4; ++t) {
        const unsigned qo = WS_T + (pr * 64 + 8 * qc + 4 * qh + (t ^ (2 * kh))) * PT + P_Q;
        q1[t] = ld16(rw, oK1, qo); q2[t] = ld8(rw, h ? OOB : oK2, qo);
      }
      float mx[4] = {-3.0e38f, -3.0e38f, -3.0e38f, -3.0e38f};
      if (pass) {
#pragma unroll 1
        for (int j = 2; j < 32; ++j) {
          const bf16x8 k1 = ld16(rk, oK1, j * PT); const s16x4 k2 = ld8(rk, oK2, j * PT);
#pragma unroll
          for (int t = 0; t < 4; ++t) {
            const f32x16 sv = qk_b(k2, q2[t], qk_a(k1, q1[t]));
#pragma unroll
            for (int e = 0; e < 16; ++e) mx[t] = fmaxf(mx[t], sv[e]);
          }
        }
      }
      int idx = 0xDDDD; asm volatile("" : "+v"(idx));
      KV b0, b1_, b2_, b3;
      unsigned so = 0;
#define LDK(B, d) do { B.k1 = ld16(rk, oK1, so + (d) * PT); SB(); B.k2 = ld8(rk, oK2, so + (d) * PT); SB(); } while (0)
#define LDV(B, d) do { B.v0 = ld16(rk, oV0, so + (d) * PT); SB(); B.v1 = ld16(rk, oV1, so + (d) * PT); SB(); } while (0)
      SB();
      LDK(b0, 0); LDV(b0, 0); LDK(b1_, 1); LDV(b1_, 1); LDK(b2_, 2); LDV(b2_, 2); LDK(b3, 3);

      {
        f32x16 sm[8];
#define SMP(n) do { sm[n] = (n) < 4 ? qk_b(b0.k2, q2[(n) & 3], qk_a(b0.k1, q1[(n) & 3])) : qk_b(b1_.k2, q2[(n) & 3], qk_a(b1_.k1, q1[(n) & 3])); SB(); } while (0)
        SMP(0); SMP(1);
        asm volatile("s_nop 7"); SB();
#pragma unroll
        for (int n = 0; n < 8; ++n) { if (n < 6) SMP(n + 2); mx[n & 3] = max16(sm[n], mx[n & 3]); SB(); }
      }
#pragma unroll
      for (int t = 0; t < 4; ++t) {
        auto sw = __builtin_amdgcn_permlane32_swap(__float_as_uint(mx[t]), __float_as_uint(mx[t]), false, false);
        mx[t] = fmaxf(__uint_as_float(sw[0]), __uint_as_float(sw[1]));
      }
      volatile LAS f32x4* mp = (volatile LAS f32x4*)(lds + L_M);
      mp[aw * 32 + r] = f32x4{mx[0], mx[1], mx[2], mx[3]};
      f32x4 mo;
#pragma unroll 1
      for (int n = 0; n < (1 << 22); ++n) { mo = mp[(aw ^ 2) * 32 + r]; if (__all(mo[0] == mo[0] && mo[1] == mo[1] && mo[2] == mo[2] && mo[3] == mo[3])) break; }
#pragma unroll
      for (int t = 0; t < 4; ++t) {
        const float nm = -(fmaxf(mx[t], mo[t ^ 2]) + (pass ? 0.f : MARGIN));
        const float a0 = bhi(__float_as_uint(nm)), r0 = nm - a0, a1 = bhi(__float_as_uint(r0)), a2 = r0 - a1;
        const u32x2 pm = {(__float_as_uint(a0) >> 16) | __float_as_uint(a1), __float_as_uint(a2) >> 16};
        if (h) q2[t] = __builtin_bit_cast(s16x4, pm);
      }
      f32x16 o0 = {}, o1 = {}, o2 = {}, o3 = {}, s0, s1, s2, s3 = {};
      b3.v0 = bf16x8{}; b3.v1 = bf16x8{};
      s0 = qk_b(b0.k2, q2[0], qk_a(b0.k1, q1[0])); s1 = qk_b(b0.k2, q2[1], qk_a(b0.k1, q1[1]));
      SB(); STAMP(7); SB();
#define KT(C, P, N, d) do { \
  STEP(s0, s3, o3, P, s2, C, q1[2], q2[2]); LDV(P, d + 3); \
  STEP(s1, s0, o0, C, s3, C, q1[3], q2[3]); LDK(C, d + 4); \
  STEP(s2, s1, o1, C, s0, N, q1[0], q2[0]); \
  STEP(s3, s2, o2, C, s1, N, q1[1], q2[1]); } while (0)
      for (int it = 0; it < 8; ++it) {
        KT(b0, b3, b1_, 0); KT(b1_, b0, b2_, 1); KT(b2_, b1_, b3, 2); KT(b3, b2_, b0, 3);
        so += 4 * PT;
      }
      SB(); STAMP(8); SB();
      o3 = pv_lo(b3.v0, s3, o3, idx); o3 = pv_hi(b3.v1, s3, o3, idx);
      LAS char* sp = lds + L_MRG + aw * 6144 + lane * 16;
#pragma unroll
      for (int e = 0; e < 3; ++e) {
        *(LAS f32x4*)(sp + e * 1024) = f32x4{o0[4 * e], o0[4 * e + 1], o0[4 * e + 2], o0[4 * e + 3]};
        *(LAS f32x4*)(sp + 3072 + e * 1024) = f32x4{o1[4 * e], o1[4 * e + 1], o1[4 * e + 2], o1[4 * e + 3]};
      }
      BAR_L();
      LAS char* gp = lds + L_MRG + (aw ^ 2) * 6144 + lane * 16;
      float chk = 0.f;
#pragma unroll
      for (int z = 0; z < 2; ++z) {
        const f32x16 k = z ? o3 : o2;
        f32x4 v[3];
#pragma unroll
        for (int e = 0; e < 3; ++e) v[e] = f32x4{k[4 * e], k[4 * e + 1], k[4 * e + 2], k[4 * e + 3]} + *(LAS f32x4*)(gp + z * 3072 + e * 1024);
        const float l = __uint_as_float(__builtin_amdgcn_permlane32_swap(__float_as_uint(v[2][0]), __float_as_uint(v[2][0]), false, false)[1]);
        const float inv = __builtin_amdgcn_rcpf(l);
        const int tt = (8 * qc + 4 * qh + ((2 + z) ^ (2 * kh))) * 2 + (r >> 4);
        st16(pk8(v[0] * inv, v[1] * inv), rw, (tt * 16 + pr) * 1024 + ((2 * odd + h) * 16 + c16) * 16, WS_O);
        st8(u32x2{pk2(v[2][0] * inv, v[2][1] * inv), pk2(v[2][2] * inv, v[2][3] * inv)}, rw, h ? OOB : (tt * 32 + head) * 128 + c16 * 8, WS_O2);
#pragma unroll
        for (int e = 0; e < 3; ++e) chk += (fabsf(v[e][0]) + fabsf(v[e][1])) + (fabsf(v[e][2]) + fabsf(v[e][3]));
      }
      mp[aw * 32 + r] = f32x4{__builtin_nanf(""), __builtin_nanf(""), __builtin_nanf(""), __builtin_nanf("")};
      if (!(chk < 1.0e37f)) *(volatile LAS unsigned*)(lds + L_C + 24) = 1;
      DRAIN();
      BAR_L();
      STAMP(9);
      if (pass || __builtin_amdgcn_readfirstlane(*(volatile LAS unsigned*)(lds + L_C + 24)) == 0) break;
    }
    if (threadIdx.x == 256) publish(fl, F_O + qc * 32 + head, tag);
    if (b == 0) {
      await<8>(fl, F_O + aw * 64 + lane, tag);
      lds_meet((LAS unsigned*)(lds + L_C + 28), 4, lane);
      if (threadIdx.x == 256) publish(fl, 0, tag);
    }
  } else {
    const int rt2 = 4 * g + (idx8 & 3), ct2 = idx8 >> 2;
    {
      DRAIN();
      unsigned n = 0;
      if (lane == 0) n = __hip_atomic_fetch_add((LAS unsigned*)(lds + L_C + 16), 1u, __ATOMIC_RELAXED, __HIP_MEMORY_SCOPE_WORKGROUP);
      if (lane == 0 && n == 3) publish(fl, F_H + b, tag);
    }
    const int o = (64 * rt2 + 16 * w + c16) * D_ + 80 * ct2 + 4 * kq;
    u32x2 xv[5], bv[5];
#pragma unroll
    for (int t = 0; t < 5; ++t) { xv[t] = *(const u32x2*)(x + o + 16 * t); bv[t] = *(const u32x2*)(b2 + 80 * ct2 + 4 * kq + 16 * t); }
    __builtin_amdgcn_s_sleep(16);
    await<16>(fl, F_H + 64 * w + lane, tag);
    lds_meet((LAS unsigned*)(lds + L_C + 20), 4, lane);
    STAMP(6);
    f32x4 y[5];
    {
      f32x4 acc[4][5] = {};
      bf16x8 a0[4], w0[5], a1[4], w1_[5];
      const unsigned vo = lane * 16, ao = WS_H + (4 * rt2 * 48 + 12 * w) * 1024, wo_ = WS_W + (ct2 * 68 + 12 * w) * 5120;
#define GLD(A, W, s) do { _Pragma("unroll") for (int m = 0; m < 4; ++m) A[m] = ld16(rw, vo, ao + (m * 48 + (s)) * 1024); \
                          _Pragma("unroll") for (int t = 0; t < 5; ++t) W[t] = ld16(rw, vo, wo_ + ((s) * 5 + t) * 1024); } while (0)
#define GMM(A, W) do { __builtin_amdgcn_s_setprio(0); _Pragma("unroll") for (int t = 0; t < 5; ++t) _Pragma("unroll") for (int m = 0; m < 4; ++m) \
                         acc[m][t] = __builtin_amdgcn_mfma_f32_16x16x32_bf16(W[t], A[m], acc[m][t], 0, 0, 0); SB(); __builtin_amdgcn_s_setprio(2); } while (0)
      GLD(a0, w0, 0); SB();
#pragma unroll 1
      for (int s = 0; s < 12; s += 2) {
        GLD(a1, w1_, s + 1); SB(); GMM(a0, w0); SB();
        const int s2 = min(s + 2, 11);
        GLD(a0, w0, s2); SB(); GMM(a1, w1_); SB();
      }
      STAMP(7);
#pragma unroll
      for (int m = 0; m < 4; ++m)
#pragma unroll
        for (int t = 0; t < 5; ++t) *(LAS f32x4*)(lds + L_RED + ((m * 4 + w) * 5 + t) * 1024 + lane * 16) = acc[m][t];
      lds_meet((LAS unsigned*)(lds + L_C + 20), 8, lane);
#pragma unroll
      for (int t = 0; t < 5; ++t) {
        LAS char* p = lds + L_RED + (w * 20 + t) * 1024 + lane * 16;
        y[t] = (*(LAS f32x4*)p + *(LAS f32x4*)(p + 5120)) + (*(LAS f32x4*)(p + 10240) + *(LAS f32x4*)(p + 15360));
        y[t] += f32x4{blo(xv[t][0]) + blo(bv[t][0]), bhi(xv[t][0]) + bhi(bv[t][0]), blo(xv[t][1]) + blo(bv[t][1]), bhi(xv[t][1]) + bhi(bv[t][1])};
      }
      bf16x8 wf[25];
#pragma unroll
      for (int i = 0; i < 25; ++i) wf[i] = ld16(rw, vo, WS_W + ((ct2 * 68 + 48) * 5 + w * 25 + i) * 1024);
      lds_meet((LAS unsigned*)(lds + L_C + 20), 12, lane);
#pragma unroll
      for (int i = 0; i < 25; ++i) *(LAS bf16x8*)(lds + L_WO + (w * 25 + i) * 1024 + lane * 16) = wf[i];
    }
    STAMP(8);
    for (int pass = 0; pass < 2; ++pass) {
      BAR_L(); BAR_L();
      if (pass || __builtin_amdgcn_readfirstlane(*(volatile LAS unsigned*)(lds + L_C + 24)) == 0) break;
    }
    STAMP(9);
    bf16x8 wr[30], a[20];
    LAS char* wp = lds + L_WO + lane * 16; LAS char* wp2 = wp + 61440;
#pragma unroll
    for (int t = 0; t < 5; ++t) asm volatile("" : "+v"(y[t]));
#pragma unroll
    for (int i = 0; i < 30; ++i) wr[i] = *(LAS bf16x8*)(wp + i * 1024);
    SB();
    await<1>(fl, F_O + g * 32 + (lane & 31), tag);
    STAMP(10);
    const int tt = 4 * rt2 + w;
#pragma unroll
    for (int s = 0; s < 16; ++s) a[s] = ld16(rw, lane * 16 + (s & 3) * 1024, WS_O + (tt * 16 + (s & 12)) * 1024);
#pragma unroll
    for (int s = 0; s < 4; ++s) a[16 + s] = cat8(ld8(rw, kq * 256 + c16 * 8 + s * 1024, WS_O2 + tt * 4096), ld8(rw, kq * 256 + 128 + c16 * 8 + s * 1024, WS_O2 + tt * 4096));
    SB();
#pragma unroll
    for (int i = 0; i < 100; ++i) {
      y[i % 5] = __builtin_amdgcn_mfma_f32_16x16x32_bf16(wr[i % 30], a[i / 5], y[i % 5], 0, 0, 0);
      if (i + 30 < 100) wr[i % 30] = *(LAS bf16x8*)(i + 30 < 60 ? wp + (i + 30) * 1024 : wp2 + (i - 30) * 1024);
      SB();
    }
#pragma unroll
    for (int t = 0; t < 5; ++t) *(u32x2*)(out + o + 16 * t) = u32x2{pk2(y[t][0], y[t][1]), pk2(y[t][2], y[t][3])};
    STAMP(11);
  }
}

extern "C" void kernel_launch(void* const* d_in, const int* in_sizes, int n_in, void* d_out, int out_size, void* d_ws, size_t ws_size,
                              hipStream_t stream) {
  fused<<<256, 512, 0, stream>>>((const u16*)d_in[0], (const u16*)d_in[1], (const u16*)d_in[2], (const u16*)d_in[4], (const u16*)d_in[5],
                                 (const u16*)d_in[6], (const u16*)d_in[7], (u16*)d_out, (char*)d_ws);
}
```

```cpp
#define PF_CODE
#define PF_DATA
#define PF_TLB
#include <hip/hip_runtime.h>
#include <hip/hip_bf16.h>

typedef __attribute__((ext_vector_type(8))) __bf16 bf16x8;
typedef __attribute__((ext_vector_type(16))) __bf16 bf16x16;
typedef __attribute__((ext_vector_type(2))) __bf16 bf16x2;
typedef __attribute__((ext_vector_type(4))) short s16x4;
typedef __attribute__((ext_vector_type(16))) float f32x16;
typedef __attribute__((ext_vector_type(8))) float f32x8;
typedef __attribute__((ext_vector_type(4))) float f32x4;
typedef __attribute__((ext_vector_type(4))) unsigned u32x4;
typedef __attribute__((ext_vector_type(2))) unsigned u32x2;
typedef unsigned short u16;
typedef unsigned long long u64;
typedef __amdgpu_buffer_rsrc_t rsrc_t;
#define LAS __attribute__((address_space(3)))
#define GAS __attribute__((address_space(1)))
#define SB() __builtin_amdgcn_sched_barrier(0)
#define RLX_AGENT __ATOMIC_RELAXED, __HIP_MEMORY_SCOPE_AGENT
#define BAR_L() do { asm volatile("" ::: "memory"); __builtin_amdgcn_s_waitcnt(0xC07F); asm volatile("s_barrier" ::: "memory"); } while (0)
#define DRAIN() asm volatile("s_waitcnt vmcnt(0)" ::: "memory")

constexpr int D_ = 640, F_ = 1536;
constexpr unsigned PT = 7808, P_V = 2560, P_Q = 5120, P_ONE = 7680;
constexpr unsigned WS_T = 0;
constexpr unsigned WS_H = 8u << 20;
constexpr unsigned WS_W = 16u << 20;
constexpr unsigned WS_O = 20u << 20;
constexpr unsigned WS_O2 = 22u << 20;
constexpr unsigned WS_S = 23u << 20;
constexpr unsigned WS_F = 24u << 20;
constexpr unsigned WS_END = WS_F;
constexpr unsigned F_Q = 256, F_H = 512, F_O = 768;
constexpr float QSCALE = 0.22360679774997896f * 1.4426950408889634f;
constexpr float MARGIN = 88.f;
constexpr unsigned OOB = 0x80000000u;
constexpr unsigned XS = 16384, WSL = 16640, L_W = 5 * XS;
constexpr unsigned L_WO = 0, L_RED = 0, L_MRG = 102400;
constexpr unsigned L_ST = L_W + 4 * WSL;
constexpr unsigned L_G = L_ST + 2048;
constexpr unsigned L_M = L_G + 2048;
constexpr unsigned L_C = L_M + 2048;
constexpr unsigned L_PF = L_C + 32;
constexpr unsigned L_END = L_PF + 256;

#define STAMP(i)
#define STEPSTAMP()

__device__ __forceinline__ unsigned pk2(float a, float b) { bf16x2 r = {(__bf16)a, (__bf16)b}; return __builtin_bit_cast(unsigned, r); }
__device__ __forceinline__ u32x4 pk8(f32x4 a, f32x4 b) { return u32x4{pk2(a[0], a[1]), pk2(a[2], a[3]), pk2(b[0], b[1]), pk2(b[2], b[3])}; }
__device__ __forceinline__ float blo(unsigned u) { return __uint_as_float(u << 16); }
__device__ __forceinline__ float bhi(unsigned u) { return __uint_as_float(u & 0xffff0000u); }
__device__ __forceinline__ u64 uni64(u64 v) {
  return ((u64)(unsigned)__builtin_amdgcn_readfirstlane((unsigned)(v >> 32)) << 32) | (unsigned)__builtin_amdgcn_readfirstlane((unsigned)v);
}
__device__ __forceinline__ rsrc_t mkrs(const void* p, unsigned bytes) { return __builtin_amdgcn_make_buffer_rsrc((void*)p, 0, bytes, 0x00020000); }
__device__ __forceinline__ void dma16(rsrc_t r, unsigned vo, unsigned so, unsigned lds_base) {
  asm volatile("s_mov_b32 m0, %0\n\ts_nop 0\n\tbuffer_load_dwordx4 %1, %2, %3 offen lds" :: "s"(lds_base), "v"(vo), "s"(r), "s"(so) : "memory");
}
__device__ __forceinline__ void touch(rsrc_t r, unsigned vo, unsigned lds_base) {
  asm volatile("s_mov_b32 m0, %0\n\ts_nop 0\n\tbuffer_load_dword %1, %2, 0 offen lds" :: "s"(lds_base), "v"(vo), "s"(r) : "memory");
}
__device__ __forceinline__ bf16x8 ld16(rsrc_t r, unsigned vo, unsigned so) { return __builtin_bit_cast(bf16x8, __builtin_amdgcn_raw_buffer_load_b128(r, vo, so, 16)); }
__device__ __forceinline__ s16x4 ld8(rsrc_t r, unsigned vo, unsigned so) { return __builtin_bit_cast(s16x4, __builtin_amdgcn_raw_buffer_load_b64(r, vo, so, 16)); }
__device__ __forceinline__ void st16(u32x4 v, rsrc_t r, unsigned vo, unsigned so) { __builtin_amdgcn_raw_buffer_store_b128(v, r, vo + so, 0, 16); }
__device__ __forceinline__ void st8(u32x2 v, rsrc_t r, unsigned vo, unsigned so) { __builtin_amdgcn_raw_buffer_store_b64(v, r, vo + so, 0, 16); }
__device__ __forceinline__ void publish(GAS u64* f, unsigned i, u64 tag) { __hip_atomic_store(f + 16 * i, tag, RLX_AGENT); }
template <int SLP> __device__ __forceinline__ void await(GAS u64* f, unsigned i, u64 tag) {
#pragma unroll 1
  for (int n = 0; n < (1 << 20); ++n) {
    if (__all(__hip_atomic_load(f + 16 * i, RLX_AGENT) == tag)) break;
    __builtin_amdgcn_s_sleep(SLP);
  }
  __builtin_amdgcn_fence(__ATOMIC_ACQUIRE, "wavefront");
}
__device__ __forceinline__ void lds_meet(LAS unsigned* c, unsigned n, int lane) {
  asm volatile("s_waitcnt lgkmcnt(0)" ::: "memory");
  if (lane == 0) __hip_atomic_fetch_add(c, 1u, __ATOMIC_RELAXED, __HIP_MEMORY_SCOPE_WORKGROUP);
#pragma unroll 1
  for (int k = 0; k < (1 << 22); ++k) {
    if (__builtin_amdgcn_readfirstlane(*(volatile LAS unsigned*)c) >= n) break;
    __builtin_amdgcn_s_sleep(4);
  }
  asm volatile("" ::: "memory");
}
__device__ __forceinline__ s16x4 trd(LAS char* p) { return __builtin_amdgcn_ds_read_tr16_b64_v4i16((LAS s16x4*)p); }
__device__ __forceinline__ bf16x8 cat8(s16x4 a, s16x4 b) { return __builtin_bit_cast(bf16x8, __builtin_shufflevector(a, b, 0, 1, 2, 3, 4, 5, 6, 7)); }
template <int C> __device__ __forceinline__ float dppadd(float a) {
  return a + __int_as_float(__builtin_amdgcn_update_dpp(0, __float_as_int(a), C, 0xf, 0xf, true));
}

template <int MODE> struct G1 {
  static constexpr int NM = MODE == 2 ? 2 : 4, NI = MODE == 2 ? 6 : 4, PAD = MODE == 1 ? 32 : 8, NP = NI / 2;
  static constexpr int NMMA = 2 * NM * NI, NRD = 2 * (NM + 2 * NI), HEAD = MODE == 2 ? 6 : 12;
  struct Frag { bf16x8 x[2][NM]; s16x4 w[2][NI][2]; };
  struct Ptr { LAS char* x[2]; LAS char* w[NP]; };
  template <int R> static __device__ __forceinline__ void rd(Frag& f, const Ptr& p) {
    constexpr int u = R / (NRD / 2), r = R % (NRD / 2);
    if constexpr (r >= 2 && r < 2 + NM) f.x[u][r - 2] = *(LAS bf16x8*)(p.x[u] + (r - 2) * 2048);
    else { constexpr int q = r < 2 ? r : r - NM, i = q >> 1;
      f.w[u][i][q & 1] = trd(p.w[i >> 1] + (i & 1) * PAD + u * (8192 + 4 * PAD) + (q & 1) * 1024); }
  }
  template <int N> static __device__ __forceinline__ void mma(f32x4 (&acc)[NM][NI], const Frag& f) {
    constexpr int u = N / (NM * NI), i = (N / NM) % NI, m = N % NM;
    const bf16x8 wf = cat8(f.w[u][i][0], f.w[u][i][1]);
    if constexpr (MODE == 1) asm volatile("v_mfma_f32_16x16x32_bf16 %0, %1, %2, %0" : "+v"(acc[m][i]) : "v"(f.x[u][m]), "v"(wf));
    else asm volatile("v_mfma_f32_16x16x32_bf16 %0, %1, %2, %0" : "+v"(acc[m][i]) : "v"(wf), "v"(f.x[u][m]));
  }
  template <int R0, int R1> static __device__ __forceinline__ void rds(Frag& f, const Ptr& p) { if constexpr (R0 < R1) { rd<R0>(f, p); rds<R0 + 1, R1>(f, p); } }
  template <int N0, int N1> static __device__ __forceinline__ void mmas(f32x4 (&acc)[NM][NI], const Frag& f) { if constexpr (N0 < N1) { mma<N0>(acc, f); mmas<N0 + 1, N1>(acc, f); } }
  template <int N> static __device__ __forceinline__ void mix(f32x4 (&acc)[NM][NI], const Frag& fc, Frag& fn, const Ptr& p) {
    if constexpr (N < NMMA) {
      constexpr int SPAN = NMMA - HEAD - 3, k = N - HEAD;
      constexpr int r0 = k >= SPAN ? NRD : (k * NRD + SPAN - 1) / SPAN, r1 = k + 1 >= SPAN ? NRD : ((k + 1) * NRD + SPAN - 1) / SPAN;
      mma<N>(acc, fc); rds<r0, r1>(fn, p); SB();
      mix<N + 1>(acc, fc, fn, p);
    }
  }
  static __device__ __forceinline__ void loop(f32x4 (&acc)[NM][NI], LAS char* lds, unsigned& sx, unsigned& sw, int w, int lane, int pub, GAS u64* fl,
                                              unsigned fi, u64 tag, int& stepno, char* ws) {
    const int kq = lane >> 4, c16 = lane & 15, a = (lane >> 2) & 3;
    const unsigned xo = ((MODE == 2 ? 32 * w : 64 * (w & 1)) + c16) * 128 + ((kq ^ (c16 >> 1)) << 4);
    const unsigned wb = L_W + kq * (2048 + PAD) + a * 256 + (lane & 3) * (MODE == 1 ? 8 : 16);
    unsigned wo[NP];
#pragma unroll
    for (int j = 0; j < NP; ++j) wo[j] = wb + (((MODE == 2 ? 0 : 2 * (w >> 1)) + j) ^ a) * 64;
    Frag fa, fb; Ptr p;
#define G1_PTRX() do { p.x[0] = lds + sx + xo; p.x[1] = lds + sx + (xo ^ 64); asm volatile("" : "+v"(p.x[0]), "+v"(p.x[1])); } while (0)
#define G1_PTRW() do { _Pragma("unroll") for (int j = 0; j < NP; ++j) { p.w[j] = lds + sw + wo[j]; asm volatile("" : "+v"(p.w[j])); } } while (0)
    G1_PTRX(); G1_PTRW();
    if (MODE == 2) rds<0, NRD>(fa, p);
#pragma unroll
    for (int n = 0; n < NM * NI; ++n) { acc[n % NM][n / NM] = f32x4{}; asm volatile("" : "+v"(acc[n % NM][n / NM])); }
    asm volatile("s_nop 1");
    if (MODE != 2) { BAR_L(); STAMP(12); rds<0, NRD>(fa, p); }
#define G1_STEP(FC, FN, PUB) do { \
      mma<0>(acc, FC); sx = (sx == 4 * XS) ? 0 : sx + XS; asm volatile("" : "+s"(sx)); SB(); \
      mma<1>(acc, FC); sw = (sw == 3 * WSL) ? 0 : sw + WSL; asm volatile("" : "+s"(sw)); SB(); \
      mma<2>(acc, FC); G1_PTRX(); SB(); \
      mma<3>(acc, FC); G1_PTRW(); SB(); \
      mmas<4, HEAD>(acc, FC); SB(); \
      STEPSTAMP(); \
      if (PUB) DRAIN(); \
      BAR_L(); \
      STEPSTAMP(); \
      if ((PUB) && threadIdx.x == 0) publish(fl, fi, tag); \
      SB(); \
      mix<HEAD>(acc, FC, FN, p); } while (0)
    SB();
#pragma unroll 1
    for (int s = 0; s < 5; ++s) {
      G1_STEP(fa, fb, MODE == 2 && s == pub);
      G1_STEP(fb, fa, false);
    }
    asm volatile("s_nop 7");
    SB();
  }
};

struct KV { bf16x8 k1; s16x4 k2; bf16x8 v0, v1; };
__device__ __forceinline__ f32x16 qk_a(bf16x8 k, bf16x8 q) { f32x16 z = {}; return __builtin_amdgcn_mfma_f32_32x32x16_bf16(k, q, z, 0, 0, 0); }
__device__ __forceinline__ f32x16 qk_b(s16x4 k, s16x4 q, f32x16 c) { return __builtin_amdgcn_mfma_f32_32x32x8bf16_1k(k, q, c, 0, 0, 0); }
__device__ __forceinline__ f32x16 pv_lo(bf16x8 v, f32x16 p, f32x16 o, int idx) {
  f32x8 h = __builtin_shufflevector(p, p, 0, 1, 2, 3, 4, 5, 6, 7);
  return __builtin_amdgcn_smfmac_f32_32x32x32_bf16(v, __builtin_bit_cast(bf16x16, h), o, idx, 0, 0); }
__device__ __forceinline__ f32x16 pv_hi(bf16x8 v, f32x16 p, f32x16 o, int idx) {
  f32x8 h = __builtin_shufflevector(p, p, 8, 9, 10, 11, 12, 13, 14, 15);
  return __builtin_amdgcn_smfmac_f32_32x32x32_bf16(v, __builtin_bit_cast(bf16x16, h), o, idx, 0, 0); }
__device__ __forceinline__ float max16(f32x16 s, float m) {
#pragma unroll
  for (int e = 0; e < 16; e += 2) asm volatile("v_max3_f32 %0, %0, %1, %2" : "+v"(m) : "v"(s[e]), "v"(s[e + 1]));
  return m;
}
#define EX4(s, b) do { s[b] = __builtin_amdgcn_exp2f(s[b]); s[b+1] = __builtin_amdgcn_exp2f(s[b+1]); s[b+2] = __builtin_amdgcn_exp2f(s[b+2]); s[b+3] = __builtin_amdgcn_exp2f(s[b+3]); } while (0)
#define STEP(SC, SP, OP, VP, SN, KN, Q1, Q2) do { \
  EX4(SC, 0); SB(); OP = pv_lo(VP.v0, SP, OP, idx); SB(); \
  EX4(SC, 4); SB(); SN = qk_a(KN.k1, Q1); SB(); \
  EX4(SC, 8); SB(); OP = pv_hi(VP.v1, SP, OP, idx); SB(); \
  EX4(SC, 12); SB(); SN = qk_b(KN.k2, Q2, SN); SB(); } while (0)

__global__ void __launch_bounds__(512, 2)
fused(const u16* __restrict__ x, const u16* __restrict__ wqkv, const u16* __restrict__ wo, const u16* __restrict__ w1,
      const u16* __restrict__ b1, const u16* __restrict__ w2, const u16* __restrict__ b2, u16* __restrict__ out, char* __restrict__ ws) {
  __shared__ __attribute__((aligned(16))) char smem[L_END + 256];
  LAS char* lds = (LAS char*)smem;
  const unsigned lds0 = (unsigned)(size_t)lds;
  const int lane = threadIdx.x & 63, w = __builtin_amdgcn_readfirstlane(threadIdx.x >> 6);
  const int b = blockIdx.x, g = b & 7, idx8 = b >> 3;
  asm volatile("" :: "s"(x), "s"(wqkv), "s"(wo), "s"(w1), "s"(b1), "s"(w2), "s"(b2), "s"(out), "s"(ws));
  const int rt = (g & 3) * 4 + (idx8 & 3), ct = (g >> 2) * 8 + (idx8 >> 2);
  const bool vtype = ct >= 10;
  const int kq = lane >> 4, c16 = lane & 15;
  LAS float* st_r = (LAS float*)(lds + L_ST);
  LAS float* st_t = st_r + 128;
  LAS float* st_c = st_r + 256;
  GAS u64* fl = (GAS u64*)(ws + WS_F);
  const rsrc_t rw = mkrs(ws, WS_END), rx = mkrs(x, 2048 * D_ * 2);
  u64 tag;
  f32x4 cs0 = {}, cs1 = {};
  unsigned hsw = 0, hoff = 0, hu = 0;
#define CSUM_LD() LAS char* cp_ = lds + hsw + hoff; LAS char* cq_ = cp_ + hu; \
      const bf16x8 c0_ = cat8(trd(cp_), trd(cp_ + 1024)), c1_ = cat8(trd(cq_), trd(cq_ + 1024)); \
      const bf16x8 c2_ = cat8(trd(cp_ + 32), trd(cp_ + 1056)), c3_ = cat8(trd(cq_ + 32), trd(cq_ + 1056))
#define CSUM_MM() do { const bf16x8 on_ = __builtin_bit_cast(bf16x8, u32x4{0x3f803f80u, 0x3f803f80u, 0x3f803f80u, 0x3f803f80u}); const int si_ = (lane & 1) ? 0xEEEE : 0x4444; SB(); \
      cs0 = __builtin_amdgcn_smfmac_f32_16x16x64_bf16(on_, __builtin_shufflevector(c0_, c1_, 0, 1, 2, 3, 4, 5, 6, 7, 8, 9, 10, 11, 12, 13, 14, 15), cs0, si_, 0, 0); \
      cs1 = __builtin_amdgcn_smfmac_f32_16x16x64_bf16(on_, __builtin_shufflevector(c2_, c3_, 0, 1, 2, 3, 4, 5, 6, 7, 8, 9, 10, 11, 12, 13, 14, 15), cs1, si_, 0, 0); SB(); \
      hsw = (hsw == 3 * WSL) ? 0 : hsw + WSL; } while (0)
#define CSUM_PUT() st_c[(w - 4) * 32 + (lane & 31)] = (lane & 16) ? cs1[0] + cs1[1] : cs0[0] + cs0[1]
  STAMP(0);

  if (w >= 4) {
    const int hw = w - 4;
    rsrc_t rwt = mkrs(wqkv, D_ * 3 * D_ * 2);
    unsigned rs = 3 * D_ * 2, pad = vtype ? 32 : 8;
    unsigned wso = ct * 256 + 16 * hw * rs, xso = (rt * 128 + hw * 32) * (D_ * 2);
    const unsigned xv = (lane >> 3) * (D_ * 2) + (((lane & 7) ^ kq) << 4), xv1 = xv ^ 64;
    const int wc = c16 ^ (kq << 2);
    unsigned wv = ct < 15 ? kq * rs + (wc << 4) : OOB;
    unsigned nx = lds0 + hw * 4096, nw = lds0 + L_W + hw * 4096;
#define DMAX() do { _Pragma("unroll") for (int i = 0; i < 4; ++i) dma16(rx, (i & 1) ? xv1 : xv, xso + i * 8 * (D_ * 2), nx + i * 1024); xso += 128; } while (0)
#define DMAW() do { _Pragma("unroll") for (int i = 0; i < 4; ++i) dma16(rwt, wv, wso + 4 * i * rs, nw + i * 1024 + (2 * hw + (i >> 1)) * pad); wso += 64 * rs; } while (0)
#define DMANEXT() do { nx = (nx >= lds0 + 4 * XS) ? nx - 4 * XS : nx + XS; nw = (nw >= lds0 + L_W + 3 * WSL) ? nw - 3 * WSL : nw + WSL; } while (0)
#pragma unroll
    for (int s = 0; s < 3; ++s) { DMAX(); DMAW(); DMANEXT(); }
    __builtin_amdgcn_s_setprio(1);
    const unsigned hb = L_W + kq * 2048 + ((lane >> 2) & 3) * 256 + (hw ^ ((lane >> 2) & 3)) * 64 + (lane & 3) * 8;
    hoff = hb + kq * pad; hu = 8192 + 4 * pad;
    asm volatile("s_waitcnt vmcnt(16)\n\ts_barrier" ::: "memory");
    tag = uni64(*(LAS u64*)(lds + L_C));
#pragma unroll 1
    for (int s = 0; s < 17; ++s) {
      if (s == 7) { rwt = mkrs(w1, D_ * F_ * 2); rs = F_ * 2; pad = 8; wso = ct * 192 + (320 + 16 * hw) * rs; wv = wc < 12 ? kq * rs + (wc << 4) : OOB; }
      if (s == 12) { wso -= 640 * rs; xso -= 1280; }
      if (s == 5 && hw < 2)
        asm volatile("s_mov_b32 m0, %0\n\ts_nop 0\n\tbuffer_load_dwordx4 %1, %2, %3 offen sc1 lds" :: "s"(lds0 + L_G + hw * 1024), "v"(lane * 16), "s"(rw), "s"(WS_S + (rt * 128 + hw * 64) * 16) : "memory");
      CSUM_LD();
      if (s < 7 || s >= 12) DMAX();
      DMAW(); DMANEXT();
      CSUM_MM();
      if (s == 9) {
        CSUM_PUT();
        cs0 = f32x4{}; cs1 = f32x4{}; hoff = hb + kq * 8; hu = 8192 + 32;
        if (hw < 2) {
          u32x4 gr = *(LAS u32x4*)(lds + L_G + hw * 1024 + lane * 16);
#pragma unroll 1
          for (int n = 0; n < (1 << 20); ++n) {
            if (__all(gr[1] == (unsigned)tag && gr[3] == (unsigned)tag)) break;
            asm volatile("buffer_load_dwordx4 %0, %1, %2, %3 offen sc1\n\ts_waitcnt vmcnt(0)" : "=&v"(gr) : "v"(lane * 16), "s"(rw), "s"(WS_S + (rt * 128 + hw * 64) * 16) : "memory");
          }
          st_r[hw * 64 + lane] = __uint_as_float(gr[0]); st_t[hw * 64 + lane] = __uint_as_float(gr[2]);
        }
      }
      if (s == 7 || s == 12) asm volatile("s_waitcnt vmcnt(12) lgkmcnt(0)\n\ts_barrier" ::: "memory");
      else if (s > 7 && s < 12) asm volatile("s_waitcnt vmcnt(8) lgkmcnt(0)\n\ts_barrier" ::: "memory");
      else asm volatile("s_waitcnt vmcnt(16) lgkmcnt(0)\n\ts_barrier" ::: "memory");
    }
  } else {
    const int wm = w & 1, wn = w >> 1;
    {
      { u64 pc; asm volatile("s_getpc_b64 %0" : "=s"(pc));
        touch(mkrs((const void*)(pc & ~(u64)255), 30000), threadIdx.x * 128, lds0 + L_PF); }
      const u64 e0 = __hip_atomic_load(fl, RLX_AGENT);
      const int row = rt * 128 + ct * 8 + 2 * w + (lane >> 5), l5 = lane & 31;
      u32x4 xr[3];
#pragma unroll
      for (int i = 0; i < 3; ++i) xr[i] = __builtin_amdgcn_raw_buffer_load_b128(rx, (i < 2 || l5 < 16) ? row * (D_ * 2) + i * 512 + l5 * 16 : OOB, 0, 0);
      const unsigned bias = __builtin_amdgcn_raw_buffer_load_b16(mkrs(b1, F_ * 2), ct * 192 + threadIdx.x * 2, 0, 0);
      {
        const unsigned l2 = (b * 64 + lane) * 128;
        touch(w == 0 ? mkrs(w1, D_ * F_ * 2) : (w == 1 ? mkrs(w2, D_ * F_ * 2) : (w == 2 ? mkrs(wo, D_ * D_ * 2) : mkrs(wqkv, 3 * D_ * D_ * 2))), w == 3 ? l2 + 3840 * 192 : l2, lds0 + L_PF); }
      if (w == 0) touch(mkrs(ws, 32u << 20), lane < 26 ? lane * (1u << 20) + (1u << 20) - 128 : OOB, lds0 + L_PF);
      if (w == 1) touch(mkrs(out, 2048 * D_ * 2), lane * (1u << 19), lds0 + L_PF);
      if (w == 2) touch(mkrs(b2, D_ * 2), lane * 128, lds0 + L_PF);
      *(LAS u32x2*)(lds + L_M + threadIdx.x * 8) = u32x2{0x7fc00000u, 0x7fc00000u};
      tag = uni64(e0 + 1);
      if (threadIdx.x == 0) { *(LAS u64*)(lds + L_C) = tag; *(LAS u32x4*)(lds + L_C + 16) = u32x4{0, 0, 0, 0}; }
      float p = 0.f, q = 0.f;
#pragma unroll
      for (int i = 0; i < 12; ++i) { const float lo = blo(xr[i >> 2][i & 3]), hi = bhi(xr[i >> 2][i & 3]); p += lo + hi; q = fmaf(lo, lo, fmaf(hi, hi, q)); }
      p = dppadd<0x140>(dppadd<0x141>(dppadd<0x4E>(dppadd<0xB1>(p)))); q = dppadd<0x140>(dppadd<0x141>(dppadd<0x4E>(dppadd<0xB1>(q))));
      p = dppadd<0x142>(p); q = dppadd<0x142>(q);
      const float mu = p * (1.f / D_), rstd = __builtin_amdgcn_rsqf(fmaxf(q * (1.f / D_) - mu * mu, 0.f) + 1e-5f);
      st16(u32x4{__float_as_uint(rstd), (unsigned)tag, __float_as_uint(-mu * rstd), (unsigned)tag}, rw, l5 == 16 ? row * 16 : OOB, WS_S);
      if (threadIdx.x < 128) st_r[384 + threadIdx.x] = blo(bias);
    }
    unsigned sx = 0, sw = 0; int stepno = 0;
    {
      f32x4 acc[4][4];
      if (vtype) {
        unsigned vo[4];
#pragma unroll
        for (int i = 0; i < 4; ++i) {
          const int fv = (ct - 10) * 128 + 64 * wn + 16 * i + c16, hd = fv / 20, d = fv - hd * 20;
          vo[i] = ct < 15 ? WS_T + ((hd >> 1) * 64 + 4 * rt + 2 * wm) * PT + P_V + (hd & 1) * 1280 + kq * 320 + d * 16 : OOB;
          asm volatile("" : "+v"(vo[i]));
        }
        STAMP(1);
        G1<1>::loop(acc, lds, sx, sw, w, lane, -1, fl, 0, tag, stepno, ws);
        STAMP(2);
        f32x4 r4[4], t4[4];
#pragma unroll
        for (int m = 0; m < 4; ++m) { r4[m] = *(LAS f32x4*)(st_r + 64 * wm + 16 * m + 4 * kq); t4[m] = *(LAS f32x4*)(st_t + 64 * wm + 16 * m + 4 * kq); }
#pragma unroll
        for (int i = 0; i < 4; ++i) {
          const float c = st_c[64 * wn + 16 * i + c16];
#pragma unroll
          for (int a = 0; a < 2; ++a)
            st16(pk8(acc[2 * a][i] * r4[2 * a] + t4[2 * a] * c, acc[2 * a + 1][i] * r4[2 * a + 1] + t4[2 * a + 1] * c), rw, vo[i], a * PT);
        }
      } else {
        unsigned vo[2], oo[2];
        const unsigned dm = ct < 5 ? 256 : 64;
#pragma unroll
        for (int pp = 0; pp < 2; ++pp) {
          const int c = (ct < 5 ? ct : ct - 5) * 16 + 8 * wn + 4 * pp + kq, pr = c / 5, pos = c - pr * 5;
          const unsigned tb = WS_T + (pr * 64 + 4 * rt + 2 * wm) * PT;
          vo[pp] = tb + pos * 512 + (ct < 5 ? P_Q + c16 * 16 : (8 * (c16 >> 2) + (c16 & 3)) * 16);
          oo[pp] = (ct >= 5 && pos == 2 && c16 < 2) ? tb + P_ONE + c16 * PT : OOB;
          asm volatile("" : "+v"(vo[pp]));
          st16(u32x4{0x3f803f80u, 0x3f803f80u, 0x3f803f80u, 0x3f803f80u}, rw, oo[pp], 0);
        }
        STAMP(1);
        G1<0>::loop(acc, lds, sx, sw, w, lane, -1, fl, 0, tag, stepno, ws);
        STAMP(2);
        const float sc = ct < 5 ? QSCALE : 1.f;
        float r1[4], t1[4];
#pragma unroll
        for (int m = 0; m < 4; ++m) { r1[m] = st_r[64 * wm + 16 * m + c16] * sc; t1[m] = st_t[64 * wm + 16 * m + c16] * sc; }
#pragma unroll
        for (int pp = 0; pp < 2; ++pp) {
          const f32x4 ca = *(LAS f32x4*)(st_c + 64 * wn + 32 * pp + 8 * kq), cb = *(LAS f32x4*)(st_c + 64 * wn + 32 * pp + 8 * kq + 4);
#pragma unroll
          for (int m = 0; m < 4; ++m)
            st16(pk8(acc[m][2 * pp] * r1[m] + ca * t1[m], acc[m][2 * pp + 1] * r1[m] + cb * t1[m]), rw, vo[pp] + (m & 1) * dm, (m >> 1) * PT);
        }
      }
    }
    STAMP(3);
    f32x4 acc[2][6];
    G1<2>::loop(acc, lds, sx, sw, w, lane, 1, fl, F_Q + ct * 16 + rt, tag, stepno, ws);
    STAMP(4);
    __builtin_amdgcn_s_setprio(2);
    const int u = w * 256 + b, slab = u / 10, cg = u - slab * 10;
    if (u < 680) {
      const rsrc_t rs = slab < 48 ? mkrs(w2, F_ * D_ * 2) : mkrs(wo, D_ * D_ * 2);
      const int s2 = slab - 48, l8 = lane >> 3;
      const unsigned r0 = slab < 48 ? slab * 32 : (s2 < 16 ? s2 * 40 : (s2 - 16) * 160 + 16);
#pragma unroll
      for (int i = 0; i < 4; ++i) {
        const int row = slab < 48 ? 8 * i + l8 : (s2 < 16 ? (i >> 1) * 20 + (i & 1) * 8 + l8 : (2 * i + (l8 >> 2)) * 20 + (l8 & 3));
        dma16(rs, row * (D_ * 2) + (lane & 7) * 16, r0 * (D_ * 2) + cg * 128, lds0 + w * 4096 + i * 1024);
      }
    }
    {
      float r1[2], t1[2];
#pragma unroll
      for (int m = 0; m < 2; ++m) { r1[m] = st_r[32 * w + 16 * m + c16]; t1[m] = st_t[32 * w + 16 * m + c16]; }
#pragma unroll
      for (int pp = 0; pp < 3; ++pp) {
        const f32x4 ca = *(LAS f32x4*)(st_c + 32 * pp + 8 * kq), cb = *(LAS f32x4*)(st_c + 32 * pp + 8 * kq + 4);
        const f32x4 ba = *(LAS f32x4*)(st_c + 128 + 32 * pp + 8 * kq), bc = *(LAS f32x4*)(st_c + 128 + 32 * pp + 8 * kq + 4);
#pragma unroll
        for (int m = 0; m < 2; ++m) {
          f32x4 v0 = acc[m][2 * pp] * r1[m] + (ca * t1[m] + ba), v1 = acc[m][2 * pp + 1] * r1[m] + (cb * t1[m] + bc);
#pragma unroll
          for (int j = 0; j < 4; ++j) { v0[j] = fmaxf(v0[j], 0.f); v1[j] = fmaxf(v1[j], 0.f); }
          st16(pk8(v0 * v0, v1 * v1), rw, lane * 16, WS_H + ((8 * rt + 2 * w + m) * 48 + 3 * ct + pp) * 1024);
        }
      }
    }
    if (u < 680) {
      asm volatile("s_waitcnt vmcnt(6)" ::: "memory");
      LAS char* p = lds + w * 4096 + (8 * kq + ((lane >> 2) & 3)) * 128 + (lane & 3) * 8;
#pragma unroll
      for (int nt = 0; nt < 4; ++nt) {
        const int T = 4 * cg + nt, c2 = T / 5, t = T - c2 * 5;
        st16(__builtin_bit_cast(u32x4, cat8(trd(p + nt * 32), trd(p + nt * 32 + 512))), rw, lane * 16, WS_W + ((c2 * 68 + slab) * 5 + t) * 1024);
      }
    }
    STAMP(5);
  }

  const int head = 4 * g + (idx8 >> 3), qc = idx8 & 7;
  if (w >= 4) {
    const int aw = w - 4, qh = aw & 1, kh = aw >> 1, r = lane & 31, h = lane >> 5, odd = head & 1, pr = head >> 1;
    {
      const int c0 = pr * 5 + 2 * odd, e = (lane >> 3) & 1;
      const int tqk = (c0 + 2 * e) >> 4, tv = (head * 20 + 19 * e) >> 7;
      unsigned fi = F_Q + (lane < 16 ? 5 + tqk : 10 + tv) * 16 + 8 * kh + (lane & 7);
      if (lane >= 32) fi = F_Q + tqk * 16 + 2 * qc + qh;
      const u64 f0 = __hip_atomic_load(fl + 16 * fi, RLX_AGENT);
      { CSUM_LD(); CSUM_MM(); }
      asm volatile("s_waitcnt vmcnt(9) lgkmcnt(0)\n\ts_barrier" ::: "memory");
      { CSUM_LD(); CSUM_MM(); }
      DRAIN();
      BAR_L();
      { CSUM_LD(); CSUM_MM(); CSUM_PUT(); }
      BAR_L();
      if (!__all(f0 == tag)) await<2>(fl, fi, tag);
      __builtin_amdgcn_fence(__ATOMIC_ACQUIRE, "wavefront");
    }
    STAMP(6);
    const rsrc_t rk = mkrs(ws + WS_T + (pr * 64 + kh * 32) * PT, 32 * PT);
    const unsigned oK1 = odd * 1536 + lane * 16, oK2 = h ? P_ONE : 1024 + r * 16 + odd * 8;
    const int dv = r < 16 ? (r & 3) + 4 * (r >> 3) + 8 * ((r >> 2) & 1) : r;
    const unsigned oV0 = r < 20 ? P_V + odd * 1280 + h * 320 + dv * 16 : (r == 20 ? P_ONE : OOB), oV1 = r < 20 ? oV0 + 640 : oV0;
#pragma unroll 1
    for (int pass = 0; pass < 2; ++pass) {
      bf16x8 q1[4]; s16x4 q2[4];
#pragma unroll
      for (int t = 0; t < 4; ++t) {
        const unsigned qo = WS_T + (pr * 64 + 8 * qc + 4 * qh + (t ^ (2 * kh))) * PT + P_Q;
        q1[t] = ld16(rw, oK1, qo); q2[t] = ld8(rw, h ? OOB : oK2, qo);
      }
      float mx[4] = {-3.0e38f, -3.0e38f, -3.0e38f, -3.0e38f};
      if (pass) {
#pragma unroll 1
        for (int j = 2; j < 32; ++j) {
          const bf16x8 k1 = ld16(rk, oK1, j * PT); const s16x4 k2 = ld8(rk, oK2, j * PT);
#pragma unroll
          for (int t = 0; t < 4; ++t) {
            const f32x16 sv = qk_b(k2, q2[t], qk_a(k1, q1[t]));
#pragma unroll
            for (int e = 0; e < 16; ++e) mx[t] = fmaxf(mx[t], sv[e]);
          }
        }
      }
      int idx = 0xDDDD; asm volatile("" : "+v"(idx));
      KV b0, b1_, b2_, b3;
      unsigned so = 0;
#define LDK(B, d) do { B.k1 = ld16(rk, oK1, so + (d) * PT); SB(); B.k2 = ld8(rk, oK2, so + (d) * PT); SB(); } while (0)
#define LDV(B, d) do { B.v0 = ld16(rk, oV0, so + (d) * PT); SB(); B.v1 = ld16(rk, oV1, so + (d) * PT); SB(); } while (0)
      SB();
      LDK(b0, 0); LDV(b0, 0); LDK(b1_, 1); LDV(b1_, 1); LDK(b2_, 2); LDV(b2_, 2); LDK(b3, 3);

      {
        f32x16 sm[8];
#define SMP(n) do { sm[n] = (n) < 4 ? qk_b(b0.k2, q2[(n) & 3], qk_a(b0.k1, q1[(n) & 3])) : qk_b(b1_.k2, q2[(n) & 3], qk_a(b1_.k1, q1[(n) & 3])); SB(); } while (0)
        SMP(0); SMP(1);
        asm volatile("s_nop 7"); SB();
#pragma unroll
        for (int n = 0; n < 8; ++n) { if (n < 6) SMP(n + 2); mx[n & 3] = max16(sm[n], mx[n & 3]); SB(); }
      }
#pragma unroll
      for (int t = 0; t < 4; ++t) {
        auto sw = __builtin_amdgcn_permlane32_swap(__float_as_uint(mx[t]), __float_as_uint(mx[t]), false, false);
        mx[t] = fmaxf(__uint_as_float(sw[0]), __uint_as_float(sw[1]));
      }
      volatile LAS f32x4* mp = (volatile LAS f32x4*)(lds + L_M);
      mp[aw * 32 + r] = f32x4{mx[0], mx[1], mx[2], mx[3]};
      f32x4 mo;
#pragma unroll 1
      for (int n = 0; n < (1 << 22); ++n) { mo = mp[(aw ^ 2) * 32 + r]; if (__all(mo[0] == mo[0] && mo[1] == mo[1] && mo[2] == mo[2] && mo[3] == mo[3])) break; }
#pragma unroll
      for (int t = 0; t < 4; ++t) {
        const float nm = -(fmaxf(mx[t], mo[t ^ 2]) + (pass ? 0.f : MARGIN));
        const float a0 = bhi(__float_as_uint(nm)), r0 = nm - a0, a1 = bhi(__float_as_uint(r0)), a2 = r0 - a1;
        const u32x2 pm = {(__float_as_uint(a0) >> 16) | __float_as_uint(a1), __float_as_uint(a2) >> 16};
        if (h) q2[t] = __builtin_bit_cast(s16x4, pm);
      }
      f32x16 o0 = {}, o1 = {}, o2 = {}, o3 = {}, s0, s1, s2, s3 = {};
      b3.v0 = bf16x8{}; b3.v1 = bf16x8{};
      s0 = qk_b(b0.k2, q2[0], qk_a(b0.k1, q1[0])); s1 = qk_b(b0.k2, q2[1], qk_a(b0.k1, q1[1]));
      SB(); STAMP(7); SB();
#define KT(C, P, N, d) do { \
  STEP(s0, s3, o3, P, s2, C, q1[2], q2[2]); LDV(P, d + 3); \
  STEP(s1, s0, o0, C, s3, C, q1[3], q2[3]); LDK(C, d + 4); \
  STEP(s2, s1, o1, C, s0, N, q1[0], q2[0]); \
  STEP(s3, s2, o2, C, s1, N, q1[1], q2[1]); } while (0)
      for (int it = 0; it < 8; ++it) {
        KT(b0, b3, b1_, 0); KT(b1_, b0, b2_, 1); KT(b2_, b1_, b3, 2); KT(b3, b2_, b0, 3);
        so += 4 * PT;
      }
      SB(); STAMP(8); SB();
      o3 = pv_lo(b3.v0, s3, o3, idx); o3 = pv_hi(b3.v1, s3, o3, idx);
      LAS char* sp = lds + L_MRG + aw * 6144 + lane * 16;
#pragma unroll
      for (int e = 0; e < 3; ++e) {
        *(LAS f32x4*)(sp + e * 1024) = f32x4{o0[4 * e], o0[4 * e + 1], o0[4 * e + 2], o0[4 * e + 3]};
        *(LAS f32x4*)(sp + 3072 + e * 1024) = f32x4{o1[4 * e], o1[4 * e + 1], o1[4 * e + 2], o1[4 * e + 3]};
      }
      BAR_L();
      LAS char* gp = lds + L_MRG + (aw ^ 2) * 6144 + lane * 16;
      float chk = 0.f;
#pragma unroll
      for (int z = 0; z < 2; ++z) {
        const f32x16 k = z ? o3 : o2;
        f32x4 v[3];
#pragma unroll
        for (int e = 0; e < 3; ++e) v[e] = f32x4{k[4 * e], k[4 * e + 1], k[4 * e + 2], k[4 * e + 3]} + *(LAS f32x4*)(gp + z * 3072 + e * 1024);
        const float l = __uint_as_float(__builtin_amdgcn_permlane32_swap(__float_as_uint(v[2][0]), __float_as_uint(v[2][0]), false, false)[1]);
        const float inv = __builtin_amdgcn_rcpf(l);
        const int tt = (8 * qc + 4 * qh + ((2 + z) ^ (2 * kh))) * 2 + (r >> 4);
        st16(pk8(v[0] * inv, v[1] * inv), rw, (tt * 16 + pr) * 1024 + ((2 * odd + h) * 16 + c16) * 16, WS_O);
        st8(u32x2{pk2(v[2][0] * inv, v[2][1] * inv), pk2(v[2][2] * inv, v[2][3] * inv)}, rw, h ? OOB : (tt * 32 + head) * 128 + c16 * 8, WS_O2);
#pragma unroll
        for (int e = 0; e < 3; ++e) chk += (fabsf(v[e][0]) + fabsf(v[e][1])) + (fabsf(v[e][2]) + fabsf(v[e][3]));
      }
      mp[aw * 32 + r] = f32x4{__builtin_nanf(""), __builtin_nanf(""), __builtin_nanf(""), __builtin_nanf("")};
      if (!(chk < 1.0e37f)) *(volatile LAS unsigned*)(lds + L_C + 24) = 1;
      DRAIN();
      BAR_L();
      STAMP(9);
      if (pass || __builtin_amdgcn_readfirstlane(*(volatile LAS unsigned*)(lds + L_C + 24)) == 0) break;
    }
    if (threadIdx.x == 256) publish(fl, F_O + qc * 32 + head, tag);
    if (b == 0) {
      await<8>(fl, F_O + aw * 64 + lane, tag);
      lds_meet((LAS unsigned*)(lds + L_C + 28), 4, lane);
      if (threadIdx.x == 256) publish(fl, 0, tag);
    }
  } else {
    const int rt2 = 4 * g + (idx8 & 3), ct2 = idx8 >> 2;
    {
      DRAIN();
      unsigned n = 0;
      if (lane == 0) n = __hip_atomic_fetch_add((LAS unsigned*)(lds + L_C + 16), 1u, __ATOMIC_RELAXED, __HIP_MEMORY_SCOPE_WORKGROUP);
      if (lane == 0 && n == 3) publish(fl, F_H + b, tag);
    }
    const int o = (64 * rt2 + 16 * w + c16) * D_ + 80 * ct2 + 4 * kq;
    u32x2 xv[5], bv[5];
#pragma unroll
    for (int t = 0; t < 5; ++t) { xv[t] = *(const u32x2*)(x + o + 16 * t); bv[t] = *(const u32x2*)(b2 + 80 * ct2 + 4 * kq + 16 * t); }
    __builtin_amdgcn_s_sleep(16);
    await<16>(fl, F_H + 64 * w + lane, tag);
    lds_meet((LAS unsigned*)(lds + L_C + 20), 4, lane);
    STAMP(6);
    f32x4 y[5];
    {
      f32x4 acc[4][5] = {};
      bf16x8 a0[4], w0[5], a1[4], w1_[5];
      const unsigned vo = lane * 16, ao = WS_H + (4 * rt2 * 48 + 12 * w) * 1024, wo_ = WS_W + (ct2 * 68 + 12 * w) * 5120;
#define GLD(A, W, s) do { _Pragma("unroll") for (int m = 0; m < 4; ++m) A[m] = ld16(rw, vo, ao + (m * 48 + (s)) * 1024); \
                          _Pragma("unroll") for (int t = 0; t < 5; ++t) W[t] = ld16(rw, vo, wo_ + ((s) * 5 + t) * 1024); } while (0)
#define GMM(A, W) do { __builtin_amdgcn_s_setprio(0); _Pragma("unroll") for (int t = 0; t < 5; ++t) _Pragma("unroll") for (int m = 0; m < 4; ++m) \
                         acc[m][t] = __builtin_amdgcn_mfma_f32_16x16x32_bf16(W[t], A[m], acc[m][t], 0, 0, 0); SB(); __builtin_amdgcn_s_setprio(2); } while (0)
      GLD(a0, w0, 0); SB();
#pragma unroll 1
      for (int s = 0; s < 12; s += 2) {
        GLD(a1, w1_, s + 1); SB(); GMM(a0, w0); SB();
        const int s2 = min(s + 2, 11);
        GLD(a0, w0, s2); SB(); GMM(a1, w1_); SB();
      }
      STAMP(7);
#pragma unroll
      for (int m = 0; m < 4; ++m)
#pragma unroll
        for (int t = 0; t < 5; ++t) *(LAS f32x4*)(lds + L_RED + ((m * 4 + w) * 5 + t) * 1024 + lane * 16) = acc[m][t];
      lds_meet((LAS unsigned*)(lds + L_C + 20), 8, lane);
#pragma unroll
      for (int t = 0; t < 5; ++t) {
        LAS char* p = lds + L_RED + (w * 20 + t) * 1024 + lane * 16;
        y[t] = (*(LAS f32x4*)p + *(LAS f32x4*)(p + 5120)) + (*(LAS f32x4*)(p + 10240) + *(LAS f32x4*)(p + 15360));
        y[t] += f32x4{blo(xv[t][0]) + blo(bv[t][0]), bhi(xv[t][0]) + bhi(bv[t][0]), blo(xv[t][1]) + blo(bv[t][1]), bhi(xv[t][1]) + bhi(bv[t][1])};
      }
      bf16x8 wf[25];
#pragma unroll
      for (int i = 0; i < 25; ++i) wf[i] = ld16(rw, vo, WS_W + ((ct2 * 68 + 48) * 5 + w * 25 + i) * 1024);
      lds_meet((LAS unsigned*)(lds + L_C + 20), 12, lane);
#pragma unroll
      for (int i = 0; i < 25; ++i) *(LAS bf16x8*)(lds + L_WO + (w * 25 + i) * 1024 + lane * 16) = wf[i];
    }
    STAMP(8);
    for (int pass = 0; pass < 2; ++pass) {
      BAR_L(); BAR_L();
      if (pass || __builtin_amdgcn_readfirstlane(*(volatile LAS unsigned*)(lds + L_C + 24)) == 0) break;
    }
    STAMP(9);
    bf16x8 wr[30], a[20];
    LAS char* wp = lds + L_WO + lane * 16; LAS char* wp2 = wp + 61440;
#pragma unroll
    for (int t = 0; t < 5; ++t) asm volatile("" : "+v"(y[t]));
#pragma unroll
    for (int i = 0; i < 30; ++i) wr[i] = *(LAS bf16x8*)(wp + i * 1024);
    SB();
    await<1>(fl, F_O + g * 32 + (lane & 31), tag);
    STAMP(10);
    const int tt = 4 * rt2 + w;
#pragma unroll
    for (int s = 0; s < 16; ++s) a[s] = ld16(rw, lane * 16 + (s & 3) * 1024, WS_O + (tt * 16 + (s & 12)) * 1024);
#pragma unroll
    for (int s = 0; s < 4; ++s) a[16 + s] = cat8(ld8(rw, kq * 256 + c16 * 8 + s * 1024, WS_O2 + tt * 4096), ld8(rw, kq * 256 + 128 + c16 * 8 + s * 1024, WS_O2 + tt * 4096));
    SB();
#pragma unroll
    for (int i = 0; i < 100; ++i) {
      y[i % 5] = __builtin_amdgcn_mfma_f32_16x16x32_bf16(wr[i % 30], a[i / 5], y[i % 5], 0, 0, 0);
      if (i + 30 < 100) wr[i % 30] = *(LAS bf16x8*)(i + 30 < 60 ? wp + (i + 30) * 1024 : wp2 + (i - 30) * 1024);
      SB();
    }
#pragma unroll
    for (int t = 0; t < 5; ++t) *(u32x2*)(out + o + 16 * t) = u32x2{pk2(y[t][0], y[t][1]), pk2(y[t][2], y[t][3])};
    STAMP(11);
  }
}

extern "C" void kernel_launch(void* const* d_in, const int* in_sizes, int n_in, void* d_out, int out_size, void* d_ws, size_t ws_size,
                              hipStream_t stream) {
  fused<<<256, 512, 0, stream>>>((const u16*)d_in[0], (const u16*)d_in[1], (const u16*)d_in[2], (const u16*)d_in[4], (const u16*)d_in[5],
                                 (const u16*)d_in[6], (const u16*)d_in[7], (u16*)d_out, (char*)d_ws);
}
```

```cpp
#define PF_DATA
#define PF_TLB
#define PROBE 0
#include <hip/hip_runtime.h>
#include <hip/hip_bf16.h>

typedef __attribute__((ext_vector_type(8))) __bf16 bf16x8;
typedef __attribute__((ext_vector_type(16))) __bf16 bf16x16;
typedef __attribute__((ext_vector_type(2))) __bf16 bf16x2;
typedef __attribute__((ext_vector_type(4))) short s16x4;
typedef __attribute__((ext_vector_type(16))) float f32x16;
typedef __attribute__((ext_vector_type(8))) float f32x8;
typedef __attribute__((ext_vector_type(4))) float f32x4;
typedef __attribute__((ext_vector_type(4))) unsigned u32x4;
typedef __attribute__((ext_vector_type(2))) unsigned u32x2;
typedef unsigned short u16;
typedef unsigned long long u64;
typedef __amdgpu_buffer_rsrc_t rsrc_t;
#define LAS __attribute__((address_space(3)))
#define GAS __attribute__((address_space(1)))
#define SB() __builtin_amdgcn_sched_barrier(0)
#define RLX_AGENT __ATOMIC_RELAXED, __HIP_MEMORY_SCOPE_AGENT
#define BAR_L() do { asm volatile("" ::: "memory"); __builtin_amdgcn_s_waitcnt(0xC07F); asm volatile("s_barrier" ::: "memory"); } while (0)
#define DRAIN() asm volatile("s_waitcnt vmcnt(0)" ::: "memory")

constexpr int D_ = 640, F_ = 1536;
constexpr unsigned PT = 7808, P_V = 2560, P_Q = 5120, P_ONE = 7680;
constexpr unsigned WS_T = 0;
constexpr unsigned WS_H = 8u << 20;
constexpr unsigned WS_W = 16u << 20;
constexpr unsigned WS_O = 20u << 20;
constexpr unsigned WS_O2 = 22u << 20;
constexpr unsigned WS_S = 23u << 20;
constexpr unsigned WS_F = 24u << 20;
constexpr unsigned WS_END = WS_F;
constexpr unsigned F_Q = 256, F_H = 512, F_O = 768;
constexpr float QSCALE = 0.22360679774997896f * 1.4426950408889634f;
constexpr float MARGIN = 88.f;
constexpr unsigned OOB = 0x80000000u;
constexpr unsigned XS = 16384, WSL = 16640, L_W = 5 * XS;
constexpr unsigned L_WO = 0, L_RED = 0, L_MRG = 102400;
constexpr unsigned L_ST = L_W + 4 * WSL;
constexpr unsigned L_G = L_ST + 2048;
constexpr unsigned L_M = L_G + 2048;
constexpr unsigned L_C = L_M + 2048;
constexpr unsigned L_PF = L_C + 32;
constexpr unsigned L_END = L_PF + 256;

#define STAMP(i) do { if ((i) == 0 || (i) == (PROBE % 100)) { if (threadIdx.x == (PROBE / 100) * 256) { u64 r_; asm volatile("s_memrealtime %0\n\ts_waitcnt lgkmcnt(0)" : "=s"(r_) :: "memory"); \
   if ((i) == 0) *(volatile LAS u64*)(lds + L_END) = r_; if ((i) == (PROBE % 100)) *(volatile LAS u64*)(lds + L_END + 8) = r_; } } } while (0)
#define STEPSTAMP()

__device__ __forceinline__ unsigned pk2(float a, float b) { bf16x2 r = {(__bf16)a, (__bf16)b}; return __builtin_bit_cast(unsigned, r); }
__device__ __forceinline__ u32x4 pk8(f32x4 a, f32x4 b) { return u32x4{pk2(a[0], a[1]), pk2(a[2], a[3]), pk2(b[0], b[1]), pk2(b[2], b[3])}; }
__device__ __forceinline__ float blo(unsigned u) { return __uint_as_float(u << 16); }
__device__ __forceinline__ float bhi(unsigned u) { return __uint_as_float(u & 0xffff0000u); }
__device__ __forceinline__ u64 uni64(u64 v) {
  return ((u64)(unsigned)__builtin_amdgcn_readfirstlane((unsigned)(v >> 32)) << 32) | (unsigned)__builtin_amdgcn_readfirstlane((unsigned)v);
}
__device__ __forceinline__ rsrc_t mkrs(const void* p, unsigned bytes) { return __builtin_amdgcn_make_buffer_rsrc((void*)p, 0, bytes, 0x00020000); }
__device__ __forceinline__ void dma16(rsrc_t r, unsigned vo, unsigned so, unsigned lds_base) {
  asm volatile("s_mov_b32 m0, %0\n\ts_nop 0\n\tbuffer_load_dwordx4 %1, %2, %3 offen lds" :: "s"(lds_base), "v"(vo), "s"(r), "s"(so) : "memory");
}
__device__ __forceinline__ void touch(rsrc_t r, unsigned vo, unsigned lds_base) {
  asm volatile("s_mov_b32 m0, %0\n\ts_nop 0\n\tbuffer_load_dword %1, %2, 0 offen lds" :: "s"(lds_base), "v"(vo), "s"(r) : "memory");
}
__device__ __forceinline__ bf16x8 ld16(rsrc_t r, unsigned vo, unsigned so) { return __builtin_bit_cast(bf16x8, __builtin_amdgcn_raw_buffer_load_b128(r, vo, so, 16)); }
__device__ __forceinline__ s16x4 ld8(rsrc_t r, unsigned vo, unsigned so) { return __builtin_bit_cast(s16x4, __builtin_amdgcn_raw_buffer_load_b64(r, vo, so, 16)); }
__device__ __forceinline__ void st16(u32x4 v, rsrc_t r, unsigned vo, unsigned so) { __builtin_amdgcn_raw_buffer_store_b128(v, r, vo + so, 0, 16); }
__device__ __forceinline__ void st8(u32x2 v, rsrc_t r, unsigned vo, unsigned so) { __builtin_amdgcn_raw_buffer_store_b64(v, r, vo + so, 0, 16); }
__device__ __forceinline__ void publish(GAS u64* f, unsigned i, u64 tag) { __hip_atomic_store(f + 16 * i, tag, RLX_AGENT); }
template <int SLP> __device__ __forceinline__ void await(GAS u64* f, unsigned i, u64 tag) {
#pragma unroll 1
  for (int n = 0; n < (1 << 20); ++n) {
    if (__all(__hip_atomic_load(f + 16 * i, RLX_AGENT) == tag)) break;
    __builtin_amdgcn_s_sleep(SLP);
  }
  __builtin_amdgcn_fence(__ATOMIC_ACQUIRE, "wavefront");
}
__device__ __forceinline__ void lds_meet(LAS unsigned* c, unsigned n, int lane) {
  asm volatile("s_waitcnt lgkmcnt(0)" ::: "memory");
  if (lane == 0) __hip_atomic_fetch_add(c, 1u, __ATOMIC_RELAXED, __HIP_MEMORY_SCOPE_WORKGROUP);
#pragma unroll 1
  for (int k = 0; k < (1 << 22); ++k) {
    if (__builtin_amdgcn_readfirstlane(*(volatile LAS unsigned*)c) >= n) break;
    __builtin_amdgcn_s_sleep(4);
  }
  asm volatile("" ::: "memory");
}
__device__ __forceinline__ s16x4 trd(LAS char* p) { return __builtin_amdgcn_ds_read_tr16_b64_v4i16((LAS s16x4*)p); }
__device__ __forceinline__ bf16x8 cat8(s16x4 a, s16x4 b) { return __builtin_bit_cast(bf16x8, __builtin_shufflevector(a, b, 0, 1, 2, 3, 4, 5, 6, 7)); }
template <int C> __device__ __forceinline__ float dppadd(float a) {
  return a + __int_as_float(__builtin_amdgcn_update_dpp(0, __float_as_int(a), C, 0xf, 0xf, true));
}

template <int MODE> struct G1 {
  static constexpr int NM = MODE == 2 ? 2 : 4, NI = MODE == 2 ? 6 : 4, PAD = MODE == 1 ? 32 : 8, NP = NI / 2;
  static constexpr int NMMA = 2 * NM * NI, NRD = 2 * (NM + 2 * NI), HEAD = MODE == 2 ? 6 : 12;
  struct Frag { bf16x8 x[2][NM]; s16x4 w[2][NI][2]; };
  struct Ptr { LAS char* x[2]; LAS char* w[NP]; };
  template <int R> static __device__ __forceinline__ void rd(Frag& f, const Ptr& p) {
    constexpr int u = R / (NRD / 2), r = R % (NRD / 2);
    if constexpr (r >= 2 && r < 2 + NM) f.x[u][r - 2] = *(LAS bf16x8*)(p.x[u] + (r - 2) * 2048);
    else { constexpr int q = r < 2 ? r : r - NM, i = q >> 1;
      f.w[u][i][q & 1] = trd(p.w[i >> 1] + (i & 1) * PAD + u * (8192 + 4 * PAD) + (q & 1) * 1024); }
  }
  template <int N> static __device__ __forceinline__ void mma(f32x4 (&acc)[NM][NI], const Frag& f) {
    constexpr int u = N / (NM * NI), i = (N / NM) % NI, m = N % NM;
    const bf16x8 wf = cat8(f.w[u][i][0], f.w[u][i][1]);
    if constexpr (MODE == 1) asm volatile("v_mfma_f32_16x16x32_bf16 %0, %1, %2, %0" : "+v"(acc[m][i]) : "v"(f.x[u][m]), "v"(wf));
    else asm volatile("v_mfma_f32_16x16x32_bf16 %0, %1, %2, %0" : "+v"(acc[m][i]) : "v"(wf), "v"(f.x[u][m]));
  }
  template <int R0, int R1> static __device__ __forceinline__ void rds(Frag& f, const Ptr& p) { if constexpr (R0 < R1) { rd<R0>(f, p); rds<R0 + 1, R1>(f, p); } }
  template <int N0, int N1> static __device__ __forceinline__ void mmas(f32x4 (&acc)[NM][NI], const Frag& f) { if constexpr (N0 < N1) { mma<N0>(acc, f); mmas<N0 + 1, N1>(acc, f); } }
  template <int N> static __device__ __forceinline__ void mix(f32x4 (&acc)[NM][NI], const Frag& fc, Frag& fn, const Ptr& p) {
    if constexpr (N < NMMA) {
      constexpr int SPAN = NMMA - HEAD - 3, k = N - HEAD;
      constexpr int r0 = k >= SPAN ? NRD : (k * NRD + SPAN - 1) / SPAN, r1 = k + 1 >= SPAN ? NRD : ((k + 1) * NRD + SPAN - 1) / SPAN;
      mma<N>(acc, fc); rds<r0, r1>(fn, p); SB();
      mix<N + 1>(acc, fc, fn, p);
    }
  }
  static __device__ __forceinline__ void loop(f32x4 (&acc)[NM][NI], LAS char* lds, unsigned& sx, unsigned& sw, int w, int lane, int pub, GAS u64* fl,
                                              unsigned fi, u64 tag, int& stepno, char* ws) {
    const int kq = lane >> 4, c16 = lane & 15, a = (lane >> 2) & 3;
    const unsigned xo = ((MODE == 2 ? 32 * w : 64 * (w & 1)) + c16) * 128 + ((kq ^ (c16 >> 1)) << 4);
    const unsigned wb = L_W + kq * (2048 + PAD) + a * 256 + (lane & 3) * (MODE == 1 ? 8 : 16);
    unsigned wo[NP];
#pragma unroll
    for (int j = 0; j < NP; ++j) wo[j] = wb + (((MODE == 2 ? 0 : 2 * (w >> 1)) + j) ^ a) * 64;
    Frag fa, fb; Ptr p;
#define G1_PTRX() do { p.x[0] = lds + sx + xo; p.x[1] = lds + sx + (xo ^ 64); asm volatile("" : "+v"(p.x[0]), "+v"(p.x[1])); } while (0)
#define G1_PTRW() do { _Pragma("unroll") for (int j = 0; j < NP; ++j) { p.w[j] = lds + sw + wo[j]; asm volatile("" : "+v"(p.w[j])); } } while (0)
    G1_PTRX(); G1_PTRW();
    if (MODE == 2) rds<0, NRD>(fa, p);
#pragma unroll
    for (int n = 0; n < NM * NI; ++n) { acc[n % NM][n / NM] = f32x4{}; asm volatile("" : "+v"(acc[n % NM][n / NM])); }
    asm volatile("s_nop 1");
    if (MODE != 2) { BAR_L(); STAMP(12); rds<0, NRD>(fa, p); }
#define G1_STEP(FC, FN, PUB) do { \
      mma<0>(acc, FC); sx = (sx == 4 * XS) ? 0 : sx + XS; asm volatile("" : "+s"(sx)); SB(); \
      mma<1>(acc, FC); sw = (sw == 3 * WSL) ? 0 : sw + WSL; asm volatile("" : "+s"(sw)); SB(); \
      mma<2>(acc, FC); G1_PTRX(); SB(); \
      mma<3>(acc, FC); G1_PTRW(); SB(); \
      mmas<4, HEAD>(acc, FC); SB(); \
      STEPSTAMP(); \
      if (PUB) DRAIN(); \
      BAR_L(); \
      STEPSTAMP(); \
      if ((PUB) && threadIdx.x == 0) publish(fl, fi, tag); \
      SB(); \
      mix<HEAD>(acc, FC, FN, p); } while (0)
    SB();
#pragma unroll 1
    for (int s = 0; s < 5; ++s) {
      G1_STEP(fa, fb, MODE == 2 && s == pub);
      G1_STEP(fb, fa, false);
    }
    asm volatile("s_nop 7");
    SB();
  }
};

struct KV { bf16x8 k1; s16x4 k2; bf16x8 v0, v1; };
__device__ __forceinline__ f32x16 qk_a(bf16x8 k, bf16x8 q) { f32x16 z = {}; return __builtin_amdgcn_mfma_f32_32x32x16_bf16(k, q, z, 0, 0, 0); }
__device__ __forceinline__ f32x16 qk_b(s16x4 k, s16x4 q, f32x16 c) { return __builtin_amdgcn_mfma_f32_32x32x8bf16_1k(k, q, c, 0, 0, 0); }
__device__ __forceinline__ f32x16 pv_lo(bf16x8 v, f32x16 p, f32x16 o, int idx) {
  f32x8 h = __builtin_shufflevector(p, p, 0, 1, 2, 3, 4, 5, 6, 7);
  return __builtin_amdgcn_smfmac_f32_32x32x32_bf16(v, __builtin_bit_cast(bf16x16, h), o, idx, 0, 0); }
__device__ __forceinline__ f32x16 pv_hi(bf16x8 v, f32x16 p, f32x16 o, int idx) {
  f32x8 h = __builtin_shufflevector(p, p, 8, 9, 10, 11, 12, 13, 14, 15);
  return __builtin_amdgcn_smfmac_f32_32x32x32_bf16(v, __builtin_bit_cast(bf16x16, h), o, idx, 0, 0); }
__device__ __forceinline__ float max16(f32x16 s, float m) {
#pragma unroll
  for (int e = 0; e < 16; e += 2) asm volatile("v_max3_f32 %0, %0, %1, %2" : "+v"(m) : "v"(s[e]), "v"(s[e + 1]));
  return m;
}
#define EX4(s, b) do { s[b] = __builtin_amdgcn_exp2f(s[b]); s[b+1] = __builtin_amdgcn_exp2f(s[b+1]); s[b+2] = __builtin_amdgcn_exp2f(s[b+2]); s[b+3] = __builtin_amdgcn_exp2f(s[b+3]); } while (0)
#define STEP(SC, SP, OP, VP, SN, KN, Q1, Q2) do { \
  EX4(SC, 0); SB(); OP = pv_lo(VP.v0, SP, OP, idx); SB(); \
  EX4(SC, 4); SB(); SN = qk_a(KN.k1, Q1); SB(); \
  EX4(SC, 8); SB(); OP = pv_hi(VP.v1, SP, OP, idx); SB(); \
  EX4(SC, 12); SB(); SN = qk_b(KN.k2, Q2, SN); SB(); } while (0)

__global__ void __launch_bounds__(512, 2)
fused(const u16* __restrict__ x, const u16* __restrict__ wqkv, const u16* __restrict__ wo, const u16* __restrict__ w1,
      const u16* __restrict__ b1, const u16* __restrict__ w2, const u16* __restrict__ b2, u16* __restrict__ out, char* __restrict__ ws) {
  __shared__ __attribute__((aligned(16))) char smem[L_END + 256];
  LAS char* lds = (LAS char*)smem;
  const unsigned lds0 = (unsigned)(size_t)lds;
  const int lane = threadIdx.x & 63, w = __builtin_amdgcn_readfirstlane(threadIdx.x >> 6);
  const int b = blockIdx.x, g = b & 7, idx8 = b >> 3;
  asm volatile("" :: "s"(x), "s"(wqkv), "s"(wo), "s"(w1), "s"(b1), "s"(w2), "s"(b2), "s"(out), "s"(ws));
  const int rt = (g & 3) * 4 + (idx8 & 3), ct = (g >> 2) * 8 + (idx8 >> 2);
  const bool vtype = ct >= 10;
  const int kq = lane >> 4, c16 = lane & 15;
  LAS float* st_r = (LAS float*)(lds + L_ST);
  LAS float* st_t = st_r + 128;
  LAS float* st_c = st_r + 256;
  GAS u64* fl = (GAS u64*)(ws + WS_F);
  const rsrc_t rw = mkrs(ws, WS_END), rx = mkrs(x, 2048 * D_ * 2);
  u64 tag;
  f32x4 cs0 = {}, cs1 = {};
  unsigned hsw = 0, hoff = 0, hu = 0;
#define CSUM_LD() LAS char* cp_ = lds + hsw + hoff; LAS char* cq_ = cp_ + hu; \
      const bf16x8 c0_ = cat8(trd(cp_), trd(cp_ + 1024)), c1_ = cat8(trd(cq_), trd(cq_ + 1024)); \
      const bf16x8 c2_ = cat8(trd(cp_ + 32), trd(cp_ + 1056)), c3_ = cat8(trd(cq_ + 32), trd(cq_ + 1056))
#define CSUM_MM() do { const bf16x8 on_ = __builtin_bit_cast(bf16x8, u32x4{0x3f803f80u, 0x3f803f80u, 0x3f803f80u, 0x3f803f80u}); const int si_ = (lane & 1) ? 0xEEEE : 0x4444; SB(); \
      cs0 = __builtin_amdgcn_smfmac_f32_16x16x64_bf16(on_, __builtin_shufflevector(c0_, c1_, 0, 1, 2, 3, 4, 5, 6, 7, 8, 9, 10, 11, 12, 13, 14, 15), cs0, si_, 0, 0); \
      cs1 = __builtin_amdgcn_smfmac_f32_16x16x64_bf16(on_, __builtin_shufflevector(c2_, c3_, 0, 1, 2, 3, 4, 5, 6, 7, 8, 9, 10, 11, 12, 13, 14, 15), cs1, si_, 0, 0); SB(); \
      hsw = (hsw == 3 * WSL) ? 0 : hsw + WSL; } while (0)
#define CSUM_PUT() st_c[(w - 4) * 32 + (lane & 31)] = (lane & 16) ? cs1[0] + cs1[1] : cs0[0] + cs0[1]
  STAMP(0);

  if (w >= 4) {
    const int hw = w - 4;
    rsrc_t rwt = mkrs(wqkv, D_ * 3 * D_ * 2);
    unsigned rs = 3 * D_ * 2, pad = vtype ? 32 : 8;
    unsigned wso = ct * 256 + 16 * hw * rs, xso = (rt * 128 + hw * 32) * (D_ * 2);
    const unsigned xv = (lane >> 3) * (D_ * 2) + (((lane & 7) ^ kq) << 4), xv1 = xv ^ 64;
    const int wc = c16 ^ (kq << 2);
    unsigned wv = ct < 15 ? kq * rs + (wc << 4) : OOB;
    unsigned nx = lds0 + hw * 4096, nw = lds0 + L_W + hw * 4096;
#define DMAX() do { _Pragma("unroll") for (int i = 0; i < 4; ++i) dma16(rx, (i & 1) ? xv1 : xv, xso + i * 8 * (D_ * 2), nx + i * 1024); xso += 128; } while (0)
#define DMAW() do { _Pragma("unroll") for (int i = 0; i < 4; ++i) dma16(rwt, wv, wso + 4 * i * rs, nw + i * 1024 + (2 * hw + (i >> 1)) * pad); wso += 64 * rs; } while (0)
#define DMANEXT() do { nx = (nx >= lds0 + 4 * XS) ? nx - 4 * XS : nx + XS; nw = (nw >= lds0 + L_W + 3 * WSL) ? nw - 3 * WSL : nw + WSL; } while (0)
#pragma unroll
    for (int s = 0; s < 3; ++s) { DMAX(); DMAW(); DMANEXT(); }
    __builtin_amdgcn_s_setprio(1);
    const unsigned hb = L_W + kq * 2048 + ((lane >> 2) & 3) * 256 + (hw ^ ((lane >> 2) & 3)) * 64 + (lane & 3) * 8;
    hoff = hb + kq * pad; hu = 8192 + 4 * pad;
    asm volatile("s_waitcnt vmcnt(16)\n\ts_barrier" ::: "memory");
    tag = uni64(*(LAS u64*)(lds + L_C));
#pragma unroll 1
    for (int s = 0; s < 17; ++s) {
      if (s == 7) { rwt = mkrs(w1, D_ * F_ * 2); rs = F_ * 2; pad = 8; wso = ct * 192 + (320 + 16 * hw) * rs; wv = wc < 12 ? kq * rs + (wc << 4) : OOB; }
      if (s == 12) { wso -= 640 * rs; xso -= 1280; }
      if (s == 5 && hw < 2)
        asm volatile("s_mov_b32 m0, %0\n\ts_nop 0\n\tbuffer_load_dwordx4 %1, %2, %3 offen sc1 lds" :: "s"(lds0 + L_G + hw * 1024), "v"(lane * 16), "s"(rw), "s"(WS_S + (rt * 128 + hw * 64) * 16) : "memory");
      CSUM_LD();
      if (s < 7 || s >= 12) DMAX();
      DMAW(); DMANEXT();
      CSUM_MM();
      if (s == 9) {
        CSUM_PUT();
        cs0 = f32x4{}; cs1 = f32x4{}; hoff = hb + kq * 8; hu = 8192 + 32;
        if (hw < 2) {
          u32x4 gr = *(LAS u32x4*)(lds + L_G + hw * 1024 + lane * 16);
#pragma unroll 1
          for (int n = 0; n < (1 << 20); ++n) {
            if (__all(gr[1] == (unsigned)tag && gr[3] == (unsigned)tag)) break;
            asm volatile("buffer_load_dwordx4 %0, %1, %2, %3 offen sc1\n\ts_waitcnt vmcnt(0)" : "=&v"(gr) : "v"(lane * 16), "s"(rw), "s"(WS_S + (rt * 128 + hw * 64) * 16) : "memory");
          }
          st_r[hw * 64 + lane] = __uint_as_float(gr[0]); st_t[hw * 64 + lane] = __uint_as_float(gr[2]);
        }
      }
      if (s == 7 || s == 12) asm volatile("s_waitcnt vmcnt(12) lgkmcnt(0)\n\ts_barrier" ::: "memory");
      else if (s > 7 && s < 12) asm volatile("s_waitcnt vmcnt(8) lgkmcnt(0)\n\ts_barrier" ::: "memory");
      else asm volatile("s_waitcnt vmcnt(16) lgkmcnt(0)\n\ts_barrier" ::: "memory");
    }
  } else {
    const int wm = w & 1, wn = w >> 1;
    {
      const u64 e0 = __hip_atomic_load(fl, RLX_AGENT);
      const int row = rt * 128 + ct * 8 + 2 * w + (lane >> 5), l5 = lane & 31;
      u32x4 xr[3];
#pragma unroll
      for (int i = 0; i < 3; ++i) xr[i] = __builtin_amdgcn_raw_buffer_load_b128(rx, (i < 2 || l5 < 16) ? row * (D_ * 2) + i * 512 + l5 * 16 : OOB, 0, 0);
      const unsigned bias = __builtin_amdgcn_raw_buffer_load_b16(mkrs(b1, F_ * 2), ct * 192 + threadIdx.x * 2, 0, 0);
      {
        const unsigned l2 = (b * 64 + lane) * 128;
        touch(w == 0 ? mkrs(w1, D_ * F_ * 2) : (w == 1 ? mkrs(w2, D_ * F_ * 2) : (w == 2 ? mkrs(wo, D_ * D_ * 2) : mkrs(wqkv, 3 * D_ * D_ * 2))), w == 3 ? l2 + 3840 * 192 : l2, lds0 + L_PF); }
      if (w == 0) touch(mkrs(ws, 32u << 20), lane < 26 ? lane * (1u << 20) + (1u << 20) - 128 : OOB, lds0 + L_PF);
      if (w == 1) touch(mkrs(out, 2048 * D_ * 2), lane * (1u << 19), lds0 + L_PF);
      if (w == 2) touch(mkrs(b2, D_ * 2), lane * 128, lds0 + L_PF);
      *(LAS u32x2*)(lds + L_M + threadIdx.x * 8) = u32x2{0x7fc00000u, 0x7fc00000u};
      tag = uni64(e0 + 1);
      if (threadIdx.x == 0) { *(LAS u64*)(lds + L_C) = tag; *(LAS u32x4*)(lds + L_C + 16) = u32x4{0, 0, 0, 0}; }
      float p = 0.f, q = 0.f;
#pragma unroll
      for (int i = 0; i < 12; ++i) { const float lo = blo(xr[i >> 2][i & 3]), hi = bhi(xr[i >> 2][i & 3]); p += lo + hi; q = fmaf(lo, lo, fmaf(hi, hi, q)); }
      p = dppadd<0x140>(dppadd<0x141>(dppadd<0x4E>(dppadd<0xB1>(p)))); q = dppadd<0x140>(dppadd<0x141>(dppadd<0x4E>(dppadd<0xB1>(q))));
      p = dppadd<0x142>(p); q = dppadd<0x142>(q);
      const float mu = p * (1.f / D_), rstd = __builtin_amdgcn_rsqf(fmaxf(q * (1.f / D_) - mu * mu, 0.f) + 1e-5f);
      st16(u32x4{__float_as_uint(rstd), (unsigned)tag, __float_as_uint(-mu * rstd), (unsigned)tag}, rw, l5 == 16 ? row * 16 : OOB, WS_S);
      if (threadIdx.x < 128) st_r[384 + threadIdx.x] = blo(bias);
    }
    unsigned sx = 0, sw = 0; int stepno = 0;
    {
      f32x4 acc[4][4];
      if (vtype) {
        unsigned vo[4];
#pragma unroll
        for (int i = 0; i < 4; ++i) {
          const int fv = (ct - 10) * 128 + 64 * wn + 16 * i + c16, hd = fv / 20, d = fv - hd * 20;
          vo[i] = ct < 15 ? WS_T + ((hd >> 1) * 64 + 4 * rt + 2 * wm) * PT + P_V + (hd & 1) * 1280 + kq * 320 + d * 16 : OOB;
          asm volatile("" : "+v"(vo[i]));
        }
        STAMP(1);
        G1<1>::loop(acc, lds, sx, sw, w, lane, -1, fl, 0, tag, stepno, ws);
        STAMP(2);
        f32x4 r4[4], t4[4];
#pragma unroll
        for (int m = 0; m < 4; ++m) { r4[m] = *(LAS f32x4*)(st_r + 64 * wm + 16 * m + 4 * kq); t4[m] = *(LAS f32x4*)(st_t + 64 * wm + 16 * m + 4 * kq); }
#pragma unroll
        for (int i = 0; i < 4; ++i) {
          const float c = st_c[64 * wn + 16 * i + c16];
#pragma unroll
          for (int a = 0; a < 2; ++a)
            st16(pk8(acc[2 * a][i] * r4[2 * a] + t4[2 * a] * c, acc[2 * a + 1][i] * r4[2 * a + 1] + t4[2 * a + 1] * c), rw, vo[i], a * PT);
        }
      } else {
        unsigned vo[2], oo[2];
        const unsigned dm = ct < 5 ? 256 : 64;
#pragma unroll
        for (int pp = 0; pp < 2; ++pp) {
          const int c = (ct < 5 ? ct : ct - 5) * 16 + 8 * wn + 4 * pp + kq, pr = c / 5, pos = c - pr * 5;
          const unsigned tb = WS_T + (pr * 64 + 4 * rt + 2 * wm) * PT;
          vo[pp] = tb + pos * 512 + (ct < 5 ? P_Q + c16 * 16 : (8 * (c16 >> 2) + (c16 & 3)) * 16);
          oo[pp] = (ct >= 5 && pos == 2 && c16 < 2) ? tb + P_ONE + c16 * PT : OOB;
          asm volatile("" : "+v"(vo[pp]));
          st16(u32x4{0x3f803f80u, 0x3f803f80u, 0x3f803f80u, 0x3f803f80u}, rw, oo[pp], 0);
        }
        STAMP(1);
        G1<0>::loop(acc, lds, sx, sw, w, lane, -1, fl, 0, tag, stepno, ws);
        STAMP(2);
        const float sc = ct < 5 ? QSCALE : 1.f;
        float r1[4], t1[4];
#pragma unroll
        for (int m = 0; m < 4; ++m) { r1[m] = st_r[64 * wm + 16 * m + c16] * sc; t1[m] = st_t[64 * wm + 16 * m + c16] * sc; }
#pragma unroll
        for (int pp = 0; pp < 2; ++pp) {
          const f32x4 ca = *(LAS f32x4*)(st_c + 64 * wn + 32 * pp + 8 * kq), cb = *(LAS f32x4*)(st_c + 64 * wn + 32 * pp + 8 * kq + 4);
#pragma unroll
          for (int m = 0; m < 4; ++m)
            st16(pk8(acc[m][2 * pp] * r1[m] + ca * t1[m], acc[m][2 * pp + 1] * r1[m] + cb * t1[m]), rw, vo[pp] + (m & 1) * dm, (m >> 1) * PT);
        }
      }
    }
    STAMP(3);
    f32x4 acc[2][6];
    G1<2>::loop(acc, lds, sx, sw, w, lane, 1, fl, F_Q + ct * 16 + rt, tag, stepno, ws);
    STAMP(4);
    __builtin_amdgcn_s_setprio(2);
    const int u = w * 256 + b, slab = u / 10, cg = u - slab * 10;
    if (u < 680) {
      const rsrc_t rs = slab < 48 ? mkrs(w2, F_ * D_ * 2) : mkrs(wo, D_ * D_ * 2);
      const int s2 = slab - 48, l8 = lane >> 3;
      const unsigned r0 = slab < 48 ? slab * 32 : (s2 < 16 ? s2 * 40 : (s2 - 16) * 160 + 16);
#pragma unroll
      for (int i = 0; i < 4; ++i) {
        const int row = slab < 48 ? 8 * i + l8 : (s2 < 16 ? (i >> 1) * 20 + (i & 1) * 8 + l8 : (2 * i + (l8 >> 2)) * 20 + (l8 & 3));
        dma16(rs, row * (D_ * 2) + (lane & 7) * 16, r0 * (D_ * 2) + cg * 128, lds0 + w * 4096 + i * 1024);
      }
    }
    {
      float r1[2], t1[2];
#pragma unroll
      for (int m = 0; m < 2; ++m) { r1[m] = st_r[32 * w + 16 * m + c16]; t1[m] = st_t[32 * w + 16 * m + c16]; }
#pragma unroll
      for (int pp = 0; pp < 3; ++pp) {
        const f32x4 ca = *(LAS f32x4*)(st_c + 32 * pp + 8 * kq), cb = *(LAS f32x4*)(st_c + 32 * pp + 8 * kq + 4);
        const f32x4 ba = *(LAS f32x4*)(st_c + 128 + 32 * pp + 8 * kq), bc = *(LAS f32x4*)(st_c + 128 + 32 * pp + 8 * kq + 4);
#pragma unroll
        for (int m = 0; m < 2; ++m) {
          f32x4 v0 = acc[m][2 * pp] * r1[m] + (ca * t1[m] + ba), v1 = acc[m][2 * pp + 1] * r1[m] + (cb * t1[m] + bc);
#pragma unroll
          for (int j = 0; j < 4; ++j) { v0[j] = fmaxf(v0[j], 0.f); v1[j] = fmaxf(v1[j], 0.f); }
          st16(pk8(v0 * v0, v1 * v1), rw, lane * 16, WS_H + ((8 * rt + 2 * w + m) * 48 + 3 * ct + pp) * 1024);
        }
      }
    }
    if (u < 680) {
      asm volatile("s_waitcnt vmcnt(6)" ::: "memory");
      LAS char* p = lds + w * 4096 + (8 * kq + ((lane >> 2) & 3)) * 128 + (lane & 3) * 8;
#pragma unroll
      for (int nt = 0; nt < 4; ++nt) {
        const int T = 4 * cg + nt, c2 = T / 5, t = T - c2 * 5;
        st16(__builtin_bit_cast(u32x4, cat8(trd(p + nt * 32), trd(p + nt * 32 + 512))), rw, lane * 16, WS_W + ((c2 * 68 + slab) * 5 + t) * 1024);
      }
    }
    STAMP(5);
  }

  const int head = 4 * g + (idx8 >> 3), qc = idx8 & 7;
  if (w >= 4) {
    const int aw = w - 4, qh = aw & 1, kh = aw >> 1, r = lane & 31, h = lane >> 5, odd = head & 1, pr = head >> 1;
    {
      const int c0 = pr * 5 + 2 * odd, e = (lane >> 3) & 1;
      const int tqk = (c0 + 2 * e) >> 4, tv = (head * 20 + 19 * e) >> 7;
      unsigned fi = F_Q + (lane < 16 ? 5 + tqk : 10 + tv) * 16 + 8 * kh + (lane & 7);
      if (lane >= 32) fi = F_Q + tqk * 16 + 2 * qc + qh;
      const u64 f0 = __hip_atomic_load(fl + 16 * fi, RLX_AGENT);
      { CSUM_LD(); CSUM_MM(); }
      asm volatile("s_waitcnt vmcnt(9) lgkmcnt(0)\n\ts_barrier" ::: "memory");
      { CSUM_LD(); CSUM_MM(); }
      DRAIN();
      BAR_L();
      { CSUM_LD(); CSUM_MM(); CSUM_PUT(); }
      BAR_L();
      if (!__all(f0 == tag)) await<2>(fl, fi, tag);
      __builtin_amdgcn_fence(__ATOMIC_ACQUIRE, "wavefront");
    }
    STAMP(6);
    const rsrc_t rk = mkrs(ws + WS_T + (pr * 64 + kh * 32) * PT, 32 * PT);
    const unsigned oK1 = odd * 1536 + lane * 16, oK2 = h ? P_ONE : 1024 + r * 16 + odd * 8;
    const int dv = r < 16 ? (r & 3) + 4 * (r >> 3) + 8 * ((r >> 2) & 1) : r;
    const unsigned oV0 = r < 20 ? P_V + odd * 1280 + h * 320 + dv * 16 : (r == 20 ? P_ONE : OOB), oV1 = r < 20 ? oV0 + 640 : oV0;
#pragma unroll 1
    for (int pass = 0; pass < 2; ++pass) {
      bf16x8 q1[4]; s16x4 q2[4];
#pragma unroll
      for (int t = 0; t < 4; ++t) {
        const unsigned qo = WS_T + (pr * 64 + 8 * qc + 4 * qh + (t ^ (2 * kh))) * PT + P_Q;
        q1[t] = ld16(rw, oK1, qo); q2[t] = ld8(rw, h ? OOB : oK2, qo);
      }
      float mx[4] = {-3.0e38f, -3.0e38f, -3.0e38f, -3.0e38f};
      if (pass) {
#pragma unroll 1
        for (int j = 2; j < 32; ++j) {
          const bf16x8 k1 = ld16(rk, oK1, j * PT); const s16x4 k2 = ld8(rk, oK2, j * PT);
#pragma unroll
          for (int t = 0; t < 4; ++t) {
            const f32x16 sv = qk_b(k2, q2[t], qk_a(k1, q1[t]));
#pragma unroll
            for (int e = 0; e < 16; ++e) mx[t] = fmaxf(mx[t], sv[e]);
          }
        }
      }
      int idx = 0xDDDD; asm volatile("" : "+v"(idx));
      KV b0, b1_, b2_, b3;
      unsigned so = 0;
#define LDK(B, d) do { B.k1 = ld16(rk, oK1, so + (d) * PT); SB(); B.k2 = ld8(rk, oK2, so + (d) * PT); SB(); } while (0)
#define LDV(B, d) do { B.v0 = ld16(rk, oV0, so + (d) * PT); SB(); B.v1 = ld16(rk, oV1, so + (d) * PT); SB(); } while (0)
      SB();
      LDK(b0, 0); LDV(b0, 0); LDK(b1_, 1); LDV(b1_, 1); LDK(b2_, 2); LDV(b2_, 2); LDK(b3, 3);

      {
        f32x16 sm[8];
#define SMP(n) do { sm[n] = (n) < 4 ? qk_b(b0.k2, q2[(n) & 3], qk_a(b0.k1, q1[(n) & 3])) : qk_b(b1_.k2, q2[(n) & 3], qk_a(b1_.k1, q1[(n) & 3])); SB(); } while (0)
        SMP(0); SMP(1);
        asm volatile("s_nop 7"); SB();
#pragma unroll
        for (int n = 0; n < 8; ++n) { if (n < 6) SMP(n + 2); mx[n & 3] = max16(sm[n], mx[n & 3]); SB(); }
      }
#pragma unroll
      for (int t = 0; t < 4; ++t) {
        auto sw = __builtin_amdgcn_permlane32_swap(__float_as_uint(mx[t]), __float_as_uint(mx[t]), false, false);
        mx[t] = fmaxf(__uint_as_float(sw[0]), __uint_as_float(sw[1]));
      }
      volatile LAS f32x4* mp = (volatile LAS f32x4*)(lds + L_M);
      mp[aw * 32 + r] = f32x4{mx[0], mx[1], mx[2], mx[3]};
      f32x4 mo;
#pragma unroll 1
      for (int n = 0; n < (1 << 22); ++n) { mo = mp[(aw ^ 2) * 32 + r]; if (__all(mo[0] == mo[0] && mo[1] == mo[1] && mo[2] == mo[2] && mo[3] == mo[3])) break; }
#pragma unroll
      for (int t = 0; t < 4; ++t) {
        const float nm = -(fmaxf(mx[t], mo[t ^ 2]) + (pass ? 0.f : MARGIN));
        const float a0 = bhi(__float_as_uint(nm)), r0 = nm - a0, a1 = bhi(__float_as_uint(r0)), a2 = r0 - a1;
        const u32x2 pm = {(__float_as_uint(a0) >> 16) | __float_as_uint(a1), __float_as_uint(a2) >> 16};
        if (h) q2[t] = __builtin_bit_cast(s16x4, pm);
      }
      f32x16 o0 = {}, o1 = {}, o2 = {}, o3 = {}, s0, s1, s2, s3 = {};
      b3.v0 = bf16x8{}; b3.v1 = bf16x8{};
      s0 = qk_b(b0.k2, q2[0], qk_a(b0.k1, q1[0])); s1 = qk_b(b0.k2, q2[1], qk_a(b0.k1, q1[1]));
      SB(); STAMP(7); SB();
#define KT(C, P, N, d) do { \
  STEP(s0, s3, o3, P, s2, C, q1[2], q2[2]); LDV(P, d + 3); \
  STEP(s1, s0, o0, C, s3, C, q1[3], q2[3]); LDK(C, d + 4); \
  STEP(s2, s1, o1, C, s0, N, q1[0], q2[0]); \
  STEP(s3, s2, o2, C, s1, N, q1[1], q2[1]); } while (0)
      for (int it = 0; it < 8; ++it) {
        KT(b0, b3, b1_, 0); KT(b1_, b0, b2_, 1); KT(b2_, b1_, b3, 2); KT(b3, b2_, b0, 3);
        so += 4 * PT;
      }
      SB(); STAMP(8); SB();
      o3 = pv_lo(b3.v0, s3, o3, idx); o3 = pv_hi(b3.v1, s3, o3, idx);
      LAS char* sp = lds + L_MRG + aw * 6144 + lane * 16;
#pragma unroll
      for (int e = 0; e < 3; ++e) {
        *(LAS f32x4*)(sp + e * 1024) = f32x4{o0[4 * e], o0[4 * e + 1], o0[4 * e + 2], o0[4 * e + 3]};
        *(LAS f32x4*)(sp + 3072 + e * 1024) = f32x4{o1[4 * e], o1[4 * e + 1], o1[4 * e + 2], o1[4 * e + 3]};
      }
      BAR_L();
      LAS char* gp = lds + L_MRG + (aw ^ 2) * 6144 + lane * 16;
      float chk = 0.f;
#pragma unroll
      for (int z = 0; z < 2; ++z) {
        const f32x16 k = z ? o3 : o2;
        f32x4 v[3];
#pragma unroll
        for (int e = 0; e < 3; ++e) v[e] = f32x4{k[4 * e], k[4 * e + 1], k[4 * e + 2], k[4 * e + 3]} + *(LAS f32x4*)(gp + z * 3072 + e * 1024);
        const float l = __uint_as_float(__builtin_amdgcn_permlane32_swap(__float_as_uint(v[2][0]), __float_as_uint(v[2][0]), false, false)[1]);
        const float inv = __builtin_amdgcn_rcpf(l);
        const int tt = (8 * qc + 4 * qh + ((2 + z) ^ (2 * kh))) * 2 + (r >> 4);
        st16(pk8(v[0] * inv, v[1] * inv), rw, (tt * 16 + pr) * 1024 + ((2 * odd + h) * 16 + c16) * 16, WS_O);
        st8(u32x2{pk2(v[2][0] * inv, v[2][1] * inv), pk2(v[2][2] * inv, v[2][3] * inv)}, rw, h ? OOB : (tt * 32 + head) * 128 + c16 * 8, WS_O2);
#pragma unroll
        for (int e = 0; e < 3; ++e) chk += (fabsf(v[e][0]) + fabsf(v[e][1])) + (fabsf(v[e][2]) + fabsf(v[e][3]));
      }
      mp[aw * 32 + r] = f32x4{__builtin_nanf(""), __builtin_nanf(""), __builtin_nanf(""), __builtin_nanf("")};
      if (!(chk < 1.0e37f)) *(volatile LAS unsigned*)(lds + L_C + 24) = 1;
      DRAIN();
      BAR_L();
      STAMP(9);
      if (pass || __builtin_amdgcn_readfirstlane(*(volatile LAS unsigned*)(lds + L_C + 24)) == 0) break;
    }
    if (threadIdx.x == 256) publish(fl, F_O + qc * 32 + head, tag);
    if (b == 0) {
      await<8>(fl, F_O + aw * 64 + lane, tag);
      lds_meet((LAS unsigned*)(lds + L_C + 28), 4, lane);
      if (threadIdx.x == 256) publish(fl, 0, tag);
    }
  } else {
    const int rt2 = 4 * g + (idx8 & 3), ct2 = idx8 >> 2;
    {
      DRAIN();
      unsigned n = 0;
      if (lane == 0) n = __hip_atomic_fetch_add((LAS unsigned*)(lds + L_C + 16), 1u, __ATOMIC_RELAXED, __HIP_MEMORY_SCOPE_WORKGROUP);
      if (lane == 0 && n == 3) publish(fl, F_H + b, tag);
    }
    const int o = (64 * rt2 + 16 * w + c16) * D_ + 80 * ct2 + 4 * kq;
    u32x2 xv[5], bv[5];
#pragma unroll
    for (int t = 0; t < 5; ++t) { xv[t] = *(const u32x2*)(x + o + 16 * t); bv[t] = *(const u32x2*)(b2 + 80 * ct2 + 4 * kq + 16 * t); }
    __builtin_amdgcn_s_sleep(16);
    await<16>(fl, F_H + 64 * w + lane, tag);
    lds_meet((LAS unsigned*)(lds + L_C + 20), 4, lane);
    STAMP(6);
    f32x4 y[5];
    {
      f32x4 acc[4][5] = {};
      bf16x8 a0[4], w0[5], a1[4], w1_[5];
      const unsigned vo = lane * 16, ao = WS_H + (4 * rt2 * 48 + 12 * w) * 1024, wo_ = WS_W + (ct2 * 68 + 12 * w) * 5120;
#define GLD(A, W, s) do { _Pragma("unroll") for (int m = 0; m < 4; ++m) A[m] = ld16(rw, vo, ao + (m * 48 + (s)) * 1024); \
                          _Pragma("unroll") for (int t = 0; t < 5; ++t) W[t] = ld16(rw, vo, wo_ + ((s) * 5 + t) * 1024); } while (0)
#define GMM(A, W) do { __builtin_amdgcn_s_setprio(0); _Pragma("unroll") for (int t = 0; t < 5; ++t) _Pragma("unroll") for (int m = 0; m < 4; ++m) \
                         acc[m][t] = __builtin_amdgcn_mfma_f32_16x16x32_bf16(W[t], A[m], acc[m][t], 0, 0, 0); SB(); __builtin_amdgcn_s_setprio(2); } while (0)
      GLD(a0, w0, 0); SB();
#pragma unroll 1
      for (int s = 0; s < 12; s += 2) {
        GLD(a1, w1_, s + 1); SB(); GMM(a0, w0); SB();
        const int s2 = min(s + 2, 11);
        GLD(a0, w0, s2); SB(); GMM(a1, w1_); SB();
      }
      STAMP(7);
#pragma unroll
      for (int m = 0; m < 4; ++m)
#pragma unroll
        for (int t = 0; t < 5; ++t) *(LAS f32x4*)(lds + L_RED + ((m * 4 + w) * 5 + t) * 1024 + lane * 16) = acc[m][t];
      lds_meet((LAS unsigned*)(lds + L_C + 20), 8, lane);
#pragma unroll
      for (int t = 0; t < 5; ++t) {
        LAS char* p = lds + L_RED + (w * 20 + t) * 1024 + lane * 16;
        y[t] = (*(LAS f32x4*)p + *(LAS f32x4*)(p + 5120)) + (*(LAS f32x4*)(p + 10240) + *(LAS f32x4*)(p + 15360));
        y[t] += f32x4{blo(xv[t][0]) + blo(bv[t][0]), bhi(xv[t][0]) + bhi(bv[t][0]), blo(xv[t][1]) + blo(bv[t][1]), bhi(xv[t][1]) + bhi(bv[t][1])};
      }
      bf16x8 wf[25];
#pragma unroll
      for (int i = 0; i < 25; ++i) wf[i] = ld16(rw, vo, WS_W + ((ct2 * 68 + 48) * 5 + w * 25 + i) * 1024);
      lds_meet((LAS unsigned*)(lds + L_C + 20), 12, lane);
#pragma unroll
      for (int i = 0; i < 25; ++i) *(LAS bf16x8*)(lds + L_WO + (w * 25 + i) * 1024 + lane * 16) = wf[i];
    }
    STAMP(8);
    for (int pass = 0; pass < 2; ++pass) {
      BAR_L(); BAR_L();
      if (pass || __builtin_amdgcn_readfirstlane(*(volatile LAS unsigned*)(lds + L_C + 24)) == 0) break;
    }
    STAMP(9);
    bf16x8 wr[30], a[20];
    LAS char* wp = lds + L_WO + lane * 16; LAS char* wp2 = wp + 61440;
#pragma unroll
    for (int t = 0; t < 5; ++t) asm volatile("" : "+v"(y[t]));
#pragma unroll
    for (int i = 0; i < 30; ++i) wr[i] = *(LAS bf16x8*)(wp + i * 1024);
    SB();
    await<1>(fl, F_O + g * 32 + (lane & 31), tag);
    STAMP(10);
    const int tt = 4 * rt2 + w;
#pragma unroll
    for (int s = 0; s < 16; ++s) a[s] = ld16(rw, lane * 16 + (s & 3) * 1024, WS_O + (tt * 16 + (s & 12)) * 1024);
#pragma unroll
    for (int s = 0; s < 4; ++s) a[16 + s] = cat8(ld8(rw, kq * 256 + c16 * 8 + s * 1024, WS_O2 + tt * 4096), ld8(rw, kq * 256 + 128 + c16 * 8 + s * 1024, WS_O2 + tt * 4096));
    SB();
#pragma unroll
    for (int i = 0; i < 100; ++i) {
      y[i % 5] = __builtin_amdgcn_mfma_f32_16x16x32_bf16(wr[i % 30], a[i / 5], y[i % 5], 0, 0, 0);
      if (i + 30 < 100) wr[i % 30] = *(LAS bf16x8*)(i + 30 < 60 ? wp + (i + 30) * 1024 : wp2 + (i - 30) * 1024);
      SB();
    }
#pragma unroll
    for (int t = 0; t < 5; ++t) *(u32x2*)(out + o + 16 * t) = u32x2{pk2(y[t][0], y[t][1]), pk2(y[t][2], y[t][3])};
    STAMP(11);
    if (b == 37 && w == 0) { const u64 t0 = *(volatile LAS u64*)(lds + L_END), tk = *(volatile LAS u64*)(lds + L_END + 8);
      for (int n = 0; n < (1 << 22); ++n) { if (__builtin_amdgcn_s_memrealtime() - t0 >= 10000 + 10 * (tk - t0)) break; } }
  }
}

extern "C" void kernel_launch(void* const* d_in, const int* in_sizes, int n_in, void* d_out, int out_size, void* d_ws, size_t ws_size,
                              hipStream_t stream) {
  fused<<<256, 512, 0, stream>>>((const u16*)d_in[0], (const u16*)d_in[1], (const u16*)d_in[2], (const u16*)d_in[4], (const u16*)d_in[5],
                                 (const u16*)d_in[6], (const u16*)d_in[7], (u16*)d_out, (char*)d_ws);
}
```

```cpp
#define PF_DATA
#define PF_TLB
#include <hip/hip_runtime.h>
#include <hip/hip_bf16.h>

typedef __attribute__((ext_vector_type(8))) __bf16 bf16x8;
typedef __attribute__((ext_vector_type(16))) __bf16 bf16x16;
typedef __attribute__((ext_vector_type(2))) __bf16 bf16x2;
typedef __attribute__((ext_vector_type(4))) short s16x4;
typedef __attribute__((ext_vector_type(16))) float f32x16;
typedef __attribute__((ext_vector_type(8))) float f32x8;
typedef __attribute__((ext_vector_type(4))) float f32x4;
typedef __attribute__((ext_vector_type(4))) unsigned u32x4;
typedef __attribute__((ext_vector_type(2))) unsigned u32x2;
typedef unsigned short u16;
typedef unsigned long long u64;
typedef __amdgpu_buffer_rsrc_t rsrc_t;
#define LAS __attribute__((address_space(3)))
#define GAS __attribute__((address_space(1)))
#define SB() __builtin_amdgcn_sched_barrier(0)
#define RLX_AGENT __ATOMIC_RELAXED, __HIP_MEMORY_SCOPE_AGENT
#define BAR_L() do { asm volatile("" ::: "memory"); __builtin_amdgcn_s_waitcnt(0xC07F); asm volatile("s_barrier" ::: "memory"); } while (0)
#define DRAIN() asm volatile("s_waitcnt vmcnt(0)" ::: "memory")

constexpr int D_ = 640, F_ = 1536;
constexpr unsigned PT = 7808, P_V = 2560, P_Q = 5120, P_ONE = 7680;
constexpr unsigned WS_T = 0;
constexpr unsigned WS_H = 8u << 20;
constexpr unsigned WS_W = 16u << 20;
constexpr unsigned WS_O = 20u << 20;
constexpr unsigned WS_O2 = 22u << 20;
constexpr unsigned WS_S = 23u << 20;
constexpr unsigned WS_F = 24u << 20;
constexpr unsigned WS_END = WS_F;
constexpr unsigned F_Q = 256, F_H = 512, F_O = 768;
constexpr float QSCALE = 0.22360679774997896f * 1.4426950408889634f;
constexpr float MARGIN = 88.f;
constexpr unsigned OOB = 0x80000000u;
constexpr unsigned XS = 16384, WSL = 16640, L_W = 5 * XS;
constexpr unsigned L_WO = 0, L_RED = 0, L_MRG = 102400;
constexpr unsigned L_ST = L_W + 4 * WSL;
constexpr unsigned L_G = L_ST + 2048;
constexpr unsigned L_M = L_G + 2048;
constexpr unsigned L_C = L_M + 2048;
constexpr unsigned L_PF = L_C + 32;
constexpr unsigned L_END = L_PF + 256;

#define STAMP(i)
#define STEPSTAMP()

__device__ __forceinline__ unsigned pk2(float a, float b) { bf16x2 r = {(__bf16)a, (__bf16)b}; return __builtin_bit_cast(unsigned, r); }
__device__ __forceinline__ u32x4 pk8(f32x4 a, f32x4 b) { return u32x4{pk2(a[0], a[1]), pk2(a[2], a[3]), pk2(b[0], b[1]), pk2(b[2], b[3])}; }
__device__ __forceinline__ float blo(unsigned u) { return __uint_as_float(u << 16); }
__device__ __forceinline__ float bhi(unsigned u) { return __uint_as_float(u & 0xffff0000u); }
__device__ __forceinline__ u64 uni64(u64 v) {
  return ((u64)(unsigned)__builtin_amdgcn_readfirstlane((unsigned)(v >> 32)) << 32) | (unsigned)__builtin_amdgcn_readfirstlane((unsigned)v);
}
__device__ __forceinline__ rsrc_t mkrs(const void* p, unsigned bytes) { return __builtin_amdgcn_make_buffer_rsrc((void*)p, 0, bytes, 0x00020000); }
__device__ __forceinline__ void dma16(rsrc_t r, unsigned vo, unsigned so, unsigned lds_base) {
  asm volatile("s_mov_b32 m0, %0\n\ts_nop 0\n\tbuffer_load_dwordx4 %1, %2, %3 offen lds" :: "s"(lds_base), "v"(vo), "s"(r), "s"(so) : "memory");
}
__device__ __forceinline__ void touch(rsrc_t r, unsigned vo, unsigned lds_base) {
  asm volatile("s_mov_b32 m0, %0\n\ts_nop 0\n\tbuffer_load_dword %1, %2, 0 offen lds" :: "s"(lds_base), "v"(vo), "s"(r) : "memory");
}
__device__ __forceinline__ bf16x8 ld16(rsrc_t r, unsigned vo, unsigned so) { return __builtin_bit_cast(bf16x8, __builtin_amdgcn_raw_buffer_load_b128(r, vo, so, 16)); }
__device__ __forceinline__ s16x4 ld8(rsrc_t r, unsigned vo, unsigned so) { return __builtin_bit_cast(s16x4, __builtin_amdgcn_raw_buffer_load_b64(r, vo, so, 16)); }
__device__ __forceinline__ void st16(u32x4 v, rsrc_t r, unsigned vo, unsigned so) { __builtin_amdgcn_raw_buffer_store_b128(v, r, vo + so, 0, 16); }
__device__ __forceinline__ void st8(u32x2 v, rsrc_t r, unsigned vo, unsigned so) { __builtin_amdgcn_raw_buffer_store_b64(v, r, vo + so, 0, 16); }
__device__ __forceinline__ void publish(GAS u64* f, unsigned i, u64 tag) { __hip_atomic_store(f + 16 * i, tag, RLX_AGENT); }
template <int SLP> __device__ __forceinline__ void await(GAS u64* f, unsigned i, u64 tag) {
#pragma unroll 1
  for (int n = 0; n < (1 << 20); ++n) {
    if (__all(__hip_atomic_load(f + 16 * i, RLX_AGENT) == tag)) break;
    __builtin_amdgcn_s_sleep(SLP);
  }
  __builtin_amdgcn_fence(__ATOMIC_ACQUIRE, "wavefront");
}
__device__ __forceinline__ void lds_meet(LAS unsigned* c, unsigned n, int lane) {
  asm volatile("s_waitcnt lgkmcnt(0)" ::: "memory");
  if (lane == 0) __hip_atomic_fetch_add(c, 1u, __ATOMIC_RELAXED, __HIP_MEMORY_SCOPE_WORKGROUP);
#pragma unroll 1
  for (int k = 0; k < (1 << 22); ++k) {
    if (__builtin_amdgcn_readfirstlane(*(volatile LAS unsigned*)c) >= n) break;
    __builtin_amdgcn_s_sleep(4);
  }
  asm volatile("" ::: "memory");
}
__device__ __forceinline__ s16x4 trd(LAS char* p) { return __builtin_amdgcn_ds_read_tr16_b64_v4i16((LAS s16x4*)p); }
__device__ __forceinline__ bf16x8 cat8(s16x4 a, s16x4 b) { return __builtin_bit_cast(bf16x8, __builtin_shufflevector(a, b, 0, 1, 2, 3, 4, 5, 6, 7)); }
template <int C> __device__ __forceinline__ float dppadd(float a) {
  return a + __int_as_float(__builtin_amdgcn_update_dpp(0, __float_as_int(a), C, 0xf, 0xf, true));
}

template <int MODE> struct G1 {
  static constexpr int NM = MODE == 2 ? 2 : 4, NI = MODE == 2 ? 6 : 4, PAD = MODE == 1 ? 32 : 8, NP = NI / 2;
  static constexpr int NMMA = 2 * NM * NI, NRD = 2 * (NM + 2 * NI), HEAD = MODE == 2 ? 6 : 12;
  struct Frag { bf16x8 x[2][NM]; s16x4 w[2][NI][2]; };
  struct Ptr { LAS char* x[2]; LAS char* w[NP]; };
  template <int R> static __device__ __forceinline__ void rd(Frag& f, const Ptr& p) {
    constexpr int u = R / (NRD / 2), r = R % (NRD / 2);
    if constexpr (r >= 2 && r < 2 + NM) f.x[u][r - 2] = *(LAS bf16x8*)(p.x[u] + (r - 2) * 2048);
    else { constexpr int q = r < 2 ? r : r - NM, i = q >> 1;
      f.w[u][i][q & 1] = trd(p.w[i >> 1] + (i & 1) * PAD + u * (8192 + 4 * PAD) + (q & 1) * 1024); }
  }
  template <int N> static __device__ __forceinline__ void mma(f32x4 (&acc)[NM][NI], const Frag& f) {
    constexpr int u = N / (NM * NI), i = (N / NM) % NI, m = N % NM;
    const bf16x8 wf = cat8(f.w[u][i][0], f.w[u][i][1]);
    if constexpr (MODE == 1) asm volatile("v_mfma_f32_16x16x32_bf16 %0, %1, %2, %0" : "+v"(acc[m][i]) : "v"(f.x[u][m]), "v"(wf));
    else asm volatile("v_mfma_f32_16x16x32_bf16 %0, %1, %2, %0" : "+v"(acc[m][i]) : "v"(wf), "v"(f.x[u][m]));
  }
  template <int R0, int R1> static __device__ __forceinline__ void rds(Frag& f, const Ptr& p) { if constexpr (R0 < R1) { rd<R0>(f, p); rds<R0 + 1, R1>(f, p); } }
  template <int N0, int N1> static __device__ __forceinline__ void mmas(f32x4 (&acc)[NM][NI], const Frag& f) { if constexpr (N0 < N1) { mma<N0>(acc, f); mmas<N0 + 1, N1>(acc, f); } }
  template <int N> static __device__ __forceinline__ void mix(f32x4 (&acc)[NM][NI], const Frag& fc, Frag& fn, const Ptr& p) {
    if constexpr (N < NMMA) {
      constexpr int SPAN = NMMA - HEAD - 3, k = N - HEAD;
      constexpr int r0 = k >= SPAN ? NRD : (k * NRD + SPAN - 1) / SPAN, r1 = k + 1 >= SPAN ? NRD : ((k + 1) * NRD + SPAN - 1) / SPAN;
      mma<N>(acc, fc); rds<r0, r1>(fn, p); SB();
      mix<N + 1>(acc, fc, fn, p);
    }
  }
  static __device__ __forceinline__ void loop(f32x4 (&acc)[NM][NI], LAS char* lds, unsigned& sx, unsigned& sw, int w, int lane, int pub, GAS u64* fl,
                                              unsigned fi, u64 tag, int& stepno, char* ws) {
    const int kq = lane >> 4, c16 = lane & 15, a = (lane >> 2) & 3;
    const unsigned xo = ((MODE == 2 ? 32 * w : 64 * (w & 1)) + c16) * 128 + ((kq ^ (c16 >> 1)) << 4);
    const unsigned wb = L_W + kq * (2048 + PAD) + a * 256 + (lane & 3) * (MODE == 1 ? 8 : 16);
    unsigned wo[NP];
#pragma unroll
    for (int j = 0; j < NP; ++j) wo[j] = wb + (((MODE == 2 ? 0 : 2 * (w >> 1)) + j) ^ a) * 64;
    Frag fa, fb; Ptr p;
#define G1_PTRX() do { p.x[0] = lds + sx + xo; p.x[1] = lds + sx + (xo ^ 64); asm volatile("" : "+v"(p.x[0]), "+v"(p.x[1])); } while (0)
#define G1_PTRW() do { _Pragma("unroll") for (int j = 0; j < NP; ++j) { p.w[j] = lds + sw + wo[j]; asm volatile("" : "+v"(p.w[j])); } } while (0)
    G1_PTRX(); G1_PTRW();
    if (MODE == 2) rds<0, NRD>(fa, p);
#pragma unroll
    for (int n = 0; n < NM * NI; ++n) { acc[n % NM][n / NM] = f32x4{}; asm volatile("" : "+v"(acc[n % NM][n / NM])); }
    asm volatile("s_nop 1");
    if (MODE != 2) { BAR_L(); STAMP(12); rds<0, NRD>(fa, p); }
#define G1_STEP(FC, FN, PUB) do { \
      mma<0>(acc, FC); sx = (sx == 4 * XS) ? 0 : sx + XS; asm volatile("" : "+s"(sx)); SB(); \
      mma<1>(acc, FC); sw = (sw == 3 * WSL) ? 0 : sw + WSL; asm volatile("" : "+s"(sw)); SB(); \
      mma<2>(acc, FC); G1_PTRX(); SB(); \
      mma<3>(acc, FC); G1_PTRW(); SB(); \
      mmas<4, HEAD>(acc, FC); SB(); \
      STEPSTAMP(); \
      if (PUB) DRAIN(); \
      BAR_L(); \
      STEPSTAMP(); \
      if ((PUB) && threadIdx.x == 0) publish(fl, fi, tag); \
      SB(); \
      mix<HEAD>(acc, FC, FN, p); } while (0)
    SB();
#pragma unroll 1
    for (int s = 0; s < 5; ++s) {
      G1_STEP(fa, fb, MODE == 2 && s == pub);
      G1_STEP(fb, fa, false);
    }
    asm volatile("s_nop 7");
    SB();
  }
};

struct KV { bf16x8 k1; s16x4 k2; bf16x8 v0, v1; };
__device__ __forceinline__ f32x16 qk_a(bf16x8 k, bf16x8 q) { f32x16 z = {}; return __builtin_amdgcn_mfma_f32_32x32x16_bf16(k, q, z, 0, 0, 0); }
__device__ __forceinline__ f32x16 qk_b(s16x4 k, s16x4 q, f32x16 c) { return __builtin_amdgcn_mfma_f32_32x32x8bf16_1k(k, q, c, 0, 0, 0); }
__device__ __forceinline__ f32x16 pv_lo(bf16x8 v, f32x16 p, f32x16 o, int idx) {
  f32x8 h = __builtin_shufflevector(p, p, 0, 1, 2, 3, 4, 5, 6, 7);
  return __builtin_amdgcn_smfmac_f32_32x32x32_bf16(v, __builtin_bit_cast(bf16x16, h), o, idx, 0, 0); }
__device__ __forceinline__ f32x16 pv_hi(bf16x8 v, f32x16 p, f32x16 o, int idx) {
  f32x8 h = __builtin_shufflevector(p, p, 8, 9, 10, 11, 12, 13, 14, 15);
  return __builtin_amdgcn_smfmac_f32_32x32x32_bf16(v, __builtin_bit_cast(bf16x16, h), o, idx, 0, 0); }
__device__ __forceinline__ float max16(f32x16 s, float m) {
#pragma unroll
  for (int e = 0; e < 16; e += 2) asm volatile("v_max3_f32 %0, %0, %1, %2" : "+v"(m) : "v"(s[e]), "v"(s[e + 1]));
  return m;
}
#define EX4(s, b) do { s[b] = __builtin_amdgcn_exp2f(s[b]); s[b+1] = __builtin_amdgcn_exp2f(s[b+1]); s[b+2] = __builtin_amdgcn_exp2f(s[b+2]); s[b+3] = __builtin_amdgcn_exp2f(s[b+3]); } while (0)
#define STEP(SC, SP, OP, VP, SN, KN, Q1, Q2) do { \
  EX4(SC, 0); SB(); OP = pv_lo(VP.v0, SP, OP, idx); SB(); \
  EX4(SC, 4); SB(); SN = qk_a(KN.k1, Q1); SB(); \
  EX4(SC, 8); SB(); OP = pv_hi(VP.v1, SP, OP, idx); SB(); \
  EX4(SC, 12); SB(); SN = qk_b(KN.k2, Q2, SN); SB(); } while (0)

__global__ void __launch_bounds__(512, 2)
fused(const u16* __restrict__ x, const u16* __restrict__ wqkv, const u16* __restrict__ wo, const u16* __restrict__ w1,
      const u16* __restrict__ b1, const u16* __restrict__ w2, const u16* __restrict__ b2, u16* __restrict__ out, char* __restrict__ ws) {
  __shared__ __attribute__((aligned(16))) char smem[L_END + 256];
  LAS char* lds = (LAS char*)smem;
  const unsigned lds0 = (unsigned)(size_t)lds;
  const int lane = threadIdx.x & 63, w = __builtin_amdgcn_readfirstlane(threadIdx.x >> 6);
  const int b = blockIdx.x, g = b & 7, idx8 = b >> 3;
  asm volatile("" :: "s"(x), "s"(wqkv), "s"(wo), "s"(w1), "s"(b1), "s"(w2), "s"(b2), "s"(out), "s"(ws));
  const int rt = (g & 3) * 4 + (idx8 & 3), ct = (g >> 2) * 8 + (idx8 >> 2);
  const bool vtype = ct >= 10;
  const int kq = lane >> 4, c16 = lane & 15;
  LAS float* st_r = (LAS float*)(lds + L_ST);
  LAS float* st_t = st_r + 128;
  LAS float* st_c = st_r + 256;
  GAS u64* fl = (GAS u64*)(ws + WS_F);
  const rsrc_t rw = mkrs(ws, WS_END), rx = mkrs(x, 2048 * D_ * 2);
  u64 tag;
  f32x4 cs0 = {}, cs1 = {};
  unsigned hsw = 0, hoff = 0, hu = 0;
#define CSUM_LD() LAS char* cp_ = lds + hsw + hoff; LAS char* cq_ = cp_ + hu; \
      const bf16x8 c0_ = cat8(trd(cp_), trd(cp_ + 1024)), c1_ = cat8(trd(cq_), trd(cq_ + 1024)); \
      const bf16x8 c2_ = cat8(trd(cp_ + 32), trd(cp_ + 1056)), c3_ = cat8(trd(cq_ + 32), trd(cq_ + 1056))
#define CSUM_MM() do { const bf16x8 on_ = __builtin_bit_cast(bf16x8, u32x4{0x3f803f80u, 0x3f803f80u, 0x3f803f80u, 0x3f803f80u}); const int si_ = (lane & 1) ? 0xEEEE : 0x4444; SB(); \
      cs0 = __builtin_amdgcn_smfmac_f32_16x16x64_bf16(on_, __builtin_shufflevector(c0_, c1_, 0, 1, 2, 3, 4, 5, 6, 7, 8, 9, 10, 11, 12, 13, 14, 15), cs0, si_, 0, 0); \
      cs1 = __builtin_amdgcn_smfmac_f32_16x16x64_bf16(on_, __builtin_shufflevector(c2_, c3_, 0, 1, 2, 3, 4, 5, 6, 7, 8, 9, 10, 11, 12, 13, 14, 15), cs1, si_, 0, 0); SB(); \
      hsw = (hsw == 3 * WSL) ? 0 : hsw + WSL; } while (0)
#define CSUM_PUT() st_c[(w - 4) * 32 + (lane & 31)] = (lane & 16) ? cs1[0] + cs1[1] : cs0[0] + cs0[1]
  STAMP(0);

  if (w >= 4) {
    const int hw = w - 4;
    rsrc_t rwt = mkrs(wqkv, D_ * 3 * D_ * 2);
    unsigned rs = 3 * D_ * 2, pad = vtype ? 32 : 8;
    unsigned wso = ct * 256 + 16 * hw * rs, xso = (rt * 128 + hw * 32) * (D_ * 2);
    const unsigned xv = (lane >> 3) * (D_ * 2) + (((lane & 7) ^ kq) << 4), xv1 = xv ^ 64;
    const int wc = c16 ^ (kq << 2);
    unsigned wv = ct < 15 ? kq * rs + (wc << 4) : OOB;
    unsigned nx = lds0 + hw * 4096, nw = lds0 + L_W + hw * 4096;
#define DMAX() do { _Pragma("unroll") for (int i = 0; i < 4; ++i) dma16(rx, (i & 1) ? xv1 : xv, xso + i * 8 * (D_ * 2), nx + i * 1024); xso += 128; } while (0)
#define DMAW() do { _Pragma("unroll") for (int i = 0; i < 4; ++i) dma16(rwt, wv, wso + 4 * i * rs, nw + i * 1024 + (2 * hw + (i >> 1)) * pad); wso += 64 * rs; } while (0)
#define DMANEXT() do { nx = (nx >= lds0 + 4 * XS) ? nx - 4 * XS : nx + XS; nw = (nw >= lds0 + L_W + 3 * WSL) ? nw - 3 * WSL : nw + WSL; } while (0)
#pragma unroll
    for (int s = 0; s < 3; ++s) { DMAX(); DMAW(); DMANEXT(); }
    __builtin_amdgcn_s_setprio(1);
    const unsigned hb = L_W + kq * 2048 + ((lane >> 2) & 3) * 256 + (hw ^ ((lane >> 2) & 3)) * 64 + (lane & 3) * 8;
    hoff = hb + kq * pad; hu = 8192 + 4 * pad;
    asm volatile("s_waitcnt vmcnt(16)\n\ts_barrier" ::: "memory");
    tag = uni64(*(LAS u64*)(lds + L_C));
#pragma unroll 1
    for (int s = 0; s < 17; ++s) {
      if (s == 7) { rwt = mkrs(w1, D_ * F_ * 2); rs = F_ * 2; pad = 8; wso = ct * 192 + (320 + 16 * hw) * rs; wv = wc < 12 ? kq * rs + (wc << 4) : OOB; }
      if (s == 12) { wso -= 640 * rs; xso -= 1280; }
      if (s == 5 && hw < 2)
        asm volatile("s_mov_b32 m0, %0\n\ts_nop 0\n\tbuffer_load_dwordx4 %1, %2, %3 offen sc1 lds" :: "s"(lds0 + L_G + hw * 1024), "v"(lane * 16), "s"(rw), "s"(WS_S + (rt * 128 + hw * 64) * 16) : "memory");
      CSUM_LD();
      if (s < 7 || s >= 12) DMAX();
      DMAW(); DMANEXT();
      CSUM_MM();
      if (s == 9) {
        CSUM_PUT();
        cs0 = f32x4{}; cs1 = f32x4{}; hoff = hb + kq * 8; hu = 8192 + 32;
        if (hw < 2) {
          u32x4 gr = *(LAS u32x4*)(lds + L_G + hw * 1024 + lane * 16);
#pragma unroll 1
          for (int n = 0; n < (1 << 20); ++n) {
            if (__all(gr[1] == (unsigned)tag && gr[3] == (unsigned)tag)) break;
            asm volatile("buffer_load_dwordx4 %0, %1, %2, %3 offen sc1\n\ts_waitcnt vmcnt(0)" : "=&v"(gr) : "v"(lane * 16), "s"(rw), "s"(WS_S + (rt * 128 + hw * 64) * 16) : "memory");
          }
          st_r[hw * 64 + lane] = __uint_as_float(gr[0]); st_t[hw * 64 + lane] = __uint_as_float(gr[2]);
        }
      }
      if (s == 7 || s == 12) asm volatile("s_waitcnt vmcnt(12) lgkmcnt(0)\n\ts_barrier" ::: "memory");
      else if (s > 7 && s < 12) asm volatile("s_waitcnt vmcnt(8) lgkmcnt(0)\n\ts_barrier" ::: "memory");
      else asm volatile("s_waitcnt vmcnt(16) lgkmcnt(0)\n\ts_barrier" ::: "memory");
    }
  } else {
    const int wm = w & 1, wn = w >> 1;
    {
      const u64 e0 = __hip_atomic_load(fl, RLX_AGENT);
      const int row = rt * 128 + ct * 8 + 2 * w + (lane >> 5), l5 = lane & 31;
      u32x4 xr[3];
#pragma unroll
      for (int i = 0; i < 3; ++i) xr[i] = __builtin_amdgcn_raw_buffer_load_b128(rx, (i < 2 || l5 < 16) ? row * (D_ * 2) + i * 512 + l5 * 16 : OOB, 0, 0);
      const unsigned bias = __builtin_amdgcn_raw_buffer_load_b16(mkrs(b1, F_ * 2), ct * 192 + threadIdx.x * 2, 0, 0);
      {
        const unsigned l2 = (b * 64 + lane) * 128;
        touch(w == 0 ? mkrs(w1, D_ * F_ * 2) : (w == 1 ? mkrs(w2, D_ * F_ * 2) : (w == 2 ? mkrs(wo, D_ * D_ * 2) : mkrs(wqkv, 3 * D_ * D_ * 2))), w == 3 ? l2 + 3840 * 192 : l2, lds0 + L_PF); }
      if (w == 0) touch(mkrs(ws, 32u << 20), lane < 26 ? lane * (1u << 20) + (1u << 20) - 128 : OOB, lds0 + L_PF);
      if (w == 1) touch(mkrs(out, 2048 * D_ * 2), lane * (1u << 19), lds0 + L_PF);
      if (w == 2) touch(mkrs(b2, D_ * 2), lane * 128, lds0 + L_PF);
      *(LAS u32x2*)(lds + L_M + threadIdx.x * 8) = u32x2{0x7fc00000u, 0x7fc00000u};
      tag = uni64(e0 + 1);
      if (threadIdx.x == 0) { *(LAS u64*)(lds + L_C) = tag; *(LAS u32x4*)(lds + L_C + 16) = u32x4{0, 0, 0, 0}; }
      float p = 0.f, q = 0.f;
#pragma unroll
      for (int i = 0; i < 12; ++i) { const float lo = blo(xr[i >> 2][i & 3]), hi = bhi(xr[i >> 2][i & 3]); p += lo + hi; q = fmaf(lo, lo, fmaf(hi, hi, q)); }
      p = dppadd<0x140>(dppadd<0x141>(dppadd<0x4E>(dppadd<0xB1>(p)))); q = dppadd<0x140>(dppadd<0x141>(dppadd<0x4E>(dppadd<0xB1>(q))));
      p = dppadd<0x142>(p); q = dppadd<0x142>(q);
      const float mu = p * (1.f / D_), rstd = __builtin_amdgcn_rsqf(fmaxf(q * (1.f / D_) - mu * mu, 0.f) + 1e-5f);
      st16(u32x4{__float_as_uint(rstd), (unsigned)tag, __float_as_uint(-mu * rstd), (unsigned)tag}, rw, l5 == 16 ? row * 16 : OOB, WS_S);
      if (threadIdx.x < 128) st_r[384 + threadIdx.x] = blo(bias);
    }
    unsigned sx = 0, sw = 0; int stepno = 0;
    {
      f32x4 acc[4][4];
      if (vtype) {
        unsigned vo[4];
#pragma unroll
        for (int i = 0; i < 4; ++i) {
          const int fv = (ct - 10) * 128 + 64 * wn + 16 * i + c16, hd = fv / 20, d = fv - hd * 20;
          vo[i] = ct < 15 ? WS_T + ((hd >> 1) * 64 + 4 * rt + 2 * wm) * PT + P_V + (hd & 1) * 1280 + kq * 320 + d * 16 : OOB;
          asm volatile("" : "+v"(vo[i]));
        }
        STAMP(1);
        G1<1>::loop(acc, lds, sx, sw, w, lane, -1, fl, 0, tag, stepno, ws);
        STAMP(2);
        f32x4 r4[4], t4[4];
#pragma unroll
        for (int m = 0; m < 4; ++m) { r4[m] = *(LAS f32x4*)(st_r + 64 * wm + 16 * m + 4 * kq); t4[m] = *(LAS f32x4*)(st_t + 64 * wm + 16 * m + 4 * kq); }
#pragma unroll
        for (int i = 0; i < 4; ++i) {
          const float c = st_c[64 * wn + 16 * i + c16];
#pragma unroll
          for (int a = 0; a < 2; ++a)
            st16(pk8(acc[2 * a][i] * r4[2 * a] + t4[2 * a] * c, acc[2 * a + 1][i] * r4[2 * a + 1] + t4[2 * a + 1] * c), rw, vo[i], a * PT);
        }
      } else {
        unsigned vo[2], oo[2];
        const unsigned dm = ct < 5 ? 256 : 64;
#pragma unroll
        for (int pp = 0; pp < 2; ++pp) {
          const int c = (ct < 5 ? ct : ct - 5) * 16 + 8 * wn + 4 * pp + kq, pr = c / 5, pos = c - pr * 5;
          const unsigned tb = WS_T + (pr * 64 + 4 * rt + 2 * wm) * PT;
          vo[pp] = tb + pos * 512 + (ct < 5 ? P_Q + c16 * 16 : (8 * (c16 >> 2) + (c16 & 3)) * 16);
          oo[pp] = (ct >= 5 && pos == 2 && c16 < 2) ? tb + P_ONE + c16 * PT : OOB;
          asm volatile("" : "+v"(vo[pp]));
          st16(u32x4{0x3f803f80u, 0x3f803f80u, 0x3f803f80u, 0x3f803f80u}, rw, oo[pp], 0);
        }
        STAMP(1);
        G1<0>::loop(acc, lds, sx, sw, w, lane, -1, fl, 0, tag, stepno, ws);
        STAMP(2);
        const float sc = ct < 5 ? QSCALE : 1.f;
        float r1[4], t1[4];
#pragma unroll
        for (int m = 0; m < 4; ++m) { r1[m] = st_r[64 * wm + 16 * m + c16] * sc; t1[m] = st_t[64 * wm + 16 * m + c16] * sc; }
#pragma unroll
        for (int pp = 0; pp < 2; ++pp) {
          const f32x4 ca = *(LAS f32x4*)(st_c + 64 * wn + 32 * pp + 8 * kq), cb = *(LAS f32x4*)(st_c + 64 * wn + 32 * pp + 8 * kq + 4);
#pragma unroll
          for (int m = 0; m < 4; ++m)
            st16(pk8(acc[m][2 * pp] * r1[m] + ca * t1[m], acc[m][2 * pp + 1] * r1[m] + cb * t1[m]), rw, vo[pp] + (m & 1) * dm, (m >> 1) * PT);
        }
      }
    }
    STAMP(3);
    f32x4 acc[2][6];
    G1<2>::loop(acc, lds, sx, sw, w, lane, 1, fl, F_Q + ct * 16 + rt, tag, stepno, ws);
    STAMP(4);
    __builtin_amdgcn_s_setprio(2);
    const int u = w * 256 + b, slab = u / 10, cg = u - slab * 10;
    if (u < 680) {
      const rsrc_t rs = slab < 48 ? mkrs(w2, F_ * D_ * 2) : mkrs(wo, D_ * D_ * 2);
      const int s2 = slab - 48, l8 = lane >> 3;
      const unsigned r0 = slab < 48 ? slab * 32 : (s2 < 16 ? s2 * 40 : (s2 - 16) * 160 + 16);
#pragma unroll
      for (int i = 0; i < 4; ++i) {
        const int row = slab < 48 ? 8 * i + l8 : (s2 < 16 ? (i >> 1) * 20 + (i & 1) * 8 + l8 : (2 * i + (l8 >> 2)) * 20 + (l8 & 3));
        dma16(rs, row * (D_ * 2) + (lane & 7) * 16, r0 * (D_ * 2) + cg * 128, lds0 + w * 4096 + i * 1024);
      }
    }
    {
      float r1[2], t1[2];
#pragma unroll
      for (int m = 0; m < 2; ++m) { r1[m] = st_r[32 * w + 16 * m + c16]; t1[m] = st_t[32 * w + 16 * m + c16]; }
#pragma unroll
      for (int pp = 0; pp < 3; ++pp) {
        const f32x4 ca = *(LAS f32x4*)(st_c + 32 * pp + 8 * kq), cb = *(LAS f32x4*)(st_c + 32 * pp + 8 * kq + 4);
        const f32x4 ba = *(LAS f32x4*)(st_c + 128 + 32 * pp + 8 * kq), bc = *(LAS f32x4*)(st_c + 128 + 32 * pp + 8 * kq + 4);
#pragma unroll
        for (int m = 0; m < 2; ++m) {
          f32x4 v0 = acc[m][2 * pp] * r1[m] + (ca * t1[m] + ba), v1 = acc[m][2 * pp + 1] * r1[m] + (cb * t1[m] + bc);
#pragma unroll
          for (int j = 0; j < 4; ++j) { v0[j] = fmaxf(v0[j], 0.f); v1[j] = fmaxf(v1[j], 0.f); }
          st16(pk8(v0 * v0, v1 * v1), rw, lane * 16, WS_H + ((8 * rt + 2 * w + m) * 48 + 3 * ct + pp) * 1024);
        }
      }
    }
    if (u < 680) {
      asm volatile("s_waitcnt vmcnt(6)" ::: "memory");
      LAS char* p = lds + w * 4096 + (8 * kq + ((lane >> 2) & 3)) * 128 + (lane & 3) * 8;
#pragma unroll
      for (int nt = 0; nt < 4; ++nt) {
        const int T = 4 * cg + nt, c2 = T / 5, t = T - c2 * 5;
        st16(__builtin_bit_cast(u32x4, cat8(trd(p + nt * 32), trd(p + nt * 32 + 512))), rw, lane * 16, WS_W + ((c2 * 68 + slab) * 5 + t) * 1024);
      }
    }
    STAMP(5);
  }

  const int head = 4 * g + (idx8 >> 3), qc = idx8 & 7;
  if (w >= 4) {
    const int aw = w - 4, qh = aw & 1, kh = aw >> 1, r = lane & 31, h = lane >> 5, odd = head & 1, pr = head >> 1;
    {
      const int c0 = pr * 5 + 2 * odd, e = (lane >> 3) & 1;
      const int tqk = (c0 + 2 * e) >> 4, tv = (head * 20 + 19 * e) >> 7;
      unsigned fi = F_Q + (lane < 16 ? 5 + tqk : 10 + tv) * 16 + 8 * kh + (lane & 7);
      if (lane >= 32) fi = F_Q + tqk * 16 + 2 * qc + qh;
      const u64 f0 = __hip_atomic_load(fl + 16 * fi, RLX_AGENT);
      { CSUM_LD(); CSUM_MM(); }
      asm volatile("s_waitcnt vmcnt(9) lgkmcnt(0)\n\ts_barrier" ::: "memory");
      { CSUM_LD(); CSUM_MM(); }
      DRAIN();
      BAR_L();
      { CSUM_LD(); CSUM_MM(); CSUM_PUT(); }
      BAR_L();
      if (!__all(f0 == tag)) await<2>(fl, fi, tag);
      __builtin_amdgcn_fence(__ATOMIC_ACQUIRE, "wavefront");
    }
    STAMP(6);
    const rsrc_t rk = mkrs(ws + WS_T + (pr * 64 + kh * 32) * PT, 32 * PT);
    const unsigned oK1 = odd * 1536 + lane * 16, oK2 = h ? P_ONE : 1024 + r * 16 + odd * 8;
    const int dv = r < 16 ? (r & 3) + 4 * (r >> 3) + 8 * ((r >> 2) & 1) : r;
    const unsigned oV0 = r < 20 ? P_V + odd * 1280 + h * 320 + dv * 16 : (r == 20 ? P_ONE : OOB), oV1 = r < 20 ? oV0 + 640 : oV0;
#pragma unroll 1
    for (int pass = 0; pass < 2; ++pass) {
      bf16x8 q1[4]; s16x4 q2[4];
#pragma unroll
      for (int t = 0; t < 4; ++t) {
        const unsigned qo = WS_T + (pr * 64 + 8 * qc + 4 * qh + (t ^ (2 * kh))) * PT + P_Q;
        q1[t] = ld16(rw, oK1, qo); q2[t] = ld8(rw, h ? OOB : oK2, qo);
      }
      float mx[4] = {-3.0e38f, -3.0e38f, -3.0e38f, -3.0e38f};
      if (pass) {
#pragma unroll 1
        for (int j = 2; j < 32; ++j) {
          const bf16x8 k1 = ld16(rk, oK1, j * PT); const s16x4 k2 = ld8(rk, oK2, j * PT);
#pragma unroll
          for (int t = 0; t < 4; ++t) {
            const f32x16 sv = qk_b(k2, q2[t], qk_a(k1, q1[t]));
#pragma unroll
            for (int e = 0; e < 16; ++e) mx[t] = fmaxf(mx[t], sv[e]);
          }
        }
      }
      int idx = 0xDDDD; asm volatile("" : "+v"(idx));
      KV b0, b1_, b2_, b3;
      unsigned so = 0;
#define LDK(B, d) do { B.k1 = ld16(rk, oK1, so + (d) * PT); SB(); B.k2 = ld8(rk, oK2, so + (d) * PT); SB(); } while (0)
#define LDV(B, d) do { B.v0 = ld16(rk, oV0, so + (d) * PT); SB(); B.v1 = ld16(rk, oV1, so + (d) * PT); SB(); } while (0)
      SB();
      LDK(b0, 0); LDV(b0, 0); LDK(b1_, 1); LDV(b1_, 1); LDK(b2_, 2); LDV(b2_, 2); LDK(b3, 3);

      {
        f32x16 sm[8];
#define SMP(n) do { sm[n] = (n) < 4 ? qk_b(b0.k2, q2[(n) & 3], qk_a(b0.k1, q1[(n) & 3])) : qk_b(b1_.k2, q2[(n) & 3], qk_a(b1_.k1, q1[(n) & 3])); SB(); } while (0)
        SMP(0); SMP(1);
        asm volatile("s_nop 7"); SB();
#pragma unroll
        for (int n = 0; n < 8; ++n) { if (n < 6) SMP(n + 2); mx[n & 3] = max16(sm[n], mx[n & 3]); SB(); }
      }
#pragma unroll
      for (int t = 0; t < 4; ++t) {
        auto sw = __builtin_amdgcn_permlane32_swap(__float_as_uint(mx[t]), __float_as_uint(mx[t]), false, false);
        mx[t] = fmaxf(__uint_as_float(sw[0]), __uint_as_float(sw[1]));
      }
      volatile LAS f32x4* mp = (volatile LAS f32x4*)(lds + L_M);
      mp[aw * 32 + r] = f32x4{mx[0], mx[1], mx[2], mx[3]};
      f32x4 mo;
#pragma unroll 1
      for (int n = 0; n < (1 << 22); ++n) { mo = mp[(aw ^ 2) * 32 + r]; if (__all(mo[0] == mo[0] && mo[1] == mo[1] && mo[2] == mo[2] && mo[3] == mo[3])) break; }
#pragma unroll
      for (int t = 0; t < 4; ++t) {
        const float nm = -(fmaxf(mx[t], mo[t ^ 2]) + (pass ? 0.f : MARGIN));
        const float a0 = bhi(__float_as_uint(nm)), r0 = nm - a0, a1 = bhi(__float_as_uint(r0)), a2 = r0 - a1;
        const u32x2 pm = {(__float_as_uint(a0) >> 16) | __float_as_uint(a1), __float_as_uint(a2) >> 16};
        if (h) q2[t] = __builtin_bit_cast(s16x4, pm);
      }
      f32x16 o0 = {}, o1 = {}, o2 = {}, o3 = {}, s0, s1, s2, s3 = {};
      b3.v0 = bf16x8{}; b3.v1 = bf16x8{};
      s0 = qk_b(b0.k2, q2[0], qk_a(b0.k1, q1[0])); s1 = qk_b(b0.k2, q2[1], qk_a(b0.k1, q1[1]));
      SB(); STAMP(7); SB();
#define KT(C, P, N, d) do { \
  STEP(s0, s3, o3, P, s2, C, q1[2], q2[2]); LDV(P, d + 3); \
  STEP(s1, s0, o0, C, s3, C, q1[3], q2[3]); LDK(C, d + 4); \
  STEP(s2, s1, o1, C, s0, N, q1[0], q2[0]); \
  STEP(s3, s2, o2, C, s1, N, q1[1], q2[1]); } while (0)
      for (int it = 0; it < 8; ++it) {
        KT(b0, b3, b1_, 0); KT(b1_, b0, b2_, 1); KT(b2_, b1_, b3, 2); KT(b3, b2_, b0, 3);
        so += 4 * PT;
      }
      SB(); STAMP(8); SB();
      o3 = pv_lo(b3.v0, s3, o3, idx); o3 = pv_hi(b3.v1, s3, o3, idx);
      LAS char* sp = lds + L_MRG + aw * 6144 + lane * 16;
#pragma unroll
      for (int e = 0; e < 3; ++e) {
        *(LAS f32x4*)(sp + e * 1024) = f32x4{o0[4 * e], o0[4 * e + 1], o0[4 * e + 2], o0[4 * e + 3]};
        *(LAS f32x4*)(sp + 3072 + e * 1024) = f32x4{o1[4 * e], o1[4 * e + 1], o1[4 * e + 2], o1[4 * e + 3]};
      }
      BAR_L();
      LAS char* gp = lds + L_MRG + (aw ^ 2) * 6144 + lane * 16;
      float chk = 0.f;
#pragma unroll
      for (int z = 0; z < 2; ++z) {
        const f32x16 k = z ? o3 : o2;
        f32x4 v[3];
#pragma unroll
        for (int e = 0; e < 3; ++e) v[e] = f32x4{k[4 * e], k[4 * e + 1], k[4 * e + 2], k[4 * e + 3]} + *(LAS f32x4*)(gp + z * 3072 + e * 1024);
        const float l = __uint_as_float(__builtin_amdgcn_permlane32_swap(__float_as_uint(v[2][0]), __float_as_uint(v[2][0]), false, false)[1]);
        const float inv = __builtin_amdgcn_rcpf(l);
        const int tt = (8 * qc + 4 * qh + ((2 + z) ^ (2 * kh))) * 2 + (r >> 4);
        st16(pk8(v[0] * inv, v[1] * inv), rw, (tt * 16 + pr) * 1024 + ((2 * odd + h) * 16 + c16) * 16, WS_O);
        st8(u32x2{pk2(v[2][0] * inv, v[2][1] * inv), pk2(v[2][2] * inv, v[2][3] * inv)}, rw, h ? OOB : (tt * 32 + head) * 128 + c16 * 8, WS_O2);
#pragma unroll
        for (int e = 0; e < 3; ++e) chk += (fabsf(v[e][0]) + fabsf(v[e][1])) + (fabsf(v[e][2]) + fabsf(v[e][3]));
      }
      mp[aw * 32 + r] = f32x4{__builtin_nanf(""), __builtin_nanf(""), __builtin_nanf(""), __builtin_nanf("")};
      if (!(chk < 1.0e37f)) *(volatile LAS unsigned*)(lds + L_C + 24) = 1;
      DRAIN();
      BAR_L();
      STAMP(9);
      if (pass || __builtin_amdgcn_readfirstlane(*(volatile LAS unsigned*)(lds + L_C + 24)) == 0) break;
    }
    if (threadIdx.x == 256) publish(fl, F_O + qc * 32 + head, tag);
    if (b == 0) {
      await<8>(fl, F_O + aw * 64 + lane, tag);
      lds_meet((LAS unsigned*)(lds + L_C + 28), 4, lane);
      if (threadIdx.x == 256) publish(fl, 0, tag);
    }
  } else {
    const int rt2 = 4 * g + (idx8 & 3), ct2 = idx8 >> 2;
    {
      DRAIN();
      unsigned n = 0;
      if (lane == 0) n = __hip_atomic_fetch_add((LAS unsigned*)(lds + L_C + 16), 1u, __ATOMIC_RELAXED, __HIP_MEMORY_SCOPE_WORKGROUP);
      if (lane == 0 && n == 3) publish(fl, F_H + b, tag);
    }
    const int o = (64 * rt2 + 16 * w + c16) * D_ + 80 * ct2 + 4 * kq;
    u32x2 xv[5], bv[5];
#pragma unroll
    for (int t = 0; t < 5; ++t) { xv[t] = *(const u32x2*)(x + o + 16 * t); bv[t] = *(const u32x2*)(b2 + 80 * ct2 + 4 * kq + 16 * t); }
    __builtin_amdgcn_s_sleep(16);
    await<16>(fl, F_H + 64 * w + lane, tag);
    lds_meet((LAS unsigned*)(lds + L_C + 20), 4, lane);
    STAMP(6);
    f32x4 y[5];
    {
      f32x4 acc[4][5] = {};
      bf16x8 a0[4], w0[5], a1[4], w1_[5];
      const unsigned vo = lane * 16, ao = WS_H + (4 * rt2 * 48 + 12 * w) * 1024, wo_ = WS_W + (ct2 * 68 + 12 * w) * 5120;
#define GLD(A, W, s) do { _Pragma("unroll") for (int m = 0; m < 4; ++m) A[m] = ld16(rw, vo, ao + (m * 48 + (s)) * 1024); \
                          _Pragma("unroll") for (int t = 0; t < 5; ++t) W[t] = ld16(rw, vo, wo_ + ((s) * 5 + t) * 1024); } while (0)
#define GMM(A, W) do { __builtin_amdgcn_s_setprio(0); _Pragma("unroll") for (int t = 0; t < 5; ++t) _Pragma("unroll") for (int m = 0; m < 4; ++m) \
                         acc[m][t] = __builtin_amdgcn_mfma_f32_16x16x32_bf16(W[t], A[m], acc[m][t], 0, 0, 0); SB(); __builtin_amdgcn_s_setprio(2); } while (0)
      GLD(a0, w0, 0); SB();
#pragma unroll 1
      for (int s = 0; s < 12; s += 2) {
        GLD(a1, w1_, s + 1); SB(); GMM(a0, w0); SB();
        const int s2 = min(s + 2, 11);
        GLD(a0, w0, s2); SB(); GMM(a1, w1_); SB();
      }
      STAMP(7);
#pragma unroll
      for (int m = 0; m < 4; ++m)
#pragma unroll
        for (int t = 0; t < 5; ++t) *(LAS f32x4*)(lds + L_RED + ((m * 4 + w) * 5 + t) * 1024 + lane * 16) = acc[m][t];
      lds_meet((LAS unsigned*)(lds + L_C + 20), 8, lane);
#pragma unroll
      for (int t = 0; t < 5; ++t) {
        LAS char* p = lds + L_RED + (w * 20 + t) * 1024 + lane * 16;
        y[t] = (*(LAS f32x4*)p + *(LAS f32x4*)(p + 5120)) + (*(LAS f32x4*)(p + 10240) + *(LAS f32x4*)(p + 15360));
        y[t] += f32x4{blo(xv[t][0]) + blo(bv[t][0]), bhi(xv[t][0]) + bhi(bv[t][0]), blo(xv[t][1]) + blo(bv[t][1]), bhi(xv[t][1]) + bhi(bv[t][1])};
      }
      bf16x8 wf[25];
#pragma unroll
      for (int i = 0; i < 25; ++i) wf[i] = ld16(rw, vo, WS_W + ((ct2 * 68 + 48) * 5 + w * 25 + i) * 1024);
      lds_meet((LAS unsigned*)(lds + L_C + 20), 12, lane);
#pragma unroll
      for (int i = 0; i < 25; ++i) *(LAS bf16x8*)(lds + L_WO + (w * 25 + i) * 1024 + lane * 16) = wf[i];
    }
    STAMP(8);
    for (int pass = 0; pass < 2; ++pass) {
      BAR_L(); BAR_L();
      if (pass || __builtin_amdgcn_readfirstlane(*(volatile LAS unsigned*)(lds + L_C + 24)) == 0) break;
    }
    STAMP(9);
    bf16x8 wr[30], a[20];
    LAS char* wp = lds + L_WO + lane * 16; LAS char* wp2 = wp + 61440;
#pragma unroll
    for (int t = 0; t < 5; ++t) asm volatile("" : "+v"(y[t]));
#pragma unroll
    for (int i = 0; i < 30; ++i) wr[i] = *(LAS bf16x8*)(wp + i * 1024);
    SB();
    await<1>(fl, F_O + g * 32 + (lane & 31), tag);
    STAMP(10);
    const int tt = 4 * rt2 + w;
#pragma unroll
    for (int s = 0; s < 16; ++s) a[s] = ld16(rw, lane * 16 + (s & 3) * 1024, WS_O + (tt * 16 + (s & 12)) * 1024);
#pragma unroll
    for (int s = 0; s < 4; ++s) a[16 + s] = cat8(ld8(rw, kq * 256 + c16 * 8 + s * 1024, WS_O2 + tt * 4096), ld8(rw, kq * 256 + 128 + c16 * 8 + s * 1024, WS_O2 + tt * 4096));
    SB();
#pragma unroll
    for (int i = 0; i < 100; ++i) {
      y[i % 5] = __builtin_amdgcn_mfma_f32_16x16x32_bf16(wr[i % 30], a[i / 5], y[i % 5], 0, 0, 0);
      if (i + 30 < 100) wr[i % 30] = *(LAS bf16x8*)(i + 30 < 60 ? wp + (i + 30) * 1024 : wp2 + (i - 30) * 1024);
      SB();
    }
#pragma unroll
    for (int t = 0; t < 5; ++t) *(u32x2*)(out + o + 16 * t) = u32x2{pk2(y[t][0], y[t][1]), pk2(y[t][2], y[t][3])};
    STAMP(11);
  }
}

__global__ void rec_tiny(char* ws) { if (threadIdx.x == 0) __hip_atomic_store((GAS u64*)(ws + (40u << 20)), (u64)__builtin_amdgcn_s_memrealtime(), RLX_AGENT); }
__global__ void spin_tiny(char* ws) { const u64 t0 = __hip_atomic_load((GAS u64*)(ws + (40u << 20)), RLX_AGENT);
  for (int n = 0; n < (1 << 22); ++n) { if (__builtin_amdgcn_s_memrealtime() - t0 >= 20000) break; } }
__global__ void __launch_bounds__(512, 2) rec_big(char* ws, int blk) { u64 t = __builtin_amdgcn_s_memrealtime();
  __shared__ char sm[155000]; asm volatile("" :: "v"(sm) : "v247"); if (threadIdx.x == 0 && blockIdx.x == blk) __hip_atomic_store((GAS u64*)(ws + (40u << 20)), t, RLX_AGENT); }
extern "C" void kernel_launch(void* const* d_in, const int* in_sizes, int n_in, void* d_out, int out_size, void* d_ws, size_t ws_size,
                              hipStream_t stream) {
  rec_big<<<256, 512, 0, stream>>>((char*)d_ws, 255);
  fused<<<256, 512, 0, stream>>>((const u16*)d_in[0], (const u16*)d_in[1], (const u16*)d_in[2], (const u16*)d_in[4], (const u16*)d_in[5],
                                 (const u16*)d_in[6], (const u16*)d_in[7], (u16*)d_out, (char*)d_ws);
  spin_tiny<<<1, 64, 0, stream>>>((char*)d_ws);
}
```

```cpp
#include <hip/hip_runtime.h>
#include <hip/hip_bf16.h>

typedef __attribute__((ext_vector_type(8))) __bf16 bf16x8;
typedef __attribute__((ext_vector_type(16))) __bf16 bf16x16;
typedef __attribute__((ext_vector_type(2))) __bf16 bf16x2;
typedef __attribute__((ext_vector_type(4))) short s16x4;
typedef __attribute__((ext_vector_type(16))) float f32x16;
typedef __attribute__((ext_vector_type(8))) float f32x8;
typedef __attribute__((ext_vector_type(4))) float f32x4;
typedef __attribute__((ext_vector_type(4))) unsigned u32x4;
typedef __attribute__((ext_vector_type(2))) unsigned u32x2;
typedef unsigned short u16;
typedef unsigned long long u64;
typedef __amdgpu_buffer_rsrc_t rsrc_t;
#define LAS __attribute__((address_space(3)))
#define GAS __attribute__((address_space(1)))
#define SB() __builtin_amdgcn_sched_barrier(0)
#define RLX_AGENT __ATOMIC_RELAXED, __HIP_MEMORY_SCOPE_AGENT
#define BAR_L() do { asm volatile("" ::: "memory"); __builtin_amdgcn_s_waitcnt(0xC07F); asm volatile("s_barrier" ::: "memory"); } while (0)
#define DRAIN() asm volatile("s_waitcnt vmcnt(0)" ::: "memory")

constexpr int D_ = 640, F_ = 1536;
constexpr unsigned PT = 7808, P_V = 2560, P_Q = 5120, P_ONE = 7680;
constexpr unsigned WS_T = 0;
constexpr unsigned WS_H = 8u << 20;
constexpr unsigned WS_W = 16u << 20;
constexpr unsigned WS_O = 20u << 20;
constexpr unsigned WS_O2 = 22u << 20;
constexpr unsigned WS_S = 23u << 20;
constexpr unsigned WS_F = 24u << 20;
constexpr unsigned WS_END = WS_F;
constexpr unsigned F_Q = 256, F_H = 512, F_O = 768;
constexpr float QSCALE = 0.22360679774997896f * 1.4426950408889634f;
constexpr float MARGIN = 88.f;
constexpr unsigned OOB = 0x80000000u;
constexpr unsigned CODE_SKIP = 4608;
constexpr unsigned XS = 16448, WSL = 16640, L_W = 6 * XS;
constexpr unsigned L_WO = 0, L_RED = 102400, L_MRG = 102400;
constexpr unsigned L_ST = L_W + 3 * WSL;
constexpr unsigned L_G = L_ST + 2048;
constexpr unsigned L_M = L_G + 4096;
constexpr unsigned L_C = L_M + 2048;
constexpr unsigned L_PF = L_C + 32;
constexpr unsigned L_END = L_PF + 256;

#define STAMP(i)
#define STEPSTAMP()

#define FS(v)
__device__ __forceinline__ unsigned pk2(float a, float b) { bf16x2 r = {(__bf16)a, (__bf16)b}; return __builtin_bit_cast(unsigned, r); }
__device__ __forceinline__ u32x4 pk8(f32x4 a, f32x4 b) { return u32x4{pk2(a[0], a[1]), pk2(a[2], a[3]), pk2(b[0], b[1]), pk2(b[2], b[3])}; }
__device__ __forceinline__ float blo(unsigned u) { return __uint_as_float(u << 16); }
__device__ __forceinline__ float bhi(unsigned u) { return __uint_as_float(u & 0xffff0000u); }
__device__ __forceinline__ u64 uni64(u64 v) {
  return ((u64)(unsigned)__builtin_amdgcn_readfirstlane((unsigned)(v >> 32)) << 32) | (unsigned)__builtin_amdgcn_readfirstlane((unsigned)v);
}
__device__ __forceinline__ rsrc_t mkrs(const void* p, unsigned bytes) { return __builtin_amdgcn_make_buffer_rsrc((void*)p, 0, bytes, 0x00020000); }
__device__ __forceinline__ void dma16(rsrc_t r, unsigned vo, unsigned so, unsigned lds_base) {
  asm volatile("s_mov_b32 m0, %0\n\ts_nop 0\n\tbuffer_load_dwordx4 %1, %2, %3 offen lds" :: "s"(lds_base), "v"(vo), "s"(r), "s"(so) : "memory");
}
__device__ __forceinline__ void touch(rsrc_t r, unsigned vo, unsigned lds_base) {
  asm volatile("s_mov_b32 m0, %0\n\ts_nop 0\n\tbuffer_load_dword %1, %2, 0 offen lds" :: "s"(lds_base), "v"(vo), "s"(r) : "memory");
}
#define STOUCH3(p) asm volatile("s_load_dword s100, %0, 0\n\ts_load_dword s100, %0, 64\n\ts_load_dword s100, %0, 128" :: "s"(p) : "s100")
#define STOUCH4(p) asm volatile("s_load_dword s100, %0, 0\n\ts_load_dword s100, %0, 64\n\ts_load_dword s100, %0, 128\n\ts_load_dword s100, %0, 192" :: "s"(p) : "s100")
__device__ __forceinline__ bf16x8 ld16(rsrc_t r, unsigned vo, unsigned so) { return __builtin_bit_cast(bf16x8, __builtin_amdgcn_raw_buffer_load_b128(r, vo, so, 16)); }
__device__ __forceinline__ s16x4 ld8(rsrc_t r, unsigned vo, unsigned so) { return __builtin_bit_cast(s16x4, __builtin_amdgcn_raw_buffer_load_b64(r, vo, so, 16)); }
__device__ __forceinline__ void st16(u32x4 v, rsrc_t r, unsigned vo, unsigned so) { __builtin_amdgcn_raw_buffer_store_b128(v, r, vo + so, 0, 16); }
__device__ __forceinline__ void st8(u32x2 v, rsrc_t r, unsigned vo, unsigned so) { __builtin_amdgcn_raw_buffer_store_b64(v, r, vo + so, 0, 16); }
__device__ __forceinline__ void publish(GAS u64* f, unsigned i, u64 tag) { __hip_atomic_store(f + 16 * i, tag, RLX_AGENT); }
template <int SLP> __device__ __forceinline__ void await(GAS u64* f, unsigned i, u64 tag) {
#pragma unroll 1
  for (int n = 0; n < (1 << 20); ++n) {
    if (__all(__hip_atomic_load(f + 16 * i, RLX_AGENT) == tag)) break;
    __builtin_amdgcn_s_sleep(SLP);
  }
  __builtin_amdgcn_fence(__ATOMIC_ACQUIRE, "wavefront");
}
__device__ __forceinline__ void lds_meet(LAS unsigned* c, unsigned n, int lane) {
  asm volatile("s_waitcnt lgkmcnt(0)" ::: "memory");
  if (lane == 0) __hip_atomic_fetch_add(c, 1u, __ATOMIC_RELAXED, __HIP_MEMORY_SCOPE_WORKGROUP);
#pragma unroll 1
  for (int k = 0; k < (1 << 22); ++k) {
    if (__builtin_amdgcn_readfirstlane(*(volatile LAS unsigned*)c) >= n) break;
    __builtin_amdgcn_s_sleep(2);
  }
  asm volatile("" ::: "memory");
}
__device__ __forceinline__ s16x4 trd(LAS char* p) { return __builtin_amdgcn_ds_read_tr16_b64_v4i16((LAS s16x4*)p); }
__device__ __forceinline__ bf16x8 cat8(s16x4 a, s16x4 b) { return __builtin_bit_cast(bf16x8, __builtin_shufflevector(a, b, 0, 1, 2, 3, 4, 5, 6, 7)); }
template <int C> __device__ __forceinline__ float dppadd(float a) {
  return a + __int_as_float(__builtin_amdgcn_update_dpp(0, __float_as_int(a), C, 0xf, 0xf, true));
}

template <int N> struct IC { static constexpr int v = N; };
template <int MODE> struct G1 {
  static constexpr int NM = MODE == 2 ? 2 : 4, NI = MODE == 2 ? 6 : 4, PAD = MODE == 1 ? 32 : 8, NP = NI / 2;
  static constexpr int NMMA = 2 * NM * NI, NRD = 2 * (NM + 2 * NI), HEAD = MODE == 2 ? 6 : 12;
  struct Frag { bf16x8 x[2][NM]; s16x4 w[2][NI][2]; };
  struct Ptr { LAS char* x[2]; LAS char* w[NP]; };
  template <int R> static __device__ __forceinline__ void rd(Frag& f, const Ptr& p) {
    constexpr int u = R / (NRD / 2), r = R % (NRD / 2);
    if constexpr (r >= 2 && r < 2 + NM) f.x[u][r - 2] = *(LAS bf16x8*)(p.x[u] + (r - 2) * 2048);
    else { constexpr int q = r < 2 ? r : r - NM, i = q >> 1;
      f.w[u][i][q & 1] = trd(p.w[i >> 1] + (i & 1) * PAD + u * (8192 + 4 * PAD) + (q & 1) * 1024); }
  }
  template <int N> static __device__ __forceinline__ void mma(f32x4 (&acc)[NM][NI], const Frag& f) {
    constexpr int u = N / (NM * NI), i = (N / NM) % NI, m = N % NM;
    const bf16x8 wf = cat8(f.w[u][i][0], f.w[u][i][1]);
    if constexpr (MODE == 1) asm volatile("v_mfma_f32_16x16x32_bf16 %0, %1, %2, %0" : "+v"(acc[m][i]) : "v"(f.x[u][m]), "v"(wf));
    else asm volatile("v_mfma_f32_16x16x32_bf16 %0, %1, %2, %0" : "+v"(acc[m][i]) : "v"(wf), "v"(f.x[u][m]));
  }
  template <int U> static __device__ __forceinline__ void csum(f32x4& cs, const Frag& f, bf16x8 sel, int idx) {
    const bf16x16 wf = __builtin_shufflevector(cat8(f.w[U][0][0], f.w[U][0][1]), cat8(f.w[U][1][0], f.w[U][1][1]), 0, 1, 2, 3, 4, 5, 6, 7, 8, 9, 10, 11, 12, 13, 14, 15);
    asm volatile("v_smfmac_f32_16x16x64_bf16 %0, %1, %2, %3" : "+v"(cs) : "v"(sel), "v"(wf), "v"(idx));
  }
  static __device__ __forceinline__ int span(int w, int j) { return MODE == 2 ? (w + j >= 3 ? w + j - 3 : w + j) : 2 * (w >> 1) + (j ^ (w & 1)); }
  template <int R0, int R1> static __device__ __forceinline__ void rds(Frag& f, const Ptr& p) { if constexpr (R0 < R1) { rd<R0>(f, p); rds<R0 + 1, R1>(f, p); } }
  template <int N0, int N1> static __device__ __forceinline__ void mmas(f32x4 (&acc)[NM][NI], const Frag& f) { if constexpr (N0 < N1) { mma<N0>(acc, f); mmas<N0 + 1, N1>(acc, f); } }
  template <int N> static __device__ __forceinline__ void mix(f32x4 (&acc)[NM][NI], const Frag& fc, Frag& fn, const Ptr& p) {
    if constexpr (N < NMMA) {
      constexpr int SPAN = NMMA - HEAD - 3, k = N - HEAD;
      constexpr int r0 = k >= SPAN ? NRD : (k * NRD + SPAN - 1) / SPAN, r1 = k + 1 >= SPAN ? NRD : ((k + 1) * NRD + SPAN - 1) / SPAN;
      mma<N>(acc, fc); rds<r0, r1>(fn, p); SB();
      mix<N + 1>(acc, fc, fn, p);
    }
  }
  struct Off { unsigned x, w[NP]; };
  static __device__ __forceinline__ Off offs(int w, int lane) {
    const int kq = lane >> 4, c16 = lane & 15, a = (lane >> 2) & 3;
    Off o;
    o.x = ((MODE == 2 ? 32 * w : 64 * (w & 1)) + c16) * 128 + ((kq ^ (c16 >> 1)) << 4);
    const unsigned wb = L_W + kq * (2048 + PAD) + a * 256 + (lane & 3) * (MODE == 1 ? 8 : 16);
#pragma unroll
    for (int j = 0; j < NP; ++j) o.w[j] = wb + (span(w, j) ^ a) * 64;
    return o;
  }
  static __device__ __forceinline__ void ptrx(Ptr& p, const Off& o, LAS char* lds, unsigned sx) {
    p.x[0] = lds + sx + o.x; p.x[1] = lds + sx + (o.x ^ 64); asm volatile("" : "+v"(p.x[0]), "+v"(p.x[1])); }
  static __device__ __forceinline__ void ptrw(Ptr& p, const Off& o, LAS char* lds, unsigned sw) {
#pragma unroll
    for (int j = 0; j < NP; ++j) { p.w[j] = lds + (sw + o.w[j]); asm volatile("" : "+v"(p.w[j])); } }
  template <int N, class T> static __device__ __forceinline__ void fin(f32x4 (&acc)[NM][NI], const Frag& fc, T& tail) {
    if constexpr (N < NMMA) { mma<N>(acc, fc); tail(IC<N - HEAD>{}); SB(); fin<N + 1>(acc, fc, tail); }
  }
  template <class T>
  static __device__ __forceinline__ void loop(f32x4 (&acc)[NM][NI], Frag& fa, const Off& o, LAS char* lds, unsigned sx, unsigned sw, int w, int lane, int pub,
                                              GAS u64* fl, unsigned fi, u64 tag, T tail, char* ws) {
    const int kq = lane >> 4, c16 = lane & 15;
    Frag fb; Ptr p;
#pragma unroll
    for (int n = 0; n < NM * NI; ++n) { acc[n % NM][n / NM] = f32x4{}; asm volatile("" : "+v"(acc[n % NM][n / NM])); }
    const unsigned one = (kq >> 1) == ((c16 >> 1) & 1) ? 0x3f803f80u : 0u;
    bf16x8 sel = __builtin_bit_cast(bf16x8, u32x4{one, one, one, one});
    int sidx = (lane & 1) ? 0xEEEE : 0x4444;
    f32x4 cs = {};
    LAS float* cp = (LAS float*)(lds + L_ST + 1024) + 32 * span(w, 0) + (MODE == 1 ? c16 : 8 * (c16 >> 2) + (c16 & 3));
    asm volatile("s_nop 1" : "+v"(sel), "+v"(sidx), "+v"(cs), "+v"(cp));
    if constexpr (MODE != 2) { ptrx(p, o, lds, sx); ptrw(p, o, lds, sw); SB(); tail(IC<-2>{}); STAMP(1); BAR_L(); STAMP(12); rds<0, NRD>(fa, p); SB(); tail(IC<-1>{}); }
#define G1_HEAD(FC, PUB, LAST, BAR) do { \
      mma<0>(acc, FC); sx = (sx == 5 * XS) ? 0 : sx + XS; asm volatile("" : "+s"(sx)); SB(); \
      csum<0>(cs, FC, sel, sidx); SB(); \
      mma<1>(acc, FC); sw = (sw == 2 * WSL) ? 0 : sw + WSL; asm volatile("" : "+s"(sw)); SB(); \
      mma<2>(acc, FC); ptrx(p, o, lds, sx); SB(); \
      mma<3>(acc, FC); ptrw(p, o, lds, sw); SB(); \
      mmas<4, HEAD - 2>(acc, FC); SB(); \
      csum<1>(cs, FC, sel, sidx); SB(); \
      mmas<HEAD - 2, HEAD>(acc, FC); SB(); \
      if (LAST) { asm volatile("s_nop 5" : "+v"(cs)); cp[0] = cs[0] + cs[1]; cp[MODE == 1 ? 16 : 4] = cs[2] + cs[3]; } \
      if (PUB) DRAIN(); \
      if (BAR) BAR_L(); \
      if ((PUB) && threadIdx.x == 0) publish(fl, fi, tag); \
      SB(); } while (0)
    SB();
    G1_HEAD(fa, false, false, true);
#pragma unroll 1
    for (int s = 0; s < 4; ++s) {
      mix<HEAD>(acc, fa, fb, p);
      G1_HEAD(fb, false, false, true);
      mix<HEAD>(acc, fb, fa, p);
      G1_HEAD(fa, MODE == 2 && s == pub, false, MODE != 2 || s < 3);
    }
    if constexpr (MODE == 2) ptrw(p, o, lds, 3 * XS - L_W);
    mix<HEAD>(acc, fa, fb, p);
    G1_HEAD(fb, false, true, MODE != 2);
    fin<HEAD>(acc, fb, tail);
    asm volatile("s_nop 7");
    SB();
  }
};

struct KV { bf16x8 k1; s16x4 k2; bf16x8 v0, v1; };
__device__ __forceinline__ f32x16 qk_a(bf16x8 k, bf16x8 q) { f32x16 z = {}; return __builtin_amdgcn_mfma_f32_32x32x16_bf16(k, q, z, 0, 0, 0); }
__device__ __forceinline__ f32x16 qk_b(s16x4 k, s16x4 q, f32x16 c) { return __builtin_amdgcn_mfma_f32_32x32x8bf16_1k(k, q, c, 0, 0, 0); }
__device__ __forceinline__ f32x16 pv_lo(bf16x8 v, f32x16 p, f32x16 o, int idx) {
  f32x8 h = __builtin_shufflevector(p, p, 0, 1, 2, 3, 4, 5, 6, 7);
  return __builtin_amdgcn_smfmac_f32_32x32x32_bf16(v, __builtin_bit_cast(bf16x16, h), o, idx, 0, 0); }
__device__ __forceinline__ f32x16 pv_hi(bf16x8 v, f32x16 p, f32x16 o, int idx) {
  f32x8 h = __builtin_shufflevector(p, p, 8, 9, 10, 11, 12, 13, 14, 15);
  return __builtin_amdgcn_smfmac_f32_32x32x32_bf16(v, __builtin_bit_cast(bf16x16, h), o, idx, 0, 0); }
__device__ __forceinline__ float max16(f32x16 s, float m) {
#pragma unroll
  for (int e = 0; e < 16; e += 2) asm volatile("v_max3_f32 %0, %0, %1, %2" : "+v"(m) : "v"(s[e]), "v"(s[e + 1]));
  return m;
}
#define EX4(s, b) do { s[b] = __builtin_amdgcn_exp2f(s[b]); s[b+1] = __builtin_amdgcn_exp2f(s[b+1]); s[b+2] = __builtin_amdgcn_exp2f(s[b+2]); s[b+3] = __builtin_amdgcn_exp2f(s[b+3]); } while (0)
#define STEP(SC, SP, OP, VP, SN, KN, Q1, Q2) do { \
  EX4(SC, 0); SB(); OP = pv_lo(VP.v0, SP, OP, idx); SB(); \
  EX4(SC, 4); SB(); SN = qk_a(KN.k1, Q1); SB(); \
  EX4(SC, 8); SB(); OP = pv_hi(VP.v1, SP, OP, idx); SB(); \
  EX4(SC, 12); SB(); SN = qk_b(KN.k2, Q2, SN); SB(); } while (0)

__global__ void __launch_bounds__(512, 2)
fused(const u16* __restrict__ x, const u16* __restrict__ wqkv, const u16* __restrict__ wo, const u16* __restrict__ w1,
      const u16* __restrict__ b1, const u16* __restrict__ w2, const u16* __restrict__ b2, u16* __restrict__ out, char* __restrict__ ws) {
  __shared__ __attribute__((aligned(16))) char smem[L_END + 256];
  LAS char* lds = (LAS char*)smem;
  const unsigned lds0 = (unsigned)(size_t)lds;
  const int lane = threadIdx.x & 63, w = __builtin_amdgcn_readfirstlane(threadIdx.x >> 6);
  const int b = blockIdx.x, g = b & 7, idx8 = b >> 3;
  asm volatile("" :: "s"(x), "s"(wqkv), "s"(wo), "s"(w1), "s"(b1), "s"(w2), "s"(b2), "s"(out), "s"(ws));
  const int rt = (g & 3) * 4 + (idx8 & 3), ct = (g >> 2) * 8 + (idx8 >> 2);
  const bool vtype = ct >= 10;
  const int kq = lane >> 4, c16 = lane & 15;
  LAS float* st_r = (LAS float*)(lds + L_ST);
  LAS float* st_t = st_r + 128;
  LAS float* st_c = st_r + 256;
  GAS u64* fl = (GAS u64*)(ws + WS_F);
  const rsrc_t rw = mkrs(ws, WS_END), rx = mkrs(x, 2048 * D_ * 2);
  u64 tag = 0;
  const int head = 4 * g + (idx8 >> 3), qc = idx8 & 7;
  const int aw = w - 4, qh = aw & 1, kh = aw >> 1, r = lane & 31, h = lane >> 5, odd = head & 1, pr = head >> 1;
  int hstep = 0;
  unsigned fi = 0;
  STAMP(0);

  if (w >= 4) {
    const int hw = w - 4;
    rsrc_t rwt = mkrs(wqkv, D_ * 3 * D_ * 2);
    unsigned rs = 3 * D_ * 2, pad = vtype ? 32 : 8;
    unsigned wso = ct * 256 + 16 * hw * rs, xso = (rt * 128 + hw * 32) * (D_ * 2);
    const unsigned xv = (lane >> 3) * (D_ * 2) + (((lane & 7) ^ kq) << 4), xv1 = xv ^ 64;
    const int wc = c16 ^ (kq << 2);
    unsigned wv = ct < 15 ? kq * rs + (wc << 4) : OOB;
    unsigned nx = lds0 + hw * 4096, nw = lds0 + L_W + hw * 4096;
#define DX(i) dma16(rx, (i & 1) ? xv1 : xv, xso + i * 8 * (D_ * 2), nx + i * 1024)
#define DW(i) dma16(rwt, wv, wso + 4 * i * rs, nw + i * 1024 + (2 * hw + (i >> 1)) * pad)
#define NEXTX() do { xso += 128; nx = (nx == lds0 + hw * 4096 + 5 * XS) ? nx - 5 * XS : nx + XS; } while (0)
#define NEXTW() do { wso += 64 * rs; nw = (nw == lds0 + L_W + hw * 4096 + 2 * WSL) ? nw - 2 * WSL : nw + WSL; } while (0)
#define HBAR(n) asm volatile("s_waitcnt vmcnt(" #n ")\n\ts_barrier" ::: "memory")
    __builtin_amdgcn_s_setprio(1);
    DX(0); DW(0); DX(1); DW(1); DX(2); DW(2); DX(3); DW(3); NEXTW(); NEXTX();
#define DW4() do { DW(0); DW(1); DW(2); DW(3); NEXTW(); } while (0)
#define DX4() do { DX(0); DX(1); DX(2); DX(3); NEXTX(); } while (0)
    const char* pw = (const char*)w1 + 256 * F_ * 2 + (b * 4 + hw) * 192;
    DW4(); DX4(); STOUCH3(pw); pw += 64 * F_ * 2; HBAR(8);
    DX4();
#pragma unroll 1
    for (int s = 0; s < 7; ++s) {
      DW4(); DX4();
      STOUCH3(pw); pw += (s == 4 ? -576 : 64) * F_ * 2;
      HBAR(12);
    }
    const unsigned go = WS_S + (rt * 128 + (hw & 1) * 64 + lane) * 16;
    asm volatile("s_mov_b32 m0, %0\n\ts_nop 0\n\tbuffer_load_dwordx4 %1, %2, 0 offen sc1 lds" :: "s"(lds0 + L_G + hw * 1024), "v"(go), "s"(rw) : "memory");
    DW4(); STOUCH3(pw); pw += 64 * F_ * 2; HBAR(9);
    rwt = mkrs(w1, D_ * F_ * 2); rs = F_ * 2; pad = 8; wso = ct * 192 + (256 + 16 * hw) * rs; wv = wc < 12 ? kq * rs + (wc << 4) : OOB;
    xso -= 1280; nx = lds0 + hw * 4096 + 4 * XS; nw = lds0 + L_W + hw * 4096 + WSL;
    DW4();
    tag = uni64(*(LAS u64*)(lds + L_C));
    {
      const int c0 = pr * 5 + 2 * odd, e = (lane >> 3) & 1;
      const int tqk = (c0 + 2 * e) >> 4, tv = (head * 20 + 19 * e) >> 7;
      fi = F_Q + (lane < 16 ? 5 + tqk : 10 + tv) * 16 + 8 * kh + (lane & 7);
      if (lane >= 32) fi = F_Q + tqk * 16 + 2 * qc + qh;
    }
    HBAR(4);
    DW4();
    {
      u32x4 gr = *(LAS u32x4*)(lds + L_G + hw * 1024 + lane * 16);
#pragma unroll 1
      for (int n = 0; n < (1 << 20); ++n) {
        if (__all(gr[1] == (unsigned)tag && gr[3] == (unsigned)tag)) break;
        asm volatile("buffer_load_dwordx4 %0, %1, %2, 0 offen sc1\n\ts_waitcnt vmcnt(0)" : "=&v"(gr) : "v"(go), "s"(rw) : "memory");
      }
      st_r[hw * 64 + lane] = __uint_as_float(hw < 2 ? gr[0] : gr[2]);
      asm volatile("s_waitcnt lgkmcnt(0)" ::: "memory");
    }
    STOUCH3(pw);
    HBAR(4);
    DW4();
      const int sg = b / 10, cg = b - sg * 10, slab = 4 * sg + hw, s2 = slab - 48, l8 = lane >> 3;
      const rsrc_t rr = mkrs(slab < 48 ? w2 : wo, b < 170 ? (slab < 48 ? F_ * D_ * 2 : D_ * D_ * 2) : 0);
      const unsigned r0 = slab < 48 ? slab * 32 : (s2 < 16 ? s2 * 40 : (s2 - 16) * 160 + 16);
      const unsigned d1 = s2 < 16 ? 8 : 40, d2 = slab < 48 ? 16 : (s2 < 16 ? 20 : 80);
      const unsigned rv = (r0 + (s2 < 16 ? l8 : (l8 >> 2) * 20 + (l8 & 3))) * (D_ * 2) + cg * 128 + (lane & 7) * 16;
    unsigned rvi[4];
#pragma unroll
    for (int i = 0; i < 4; ++i) { rvi[i] = rv + ((i & 1) * d1 + (i >> 1) * d2) * (D_ * 2); asm volatile("" : "+v"(rvi[i])); }
    HBAR(4);
    DW4(); DX4(); HBAR(8);
    DW4(); DX4(); HBAR(12);
    DW4(); DX4(); wso -= 640 * rs; HBAR(12);
    DW4(); DX4(); HBAR(12);
    DW4();
    {
#pragma unroll
      for (int i = 0; i < 4; ++i) dma16(rr, rvi[i], 0, lds0 + 2 * XS + hw * 4096 + i * 1024);
    }
    HBAR(12);
    DW4(); nw = lds0 + 3 * XS + hw * 4096; DW4(); HBAR(12);
    asm volatile("s_mov_b32 m0, %0\n\ts_nop 0\n\tbuffer_load_dwordx4 %1, %2, 0 offen sc1 lds" :: "s"(lds0 + L_G + hw * 1024), "v"(fi * 128), "s"(mkrs(ws + WS_F, 1u << 17)) : "memory");
  } else {
    const int wm = w & 1, wn = w >> 1;
    const int row = rt * 128 + ct * 8 + 2 * w + (lane >> 5);
    {
      const unsigned lb = lds0 + 5 * XS + w * 4096, xo = row * (D_ * 2) + r * 16;
      dma16(rx, xo, 0, lb); dma16(rx, xo, 512, lb + 1024); dma16(rx, r < 16 ? xo : OOB, 1024, lb + 2048);
      asm volatile("s_mov_b32 m0, %0\n\ts_nop 0\n\tbuffer_load_dword %1, %2, 0 offen sc1 lds" :: "s"(lb + 3072), "v"(lane * 4), "s"(mkrs(ws + WS_F, 8)) : "memory");
      u64 pc; asm volatile("s_getpc_b64 %0" : "=s"(pc));
      STOUCH4((pc & ~(u64)255) + CODE_SKIP + (idx8 * 4 + w) * 256u);
      *(LAS u32x2*)(lds + L_M + threadIdx.x * 8) = u32x2{0x7fc00000u, 0x7fc00000u};
    }
    unsigned vo[4];
    if (vtype) {
#pragma unroll
      for (int i = 0; i < 4; ++i) {
        const int fv = (ct - 10) * 128 + 32 * G1<1>::span(w, i >> 1) + 16 * (i & 1) + c16, hd = fv / 20, d = fv - hd * 20;
        vo[i] = (WS_T + ((hd >> 1) * 64 + 4 * rt + 2 * wm) * PT + P_V + (hd & 1) * 1280 + kq * 320 + d * 16) | (ct < 15 ? 0u : OOB);
      }
    } else {
#pragma unroll
      for (int pp = 0; pp < 2; ++pp) {
        const int c = (ct < 5 ? ct : ct - 5) * 16 + 4 * G1<0>::span(w, pp) + kq, p5 = c / 5, pos = c - p5 * 5;
        const unsigned tb = WS_T + (p5 * 64 + 4 * rt + 2 * wm) * PT;
        vo[pp] = tb + pos * 512 + (ct < 5 ? P_Q + c16 * 16 : (8 * (c16 >> 2) + (c16 & 3)) * 16);
        vo[2 + pp] = (ct >= 5 && pos == 2 && c16 < 2) ? tb + P_ONE + c16 * PT : OOB;
      }
    }
    asm volatile("" : "+v"(vo[0]), "+v"(vo[1]), "+v"(vo[2]), "+v"(vo[3]));
    if (w < 2) touch(w ? mkrs(b1 + ct * 96, 192) : mkrs(wqkv + 94 * 3 * D_, 546 * 3 * D_ * 2), w ? lane * 4 : (b * 64 + lane) * 128, lds0 + (w ? L_ST + 1536 : L_PF));
    u32x4 xr[3]; u64 e0;
    auto stats_ld = [&]() {
      if (w < 2) asm volatile("s_waitcnt vmcnt(1)" ::: "memory"); else DRAIN();
#pragma unroll
      for (int i = 0; i < 3; ++i) xr[i] = *(LAS u32x4*)(lds + 5 * XS + w * 4096 + i * 1024 + lane * 16);
      e0 = *(LAS u64*)(lds + 5 * XS + w * 4096 + 3072);
    };
    auto stats = [&]() {
      float p = 0.f, q = 0.f;
#pragma unroll
      for (int i = 0; i < 12; ++i) {
        const unsigned u2 = xr[i >> 2][i & 3];
        const bf16x2 v2 = __builtin_bit_cast(bf16x2, u2);
        p = __builtin_amdgcn_fdot2_f32_bf16(v2, __builtin_bit_cast(bf16x2, 0x3f803f80u), p, false); q = __builtin_amdgcn_fdot2_f32_bf16(v2, v2, q, false);
      }
      p = dppadd<0x140>(dppadd<0x141>(dppadd<0x4E>(dppadd<0xB1>(p)))); q = dppadd<0x140>(dppadd<0x141>(dppadd<0x4E>(dppadd<0xB1>(q))));
      p = dppadd<0x142>(p); q = dppadd<0x142>(q);
      const float mu = p * (1.f / D_), rstd = __builtin_amdgcn_rsqf(fmaxf(q * (1.f / D_) - mu * mu, 0.f) + 1e-5f);
      tag = uni64(e0 + 1);
      if (threadIdx.x == 0) { *(LAS u64*)(lds + L_C) = tag; *(LAS u32x4*)(lds + L_C + 16) = u32x4{0, 0, 0, 0}; }
      st16(u32x4{__float_as_uint(rstd), (unsigned)tag, __float_as_uint(-mu * rstd), (unsigned)tag}, rw, r == 16 ? row * 16 : OOB, WS_S);
    };
    G1<2>::Frag f2; G1<2>::Ptr p2;
    const G1<2>::Off o2 = G1<2>::offs(w, lane);
    G1<2>::ptrx(p2, o2, lds, 4 * XS); G1<2>::ptrw(p2, o2, lds, WSL);
    {
      f32x4 acc[4][4];
      if (vtype) {
        f32x4 r4[4], t4[4]; float c1[4];
        G1<1>::Frag f1;
        LAS float* pr4 = st_r + 64 * wm + 4 * kq; LAS float* pc0 = st_c + 32 * G1<1>::span(w, 0) + c16; LAS float* pc1 = st_c + 32 * G1<1>::span(w, 1) + c16;
        asm volatile("" : "+v"(pr4), "+v"(pc0), "+v"(pc1));
        G1<1>::loop(acc, f1, G1<1>::offs(w, lane), lds, 0, 0, w, lane, -1, fl, 0, tag, [&](auto k) {
          constexpr int K = decltype(k)::v;
          if constexpr (K == -2) stats_ld();
          else if constexpr (K == -1) stats();
          else if constexpr (K < 4) { r4[K] = *(LAS f32x4*)(pr4 + 16 * K); t4[K] = *(LAS f32x4*)(pr4 + 128 + 16 * K); }
          else if constexpr (K < 6) { c1[2 * K - 8] = (K == 4 ? pc0 : pc1)[0]; c1[2 * K - 7] = (K == 4 ? pc0 : pc1)[16]; }
          else G1<2>::rds<2 * K - 12, 2 * K - 10>(f2, p2);
        }, ws);
        STAMP(2);
#pragma unroll
        for (int i = 0; i < 4; ++i)
#pragma unroll
          for (int a = 0; a < 2; ++a)
            st16(pk8(acc[2 * a][i] * r4[2 * a] + t4[2 * a] * c1[i], acc[2 * a + 1][i] * r4[2 * a + 1] + t4[2 * a + 1] * c1[i]), rw, vo[i], a * PT);
      } else {
        float r1[4], t1[4]; f32x4 c4[4];
        G1<0>::Frag f0;
        LAS float* pr1 = st_r + 64 * wm + c16; LAS float* pc0 = st_c + 32 * G1<0>::span(w, 0) + 8 * kq; LAS float* pc1 = st_c + 32 * G1<0>::span(w, 1) + 8 * kq;
        asm volatile("" : "+v"(pr1), "+v"(pc0), "+v"(pc1));
        G1<0>::loop(acc, f0, G1<0>::offs(w, lane), lds, 0, 0, w, lane, -1, fl, 0, tag, [&](auto k) {
          constexpr int K = decltype(k)::v;
          if constexpr (K == -2) stats_ld();
          else if constexpr (K == -1) stats();
          else if constexpr (K < 4) { r1[K] = pr1[16 * K]; t1[K] = pr1[128 + 16 * K]; }
          else if constexpr (K < 6) { c4[2 * K - 8] = *(LAS f32x4*)(K == 4 ? pc0 : pc1); c4[2 * K - 7] = *(LAS f32x4*)((K == 4 ? pc0 : pc1) + 4); }
          else G1<2>::rds<2 * K - 12, 2 * K - 10>(f2, p2);
        }, ws);
        STAMP(2);
        const float sc = ct < 5 ? QSCALE : 1.f;
        const unsigned dm = ct < 5 ? 256 : 64;
#pragma unroll
        for (int pp = 0; pp < 2; ++pp)
#pragma unroll
          for (int m = 0; m < 4; ++m)
            st16(pk8(acc[m][2 * pp] * (r1[m] * sc) + c4[2 * pp] * (t1[m] * sc), acc[m][2 * pp + 1] * (r1[m] * sc) + c4[2 * pp + 1] * (t1[m] * sc)), rw,
                 vo[pp] + (m & 1) * dm, (m >> 1) * PT);
        st16(u32x4{0x3f803f80u, 0x3f803f80u, 0x3f803f80u, 0x3f803f80u}, rw, min(vo[2], vo[3]), 0);
      }
    }
    STAMP(3);
    f32x4 acc[2][6];
    float r1[2], t1[2]; f32x4 c4[6]; u32x4 bb[3]; s16x4 rt_[8];
    LAS float* pr1 = st_r + 32 * w + c16;
    LAS char* prt = lds + 2 * XS + w * 4096 + (8 * kq + ((lane >> 2) & 3)) * 128 + (lane & 3) * 8;
    G1<2>::loop(acc, f2, o2, lds, 4 * XS, WSL, w, lane, 0, fl, F_Q + ct * 16 + rt, tag, [&](auto k) {
      constexpr int K = decltype(k)::v;
      if constexpr (K < 4) { rt_[2 * K] = trd(prt + K * 32); rt_[2 * K + 1] = trd(prt + K * 32 + 512); }
      else if constexpr (K < 6) { r1[K - 4] = pr1[16 * (K - 4)]; t1[K - 4] = pr1[128 + 16 * (K - 4)]; }
      else if constexpr (K < 9) bb[K - 6] = *(LAS u32x4*)(st_c + 128 + 16 * G1<2>::span(w, K - 6) + 4 * kq);
    }, ws);
    STAMP(4);
    __builtin_amdgcn_s_setprio(2);
    lds_meet((LAS unsigned*)(lds + L_C + 20), 4, lane);
#pragma unroll
    for (int pp = 0; pp < 3; ++pp) { LAS float* pc = st_c + 32 * G1<2>::span(w, pp) + 8 * kq; c4[2 * pp] = *(LAS f32x4*)pc; c4[2 * pp + 1] = *(LAS f32x4*)(pc + 4); }
    if (b < 170) {
      const int sg = b / 10, cg = b - sg * 10;
#pragma unroll
      for (int nt = 0; nt < 4; ++nt) {
        const int T = 4 * cg + nt, c2 = T / 5, t = T - c2 * 5;
        st16(__builtin_bit_cast(u32x4, cat8(rt_[2 * nt], rt_[2 * nt + 1])), rw, lane * 16, WS_W + ((c2 * 68 + 4 * sg + w) * 5 + t) * 1024);
      }
    }
#pragma unroll
    for (int pp = 0; pp < 3; ++pp) {
      const f32x4 ba = {blo(bb[pp][0]), bhi(bb[pp][0]), blo(bb[pp][1]), bhi(bb[pp][1])}, bc = {blo(bb[pp][2]), bhi(bb[pp][2]), blo(bb[pp][3]), bhi(bb[pp][3])};
#pragma unroll
      for (int m = 0; m < 2; ++m) {
        f32x4 v0 = acc[m][2 * pp] * r1[m] + (c4[2 * pp] * t1[m] + ba), v1 = acc[m][2 * pp + 1] * r1[m] + (c4[2 * pp + 1] * t1[m] + bc);
#pragma unroll
        for (int j = 0; j < 4; ++j) { v0[j] = fmaxf(v0[j], 0.f); v1[j] = fmaxf(v1[j], 0.f); }
        st16(pk8(v0 * v0, v1 * v1), rw, lane * 16, WS_H + ((8 * rt + 2 * w + m) * 48 + 3 * ct + G1<2>::span(w, pp)) * 1024);
      }
    }
    STAMP(5);
  }

  if (w >= 4) {
    const rsrc_t rk = mkrs(ws + WS_T + (pr * 64 + kh * 32) * PT, 32 * PT);
    const unsigned oK1 = odd * 1536 + lane * 16, oK2 = h ? P_ONE : 1024 + r * 16 + odd * 8;
    const int dv = r < 16 ? (r & 3) + 4 * (r >> 3) + 8 * ((r >> 2) & 1) : r;
    const unsigned oV0 = r < 20 ? P_V + odd * 1280 + h * 320 + dv * 16 : (r == 20 ? P_ONE : OOB), oV1 = r < 20 ? oV0 + 640 : oV0;
    asm volatile("" :: "v"(oK1), "v"(oK2), "v"(oV0), "v"(oV1));
    HBAR(1);
    DRAIN();
    if (!__all(*(volatile LAS u64*)(lds + L_G + aw * 1024 + lane * 16) == tag)) await<2>(fl, fi, tag);
    __builtin_amdgcn_fence(__ATOMIC_ACQUIRE, "wavefront");
    STAMP(6);
#pragma unroll 1
    for (int pass = 0; pass < 2; ++pass) {
      u64 f1 = 0, f2 = 0, f3 = 0, f4 = 0;
      bf16x8 q1[4]; s16x4 q2[4];
#pragma unroll
      for (int t = 0; t < 4; ++t) {
        const unsigned qo = WS_T + (pr * 64 + 8 * qc + 4 * qh + (t ^ (2 * kh))) * PT + P_Q;
        q1[t] = ld16(rw, oK1, qo); q2[t] = ld8(rw, h ? OOB : oK2, qo);
      }
      float mx[4] = {-3.0e38f, -3.0e38f, -3.0e38f, -3.0e38f};
      if (pass) {
#pragma unroll 1
        for (int j = 2; j < 32; ++j) {
          const bf16x8 k1 = ld16(rk, oK1, j * PT); const s16x4 k2 = ld8(rk, oK2, j * PT);
#pragma unroll
          for (int t = 0; t < 4; ++t) {
            const f32x16 sv = qk_b(k2, q2[t], qk_a(k1, q1[t]));
#pragma unroll
            for (int e = 0; e < 16; ++e) mx[t] = fmaxf(mx[t], sv[e]);
          }
        }
      }
      int idx = 0xDDDD; asm volatile("" : "+v"(idx));
      KV b0, b1_, b2_, b3;
      unsigned so = 0;
#define LDK(B, d) do { B.k1 = ld16(rk, oK1, so + (d) * PT); SB(); B.k2 = ld8(rk, oK2, so + (d) * PT); SB(); } while (0)
#define LDV(B, d) do { B.v0 = ld16(rk, oV0, so + (d) * PT); SB(); B.v1 = ld16(rk, oV1, so + (d) * PT); SB(); } while (0)
      SB();
      LDK(b0, 0); LDK(b1_, 1);
      f32x16 o0 = {}, o1 = {}, o2 = {}, o3 = {}, s0, s1, s2, s3 = {};
      b3.v0 = bf16x8{}; b3.v1 = bf16x8{};
      asm volatile("" : "+v"(o0), "+v"(o1), "+v"(o2), "+v"(o3), "+v"(s3), "+v"(b3.v0), "+v"(b3.v1)); SB();
      {
        f32x16 sm[8];
#define SMP(n) do { sm[n] = (n) < 4 ? qk_b(b0.k2, q2[(n) & 3], qk_a(b0.k1, q1[(n) & 3])) : qk_b(b1_.k2, q2[(n) & 3], qk_a(b1_.k1, q1[(n) & 3])); SB(); } while (0)
        SMP(0); SMP(1);
        FS(f1);
        LDV(b0, 0); LDV(b1_, 1); LDK(b2_, 2); LDV(b2_, 2); LDK(b3, 3);
        FS(f2);
#pragma unroll
        for (int n = 0; n < 8; ++n) { if (n < 6) SMP(n + 2); mx[n & 3] = max16(sm[n], mx[n & 3]); SB(); }
      }
#pragma unroll
      for (int t = 0; t < 4; ++t) {
        auto sw = __builtin_amdgcn_permlane32_swap(__float_as_uint(mx[t]), __float_as_uint(mx[t]), false, false);
        mx[t] = fmaxf(__uint_as_float(sw[0]), __uint_as_float(sw[1]));
      }
      FS(f3);
      volatile LAS f32x4* mp = (volatile LAS f32x4*)(lds + L_M);
      mp[aw * 32 + r] = f32x4{mx[0], mx[1], mx[2], mx[3]};
      f32x4 mo;
#pragma unroll 1
      for (int n = 0; n < (1 << 22); ++n) { mo = mp[(aw ^ 2) * 32 + r]; if (__all(mo[0] == mo[0] && mo[1] == mo[1] && mo[2] == mo[2] && mo[3] == mo[3])) break; }
      FS(f4);
#pragma unroll
      for (int t = 0; t < 4; ++t) {
        const float nm = -(fmaxf(mx[t], mo[t ^ 2]) + (pass ? 0.f : MARGIN));
        const float a0 = bhi(__float_as_uint(nm)), r0 = nm - a0, a1 = bhi(__float_as_uint(r0)), a2 = r0 - a1;
        const u32x2 pm = {(__float_as_uint(a0) >> 16) | __float_as_uint(a1), __float_as_uint(a2) >> 16};
        if (h) q2[t] = __builtin_bit_cast(s16x4, pm);
      }
      s0 = qk_b(b0.k2, q2[0], qk_a(b0.k1, q1[0])); s1 = qk_b(b0.k2, q2[1], qk_a(b0.k1, q1[1]));
      SB(); STAMP(7); SB();
#define KT(C, P, N, d) do { \
  STEP(s0, s3, o3, P, s2, C, q1[2], q2[2]); LDV(P, d + 3); __builtin_amdgcn_s_waitcnt(0x0F7A); SB(); \
  STEP(s1, s0, o0, C, s3, C, q1[3], q2[3]); LDK(C, d + 4); \
  STEP(s2, s1, o1, C, s0, N, q1[0], q2[0]); \
  STEP(s3, s2, o2, C, s1, N, q1[1], q2[1]); } while (0)
      for (int it = 0; it < 8; ++it) {
        KT(b0, b3, b1_, 0); KT(b1_, b0, b2_, 1); KT(b2_, b1_, b3, 2); KT(b3, b2_, b0, 3);
        so += 4 * PT;
      }
      SB(); STAMP(8); SB();
      o3 = pv_lo(b3.v0, s3, o3, idx); o3 = pv_hi(b3.v1, s3, o3, idx);
      int ln = __builtin_amdgcn_mbcnt_hi(~0u, __builtin_amdgcn_mbcnt_lo(~0u, 0u)); asm volatile("" : "+v"(ln));
#pragma unroll 1
      for (int n = 0; n < (1 << 22); ++n) { if (__builtin_amdgcn_readfirstlane(*(volatile LAS unsigned*)(lds + L_C + 20)) >= 16) break; __builtin_amdgcn_s_sleep(1); }
      asm volatile("" ::: "memory");
      LAS char* sp = lds + L_MRG + aw * 6144 + ln * 16;
#pragma unroll
      for (int e = 0; e < 3; ++e) {
        *(LAS f32x4*)(sp + e * 1024) = f32x4{o0[4 * e], o0[4 * e + 1], o0[4 * e + 2], o0[4 * e + 3]};
        *(LAS f32x4*)(sp + 3072 + e * 1024) = f32x4{o1[4 * e], o1[4 * e + 1], o1[4 * e + 2], o1[4 * e + 3]};
      }
      BAR_L();
      LAS char* gp = lds + L_MRG + (aw ^ 2) * 6144 + ln * 16;
      float chk = 0.f;
#pragma unroll
      for (int z = 0; z < 2; ++z) {
        const f32x16 k = z ? o3 : o2;
        f32x4 v[3];
#pragma unroll
        for (int e = 0; e < 3; ++e) v[e] = f32x4{k[4 * e], k[4 * e + 1], k[4 * e + 2], k[4 * e + 3]} + *(LAS f32x4*)(gp + z * 3072 + e * 1024);
        const float l = __uint_as_float(__builtin_amdgcn_permlane32_swap(__float_as_uint(v[2][0]), __float_as_uint(v[2][0]), false, false)[1]);
        const float inv = __builtin_amdgcn_rcpf(l);
        const int tt = (8 * qc + 4 * qh + ((2 + z) ^ (2 * kh))) * 2 + ((ln >> 4) & 1);
        st16(pk8(v[0] * inv, v[1] * inv), rw, (tt * 16 + pr) * 1024 + ((2 * odd + (ln >> 5)) * 16 + (ln & 15)) * 16, WS_O);
        st8(u32x2{pk2(v[2][0] * inv, v[2][1] * inv), pk2(v[2][2] * inv, v[2][3] * inv)}, rw, ln >= 32 ? OOB : (tt * 32 + head) * 128 + (ln & 15) * 8, WS_O2);
#pragma unroll
        for (int e = 0; e < 3; ++e) chk += (fabsf(v[e][0]) + fabsf(v[e][1])) + (fabsf(v[e][2]) + fabsf(v[e][3]));
      }
      mp[aw * 32 + (ln & 31)] = f32x4{__builtin_nanf(""), __builtin_nanf(""), __builtin_nanf(""), __builtin_nanf("")};
      if (!(chk < 1.0e37f)) *(volatile LAS unsigned*)(lds + L_C + 24) = 1;
      DRAIN();
      BAR_L();
      STAMP(9);
      if (pass || __builtin_amdgcn_readfirstlane(*(volatile LAS unsigned*)(lds + L_C + 24)) == 0) break;
    }
    if (threadIdx.x == 256) publish(fl, F_O + qc * 32 + head, tag);
    if (b == 0) {
      await<8>(fl, F_O + aw * 64 + lane, tag);
      lds_meet((LAS unsigned*)(lds + L_C + 28), 4, lane);
      if (threadIdx.x == 256) publish(fl, 0, tag);
    }
  } else {
    const int rt2 = 4 * g + (idx8 & 3), ct2 = idx8 >> 2;
    {
      DRAIN();
      unsigned n = 0;
      if (lane == 0) n = __hip_atomic_fetch_add((LAS unsigned*)(lds + L_C + 16), 1u, __ATOMIC_RELAXED, __HIP_MEMORY_SCOPE_WORKGROUP);
      if (lane == 0 && n == 3) publish(fl, F_H + b, tag);
    }
    {
      const int j = c16 - 4, hc = 4 * w + (c16 & 3), hr = rt2 >> 1, sg = j < 6 ? 3 * w + (j >> 1) : 12 + w + ((j - 6) >> 1);
      int pb = c16 < 4 ? (hr >> 2) + 4 * (hc >> 3) + 8 * ((hr & 3) + 4 * (hc & 7)) : sg * 10 + ((5 * ct2) >> 2) + (j & 1);
      if (c16 >= 14) pb = b;
      __builtin_amdgcn_s_sleep(8);
      await<6>(fl, F_H + pb, tag);
    }
    STAMP(6);
    const int o = (64 * rt2 + 16 * w + c16) * D_ + 80 * ct2 + 4 * kq;
    u32x2 xv[5], bv[5];
    f32x4 y[5], ya[5], yb[5];
    {
      f32x4 acc[4][5];
      bf16x8 f0[11], f1[11];
      const unsigned vo = lane * 16, wo_ = WS_W + (ct2 * 68 + 12 * w) * 5120, wq = WS_W + ((ct2 * 68 + 48) * 5 + w * 25) * 1024;
      unsigned ao[4];
#pragma unroll
      for (int m = 0; m < 4; ++m) ao[m] = WS_H + ((4 * rt2 + (m ^ w)) * 48 + 12 * w) * 1024;
      LAS char* wl = lds + L_WO + w * 25600 + lane * 16;
#define GL1(F, j, s) F[j] = (j) < 4 ? ld16(rw, vo, ao[(j) & 3] + (s) * 1024) : ((j) < 9 ? ld16(rw, vo + (((j) - 4) & 3) * 1024, wo_ + ((s) * 5 + (((j) - 4) & 4)) * 1024) : ld16(rw, vo + ((j) - 9) * 1024, wq + (s) * 2048))
#define GSTEP(FC, FN, s, LD, Z) do { _Pragma("unroll") for (int n = 0; n < 20; ++n) { \
        acc[n & 3][n >> 2] = __builtin_amdgcn_mfma_f32_16x16x32_bf16(FC[4 + (n >> 2)], FC[n & 3], (Z) ? f32x4{} : acc[n & 3][n >> 2], 0, 0, 0); \
        if ((LD) && n < 2) { GL1(FN, n, (s) + 1); } else if ((LD) && !(n & 1)) { GL1(FN, (n >> 1) + 1, (s) + 1); } \
        if (n == 17 || n == 19) *(LAS bf16x8*)(wl + (s) * 2048 + (n == 19 ? 1024 : 0)) = FC[n == 19 ? 10 : 9]; \
        SB(); } } while (0)
#pragma unroll
      for (int j = 0; j < 11; ++j) { GL1(f0, j, 0); }
      __builtin_amdgcn_s_setprio(0);
      SB();
      GSTEP(f0, f1, 0, true, true);
#pragma unroll 1
      for (int s = 1; s < 11; s += 2) { GSTEP(f1, f0, s, true, false); GSTEP(f0, f1, s + 1, true, false); }
      const bf16x8 wlast = ld16(rw, vo, wq + 24 * 1024);
#pragma unroll
      for (int t = 0; t < 5; ++t) xv[t] = *(const u32x2*)(x + o + 16 * t);
      SB();
      GSTEP(f1, f0, 11, false, false);
      __builtin_amdgcn_s_setprio(2);
      STAMP(7);
#pragma unroll
      for (int t = 0; t < 5; ++t) bv[t] = *(const u32x2*)(b2 + 80 * ct2 + 4 * kq + 16 * t);
      *(LAS bf16x8*)(wl + 24 * 1024) = wlast;
      LAS char* rp = lds + L_RED + lane * 16;
#pragma unroll
      for (int n = 0; n < 10; ++n) *(LAS f32x4*)(rp + (w ^ 2) * 10240 + n * 1024) = acc[2 + n / 5][n % 5];
      lds_meet((LAS unsigned*)(lds + L_C + 20), 8, lane);
#pragma unroll
      for (int t = 0; t < 5; ++t) {
        const f32x4 p1 = *(LAS f32x4*)(rp + w * 10240 + (5 + t) * 1024);
        f32x4 q1;
#pragma unroll
        for (int j = 0; j < 4; ++j) asm("v_add_f32 %0, %1, %2" : "=v"(q1[j]) : "v"(acc[1][t][j]), "v"(p1[j]));
        *(LAS f32x4*)(rp + w * 10240 + (5 + t) * 1024) = q1;
        y[t] = acc[0][t]; ya[t] = *(LAS f32x4*)(rp + w * 10240 + t * 1024);
      }
      lds_meet((LAS unsigned*)(lds + L_C + 20), 12, lane);
#pragma unroll
      for (int t = 0; t < 5; ++t) yb[t] = *(LAS f32x4*)(rp + (w ^ 1) * 10240 + (5 + t) * 1024);
      asm volatile("s_waitcnt lgkmcnt(0)" ::: "memory");
      if (lane == 0) __hip_atomic_fetch_add((LAS unsigned*)(lds + L_C + 20), 1u, __ATOMIC_RELAXED, __HIP_MEMORY_SCOPE_WORKGROUP);
    }
    STAMP(8);
    for (int pass = 0; pass < 2; ++pass) {
      BAR_L(); BAR_L();
      if (pass || __builtin_amdgcn_readfirstlane(*(volatile LAS unsigned*)(lds + L_C + 24)) == 0) break;
    }
    STAMP(9);
    bf16x8 wr[30], a[20];
    LAS char* wp = lds + L_WO + lane * 16; LAS char* wp2 = wp + 61440;
#pragma unroll
    for (int t = 0; t < 5; ++t) {
      asm volatile("" : "+v"(y[t]), "+v"(ya[t]), "+v"(yb[t]), "+v"(xv[t]));
      y[t] = (y[t] + ya[t]) + (yb[t] + f32x4{blo(xv[t][0]) + blo(bv[t][0]), bhi(xv[t][0]) + bhi(bv[t][0]), blo(xv[t][1]) + blo(bv[t][1]), bhi(xv[t][1]) + bhi(bv[t][1])});
      asm volatile("" : "+v"(y[t]));
    }
#pragma unroll
    for (int i = 0; i < 30; ++i) wr[i] = *(LAS bf16x8*)(wp + i * 1024);
    SB();
    await<1>(fl, F_O + g * 32 + (lane & 31), tag);
    STAMP(10);
    const int tt = 4 * rt2 + w;
#pragma unroll
    for (int s = 0; s < 16; ++s) a[s] = ld16(rw, lane * 16 + (s & 3) * 1024, WS_O + (tt * 16 + (s & 12)) * 1024);
#pragma unroll
    for (int s = 0; s < 4; ++s) a[16 + s] = cat8(ld8(rw, kq * 256 + c16 * 8 + s * 1024, WS_O2 + tt * 4096), ld8(rw, kq * 256 + 128 + c16 * 8 + s * 1024, WS_O2 + tt * 4096));
    SB();
#pragma unroll
    for (int i = 0; i < 100; ++i) {
      y[i % 5] = __builtin_amdgcn_mfma_f32_16x16x32_bf16(wr[i % 30], a[i / 5], y[i % 5], 0, 0, 0);
      if (i + 30 < 100) wr[i % 30] = *(LAS bf16x8*)(i + 30 < 60 ? wp + (i + 30) * 1024 : wp2 + (i - 30) * 1024);
      SB();
    }
#pragma unroll
    for (int t = 0; t < 5; ++t) *(u32x2*)(out + o + 16 * t) = u32x2{pk2(y[t][0], y[t][1]), pk2(y[t][2], y[t][3])};
    STAMP(11);
  }
}

extern "C" void kernel_launch(void* const* d_in, const int* in_sizes, int n_in, void* d_out, int out_size, void* d_ws, size_t ws_size,
                              hipStream_t stream) {
  fused<<<256, 512, 0, stream>>>((const u16*)d_in[0], (const u16*)d_in[1], (const u16*)d_in[2], (const u16*)d_in[4], (const u16*)d_in[5],
                                 (const u16*)d_in[6], (const u16*)d_in[7], (u16*)d_out, (char*)d_ws + ((size_t)228  __H0__ ));
}
```

```cpp
#include <hip/hip_runtime.h>
#include <hip/hip_bf16.h>

typedef __attribute__((ext_vector_type(8))) __bf16 bf16x8;
typedef __attribute__((ext_vector_type(16))) __bf16 bf16x16;
typedef __attribute__((ext_vector_type(2))) __bf16 bf16x2;
typedef __attribute__((ext_vector_type(4))) short s16x4;
typedef __attribute__((ext_vector_type(16))) float f32x16;
typedef __attribute__((ext_vector_type(8))) float f32x8;
typedef __attribute__((ext_vector_type(4))) float f32x4;
typedef __attribute__((ext_vector_type(4))) unsigned u32x4;
typedef __attribute__((ext_vector_type(2))) unsigned u32x2;
typedef unsigned short u16;
typedef unsigned long long u64;
typedef __amdgpu_buffer_rsrc_t rsrc_t;
#define LAS __attribute__((address_space(3)))
#define GAS __attribute__((address_space(1)))
#define SB() __builtin_amdgcn_sched_barrier(0)
#define RLX_AGENT __ATOMIC_RELAXED, __HIP_MEMORY_SCOPE_AGENT
#define BAR_L() do { asm volatile("" ::: "memory"); __builtin_amdgcn_s_waitcnt(0xC07F); asm volatile("s_barrier" ::: "memory"); } while (0)
#define DRAIN() asm volatile("s_waitcnt vmcnt(0)" ::: "memory")

constexpr int D_ = 640, F_ = 1536;
constexpr unsigned PT = 7808, P_V = 2560, P_Q = 5120, P_ONE = 7680;
constexpr unsigned WS_T = 0;
constexpr unsigned WS_H = 8u << 20;
constexpr unsigned WS_W = 16u << 20;
constexpr unsigned WS_O = 20u << 20;
constexpr unsigned WS_O2 = 22u << 20;
constexpr unsigned WS_S = 23u << 20;
constexpr unsigned WS_F = 24u << 20;
constexpr unsigned WS_END = WS_F;
constexpr unsigned F_Q = 256, F_H = 512, F_O = 768;
constexpr float QSCALE = 0.22360679774997896f * 1.4426950408889634f;
constexpr float MARGIN = 88.f;
constexpr unsigned OOB = 0x80000000u;
constexpr unsigned CODE_SKIP = 4608;
constexpr unsigned XS = 16448, WSL = 16640, L_W = 6 * XS;
constexpr unsigned L_WO = 0, L_RED = 102400, L_MRG = 122880;
constexpr unsigned L_ST = L_W + 3 * WSL;
constexpr unsigned L_G = L_ST + 2048;
constexpr unsigned L_M = L_G + 4096;
constexpr unsigned L_C = L_M + 4096;
constexpr unsigned L_PF = L_C + 48;
constexpr unsigned L_END = L_PF + 256;

#define STAMP(i)
#define STEPSTAMP()

#define FS(v)
__device__ __forceinline__ unsigned pk2(float a, float b) { bf16x2 r = {(__bf16)a, (__bf16)b}; return __builtin_bit_cast(unsigned, r); }
__device__ __forceinline__ u32x4 pk8(f32x4 a, f32x4 b) { return u32x4{pk2(a[0], a[1]), pk2(a[2], a[3]), pk2(b[0], b[1]), pk2(b[2], b[3])}; }
__device__ __forceinline__ float blo(unsigned u) { return __uint_as_float(u << 16); }
__device__ __forceinline__ float bhi(unsigned u) { return __uint_as_float(u & 0xffff0000u); }
__device__ __forceinline__ u64 uni64(u64 v) {
  return ((u64)(unsigned)__builtin_amdgcn_readfirstlane((unsigned)(v >> 32)) << 32) | (unsigned)__builtin_amdgcn_readfirstlane((unsigned)v);
}
__device__ __forceinline__ int fresh_lane() { int l = __builtin_amdgcn_mbcnt_hi(~0u, __builtin_amdgcn_mbcnt_lo(~0u, 0u)); asm volatile("" : "+v"(l)); return l; }
__device__ __forceinline__ rsrc_t mkrs(const void* p, unsigned bytes) { return __builtin_amdgcn_make_buffer_rsrc((void*)p, 0, bytes, 0x00020000); }
__device__ __forceinline__ void dma16(rsrc_t r, unsigned vo, unsigned so, unsigned lds_base) {
  asm volatile("s_mov_b32 m0, %0\n\ts_nop 0\n\tbuffer_load_dwordx4 %1, %2, %3 offen lds" :: "s"(lds_base), "v"(vo), "s"(r), "s"(so) : "memory");
}
__device__ __forceinline__ void touch(rsrc_t r, unsigned vo, unsigned lds_base) {
  asm volatile("s_mov_b32 m0, %0\n\ts_nop 0\n\tbuffer_load_dword %1, %2, 0 offen lds" :: "s"(lds_base), "v"(vo), "s"(r) : "memory");
}
#define STOUCH3(p) asm volatile("s_load_dword s100, %0, 0\n\ts_load_dword s100, %0, 64\n\ts_load_dword s100, %0, 128" :: "s"(p) : "s100")
#define SPFX(x) x
#define STOUCH4(p) asm volatile("s_load_dword s100, %0, 0\n\ts_load_dword s100, %0, 64\n\ts_load_dword s100, %0, 128\n\ts_load_dword s100, %0, 192" :: "s"(p) : "s100")
__device__ __forceinline__ bf16x8 ld16(rsrc_t r, unsigned vo, unsigned so) { return __builtin_bit_cast(bf16x8, __builtin_amdgcn_raw_buffer_load_b128(r, vo, so, 16)); }
__device__ __forceinline__ s16x4 ld8(rsrc_t r, unsigned vo, unsigned so) { return __builtin_bit_cast(s16x4, __builtin_amdgcn_raw_buffer_load_b64(r, vo, so, 16)); }
__device__ __forceinline__ void st16(u32x4 v, rsrc_t r, unsigned vo, unsigned so) { __builtin_amdgcn_raw_buffer_store_b128(v, r, vo + so, 0, 16); }
__device__ __forceinline__ void st8(u32x2 v, rsrc_t r, unsigned vo, unsigned so) { __builtin_amdgcn_raw_buffer_store_b64(v, r, vo + so, 0, 16); }
__device__ __forceinline__ void publish(GAS u64* f, unsigned i, u64 tag, int lane) {
  st16(u32x4{(unsigned)tag, (unsigned)(tag >> 32), (unsigned)tag, (unsigned)(tag >> 32)}, mkrs((const void*)(f + 16 * i), 128), lane * 16, 0);
}
template <int SLP> __device__ __forceinline__ void await(GAS u64* f, unsigned i, u64 tag) {
#pragma unroll 1
  for (int n = 0; n < (1 << 20); ++n) {
    if (__all(__hip_atomic_load(f + 16 * i, RLX_AGENT) == tag)) break;
    __builtin_amdgcn_s_sleep(SLP);
  }
  __builtin_amdgcn_fence(__ATOMIC_ACQUIRE, "wavefront");
}
__device__ __forceinline__ void lds_add(LAS unsigned* c, unsigned v) {
  asm volatile("s_mov_b64 exec, 1\n\tds_add_u32 %0, %1\n\ts_mov_b64 exec, -1" :: "v"(c), "v"(v) : "memory");
}
__device__ __forceinline__ unsigned lds_add_rtn(LAS unsigned* c, unsigned v) {
  unsigned r_;
  asm volatile("s_mov_b64 exec, 1\n\tds_add_rtn_u32 %0, %1, %2\n\ts_mov_b64 exec, -1\n\ts_waitcnt lgkmcnt(0)" : "=&v"(r_) : "v"(c), "v"(v) : "memory");
  return __builtin_amdgcn_readfirstlane(r_);
}
template <int SLP> __device__ __forceinline__ void lds_meet(LAS unsigned* c, unsigned n, int lane) {
  lds_add(c, 1u);
#pragma unroll 1
  for (int k = 0; k < (1 << 22); ++k) {
    if (__builtin_amdgcn_readfirstlane(*(volatile LAS unsigned*)c) >= n) break;
    if (SLP) __builtin_amdgcn_s_sleep(SLP);
  }
  asm volatile("" ::: "memory");
}
__device__ __forceinline__ s16x4 trd(LAS char* p) { return __builtin_amdgcn_ds_read_tr16_b64_v4i16((LAS s16x4*)p); }
__device__ __forceinline__ bf16x8 cat8(s16x4 a, s16x4 b) { return __builtin_bit_cast(bf16x8, __builtin_shufflevector(a, b, 0, 1, 2, 3, 4, 5, 6, 7)); }
template <int C> __device__ __forceinline__ float dppadd(float a) {
  return a + __int_as_float(__builtin_amdgcn_update_dpp(0, __float_as_int(a), C, 0xf, 0xf, true));
}

template <int N> struct IC { static constexpr int v = N; };
template <int MODE> struct G1 {
  static constexpr int NM = MODE == 2 ? 2 : 4, NI = MODE == 2 ? 6 : 4, PAD = MODE == 1 ? 32 : 8, NP = NI / 2;
  static constexpr int NMMA = 2 * NM * NI, NRD = 2 * (NM + 2 * NI), HEAD = MODE == 2 ? 6 : 12, R2H = 3, R2S = 18;
  struct Frag { bf16x8 x[2][NM]; s16x4 w[2][NI][2]; };
  struct Ptr { LAS char* x[2][2]; LAS char* w[NP]; };
  template <int R, int XO = 0, int WO = 0> static __device__ __forceinline__ void rd(Frag& f, const Ptr& p) {
    constexpr int u = R / (NRD / 2), r = R % (NRD / 2);
    if constexpr (r >= 2 && r < 2 + NM) f.x[u][r - 2] = *(LAS bf16x8*)(p.x[u][r & 1] + XO + ((r - 2) >> 1) * 4096 + (r & 1) * (MODE == 1 ? 512 : 2048));
    else { constexpr int q = r < 2 ? r : r - NM, i = q >> 1;
      f.w[u][i][q & 1] = trd(p.w[i >> 1] + WO + (i & 1) * PAD + u * (8192 + 4 * PAD) + (q & 1) * 1024); }
  }
  template <int N> static __device__ __forceinline__ void mma(f32x4 (&acc)[NM][NI], const Frag& f) {
    constexpr int u = N / (NM * NI), i = (N / NM) % NI, m = N % NM;
    const bf16x8 wf = cat8(f.w[u][i][0], f.w[u][i][1]);
    if constexpr (MODE == 1) asm volatile("v_mfma_f32_16x16x32_bf16 %0, %1, %2, %0" : "+v"(acc[m][i]) : "v"(f.x[u][m]), "v"(wf));
    else asm volatile("v_mfma_f32_16x16x32_bf16 %0, %1, %2, %0" : "+v"(acc[m][i]) : "v"(wf), "v"(f.x[u][m]));
  }
  template <int U> static __device__ __forceinline__ void csum(f32x4& cs, const Frag& f, bf16x8 sel, int idx) {
    const bf16x16 wf = __builtin_shufflevector(cat8(f.w[U][0][0], f.w[U][0][1]), cat8(f.w[U][1][0], f.w[U][1][1]), 0, 1, 2, 3, 4, 5, 6, 7, 8, 9, 10, 11, 12, 13, 14, 15);
    asm volatile("v_smfmac_f32_16x16x64_bf16 %0, %1, %2, %3" : "+v"(cs) : "v"(sel), "v"(wf), "v"(idx));
  }
  static __device__ __forceinline__ int span(int w, int j) { return MODE == 2 ? (w + j >= 3 ? w + j - 3 : w + j) : 2 * (w >> 1) + (j ^ (w & 1)); }
  template <int R0, int R1, int XO = 0, int WO = 0> static __device__ __forceinline__ void rds(Frag& f, const Ptr& p) {
    if constexpr (R0 < R1) { rd<R0, XO, WO>(f, p); rds<R0 + 1, R1, XO, WO>(f, p); } }
  template <int N0, int N1> static __device__ __forceinline__ void mmas(f32x4 (&acc)[NM][NI], const Frag& f) { if constexpr (N0 < N1) { mma<N0>(acc, f); mmas<N0 + 1, N1>(acc, f); } }
  template <int N> static __device__ __forceinline__ void mix(f32x4 (&acc)[NM][NI], const Frag& fc, Frag& fn, const Ptr& p) {
    if constexpr (N < NMMA) {
      constexpr int SPAN = NMMA - HEAD - 3, k = N - HEAD;
      constexpr int r0 = k >= SPAN ? NRD : (k * NRD + SPAN - 1) / SPAN, r1 = k + 1 >= SPAN ? NRD : ((k + 1) * NRD + SPAN - 1) / SPAN;
      mma<N>(acc, fc); rds<r0, r1>(fn, p); SB();
      mix<N + 1>(acc, fc, fn, p);
    }
  }
  struct Off { unsigned x, w[NP]; };
  static __device__ __forceinline__ Off offs(int w, int lane) {
    const int kq = lane >> 4, c16 = lane & 15, a = (lane >> 2) & 3;
    Off o;
    o.x = MODE == 1 ? (64 * (w & 1) + 8 * (c16 >> 2) + (c16 & 3)) * 128 + ((kq ^ ((c16 >> 1) & 1) ^ ((c16 >> 3) << 1) ^ (c16 & 4)) << 4)
                    : ((MODE == 2 ? 32 * w : 64 * (w & 1)) + c16) * 128 + ((kq ^ (c16 >> 1)) << 4);
    const unsigned wb = L_W + kq * (2048 + PAD) + a * 256 + (lane & 3) * (MODE == 1 ? 8 : 16);
#pragma unroll
    for (int j = 0; j < NP; ++j) o.w[j] = wb + (span(w, j) ^ a) * 64;
    return o;
  }
  template <int U> static __device__ __forceinline__ void ptrx(Ptr& p, const Off& o, LAS char* lds, unsigned sx) {
    p.x[U][0] = lds + sx + (o.x ^ (64 * U)); p.x[U][1] = lds + sx + (o.x ^ (64 * U + 32)); asm volatile("" : "+v"(p.x[U][0]), "+v"(p.x[U][1])); }
  static __device__ __forceinline__ void ptrw(Ptr& p, const Off& o, LAS char* lds, unsigned sw) {
#pragma unroll
    for (int j = 0; j < NP; ++j) { p.w[j] = lds + (sw + o.w[j]); asm volatile("" : "+v"(p.w[j])); } }
  template <int N, class T> static __device__ __forceinline__ void fin(f32x4 (&acc)[NM][NI], const Frag& fc, T& tail) {
    if constexpr (N < NMMA) { mma<N>(acc, fc); tail(IC<N - HEAD>{}); SB(); fin<N + 1>(acc, fc, tail); }
  }
  template <class T>
  static __device__ __forceinline__ void loop(f32x4 (&acc)[NM][NI], Frag& fa, const Off& o, LAS char* lds, unsigned sx, unsigned sw, int w, int lane, int pub,
                                              GAS u64* fl, unsigned fi, u64 tag, T tail, unsigned& stv) {
    const int kq = lane >> 4, c16 = lane & 15;
    Frag fb; Ptr p;
#pragma unroll
    for (int n = 0; n < NM * NI; ++n) { acc[n % NM][n / NM] = f32x4{}; asm volatile("" : "+v"(acc[n % NM][n / NM])); }
    const unsigned one = (kq >> 1) == ((c16 >> 1) & 1) ? 0x3f803f80u : 0u;
    bf16x8 sel = __builtin_bit_cast(bf16x8, u32x4{one, one, one, one});
    int sidx = (lane & 1) ? 0xEEEE : 0x4444;
    f32x4 cs = {};
    LAS float* cp = (LAS float*)(lds + L_ST + 1024) + 32 * span(w, 0) + (MODE == 1 ? c16 : 8 * (c16 >> 2) + (c16 & 3));
    asm volatile("s_nop 1" : "+v"(sel), "+v"(sidx), "+v"(cs), "+v"(cp));
    if constexpr (MODE != 2) { ptrx<0>(p, o, lds, sx); ptrx<1>(p, o, lds, sx); ptrw(p, o, lds, sw); SB(); tail(IC<-2>{}); STAMP(1); BAR_L(); STAMP(12); rds<0, NRD>(fa, p); }
#define G1_HEAD(FC, PUB, LAST, BAR) do { \
      mma<0>(acc, FC); sx = (sx == 5 * XS) ? 0 : sx + XS; asm volatile("" : "+s"(sx)); SB(); \
      csum<0>(cs, FC, sel, sidx); SB(); \
      mma<1>(acc, FC); sw = (sw == 2 * WSL) ? 0 : sw + WSL; asm volatile("" : "+s"(sw)); SB(); \
      mma<2>(acc, FC); ptrx<0>(p, o, lds, sx); SB(); \
      mma<3>(acc, FC); ptrw(p, o, lds, sw); SB(); \
      mmas<4, HEAD - 2>(acc, FC); SB(); \
      csum<1>(cs, FC, sel, sidx); SB(); \
      mma<HEAD - 2>(acc, FC); ptrx<1>(p, o, lds, sx); SB(); \
      mma<HEAD - 1>(acc, FC); SB(); \
      if (LAST) { asm volatile("s_nop 5" : "+v"(cs)); cp[0] = cs[0] + cs[1]; cp[MODE == 1 ? 16 : 4] = cs[2] + cs[3]; } \
      if (PUB) DRAIN(); \
      if (BAR) BAR_L(); \
      if ((PUB) && w == 0) publish(fl, fi, tag, lane); \
      SB(); } while (0)
    SB();
    G1_HEAD(fa, false, false, true);
#pragma unroll 1
    for (int s = 0; s < 4; ++s) {
      mix<HEAD>(acc, fa, fb, p);
      G1_HEAD(fb, false, false, true);
      mix<HEAD>(acc, fb, fa, p);
      G1_HEAD(fa, false, false, true);
    }
    mix<HEAD>(acc, fa, fb, p);
    G1_HEAD(fb, false, true, true);
    fin<HEAD>(acc, fb, tail);
    asm volatile("s_nop 7");
    SB();
  }
  template <int J, int N> static __device__ __forceinline__ void r2mix(f32x4 (&acc)[NM][NI], f32x4& cs, bf16x8 sel, int sidx, const Frag& fc, Frag& fn, const Ptr& pl, const Ptr& ph) {
    if constexpr (N < NMMA) {
      constexpr int H2 = R2H, SPAN = R2S, k = N - H2, xs = (5 + J) % 6;
      constexpr int r0 = k >= SPAN ? NRD : (k * NRD + SPAN - 1) / SPAN, r1 = k + 1 >= SPAN ? NRD : ((k + 1) * NRD + SPAN - 1) / SPAN;
      mma<N>(acc, fc);
      Ptr p = xs >= 3 ? ph : pl;
#pragma unroll
      for (int j = 0; j < NP; ++j) p.w[j] = J == 8 ? ph.w[j] : pl.w[j];
      rds<r0, r1, (xs % 3) * XS, J == 8 ? 0 : ((2 + J) % 3) * WSL>(fn, p); SB();
      if constexpr (N == 13) { csum<1>(cs, fc, sel, sidx); SB(); }
      r2mix<J, N + 1>(acc, cs, sel, sidx, fc, fn, pl, ph);
    }
  }
  template <int J, class PUB> static __device__ __forceinline__ void r2steps(f32x4 (&acc)[NM][NI], f32x4& cs, bf16x8 sel, int sidx, Frag& fc, Frag& fn, const Ptr& pl, const Ptr& ph, PUB& pub) {
    if constexpr (J < 9) {
      mmas<0, 2>(acc, fc); csum<0>(cs, fc, sel, sidx); mmas<2, R2H>(acc, fc); SB();
      if constexpr (J == 2) DRAIN();
      if constexpr (J < 8) BAR_L();
      if constexpr (J == 2) pub();
      SB();
      r2mix<J, R2H>(acc, cs, sel, sidx, fc, fn, pl, ph);
      r2steps<J + 1>(acc, cs, sel, sidx, fn, fc, pl, ph, pub);
    }
  }
  template <class PUB, class T>
  static __device__ __forceinline__ void loop2(f32x4 (&acc)[NM][NI], Frag& fa, const Off& o, const Ptr& ph, LAS char* lds, int w, int lane, PUB pub, T tail) {
    const int kq = lane >> 4, c16 = lane & 15;
    Frag fb; Ptr pl, ph2 = ph;
    ptrx<0>(pl, o, lds, 0); ptrx<1>(pl, o, lds, 0);
#pragma unroll
    for (int j = 0; j < NP; ++j) pl.w[j] = ph.w[j];
    ptrw(ph2, o, lds, 3 * XS - L_W);
#pragma unroll
    for (int n = 0; n < NM * NI; ++n) { acc[n % NM][n / NM] = f32x4{}; asm volatile("" : "+v"(acc[n % NM][n / NM])); }
    const unsigned one = (kq >> 1) == ((c16 >> 1) & 1) ? 0x3f803f80u : 0u;
    bf16x8 sel = __builtin_bit_cast(bf16x8, u32x4{one, one, one, one});
    int sidx = (lane & 1) ? 0xEEEE : 0x4444;
    f32x4 cs = {};
    LAS float* cp = (LAS float*)(lds + L_ST + 1024) + 32 * span(w, 0) + 8 * (c16 >> 2) + (c16 & 3);
    asm volatile("s_nop 1" : "+v"(sel), "+v"(sidx), "+v"(cs), "+v"(cp));
    SB();
    r2steps<0>(acc, cs, sel, sidx, fa, fb, pl, ph2, pub);
    mmas<0, 2>(acc, fb); csum<0>(cs, fb, sel, sidx); mmas<2, 4>(acc, fb); csum<1>(cs, fb, sel, sidx); mmas<4, HEAD>(acc, fb); SB();
    fin<HEAD>(acc, fb, tail);
    asm volatile("s_nop 7" : "+v"(cs));
    cp[0] = cs[0] + cs[1]; cp[4] = cs[2] + cs[3];
    SB();
  }
};

struct KV { bf16x8 k1; s16x4 k2; bf16x8 v0, v1; };
__device__ __forceinline__ f32x16 qk_a(bf16x8 k, bf16x8 q) { f32x16 z = {}; return __builtin_amdgcn_mfma_f32_32x32x16_bf16(k, q, z, 0, 0, 0); }
__device__ __forceinline__ f32x16 qk_b(s16x4 k, s16x4 q, f32x16 c) { return __builtin_amdgcn_mfma_f32_32x32x8bf16_1k(k, q, c, 0, 0, 0); }
__device__ __forceinline__ f32x16 pv_lo(bf16x8 v, f32x16 p, f32x16 o, int idx) {
  f32x8 h = __builtin_shufflevector(p, p, 0, 1, 2, 3, 4, 5, 6, 7);
  return __builtin_amdgcn_smfmac_f32_32x32x32_bf16(v, __builtin_bit_cast(bf16x16, h), o, idx, 0, 0); }
__device__ __forceinline__ f32x16 pv_hi(bf16x8 v, f32x16 p, f32x16 o, int idx) {
  f32x8 h = __builtin_shufflevector(p, p, 8, 9, 10, 11, 12, 13, 14, 15);
  return __builtin_amdgcn_smfmac_f32_32x32x32_bf16(v, __builtin_bit_cast(bf16x16, h), o, idx, 0, 0); }
template <int E> __device__ __forceinline__ void max8(const f32x16& s, float& m) {
  asm volatile("v_max3_f32 %0, %0, %1, %2\n\tv_max3_f32 %0, %0, %3, %4\n\tv_max3_f32 %0, %0, %5, %6\n\tv_max3_f32 %0, %0, %7, %8"
               : "+v"(m) : "v"(s[E]), "v"(s[E + 1]), "v"(s[E + 2]), "v"(s[E + 3]), "v"(s[E + 4]), "v"(s[E + 5]), "v"(s[E + 6]), "v"(s[E + 7]));
}
#define EX4(s, b) do { s[b] = __builtin_amdgcn_exp2f(s[b]); s[b+1] = __builtin_amdgcn_exp2f(s[b+1]); s[b+2] = __builtin_amdgcn_exp2f(s[b+2]); s[b+3] = __builtin_amdgcn_exp2f(s[b+3]); } while (0)
#define STEP(SC, SP, OP, VP, SN, KN, Q1, Q2) do { \
  EX4(SC, 0); SB(); OP = pv_lo(VP.v0, SP, OP, idx); SB(); \
  EX4(SC, 4); SB(); SN = qk_a(KN.k1, Q1); SB(); \
  EX4(SC, 8); SB(); OP = pv_hi(VP.v1, SP, OP, idx); SB(); \
  EX4(SC, 12); SB(); SN = qk_b(KN.k2, Q2, SN); SB(); } while (0)

__global__ void __launch_bounds__(512, 2)
fused(const u16* __restrict__ x, const u16* __restrict__ wqkv, const u16* __restrict__ wo, const u16* __restrict__ w1,
      const u16* __restrict__ b1, const u16* __restrict__ w2, const u16* __restrict__ b2, u16* __restrict__ out, char* __restrict__ ws) {
  __shared__ __attribute__((aligned(16))) char smem[L_END + 256];
  LAS char* lds = (LAS char*)smem;
  const unsigned lds0 = (unsigned)(size_t)lds;
  const int lane = threadIdx.x & 63, w = __builtin_amdgcn_readfirstlane(threadIdx.x >> 6);
  const int b = blockIdx.x, g = b & 7, idx8 = b >> 3;
  asm volatile("" :: "s"(x), "s"(wqkv), "s"(wo), "s"(w1), "s"(b1), "s"(w2), "s"(b2), "s"(out), "s"(ws));
  const int rt = (g & 3) * 4 + (idx8 & 3), ct = (g >> 2) * 8 + (idx8 >> 2);
  const bool vtype = ct >= 10;
  const int kq = lane >> 4, c16 = lane & 15;
  LAS float* st_r = (LAS float*)(lds + L_ST);
  LAS float* st_t = st_r + 128;
  LAS float* st_c = st_r + 256;
  GAS u64* fl = (GAS u64*)(ws + WS_F);
  const rsrc_t rw = mkrs(ws, WS_END), rx = mkrs(x, 2048 * D_ * 2);
  u64 tag = 0;
  const int head = 4 * g + (idx8 >> 3), qc = idx8 & 7;
  const int aw = w - 4, qh = aw & 1, kh = aw >> 1, r = lane & 31, h = lane >> 5, odd = head & 1, pr = head >> 1;
  int hstep = 0; unsigned stv = 0;
  unsigned oK1 = 0, oK2 = 0, oV0 = 0, oV1 = 0, oQ2 = 0;
  auto kv_offsets = [&](int ln) {
    const int r = ln & 31, h = ln >> 5;
    oK1 = odd * 1536 + ln * 16; oK2 = h ? P_ONE : 1024 + r * 16 + odd * 8; oQ2 = h ? OOB : oK2;
    const int dv = r < 16 ? (r & 3) + 4 * (r >> 3) + 8 * ((r >> 2) & 1) : r;
    oV0 = r < 20 ? P_V + odd * 1280 + h * 320 + dv * 16 : (r == 20 ? P_ONE : OOB); oV1 = r < 20 ? oV0 + 640 : oV0;
  };
  unsigned fi = 0;
  STAMP(0);

  if (w >= 4) {
    const int hw = w - 4;
    rsrc_t rwt = mkrs(wqkv, D_ * 3 * D_ * 2);
    unsigned rs = 3 * D_ * 2, pad = vtype ? 32 : 8;
    unsigned wso = ct * 256 + 16 * hw * rs, xso = (rt * 128 + hw * 32) * (D_ * 2);
    const unsigned xv = (lane >> 3) * (D_ * 2) + (((lane & 7) ^ kq) << 4), xv1 = xv ^ 64, xv2 = xv ^ 32, xv3 = xv ^ 96;
    const int wc = c16 ^ (kq << 2);
    unsigned wv = ct < 15 ? kq * rs + (wc << 4) : OOB;
    unsigned nx = lds0 + hw * 4096, nw = lds0 + L_W + hw * 4096;
#define DX(i) dma16(rx, i == 0 ? xv : (i == 1 ? xv1 : (i == 2 ? xv2 : xv3)), xso + i * 8 * (D_ * 2), nx + i * 1024)
#define DW(i) dma16(rwt, wv, wso + 4 * i * rs, nw + i * 1024 + (2 * hw + (i >> 1)) * pad)
#define NEXTX() do { xso += 128; nx = (nx == lds0 + hw * 4096 + 5 * XS) ? nx - 5 * XS : nx + XS; } while (0)
#define NEXTW() do { wso += 64 * rs; nw = (nw == lds0 + L_W + hw * 4096 + 2 * WSL) ? nw - 2 * WSL : nw + WSL; } while (0)
#define HBAR(n) asm volatile("s_waitcnt vmcnt(" #n ")\n\ts_barrier" ::: "memory")
    __builtin_amdgcn_s_setprio(1);
#define DW4() do { DW(0); DW(1); DW(2); DW(3); NEXTW(); } while (0)
#define DX4() do { DX(0); DX(1); DX(2); DX(3); NEXTX(); } while (0)
    const char* pw = (const char*)w1 + 256 * F_ * 2 + (b * 4 + hw) * 192;
    {
      DX(0); DW(0); DX(1); DW(1); DX(2); DW(2); DX(3); DW(3); NEXTW(); NEXTX();
      DX4();
#pragma unroll 1
      for (int s = -1; s < 7; ++s) { DW4(); DX4(); SPFX(STOUCH3(pw)); pw += (s == 4 ? -576 : 64) * F_ * 2; HBAR(12); }
    }
    const unsigned go = WS_S + (rt * 128 + (hw & 1) * 64 + lane) * 16;
    asm volatile("s_mov_b32 m0, %0\n\ts_nop 0\n\tbuffer_load_dwordx4 %1, %2, 0 offen sc1 lds" :: "s"(lds0 + L_G + hw * 1024), "v"(go), "s"(rw) : "memory");
    DW4(); SPFX(STOUCH3(pw)); pw += 64 * F_ * 2; HBAR(9);
    rwt = mkrs(w1, D_ * F_ * 2); rs = F_ * 2; pad = 8; wso = ct * 192 + (256 + 16 * hw) * rs; wv = wc < 12 ? kq * rs + (wc << 4) : OOB;
    xso -= 1280; nx = lds0 + hw * 4096 + 4 * XS; nw = lds0 + L_W + hw * 4096 + WSL;
    DW4();
    tag = uni64(*(LAS u64*)(lds + L_C));
    {
      asm volatile("s_waitcnt vmcnt(8)" ::: "memory");
      u32x4 gr = *(LAS u32x4*)(lds + L_G + hw * 1024 + lane * 16);
#pragma unroll 1
      for (int n = 0; n < (1 << 20); ++n) {
        if (__all(gr[1] == (unsigned)tag && gr[3] == (unsigned)tag)) break;
        asm volatile("buffer_load_dwordx4 %0, %1, %2, 0 offen sc1\n\ts_waitcnt vmcnt(0)" : "=&v"(gr) : "v"(go), "s"(rw) : "memory");
      }
      st_r[hw * 64 + lane] = __uint_as_float(hw < 2 ? gr[0] : gr[2]);
    }
    HBAR(4);
    DW4(); asm volatile("s_waitcnt lgkmcnt(0)" ::: "memory"); SPFX(STOUCH3(pw)); HBAR(4);
    DW4();
    {
      const int c0 = pr * 5 + 2 * odd, e = (lane >> 3) & 1;
      const int tqk = (c0 + 2 * e) >> 4, tv = (head * 20 + 19 * e) >> 7;
      fi = F_Q + (lane < 16 ? 5 + tqk : 10 + tv) * 16 + 8 * kh + (lane & 7);
      if (lane >= 32) fi = F_Q + tqk * 16 + 2 * qc + qh;
      kv_offsets(lane);
      asm volatile("" : "+v"(fi), "+v"(oK1), "+v"(oK2), "+v"(oV0), "+v"(oV1), "+v"(oQ2));
    }
    HBAR(4);
    DW4(); DX4(); HBAR(8);
    DW4(); DX4(); HBAR(12);
    DW4(); DX4(); wso -= 640 * rs; HBAR(12);
    DW4(); DX4(); HBAR(12);
    DW4(); HBAR(8);
    DW4(); nw = lds0 + 3 * XS + hw * 4096; DW4(); HBAR(8);
    asm volatile("s_mov_b32 m0, %0\n\ts_nop 0\n\tbuffer_load_dwordx4 %1, %2, 0 offen sc1 lds" :: "s"(lds0 + L_G + hw * 1024), "v"(fi * 128), "s"(mkrs(ws + WS_F, 1u << 17)) : "memory");
  } else {
    const int wm = w & 1, wn = w >> 1;
    const int row = rt * 128 + ct * 8 + 2 * w + (lane >> 5);
    {
      const unsigned lb = lds0 + 5 * XS + w * 4096, xo = row * (D_ * 2) + r * 16;
      dma16(rx, xo, 0, lb); dma16(rx, xo, 512, lb + 1024); dma16(rx, r < 16 ? xo : OOB, 1024, lb + 2048);
      asm volatile("s_mov_b32 m0, %0\n\ts_nop 0\n\tbuffer_load_dword %1, %2, 0 offen sc1 lds" :: "s"(lb + 3072), "v"(lane * 4), "s"(mkrs(ws + WS_F, 8)) : "memory");
      u64 pc; asm volatile("s_getpc_b64 %0" : "=s"(pc));
      STOUCH4((pc & ~(u64)255) + CODE_SKIP + (idx8 * 4 + w) * 256u);
      *(LAS u32x4*)(lds + L_M + threadIdx.x * 16) = u32x4{0x7fc00000u, 0x7fc00000u, 0x7fc00000u, 0x7fc00000u};
    }
    unsigned vo[4];
    if (vtype) {
#pragma unroll
      for (int i = 0; i < 4; ++i) {
        const int fv = (ct - 10) * 128 + 32 * G1<1>::span(w, i >> 1) + 16 * (i & 1) + c16, hd = fv / 20, d = fv - hd * 20;
        vo[i] = (WS_T + ((hd >> 1) * 64 + 4 * rt + 2 * wm) * PT + P_V + (hd & 1) * 1280 + kq * 320 + d * 16) | (ct < 15 ? 0u : OOB);
      }
    } else {
#pragma unroll
      for (int pp = 0; pp < 2; ++pp) {
        const int c = (ct < 5 ? ct : ct - 5) * 16 + 4 * G1<0>::span(w, pp) + kq, p5 = c / 5, pos = c - p5 * 5;
        const unsigned tb = WS_T + (p5 * 64 + 4 * rt + 2 * wm) * PT;
        vo[pp] = tb + pos * 512 + (ct < 5 ? P_Q : 0u) + c16 * 16;
        vo[2 + pp] = (ct >= 5 && pos == 2) ? tb + P_ONE + (c16 >> 3) * PT + (c16 & 7) * 16 : OOB;
      }
    }
    asm volatile("" : "+v"(vo[0]), "+v"(vo[1]), "+v"(vo[2]), "+v"(vo[3]));
    if (w < 2) touch(w ? mkrs(b1 + ct * 96, 192) : mkrs(wqkv + 94 * 3 * D_, 546 * 3 * D_ * 2), w ? lane * 4 : (b * 64 + lane) * 128, lds0 + (w ? L_ST + 1536 : L_PF));
    u32x4 xr[3]; u64 e0;
    auto stats_ld = [&]() {
      if (w < 2) asm volatile("s_waitcnt vmcnt(1)" ::: "memory"); else DRAIN();
#pragma unroll
      for (int i = 0; i < 3; ++i) xr[i] = *(LAS u32x4*)(lds + 5 * XS + w * 4096 + i * 1024 + lane * 16);
      e0 = *(LAS u64*)(lds + 5 * XS + w * 4096 + 3072);
    };
    auto stats = [&]() {
      float p = 0.f, q = 0.f;
#pragma unroll
      for (int i = 0; i < 12; ++i) {
        const unsigned u2 = xr[i >> 2][i & 3];
        const bf16x2 v2 = __builtin_bit_cast(bf16x2, u2);
        p = __builtin_amdgcn_fdot2_f32_bf16(v2, __builtin_bit_cast(bf16x2, 0x3f803f80u), p, false); q = __builtin_amdgcn_fdot2_f32_bf16(v2, v2, q, false);
      }
      p = dppadd<0x140>(dppadd<0x141>(dppadd<0x4E>(dppadd<0xB1>(p)))); q = dppadd<0x140>(dppadd<0x141>(dppadd<0x4E>(dppadd<0xB1>(q))));
      p = dppadd<0x142>(p); q = dppadd<0x142>(q);
      const float mu = p * (1.f / D_), rstd = __builtin_amdgcn_rsqf(fmaxf(q * (1.f / D_) - mu * mu, 0.f) + 1e-5f);
      tag = uni64(e0 + 1);
      if (threadIdx.x == 0) { *(LAS u64*)(lds + L_C) = tag; *(LAS u32x4*)(lds + L_C + 16) = u32x4{0, 0, 0, 0}; *(LAS u32x4*)(lds + L_C + 32) = u32x4{0, 0, 0, 0}; }
      st16(u32x4{__float_as_uint(rstd), (unsigned)tag, __float_as_uint(-mu * rstd), (unsigned)tag}, rw, r == 16 ? row * 16 : OOB, WS_S);
    };
    G1<2>::Frag f2; G1<2>::Ptr p2;
    const G1<2>::Off o2 = G1<2>::offs(w, lane);
    G1<2>::ptrx<0>(p2, o2, lds, 3 * XS); G1<2>::ptrx<1>(p2, o2, lds, 3 * XS); G1<2>::ptrw(p2, o2, lds, 0);
    {
      f32x4 acc[4][4];
      if (vtype) {
        f32x4 r4[4], t4[4]; float c1[4];
        G1<1>::Frag f1;
        LAS float* pr4 = st_r + 64 * wm + 8 * kq; LAS float* pc0 = st_c + 32 * G1<1>::span(w, 0) + c16; LAS float* pc1 = st_c + 32 * G1<1>::span(w, 1) + c16;
        asm volatile("" : "+v"(pr4), "+v"(pc0), "+v"(pc1));
        G1<1>::loop(acc, f1, G1<1>::offs(w, lane), lds, 0, 0, w, lane, -1, fl, 0, tag, [&](auto k) {
          constexpr int K = decltype(k)::v;
          if constexpr (K < 0) { stats_ld(); stats(); }
          else if constexpr (K < 4) { r4[K] = *(LAS f32x4*)(pr4 + 32 * (K >> 1) + 4 * (K & 1)); t4[K] = *(LAS f32x4*)(pr4 + 128 + 32 * (K >> 1) + 4 * (K & 1)); }
          else if constexpr (K < 6) { c1[2 * K - 8] = (K == 4 ? pc0 : pc1)[0]; c1[2 * K - 7] = (K == 4 ? pc0 : pc1)[16]; }
          else G1<2>::rds<2 * K - 12, 2 * K - 10, XS, WSL>(f2, p2);
        }, stv);
        STAMP(2);
#pragma unroll
        for (int i = 0; i < 4; ++i)
#pragma unroll
          for (int a = 0; a < 2; ++a)
            st16(pk8(acc[2 * a][i] * r4[2 * a] + t4[2 * a] * c1[i], acc[2 * a + 1][i] * r4[2 * a + 1] + t4[2 * a + 1] * c1[i]), rw, vo[i], a * PT);
      } else {
        float r1[4], t1[4]; f32x4 c4[4];
        G1<0>::Frag f0;
        LAS float* pr1 = st_r + 64 * wm + c16; LAS float* pc0 = st_c + 32 * G1<0>::span(w, 0) + 8 * kq; LAS float* pc1 = st_c + 32 * G1<0>::span(w, 1) + 8 * kq;
        asm volatile("" : "+v"(pr1), "+v"(pc0), "+v"(pc1));
        G1<0>::loop(acc, f0, G1<0>::offs(w, lane), lds, 0, 0, w, lane, -1, fl, 0, tag, [&](auto k) {
          constexpr int K = decltype(k)::v;
          if constexpr (K < 0) { stats_ld(); stats(); }
          else if constexpr (K < 4) { r1[K] = pr1[16 * K]; t1[K] = pr1[128 + 16 * K]; }
          else if constexpr (K < 6) { c4[2 * K - 8] = *(LAS f32x4*)(K == 4 ? pc0 : pc1); c4[2 * K - 7] = *(LAS f32x4*)((K == 4 ? pc0 : pc1) + 4); }
          else G1<2>::rds<2 * K - 12, 2 * K - 10, XS, WSL>(f2, p2);
        }, stv);
        STAMP(2);
        const float sc = ct < 5 ? QSCALE : 1.f;
#pragma unroll
        for (int pp = 0; pp < 2; ++pp)
#pragma unroll
          for (int m = 0; m < 4; ++m)
            st16(pk8(acc[m][2 * pp] * (r1[m] * sc) + c4[2 * pp] * (t1[m] * sc), acc[m][2 * pp + 1] * (r1[m] * sc) + c4[2 * pp + 1] * (t1[m] * sc)), rw,
                 vo[pp], (m & 1) * 256 + (m >> 1) * PT);
        st16(u32x4{0x3f803f80u, 0x3f803f80u, 0x3f803f80u, 0x3f803f80u}, rw, min(vo[2], vo[3]), 0);
      }
    }
    STAMP(3);
    f32x4 acc[2][6];
    float r1[2], t1[2]; f32x4 c4[6]; u32x4 bb[3];
    LAS float* pr1 = st_r + 32 * w + c16;
    G1<2>::loop2(acc, f2, o2, p2, lds, w, lane, [&]() { if (w == 0) publish(fl, F_Q + ct * 16 + rt, tag, lane); }, [&](auto k) {
      constexpr int K = decltype(k)::v;
      if constexpr (K < 2) { r1[K] = pr1[16 * K]; t1[K] = pr1[128 + 16 * K]; }
      else if constexpr (K < 5) bb[K - 2] = *(LAS u32x4*)(st_c + 128 + 16 * G1<2>::span(w, K - 2) + 4 * kq);
    });
    STAMP(4);
    __builtin_amdgcn_s_setprio(2);
    lds_meet<2>((LAS unsigned*)(lds + L_C + 20), 4, lane);
#pragma unroll
    for (int pp = 0; pp < 3; ++pp) { LAS float* pc = st_c + 32 * G1<2>::span(w, pp) + 8 * kq; c4[2 * pp] = *(LAS f32x4*)pc; c4[2 * pp + 1] = *(LAS f32x4*)(pc + 4); }
    u32x4 hq[6];
#pragma unroll
    for (int pp = 0; pp < 3; ++pp) {
      const f32x4 ba = {blo(bb[pp][0]), bhi(bb[pp][0]), blo(bb[pp][1]), bhi(bb[pp][1])}, bc = {blo(bb[pp][2]), bhi(bb[pp][2]), blo(bb[pp][3]), bhi(bb[pp][3])};
#pragma unroll
      for (int m = 0; m < 2; ++m) {
        f32x4 v0 = acc[m][2 * pp] * r1[m] + (c4[2 * pp] * t1[m] + ba), v1 = acc[m][2 * pp + 1] * r1[m] + (c4[2 * pp + 1] * t1[m] + bc);
#pragma unroll
        for (int j = 0; j < 4; ++j) { v0[j] = fmaxf(v0[j], 0.f); v1[j] = fmaxf(v1[j], 0.f); }
        hq[2 * pp + m] = pk8(v0 * v0, v1 * v1);
      }
    }
    asm volatile("" : "+v"(hq[0]), "+v"(hq[1]), "+v"(hq[2]), "+v"(hq[3]), "+v"(hq[4]), "+v"(hq[5]));
    const bool ret = w < 2 || (w == 2 && idx8 < 4);
    const int slab = w < 2 ? 2 * idx8 + w : 64 + idx8;
#pragma unroll
    for (int i = 0; i < 6; ++i) { st16(hq[i], rw, lane * 16, WS_H + ((8 * rt + 2 * w + (i & 1)) * 48 + 3 * ct + G1<2>::span(w, i >> 1)) * 1024); SB(); __builtin_amdgcn_s_sleep(4); SB(); }
    if (ret) {
      const int s2 = slab - 48;
      const rsrc_t rr = mkrs((slab < 48 ? w2 : wo) + 80 * g, ((slab < 48 ? F_ : D_) * D_ - 80 * g) * 2);
      const unsigned r0 = slab < 48 ? slab * 32 : (s2 < 16 ? s2 * 40 : (s2 - 16) * 160 + 16), rsh = s2 < 16 ? 4 : 2, rmu = slab < 48 ? 0 : (s2 < 16 ? 4 : 16);
#pragma unroll
      for (int i = 0; i < 5; ++i) {
        const unsigned c = 64 * i + lane, k = (c * 205) >> 11;
        dma16(rr, (k + (k >> rsh) * rmu) * (D_ * 2) + (c - k * 10) * 16, r0 * (D_ * 2), lds0 + L_RED + w * 5120 + i * 1024);
      }
      const unsigned tro = (8 * kq + ((lane >> 2) & 3)) * 160 + (lane & 3) * 16, tr4 = tro - (lane & 3) * 8 + 128;
      DRAIN();
#pragma unroll
      for (int t = 0; t < 5; ++t) {
        LAS char* p = lds + L_RED + w * 5120 + (t < 4 ? tro + (t >> 1) * 64 + (t & 1) * 8 : tr4);
        st16(__builtin_bit_cast(u32x4, cat8(trd(p), trd(p + 640))), rw, lane * 16 + WS_W + (g * 68 + slab) * 5120, t * 1024); SB();
      }
    }
    STAMP(5);
  }

  if (w >= 4) {
    const rsrc_t rk = mkrs(ws + WS_T + (pr * 64 + kh * 32) * PT, 32 * PT);
    unsigned qo[4];
#pragma unroll
    for (int t = 0; t < 4; ++t) { qo[t] = WS_T + (pr * 64 + 8 * qc + 4 * qh + (t ^ (2 * kh))) * PT + P_Q; asm volatile("" : "+s"(qo[t])); }
    asm volatile("" :: "s"(rk));
    HBAR(1);
    DRAIN();
    if (!__all(*(volatile LAS u64*)(lds + L_G + aw * 1024 + (oK1 & 1023)) == tag)) await<2>(fl, fi, tag);
    __builtin_amdgcn_fence(__ATOMIC_ACQUIRE, "wavefront");
    STAMP(6);
    auto sweep = [&](auto pass_) -> unsigned {
      constexpr int PASS = decltype(pass_)::v;
      const int lane = fresh_lane(), r = lane & 31, h = lane >> 5, c16 = lane & 15;
      if constexpr (PASS) kv_offsets(lane);
      bf16x8 q1[4]; s16x4 q2[4];
      KV b0, b1_, b2_, b3;
      unsigned so = 0;
#define LDK(B, d) do { B.k1 = ld16(rk, oK1, so + (d) * PT); SB(); B.k2 = ld8(rk, oK2, so + (d) * PT); SB(); } while (0)
#define LDV(B, d) do { B.v0 = ld16(rk, oV0, so + (d) * PT); SB(); B.v1 = ld16(rk, oV1, so + (d) * PT); SB(); } while (0)
      SB();
      LDK(b1_, 1);
#pragma unroll
      for (int t = 0; t < 4; ++t) { q1[t] = ld16(rw, oK1, qo[t]); SB(); q2[t] = ld8(rw, oQ2, qo[t]); SB(); }
      LDK(b0, 0);
      float mx[4] = {-3.0e38f, -3.0e38f, -3.0e38f, -3.0e38f};
      if constexpr (PASS) {
#pragma unroll 1
        for (int j = 2; j < 32; ++j) {
          const bf16x8 k1 = ld16(rk, oK1, j * PT); const s16x4 k2 = ld8(rk, oK2, j * PT);
#pragma unroll
          for (int t = 0; t < 4; ++t) {
            const f32x16 sv = qk_b(k2, q2[t], qk_a(k1, q1[t]));
#pragma unroll
            for (int e = 0; e < 16; ++e) mx[t] = fmaxf(mx[t], sv[e]);
          }
        }
      }
      int idx = 0xDDDD;
      f32x16 o0 = {}, o1 = {}, o2 = {}, o3 = {}, s0, s1, s2, s3;
      b3.v0 = bf16x8{}; b3.v1 = bf16x8{};
      LAS char* sp = lds + L_MRG + aw * 6144 + lane * 16; LAS char* gp = lds + L_MRG + (aw ^ 2) * 6144 + lane * 16;
      const int tt = (8 * qc + 4 * qh + (2 ^ (2 * kh))) * 2 + (r >> 4);
      unsigned oa = WS_O + (tt * 16 + pr) * 1024 + ((2 * odd + h) * 16 + c16) * 16, ob = h ? OOB : WS_O2 + (tt * 32 + head) * 128 + c16 * 8;
      volatile LAS f32x4* mw = (volatile LAS f32x4*)(lds + L_M) + aw * 64 + lane; volatile LAS f32x4* mr = (volatile LAS f32x4*)(lds + L_M) + (aw ^ 2) * 64 + r;
      asm volatile("" : "+v"(o0), "+v"(o1), "+v"(o2), "+v"(o3), "+v"(b3.v0), "+v"(b3.v1), "+v"(idx), "+v"(sp), "+v"(gp), "+v"(oa), "+v"(ob), "+v"(mw), "+v"(mr)); SB();
      {
        f32x16 sm[8];
#define SQ(n) ((n) < 4 ? (n) : ((n) - 2) & 3)
#define SMA(n) do { sm[n] = qk_a((n) < 4 ? b1_.k1 : b0.k1, q1[SQ(n)]); if ((n) == 6) s0 = sm[n]; if ((n) == 7) s1 = sm[n]; SB(); } while (0)
#define SMB(n) do { sm[n] = qk_b((n) < 4 ? b1_.k2 : b0.k2, q2[SQ(n)], sm[n]); SB(); } while (0)
        SMA(0); SMB(0); SMA(1); SMB(1);
        FS(1);
        LDV(b0, 0); LDV(b1_, 1); LDK(b2_, 2); LDV(b2_, 2); LDK(b3, 3);
        FS(2);
#pragma unroll
        for (int n = 0; n < 8; ++n) {
          if (n < 6) SMA(n + 2);
          max8<0>(sm[n], mx[SQ(n)]); SB();
          if (n < 6) SMB(n + 2);
          max8<8>(sm[n], mx[SQ(n)]); SB();
        }
      }
      FS(3);
      *mw = f32x4{mx[0], mx[1], mx[2], mx[3]};
      if constexpr (PASS) lds_meet<0>((LAS unsigned*)(lds + L_C + 44), 4, lane);
#pragma unroll
      for (int t = 0; t < 4; ++t) {
        auto sw = __builtin_amdgcn_permlane32_swap(__float_as_uint(mx[t]), __float_as_uint(mx[t]), false, false);
        asm volatile("v_max_f32 %0, %1, %2" : "=v"(mx[t]) : "v"(sw[0]), "v"(sw[1]));
      }
      s3 = o3; asm volatile("" : "+v"(s3));
      f32x4 ma, mb;
#pragma unroll 1
      for (int n = 0; n < (1 << 22); ++n) {
        ma = mr[0]; mb = mr[32];
        if (PASS || !__any(__builtin_isunordered(ma[0], mb[0]) | __builtin_isunordered(ma[1], mb[1]) | __builtin_isunordered(ma[2], mb[2]) | __builtin_isunordered(ma[3], mb[3]))) break;
      }
      FS(4);
#pragma unroll
      for (int t = 0; t < 4; ++t) {
        float nm;
        asm("v_max3_f32 %0, %1, %2, %3\n\tv_sub_f32 %0, %4, %0" : "=&v"(nm) : "v"(mx[t]), "v"(ma[t ^ 2]), "v"(mb[t ^ 2]), "s"(PASS ? 0.f : -MARGIN));
        u32x2 pm = __builtin_bit_cast(u32x2, q2[t]);
        if constexpr (PASS) {
          const float a0 = bhi(__float_as_uint(nm)), r0 = nm - a0, a1 = bhi(__float_as_uint(r0)), a2 = r0 - a1;
          if (h) pm = u32x2{(__float_as_uint(a0) >> 16) | __float_as_uint(a1), __float_as_uint(a2) >> 16};
        } else if (h) pm[0] = __float_as_uint(nm) >> 16;
        q2[t] = __builtin_bit_cast(s16x4, pm);
        if (t == 0) s0 = qk_b(b0.k2, q2[0], s0);
        if (t == 1) s1 = qk_b(b0.k2, q2[1], s1);
        SB();
      }
      if constexpr (!PASS) { STAMP(7); SB(); }
#define KT(C, P, N, d) do { \
  STEP(s0, s3, o3, P, s2, C, q1[2], q2[2]); LDV(P, d + 3); __builtin_amdgcn_s_waitcnt(0x0F7A); SB(); \
  STEP(s1, s0, o0, C, s3, C, q1[3], q2[3]); LDK(C, d + 4); \
  STEP(s2, s1, o1, C, s0, N, q1[0], q2[0]); \
  STEP(s3, s2, o2, C, s1, N, q1[1], q2[1]); } while (0)
#pragma unroll 1
      for (int it = 0; it < 7; ++it) {
        KT(b0, b3, b1_, 0); KT(b1_, b0, b2_, 1); KT(b2_, b1_, b3, 2); KT(b3, b2_, b0, 3);
        so += 4 * PT;
      }
#define KTE(C, P, N) do { STEP(s0, s3, o3, P, s2, C, q1[2], q2[2]); STEP(s1, s0, o0, C, s3, C, q1[3], q2[3]); \
  STEP(s2, s1, o1, C, s0, N, q1[0], q2[0]); STEP(s3, s2, o2, C, s1, N, q1[1], q2[1]); } while (0)
#define SEND(O, z, e) do { *(LAS f32x4*)(sp + (z) * 3072 + (e) * 1024) = f32x4{O[4 * (e)], O[4 * (e) + 1], O[4 * (e) + 2], O[4 * (e) + 3]}; SB(); } while (0)
      STEP(s0, s3, o3, b3, s2, b0, q1[2], q2[2]); LDV(b3, 3);
      STEP(s1, s0, o0, b0, s3, b0, q1[3], q2[3]); STEP(s2, s1, o1, b0, s0, b1_, q1[0], q2[0]); STEP(s3, s2, o2, b0, s1, b1_, q1[1], q2[1]);
      KTE(b1_, b0, b2_); KTE(b2_, b1_, b3);
      STEP(s0, s3, o3, b2_, s2, b3, q1[2], q2[2]); STEP(s1, s0, o0, b3, s3, b3, q1[3], q2[3]);
      EX4(s2, 0); SB(); o1 = pv_lo(b3.v0, s1, o1, idx); SB(); EX4(s2, 4); SB(); o1 = pv_hi(b3.v1, s1, o1, idx); SB();
      EX4(s2, 8); SB(); SEND(o0, 0, 0); SEND(o0, 0, 1); EX4(s2, 12); SB(); SEND(o0, 0, 2);
      o2 = pv_lo(b3.v0, s2, o2, idx); SB(); EX4(s3, 0); SB(); SEND(o1, 1, 0); SEND(o1, 1, 1);
      o2 = pv_hi(b3.v1, s2, o2, idx); SB(); EX4(s3, 4); SB(); SEND(o1, 1, 2);
      lds_add((LAS unsigned*)(lds + L_C + 32 + 4 * qh), 1u); SB();
      EX4(s3, 8); SB(); o3 = pv_lo(b3.v0, s3, o3, idx); SB(); EX4(s3, 12); SB(); o3 = pv_hi(b3.v1, s3, o3, idx); SB();
      asm volatile("" : "+v"(o2), "+v"(o3));
      if constexpr (!PASS) { STAMP(8); SB(); }
#pragma unroll 1
      for (int n = 0; n < (1 << 22); ++n) { if (__builtin_amdgcn_readfirstlane(*(volatile LAS unsigned*)(lds + L_C + 32 + 4 * qh)) >= 2 + 2 * PASS) break; }
      asm volatile("" ::: "memory");
      f32x4 v[2][3]; float l[2];
#pragma unroll
      for (int z = 0; z < 2; ++z) {
        const f32x16 k = z ? o3 : o2;
#pragma unroll
        for (int e = 0; e < 3; ++e) v[z][e] = f32x4{k[4 * e], k[4 * e + 1], k[4 * e + 2], k[4 * e + 3]} + *(LAS f32x4*)(gp + z * 3072 + e * 1024);
        l[z] = __uint_as_float(__builtin_amdgcn_permlane32_swap(__float_as_uint(v[z][2][0]), __float_as_uint(v[z][2][0]), false, false)[1]);
        const float inv = __builtin_amdgcn_rcpf(l[z]);
        st16(pk8(v[z][0] * inv, v[z][1] * inv), rw, oa, z * 32768);
        st8(u32x2{pk2(v[z][2][0] * inv, v[z][2][1] * inv), pk2(v[z][2][2] * inv, v[z][2][3] * inv)}, rw, ob, z * 8192);
      }
      SB();
      unsigned bad = 1u;
      if constexpr (!PASS) {
        float chk = 0.f;
#pragma unroll
        for (int e = 0; e < 6; ++e) chk += (fabsf(v[e / 3][e % 3][0]) + fabsf(v[e / 3][e % 3][1])) + (fabsf(v[e / 3][e % 3][2]) + fabsf(v[e / 3][e % 3][3]));
        if (__any(!(chk < 1.0e37f) || !(fminf(l[0], l[1]) > 1.0e-30f))) bad = 0x101u;
      }
      int ln = fresh_lane();
      asm volatile("s_waitcnt vmcnt(0)" : "+s"(bad), "+v"(ln) :: "memory");
      unsigned cn = lds_add_rtn((LAS unsigned*)(lds + L_C + 24), bad) + bad;
      if (PASS ? (cn & 255) == 8 : cn == 4) publish(fl, F_O + qc * 32 + head, tag, ln);
      if constexpr (!PASS) {
        STAMP(9);
#pragma unroll 1
        for (int n = 0; n < (1 << 22); ++n) { if ((cn & 255) >= 4) break; cn = __builtin_amdgcn_readfirstlane(*(volatile LAS unsigned*)(lds + L_C + 24)); }
        asm volatile("" ::: "memory");
      }
      return cn;
    };
    if (__builtin_expect(sweep(IC<0>{}) >= 256, 0)) sweep(IC<1>{});
    const int lane = fresh_lane();
    {
      const rsrc_t rf = mkrs(ws + WS_F, 1u << 17);
      unsigned fo = lane < 32 ? (F_O + g * 32 + lane) * 128 : OOB;
      volatile LAS unsigned* go_ = (volatile LAS unsigned*)(lds + L_C + 40);
#pragma unroll 1
      for (int n = 0; n < (1 << 20); ++n) {
        u32x2 v;
        asm volatile("buffer_load_dwordx2 %0, %1, %2, 0 offen sc1\n\ts_waitcnt vmcnt(0)" : "=&v"(v) : "v"(fo), "s"(rf) : "memory");
        if (v[0] == (unsigned)tag && v[1] == (unsigned)(tag >> 32)) fo = OOB;
        if (__all(fo == OOB)) break;
      }
      *go_ = 1;
    }
    if (b == 0) {
      await<8>(fl, F_O + aw * 64 + lane, tag);
      lds_meet<2>((LAS unsigned*)(lds + L_C + 28), 4, lane);
      if (w == 4) publish(fl, 0, tag, lane);
    }
  } else {
    const int rt2 = 4 * g + (idx8 & 3), ct2 = idx8 >> 2;
    {
      DRAIN();
      if (lds_add_rtn((LAS unsigned*)(lds + L_C + 16), 1u) == 3) publish(fl, F_H + b, tag, lane);
      const int j = lane & 15, hc = 4 * w + (j & 3), hr = rt2 >> 1;
      const int pb = (j < 4 || j > 12) ? (hr >> 2) + 4 * (hc >> 3) + 8 * ((hr & 3) + 4 * (hc & 7)) : 8 * (j < 10 ? 6 * w + j - 4 : (j < 12 ? 14 + 2 * w + j : w)) + ct2;
      __builtin_amdgcn_s_sleep(8);
      await<6>(fl, F_H + pb, tag);
    }
    STAMP(6);
    const int o = (64 * rt2 + 16 * w + c16) * D_ + 80 * ct2;
    u32x4 xv[2], bv[2]; u32x2 xv4, bv4;
    f32x4 y[5], ya[5], yb[5], yc[5];
    {
      f32x4 acc[4][5];
      bf16x8 f0[11], f1[11];
      const unsigned vo = lane * 16, wo_ = WS_W + (ct2 * 68 + 12 * w) * 5120, wq = WS_W + (ct2 * 68 + 48) * 5120;
      unsigned ao[4];
#pragma unroll
      for (int m = 0; m < 4; ++m) ao[m] = WS_H + ((4 * rt2 + (m ^ w)) * 48 + 12 * w) * 1024;
      LAS char* wl = lds + L_WO + lane * 16;
#define WOFF(s) ((s) < 10 ? w * 20480 + (s) * 2048 : 81920 + w * 5120 + ((s) - 10) * 2048)
#define GL1(F, j, s) F[j] = (j) < 4 ? ld16(rw, vo, ao[(j) & 3] + (s) * 1024) : ((j) < 9 ? ld16(rw, vo + (((j) - 4) & 3) * 1024, wo_ + ((s) * 5 + (((j) - 4) & 4)) * 1024) : ld16(rw, vo + ((j) - 9) * 1024, wq + WOFF(s)))
#define GSTEP(FC, FN, s, LD, Z) do { _Pragma("unroll") for (int n = 0; n < 20; ++n) { \
        acc[n & 3][n >> 2] = __builtin_amdgcn_mfma_f32_16x16x32_bf16(FC[4 + (n >> 2)], FC[n & 3], (Z) ? f32x4{} : acc[n & 3][n >> 2], 0, 0, 0); \
        if ((LD) && n < 2) { GL1(FN, n, (s) + 1); } else if ((LD) && !(n & 1)) { GL1(FN, (n >> 1) + 1, (s) + 1); } \
        if (n == 17 || n == 19) *(LAS bf16x8*)(wl + WOFF(s) + (n == 19 ? 1024 : 0)) = FC[n == 19 ? 10 : 9]; \
        SB(); } } while (0)
#pragma unroll
      for (int j = 0; j < 11; ++j) { GL1(f0, j, 0); }
      __builtin_amdgcn_s_setprio(0);
      SB();
      GSTEP(f0, f1, 0, true, true);
#pragma unroll 1
      for (int s = 1; s < 11; s += 2) { GSTEP(f1, f0, s, true, false); GSTEP(f0, f1, s + 1, true, false); }
      const bf16x8 wlast = ld16(rw, vo, wq + WOFF(12));
      xv[0] = *(const u32x4*)(x + o + 8 * kq); xv[1] = *(const u32x4*)(x + o + 32 + 8 * kq); xv4 = *(const u32x2*)(x + o + 64 + 4 * kq);
      bv[0] = *(const u32x4*)(b2 + 80 * ct2 + 8 * kq); bv[1] = *(const u32x4*)(b2 + 80 * ct2 + 32 + 8 * kq); bv4 = *(const u32x2*)(b2 + 80 * ct2 + 64 + 4 * kq);
      SB();
      GSTEP(f1, f0, 11, false, false);
      __builtin_amdgcn_s_setprio(2);
      STAMP(7);
      *(LAS bf16x8*)(wl + WOFF(12)) = wlast;
      LAS char* rp = lds + L_RED + lane * 16;
      LAS unsigned* gc = (LAS unsigned*)(lds + L_C + 20);
#pragma unroll
      for (int t = 0; t < 5; ++t) *(LAS f32x4*)(rp + w * 5120 + t * 1024) = acc[3][t];
      lds_meet<2>(gc, 8, lane);
#pragma unroll
      for (int t = 0; t < 5; ++t) { yc[t] = *(LAS f32x4*)(rp + (w ^ 3) * 5120 + t * 1024); *(LAS f32x4*)(rp + (w ^ 3) * 5120 + t * 1024) = acc[2][t]; }
      lds_meet<2>(gc, 12, lane);
#pragma unroll
      for (int t = 0; t < 5; ++t) { ya[t] = *(LAS f32x4*)(rp + (w ^ 1) * 5120 + t * 1024); *(LAS f32x4*)(rp + (w ^ 1) * 5120 + t * 1024) = acc[1][t]; }
      lds_meet<2>(gc, 16, lane);
#pragma unroll
      for (int t = 0; t < 5; ++t) { yb[t] = *(LAS f32x4*)(rp + w * 5120 + t * 1024); y[t] = acc[0][t]; }
    }
    STAMP(8);
#pragma unroll 1
    for (int n = 0; n < (1 << 22); ++n) { if (__builtin_amdgcn_readfirstlane(*(volatile LAS unsigned*)(lds + L_C + 32 + 4 * (w & 1))) >= 1) break; __builtin_amdgcn_s_sleep(4); }
    STAMP(9);
    __builtin_amdgcn_s_setprio(0);
    bf16x8 wr[30], a[20];
    LAS char* wp = lds + L_WO + lane * 16; LAS char* wp2 = wp + 61440;
#pragma unroll
    for (int t = 0; t < 5; ++t) {
      asm volatile("" : "+v"(y[t]), "+v"(ya[t]), "+v"(yb[t]), "+v"(yc[t]));
      unsigned x0 = t < 4 ? xv[(t >> 1) & 1][2 * (t & 1)] : xv4[0], x1 = t < 4 ? xv[(t >> 1) & 1][2 * (t & 1) + 1] : xv4[1];
      const unsigned b0 = t < 4 ? bv[(t >> 1) & 1][2 * (t & 1)] : bv4[0], b1v = t < 4 ? bv[(t >> 1) & 1][2 * (t & 1) + 1] : bv4[1];
      asm volatile("" : "+v"(x0), "+v"(x1));
      y[t] = (y[t] + ya[t]) + (yb[t] + yc[t]) + (f32x4{blo(x0) + blo(b0), bhi(x0) + bhi(b0), blo(x1) + blo(b1v), bhi(x1) + bhi(b1v)});
      asm volatile("" : "+v"(y[t]));
    }
#pragma unroll
    for (int i = 0; i < 30; ++i) wr[i] = *(LAS bf16x8*)(wp + i * 1024);
    SB();
#pragma unroll 1
    for (int n = 0; n < (1 << 24); ++n) { if (__builtin_amdgcn_readfirstlane(*(volatile LAS unsigned*)(lds + L_C + 40))) break; }
    __builtin_amdgcn_fence(__ATOMIC_ACQUIRE, "wavefront");
    STAMP(10);
    const int tt = 4 * rt2 + w;
#pragma unroll
    for (int s = 0; s < 16; ++s) a[s] = ld16(rw, lane * 16 + (s & 3) * 1024, WS_O + (tt * 16 + (s & 12)) * 1024);
#pragma unroll
    for (int s = 0; s < 4; ++s) a[16 + s] = cat8(ld8(rw, kq * 256 + c16 * 8 + s * 1024, WS_O2 + tt * 4096), ld8(rw, kq * 256 + 128 + c16 * 8 + s * 1024, WS_O2 + tt * 4096));
    SB();
#pragma unroll
    for (int i = 0; i < 100; ++i) {
      y[i % 5] = __builtin_amdgcn_mfma_f32_16x16x32_bf16(wr[i % 30], a[i / 5], y[i % 5], 0, 0, 0);
      if (i + 30 < 100) wr[i % 30] = *(LAS bf16x8*)(i + 30 < 60 ? wp + (i + 30) * 1024 : wp2 + (i - 30) * 1024);
      SB();
    }
    const rsrc_t rout = mkrs(out, 2048 * D_ * 2);
    __builtin_amdgcn_raw_buffer_store_b128(pk8(y[0], y[1]), rout, o * 2 + 16 * kq, 0, 0);
    __builtin_amdgcn_raw_buffer_store_b128(pk8(y[2], y[3]), rout, o * 2 + 64 + 16 * kq, 0, 0);
    __builtin_amdgcn_raw_buffer_store_b64(u32x2{pk2(y[4][0], y[4][1]), pk2(y[4][2], y[4][3])}, rout, o * 2 + 128 + 8 * kq, 0, 0);
    STAMP(11);
  }
}

extern "C" void kernel_launch(void* const* d_in, const int* in_sizes, int n_in, void* d_out, int out_size, void* d_ws, size_t ws_size,
                              hipStream_t stream) {
  fused<<<256, 512, 0, stream>>>((const u16*)d_in[0], (const u16*)d_in[1], (const u16*)d_in[2], (const u16*)d_in[4], (const u16*)d_in[5],
                                 (const u16*)d_in[6], (const u16*)d_in[7], (u16*)d_out, (char*)d_ws);
}
```

```cpp
#define FULLSPF 0
#define REARLY 0
#include <hip/hip_runtime.h>
#include <hip/hip_bf16.h>

typedef __attribute__((ext_vector_type(8))) __bf16 bf16x8;
typedef __attribute__((ext_vector_type(16))) __bf16 bf16x16;
typedef __attribute__((ext_vector_type(2))) __bf16 bf16x2;
typedef __attribute__((ext_vector_type(4))) short s16x4;
typedef __attribute__((ext_vector_type(16))) float f32x16;
typedef __attribute__((ext_vector_type(8))) float f32x8;
typedef __attribute__((ext_vector_type(4))) float f32x4;
typedef __attribute__((ext_vector_type(4))) unsigned u32x4;
typedef __attribute__((ext_vector_type(2))) unsigned u32x2;
typedef unsigned short u16;
typedef unsigned long long u64;
typedef __amdgpu_buffer_rsrc_t rsrc_t;
#define LAS __attribute__((address_space(3)))
#define GAS __attribute__((address_space(1)))
#define SB() __builtin_amdgcn_sched_barrier(0)
#define RLX_AGENT __ATOMIC_RELAXED, __HIP_MEMORY_SCOPE_AGENT
#define BAR_L() do { asm volatile("" ::: "memory"); __builtin_amdgcn_s_waitcnt(0xC07F); asm volatile("s_barrier" ::: "memory"); } while (0)
#define DRAIN() asm volatile("s_waitcnt vmcnt(0)" ::: "memory")

constexpr int D_ = 640, F_ = 1536;
constexpr unsigned PT = 7808, P_V = 2560, P_Q = 5120, P_ONE = 7680;
constexpr unsigned WS_T = 0;
constexpr unsigned WS_H = 8u << 20;
constexpr unsigned WS_W = 16u << 20;
constexpr unsigned WS_O = 20u << 20;
constexpr unsigned WS_O2 = 22u << 20;
constexpr unsigned WS_S = 23u << 20;
constexpr unsigned WS_F = 24u << 20;
constexpr unsigned WS_END = WS_F;
constexpr unsigned F_Q = 256, F_H = 512, F_O = 768;
constexpr float QSCALE = 0.22360679774997896f * 1.4426950408889634f;
constexpr float MARGIN = 88.f;
constexpr unsigned OOB = 0x80000000u;
constexpr unsigned CODE_SKIP = 4608;
constexpr unsigned XS = 16448, WSL = 16640, L_W = 6 * XS;
constexpr unsigned L_WO = 0, L_RED = 102400, L_MRG = 122880;
constexpr unsigned L_ST = L_W + 3 * WSL;
constexpr unsigned L_G = L_ST + 2048;
constexpr unsigned L_M = L_G + 4096;
constexpr unsigned L_C = L_M + 4096;
constexpr unsigned L_PF = L_C + 64;
constexpr unsigned L_END = L_PF + 256;

#define STAMP(i)
#define STEPSTAMP()

#define FS(v)
__device__ __forceinline__ unsigned pk2(float a, float b) { bf16x2 r = {(__bf16)a, (__bf16)b}; return __builtin_bit_cast(unsigned, r); }
__device__ __forceinline__ u32x4 pk8(f32x4 a, f32x4 b) { return u32x4{pk2(a[0], a[1]), pk2(a[2], a[3]), pk2(b[0], b[1]), pk2(b[2], b[3])}; }
__device__ __forceinline__ float blo(unsigned u) { return __uint_as_float(u << 16); }
__device__ __forceinline__ float bhi(unsigned u) { return __uint_as_float(u & 0xffff0000u); }
__device__ __forceinline__ u64 uni64(u64 v) {
  return ((u64)(unsigned)__builtin_amdgcn_readfirstlane((unsigned)(v >> 32)) << 32) | (unsigned)__builtin_amdgcn_readfirstlane((unsigned)v);
}
__device__ __forceinline__ int fresh_lane() { int l = __builtin_amdgcn_mbcnt_hi(~0u, __builtin_amdgcn_mbcnt_lo(~0u, 0u)); asm volatile("" : "+v"(l)); return l; }
__device__ __forceinline__ rsrc_t mkrs(const void* p, unsigned bytes) { return __builtin_amdgcn_make_buffer_rsrc((void*)p, 0, bytes, 0x00020000); }
__device__ __forceinline__ void dma16(rsrc_t r, unsigned vo, unsigned so, unsigned lds_base) {
  asm volatile("s_mov_b32 m0, %0\n\ts_nop 0\n\tbuffer_load_dwordx4 %1, %2, %3 offen lds" :: "s"(lds_base), "v"(vo), "s"(r), "s"(so) : "memory");
}
__device__ __forceinline__ void touch(rsrc_t r, unsigned vo, unsigned lds_base) {
  asm volatile("s_mov_b32 m0, %0\n\ts_nop 0\n\tbuffer_load_dword %1, %2, 0 offen lds" :: "s"(lds_base), "v"(vo), "s"(r) : "memory");
}
#define STOUCH3(p) asm volatile("s_load_dword s100, %0, 0\n\ts_load_dword s100, %0, 64\n\ts_load_dword s100, %0, 128" :: "s"(p) : "s100")
#define SPFX(x) x
#define STOUCH4(p) asm volatile("s_load_dword s100, %0, 0\n\ts_load_dword s100, %0, 64\n\ts_load_dword s100, %0, 128\n\ts_load_dword s100, %0, 192" :: "s"(p) : "s100")
__device__ __forceinline__ bf16x8 ld16(rsrc_t r, unsigned vo, unsigned so) { return __builtin_bit_cast(bf16x8, __builtin_amdgcn_raw_buffer_load_b128(r, vo, so, 16)); }
__device__ __forceinline__ s16x4 ld8(rsrc_t r, unsigned vo, unsigned so) { return __builtin_bit_cast(s16x4, __builtin_amdgcn_raw_buffer_load_b64(r, vo, so, 16)); }
__device__ __forceinline__ void st16(u32x4 v, rsrc_t r, unsigned vo, unsigned so) { __builtin_amdgcn_raw_buffer_store_b128(v, r, vo + so, 0, 16); }
__device__ __forceinline__ void st8(u32x2 v, rsrc_t r, unsigned vo, unsigned so) { __builtin_amdgcn_raw_buffer_store_b64(v, r, vo + so, 0, 16); }
__device__ __forceinline__ void publish(GAS u64* f, unsigned i, u64 tag, int lane) {
  st16(u32x4{(unsigned)tag, (unsigned)(tag >> 32), (unsigned)tag, (unsigned)(tag >> 32)}, mkrs((const void*)(f + 16 * i), 128), lane * 16, 0);
}
template <int SLP> __device__ __forceinline__ void await(GAS u64* f, unsigned i, u64 tag) {
#pragma unroll 1
  for (int n = 0; n < (1 << 20); ++n) {
    if (__all(__hip_atomic_load(f + 16 * i, RLX_AGENT) == tag)) break;
    __builtin_amdgcn_s_sleep(SLP);
  }
  __builtin_amdgcn_fence(__ATOMIC_ACQUIRE, "wavefront");
}
__device__ __forceinline__ void lds_add(LAS unsigned* c, unsigned v) {
  asm volatile("s_mov_b64 exec, 1\n\tds_add_u32 %0, %1\n\ts_mov_b64 exec, -1" :: "v"(c), "v"(v) : "memory");
}
__device__ __forceinline__ unsigned lds_add_rtn(LAS unsigned* c, unsigned v) {
  unsigned r_;
  asm volatile("s_mov_b64 exec, 1\n\tds_add_rtn_u32 %0, %1, %2\n\ts_mov_b64 exec, -1\n\ts_waitcnt lgkmcnt(0)" : "=&v"(r_) : "v"(c), "v"(v) : "memory");
  return __builtin_amdgcn_readfirstlane(r_);
}
template <int SLP> __device__ __forceinline__ void lds_meet(LAS unsigned* c, unsigned n, int lane) {
  lds_add(c, 1u);
#pragma unroll 1
  for (int k = 0; k < (1 << 22); ++k) {
    if (__builtin_amdgcn_readfirstlane(*(volatile LAS unsigned*)c) >= n) break;
    if (SLP) __builtin_amdgcn_s_sleep(SLP);
  }
  asm volatile("" ::: "memory");
}
__device__ __forceinline__ s16x4 trd(LAS char* p) { return __builtin_amdgcn_ds_read_tr16_b64_v4i16((LAS s16x4*)p); }
__device__ __forceinline__ bf16x8 cat8(s16x4 a, s16x4 b) { return __builtin_bit_cast(bf16x8, __builtin_shufflevector(a, b, 0, 1, 2, 3, 4, 5, 6, 7)); }
template <int C> __device__ __forceinline__ float dppadd(float a) {
  return a + __int_as_float(__builtin_amdgcn_update_dpp(0, __float_as_int(a), C, 0xf, 0xf, true));
}

template <int N> struct IC { static constexpr int v = N; };
template <int MODE> struct G1 {
  static constexpr int NM = MODE == 2 ? 2 : 4, NI = MODE == 2 ? 6 : 4, PAD = MODE == 1 ? 32 : 8, NP = NI / 2;
  static constexpr int NMMA = 2 * NM * NI, NRD = 2 * (NM + 2 * NI), HEAD = MODE == 2 ? 6 : 12, R2H = 3, R2S = 18;
  struct Frag { bf16x8 x[2][NM]; s16x4 w[2][NI][2]; };
  struct Ptr { LAS char* x[2][2]; LAS char* w[NP]; };
  template <int R, int XO = 0, int WO = 0> static __device__ __forceinline__ void rd(Frag& f, const Ptr& p) {
    constexpr int u = R / (NRD / 2), r = R % (NRD / 2);
    if constexpr (r >= 2 && r < 2 + NM) f.x[u][r - 2] = *(LAS bf16x8*)(p.x[u][r & 1] + XO + ((r - 2) >> 1) * 4096 + (r & 1) * (MODE == 1 ? 512 : 2048));
    else { constexpr int q = r < 2 ? r : r - NM, i = q >> 1;
      f.w[u][i][q & 1] = trd(p.w[i >> 1] + WO + (i & 1) * PAD + u * (8192 + 4 * PAD) + (q & 1) * 1024); }
  }
  template <int N> static __device__ __forceinline__ void mma(f32x4 (&acc)[NM][NI], const Frag& f) {
    constexpr int u = N / (NM * NI), i = (N / NM) % NI, m = N % NM;
    const bf16x8 wf = cat8(f.w[u][i][0], f.w[u][i][1]);
    if constexpr (MODE == 1) asm volatile("v_mfma_f32_16x16x32_bf16 %0, %1, %2, %0" : "+v"(acc[m][i]) : "v"(f.x[u][m]), "v"(wf));
    else asm volatile("v_mfma_f32_16x16x32_bf16 %0, %1, %2, %0" : "+v"(acc[m][i]) : "v"(wf), "v"(f.x[u][m]));
  }
  template <int U> static __device__ __forceinline__ void csum(f32x4& cs, const Frag& f, bf16x8 sel, int idx) {
    const bf16x16 wf = __builtin_shufflevector(cat8(f.w[U][0][0], f.w[U][0][1]), cat8(f.w[U][1][0], f.w[U][1][1]), 0, 1, 2, 3, 4, 5, 6, 7, 8, 9, 10, 11, 12, 13, 14, 15);
    asm volatile("v_smfmac_f32_16x16x64_bf16 %0, %1, %2, %3" : "+v"(cs) : "v"(sel), "v"(wf), "v"(idx));
  }
  static __device__ __forceinline__ int span(int w, int j) { return MODE == 2 ? (w + j >= 3 ? w + j - 3 : w + j) : 2 * (w >> 1) + (j ^ (w & 1)); }
  template <int R0, int R1, int XO = 0, int WO = 0> static __device__ __forceinline__ void rds(Frag& f, const Ptr& p) {
    if constexpr (R0 < R1) { rd<R0, XO, WO>(f, p); rds<R0 + 1, R1, XO, WO>(f, p); } }
  template <int N0, int N1> static __device__ __forceinline__ void mmas(f32x4 (&acc)[NM][NI], const Frag& f) { if constexpr (N0 < N1) { mma<N0>(acc, f); mmas<N0 + 1, N1>(acc, f); } }
  template <int N> static __device__ __forceinline__ void mix(f32x4 (&acc)[NM][NI], const Frag& fc, Frag& fn, const Ptr& p) {
    if constexpr (N < NMMA) {
      constexpr int SPAN = NMMA - HEAD - 3, k = N - HEAD;
      constexpr int r0 = k >= SPAN ? NRD : (k * NRD + SPAN - 1) / SPAN, r1 = k + 1 >= SPAN ? NRD : ((k + 1) * NRD + SPAN - 1) / SPAN;
      mma<N>(acc, fc); rds<r0, r1>(fn, p); SB();
      mix<N + 1>(acc, fc, fn, p);
    }
  }
  struct Off { unsigned x, w[NP]; };
  static __device__ __forceinline__ Off offs(int w, int lane) {
    const int kq = lane >> 4, c16 = lane & 15, a = (lane >> 2) & 3;
    Off o;
    o.x = MODE == 1 ? (64 * (w & 1) + 8 * (c16 >> 2) + (c16 & 3)) * 128 + ((kq ^ ((c16 >> 1) & 1) ^ ((c16 >> 3) << 1) ^ (c16 & 4)) << 4)
                    : ((MODE == 2 ? 32 * w : 64 * (w & 1)) + c16) * 128 + ((kq ^ (c16 >> 1)) << 4);
    const unsigned wb = L_W + kq * (2048 + PAD) + a * 256 + (lane & 3) * (MODE == 1 ? 8 : 16);
#pragma unroll
    for (int j = 0; j < NP; ++j) o.w[j] = wb + (span(w, j) ^ a) * 64;
    return o;
  }
  template <int U> static __device__ __forceinline__ void ptrx(Ptr& p, const Off& o, LAS char* lds, unsigned sx) {
    p.x[U][0] = lds + sx + (o.x ^ (64 * U)); p.x[U][1] = lds + sx + (o.x ^ (64 * U + 32)); asm volatile("" : "+v"(p.x[U][0]), "+v"(p.x[U][1])); }
  static __device__ __forceinline__ void ptrw(Ptr& p, const Off& o, LAS char* lds, unsigned sw) {
#pragma unroll
    for (int j = 0; j < NP; ++j) { p.w[j] = lds + (sw + o.w[j]); asm volatile("" : "+v"(p.w[j])); } }
  template <int N, class T> static __device__ __forceinline__ void fin(f32x4 (&acc)[NM][NI], const Frag& fc, T& tail) {
    if constexpr (N < NMMA) { mma<N>(acc, fc); tail(IC<N - HEAD>{}); SB(); fin<N + 1>(acc, fc, tail); }
  }
  template <class T>
  static __device__ __forceinline__ void loop(f32x4 (&acc)[NM][NI], Frag& fa, const Off& o, LAS char* lds, unsigned sx, unsigned sw, int w, int lane, int pub,
                                              GAS u64* fl, unsigned fi, u64 tag, T tail, unsigned& stv) {
    const int kq = lane >> 4, c16 = lane & 15;
    Frag fb; Ptr p;
#pragma unroll
    for (int n = 0; n < NM * NI; ++n) { acc[n % NM][n / NM] = f32x4{}; asm volatile("" : "+v"(acc[n % NM][n / NM])); }
    const unsigned one = (kq >> 1) == ((c16 >> 1) & 1) ? 0x3f803f80u : 0u;
    bf16x8 sel = __builtin_bit_cast(bf16x8, u32x4{one, one, one, one});
    int sidx = (lane & 1) ? 0xEEEE : 0x4444;
    f32x4 cs = {};
    LAS float* cp = (LAS float*)(lds + L_ST + 1024) + 32 * span(w, 0) + (MODE == 1 ? c16 : 8 * (c16 >> 2) + (c16 & 3));
    asm volatile("s_nop 1" : "+v"(sel), "+v"(sidx), "+v"(cs), "+v"(cp));
    if constexpr (MODE != 2) { ptrx<0>(p, o, lds, sx); ptrx<1>(p, o, lds, sx); ptrw(p, o, lds, sw); SB(); tail(IC<-2>{}); STAMP(1); BAR_L(); STAMP(12); rds<0, NRD>(fa, p); }
#define G1_HEAD(FC, PUB, LAST, BAR) do { \
      mma<0>(acc, FC); sx = (sx == 5 * XS) ? 0 : sx + XS; asm volatile("" : "+s"(sx)); SB(); \
      csum<0>(cs, FC, sel, sidx); SB(); \
      mma<1>(acc, FC); sw = (sw == 2 * WSL) ? 0 : sw + WSL; asm volatile("" : "+s"(sw)); SB(); \
      mma<2>(acc, FC); ptrx<0>(p, o, lds, sx); SB(); \
      mma<3>(acc, FC); ptrw(p, o, lds, sw); SB(); \
      mmas<4, HEAD - 2>(acc, FC); SB(); \
      csum<1>(cs, FC, sel, sidx); SB(); \
      mma<HEAD - 2>(acc, FC); ptrx<1>(p, o, lds, sx); SB(); \
      mma<HEAD - 1>(acc, FC); SB(); \
      if (LAST) { asm volatile("s_nop 5" : "+v"(cs)); cp[0] = cs[0] + cs[1]; cp[MODE == 1 ? 16 : 4] = cs[2] + cs[3]; } \
      if (PUB) DRAIN(); \
      if (BAR) BAR_L(); \
      if ((PUB) && w == 0) publish(fl, fi, tag, lane); \
      SB(); } while (0)
    SB();
    G1_HEAD(fa, false, false, true);
#pragma unroll 1
    for (int s = 0; s < 4; ++s) {
      mix<HEAD>(acc, fa, fb, p);
      G1_HEAD(fb, false, false, true);
      mix<HEAD>(acc, fb, fa, p);
      G1_HEAD(fa, false, false, true);
    }
    mix<HEAD>(acc, fa, fb, p);
    G1_HEAD(fb, false, true, true);
    fin<HEAD>(acc, fb, tail);
    asm volatile("s_nop 7");
    SB();
  }
  template <int J, int N> static __device__ __forceinline__ void r2mix(f32x4 (&acc)[NM][NI], f32x4& cs, bf16x8 sel, int sidx, const Frag& fc, Frag& fn, const Ptr& pl, const Ptr& ph) {
    if constexpr (N < NMMA) {
      constexpr int H2 = R2H, SPAN = R2S, k = N - H2, xs = (5 + J) % 6;
      constexpr int r0 = k >= SPAN ? NRD : (k * NRD + SPAN - 1) / SPAN, r1 = k + 1 >= SPAN ? NRD : ((k + 1) * NRD + SPAN - 1) / SPAN;
      mma<N>(acc, fc);
      Ptr p = xs >= 3 ? ph : pl;
#pragma unroll
      for (int j = 0; j < NP; ++j) p.w[j] = J == 8 ? ph.w[j] : pl.w[j];
      rds<r0, r1, (xs % 3) * XS, J == 8 ? 0 : ((2 + J) % 3) * WSL>(fn, p); SB();
      if constexpr (N == 13) { csum<1>(cs, fc, sel, sidx); SB(); }
      r2mix<J, N + 1>(acc, cs, sel, sidx, fc, fn, pl, ph);
    }
  }
  template <int J, class PUB> static __device__ __forceinline__ void r2steps(f32x4 (&acc)[NM][NI], f32x4& cs, bf16x8 sel, int sidx, Frag& fc, Frag& fn, const Ptr& pl, const Ptr& ph, PUB& pub) {
    if constexpr (J < 9) {
      mmas<0, 2>(acc, fc); csum<0>(cs, fc, sel, sidx); mmas<2, R2H>(acc, fc); SB();
      if constexpr (J == 2) DRAIN();
      if constexpr (J < 8) BAR_L();
      if constexpr (J == 2) pub();
      SB();
      r2mix<J, R2H>(acc, cs, sel, sidx, fc, fn, pl, ph);
      r2steps<J + 1>(acc, cs, sel, sidx, fn, fc, pl, ph, pub);
    }
  }
  template <class PUB, class T>
  static __device__ __forceinline__ void loop2(f32x4 (&acc)[NM][NI], Frag& fa, const Off& o, const Ptr& ph, LAS char* lds, int w, int lane, PUB pub, T tail) {
    const int kq = lane >> 4, c16 = lane & 15;
    Frag fb; Ptr pl, ph2 = ph;
    ptrx<0>(pl, o, lds, 0); ptrx<1>(pl, o, lds, 0);
#pragma unroll
    for (int j = 0; j < NP; ++j) pl.w[j] = ph.w[j];
    ptrw(ph2, o, lds, 3 * XS - L_W);
#pragma unroll
    for (int n = 0; n < NM * NI; ++n) { acc[n % NM][n / NM] = f32x4{}; asm volatile("" : "+v"(acc[n % NM][n / NM])); }
    const unsigned one = (kq >> 1) == ((c16 >> 1) & 1) ? 0x3f803f80u : 0u;
    bf16x8 sel = __builtin_bit_cast(bf16x8, u32x4{one, one, one, one});
    int sidx = (lane & 1) ? 0xEEEE : 0x4444;
    f32x4 cs = {};
    LAS float* cp = (LAS float*)(lds + L_ST + 1024) + 32 * span(w, 0) + 8 * (c16 >> 2) + (c16 & 3);
    asm volatile("s_nop 1" : "+v"(sel), "+v"(sidx), "+v"(cs), "+v"(cp));
    SB();
    r2steps<0>(acc, cs, sel, sidx, fa, fb, pl, ph2, pub);
    mmas<0, 2>(acc, fb); csum<0>(cs, fb, sel, sidx); mmas<2, 4>(acc, fb); csum<1>(cs, fb, sel, sidx); mmas<4, HEAD>(acc, fb); SB();
    fin<HEAD>(acc, fb, tail);
    asm volatile("s_nop 7" : "+v"(cs));
    cp[0] = cs[0] + cs[1]; cp[4] = cs[2] + cs[3];
    SB();
  }
};

struct KV { bf16x8 k1; s16x4 k2; bf16x8 v0, v1; };
__device__ __forceinline__ f32x16 qk_a(bf16x8 k, bf16x8 q) { f32x16 z = {}; return __builtin_amdgcn_mfma_f32_32x32x16_bf16(k, q, z, 0, 0, 0); }
__device__ __forceinline__ f32x16 qk_b(s16x4 k, s16x4 q, f32x16 c) { return __builtin_amdgcn_mfma_f32_32x32x8bf16_1k(k, q, c, 0, 0, 0); }
__device__ __forceinline__ f32x16 pv_lo(bf16x8 v, f32x16 p, f32x16 o, int idx) {
  f32x8 h = __builtin_shufflevector(p, p, 0, 1, 2, 3, 4, 5, 6, 7);
  return __builtin_amdgcn_smfmac_f32_32x32x32_bf16(v, __builtin_bit_cast(bf16x16, h), o, idx, 0, 0); }
__device__ __forceinline__ f32x16 pv_hi(bf16x8 v, f32x16 p, f32x16 o, int idx) {
  f32x8 h = __builtin_shufflevector(p, p, 8, 9, 10, 11, 12, 13, 14, 15);
  return __builtin_amdgcn_smfmac_f32_32x32x32_bf16(v, __builtin_bit_cast(bf16x16, h), o, idx, 0, 0); }
template <int E> __device__ __forceinline__ void max8(const f32x16& s, float& m) {
  asm volatile("v_max3_f32 %0, %0, %1, %2\n\tv_max3_f32 %0, %0, %3, %4\n\tv_max3_f32 %0, %0, %5, %6\n\tv_max3_f32 %0, %0, %7, %8"
               : "+v"(m) : "v"(s[E]), "v"(s[E + 1]), "v"(s[E + 2]), "v"(s[E + 3]), "v"(s[E + 4]), "v"(s[E + 5]), "v"(s[E + 6]), "v"(s[E + 7]));
}
#define EX4(s, b) do { s[b] = __builtin_amdgcn_exp2f(s[b]); s[b+1] = __builtin_amdgcn_exp2f(s[b+1]); s[b+2] = __builtin_amdgcn_exp2f(s[b+2]); s[b+3] = __builtin_amdgcn_exp2f(s[b+3]); } while (0)
#define STEP(SC, SP, OP, VP, SN, KN, Q1, Q2) do { \
  EX4(SC, 0); SB(); OP = pv_lo(VP.v0, SP, OP, idx); SB(); \
  EX4(SC, 4); SB(); SN = qk_a(KN.k1, Q1); SB(); \
  EX4(SC, 8); SB(); OP = pv_hi(VP.v1, SP, OP, idx); SB(); \
  EX4(SC, 12); SB(); SN = qk_b(KN.k2, Q2, SN); SB(); } while (0)

__global__ void __launch_bounds__(512, 2)
fused(const u16* __restrict__ x, const u16* __restrict__ wqkv, const u16* __restrict__ wo, const u16* __restrict__ w1,
      const u16* __restrict__ b1, const u16* __restrict__ w2, const u16* __restrict__ b2, u16* __restrict__ out, char* __restrict__ ws) {
  __shared__ __attribute__((aligned(16))) char smem[L_END + 256];
  LAS char* lds = (LAS char*)smem;
  const unsigned lds0 = (unsigned)(size_t)lds;
  const int lane = threadIdx.x & 63, w = __builtin_amdgcn_readfirstlane(threadIdx.x >> 6);
  const int b = blockIdx.x, g = b & 7, idx8 = b >> 3;
  asm volatile("" :: "s"(x), "s"(wqkv), "s"(wo), "s"(w1), "s"(b1), "s"(w2), "s"(b2), "s"(out), "s"(ws));
  const int rt = (g & 3) * 4 + (idx8 & 3), ct = (g >> 2) * 8 + (idx8 >> 2);
  const bool vtype = ct >= 10;
  const int kq = lane >> 4, c16 = lane & 15;
  LAS float* st_r = (LAS float*)(lds + L_ST);
  LAS float* st_t = st_r + 128;
  LAS float* st_c = st_r + 256;
  GAS u64* fl = (GAS u64*)(ws + WS_F);
  const rsrc_t rw = mkrs(ws, WS_END), rx = mkrs(x, 2048 * D_ * 2);
  u64 tag = 0;
  const int head = 4 * g + (idx8 >> 3), qc = idx8 & 7;
  const int aw = w - 4, qh = aw & 1, kh = aw >> 1, r = lane & 31, h = lane >> 5, odd = head & 1, pr = head >> 1;
  int hstep = 0; unsigned stv = 0;
  unsigned oK1 = 0, oK2 = 0, oV0 = 0, oV1 = 0, oQ2 = 0;
  auto kv_offsets = [&](int ln) {
    const int r = ln & 31, h = ln >> 5;
    oK1 = odd * 1536 + ln * 16; oK2 = h ? P_ONE : 1024 + r * 16 + odd * 8; oQ2 = h ? OOB : oK2;
    const int dv = r < 16 ? (r & 3) + 4 * (r >> 3) + 8 * ((r >> 2) & 1) : r;
    oV0 = r < 20 ? P_V + odd * 1280 + h * 320 + dv * 16 : (r == 20 ? P_ONE : OOB); oV1 = r < 20 ? oV0 + 640 : oV0;
  };
  unsigned fi = 0;
  STAMP(0);

  if (w >= 4) {
    const int hw = w - 4;
    rsrc_t rwt = mkrs(wqkv, D_ * 3 * D_ * 2);
    unsigned rs = 3 * D_ * 2, pad = vtype ? 32 : 8;
    unsigned wso = ct * 256 + 16 * hw * rs, xso = (rt * 128 + hw * 32) * (D_ * 2);
    const unsigned xv = (lane >> 3) * (D_ * 2) + (((lane & 7) ^ kq) << 4), xv1 = xv ^ 64, xv2 = xv ^ 32, xv3 = xv ^ 96;
    const int wc = c16 ^ (kq << 2);
    unsigned wv = ct < 15 ? kq * rs + (wc << 4) : OOB;
    unsigned nx = lds0 + hw * 4096, nw = lds0 + L_W + hw * 4096;
#define DX(i) dma16(rx, i == 0 ? xv : (i == 1 ? xv1 : (i == 2 ? xv2 : xv3)), xso + i * 8 * (D_ * 2), nx + i * 1024)
#define DW(i) dma16(rwt, wv, wso + 4 * i * rs, nw + i * 1024 + (2 * hw + (i >> 1)) * pad)
#define NEXTX() do { xso += 128; nx = (nx == lds0 + hw * 4096 + 5 * XS) ? nx - 5 * XS : nx + XS; } while (0)
#define NEXTW() do { wso += 64 * rs; nw = (nw == lds0 + L_W + hw * 4096 + 2 * WSL) ? nw - 2 * WSL : nw + WSL; } while (0)
#define HBAR(n) asm volatile("s_waitcnt vmcnt(" #n ")\n\ts_barrier" ::: "memory")
    __builtin_amdgcn_s_setprio(1);
#define DW4() do { DW(0); DW(1); DW(2); DW(3); NEXTW(); } while (0)
#define DX4() do { DX(0); DX(1); DX(2); DX(3); NEXTX(); } while (0)
    const char* pw = (const char*)w1 + 256 * F_ * 2 + (b * 4 + hw) * 192;
#define SPF1()
#define SPF2(j)
#define SPF3(j)
    {
      DX(0); DW(0); DX(1); DW(1); DX(2); DW(2); DX(3); DW(3); NEXTW(); NEXTX();
      DX4();
#pragma unroll 1
      for (int s = -1; s < 7; ++s) { DW4(); DX4(); SPFX(STOUCH3(pw)); pw += (s == 4 ? -576 : 64) * F_ * 2; SPF1(); HBAR(12); }
    }
    const unsigned go = WS_S + (rt * 128 + (hw & 1) * 64 + lane) * 16;
    asm volatile("s_mov_b32 m0, %0\n\ts_nop 0\n\tbuffer_load_dwordx4 %1, %2, 0 offen sc1 lds" :: "s"(lds0 + L_G + hw * 1024), "v"(go), "s"(rw) : "memory");
    DW4(); SPFX(STOUCH3(pw)); pw += 64 * F_ * 2; SPF2(0); HBAR(9);
    rwt = mkrs(w1, D_ * F_ * 2); rs = F_ * 2; pad = 8; wso = ct * 192 + (256 + 16 * hw) * rs; wv = wc < 12 ? kq * rs + (wc << 4) : OOB;
    xso -= 1280; nx = lds0 + hw * 4096 + 4 * XS; nw = lds0 + L_W + hw * 4096 + WSL;
    DW4();
    tag = uni64(*(LAS u64*)(lds + L_C));
    {
      asm volatile("s_waitcnt vmcnt(8)" ::: "memory");
      u32x4 gr = *(LAS u32x4*)(lds + L_G + hw * 1024 + lane * 16);
#pragma unroll 1
      for (int n = 0; n < (1 << 20); ++n) {
        if (__all(gr[1] == (unsigned)tag && gr[3] == (unsigned)tag)) break;
        asm volatile("buffer_load_dwordx4 %0, %1, %2, 0 offen sc1\n\ts_waitcnt vmcnt(0)" : "=&v"(gr) : "v"(go), "s"(rw) : "memory");
      }
      st_r[hw * 64 + lane] = __uint_as_float(hw < 2 ? gr[0] : gr[2]);
    }
    SPF2(1); HBAR(4);
    DW4(); asm volatile("s_waitcnt lgkmcnt(0)" ::: "memory"); SPFX(STOUCH3(pw)); SPF2(2); HBAR(4);
    DW4();
    {
      const int c0 = pr * 5 + 2 * odd, e = (lane >> 3) & 1;
      const int tqk = (c0 + 2 * e) >> 4, tv = (head * 20 + 19 * e) >> 7;
      fi = F_Q + (lane < 16 ? 5 + tqk : 10 + tv) * 16 + 8 * kh + (lane & 7);
      if (lane >= 32) fi = F_Q + tqk * 16 + 2 * qc + qh;
      kv_offsets(lane);
      asm volatile("" : "+v"(fi), "+v"(oK1), "+v"(oK2), "+v"(oV0), "+v"(oV1), "+v"(oQ2));
    }
    SPF2(3); SPF2(4); SPF2(5); HBAR(4);
    DW4(); DX4(); SPF2(6); HBAR(8);
    DW4(); DX4(); SPF2(7); HBAR(12);
    DW4(); DX4(); wso -= 640 * rs; SPF3(0); HBAR(12);
    DW4(); DX4(); SPF3(1); HBAR(12);
    DW4(); SPF3(2); SPF3(3); HBAR(8);
    DW4(); nw = lds0 + 3 * XS + hw * 4096; DW4(); HBAR(8);
    asm volatile("s_mov_b32 m0, %0\n\ts_nop 0\n\tbuffer_load_dwordx4 %1, %2, 0 offen sc1 lds" :: "s"(lds0 + L_G + hw * 1024), "v"(fi * 128), "s"(mkrs(ws + WS_F, 1u << 17)) : "memory");
  } else {
    const int wm = w & 1, wn = w >> 1;
    const int row = rt * 128 + ct * 8 + 2 * w + (lane >> 5);
    {
      const unsigned lb = lds0 + 5 * XS + w * 4096, xo = row * (D_ * 2) + r * 16;
      dma16(rx, xo, 0, lb); dma16(rx, xo, 512, lb + 1024); dma16(rx, r < 16 ? xo : OOB, 1024, lb + 2048);
      asm volatile("s_mov_b32 m0, %0\n\ts_nop 0\n\tbuffer_load_dword %1, %2, 0 offen sc1 lds" :: "s"(lb + 3072), "v"(lane * 4), "s"(mkrs(ws + WS_F, 8)) : "memory");
      u64 pc; asm volatile("s_getpc_b64 %0" : "=s"(pc));
      STOUCH4((pc & ~(u64)255) + CODE_SKIP + (idx8 * 4 + w) * 256u);
      *(LAS u32x4*)(lds + L_M + threadIdx.x * 16) = u32x4{0x7fc00000u, 0x7fc00000u, 0x7fc00000u, 0x7fc00000u};
    }
    unsigned vo[4];
    if (vtype) {
#pragma unroll
      for (int i = 0; i < 4; ++i) {
        const int fv = (ct - 10) * 128 + 32 * G1<1>::span(w, i >> 1) + 16 * (i & 1) + c16, hd = fv / 20, d = fv - hd * 20;
        vo[i] = (WS_T + ((hd >> 1) * 64 + 4 * rt + 2 * wm) * PT + P_V + (hd & 1) * 1280 + kq * 320 + d * 16) | (ct < 15 ? 0u : OOB);
      }
    } else {
#pragma unroll
      for (int pp = 0; pp < 2; ++pp) {
        const int c = (ct < 5 ? ct : ct - 5) * 16 + 4 * G1<0>::span(w, pp) + kq, p5 = c / 5, pos = c - p5 * 5;
        const unsigned tb = WS_T + (p5 * 64 + 4 * rt + 2 * wm) * PT;
        vo[pp] = tb + pos * 512 + (ct < 5 ? P_Q : 0u) + c16 * 16;
        vo[2 + pp] = (ct >= 5 && pos == 2) ? tb + P_ONE + (c16 >> 3) * PT + (c16 & 7) * 16 : OOB;
      }
    }
    asm volatile("" : "+v"(vo[0]), "+v"(vo[1]), "+v"(vo[2]), "+v"(vo[3]));
    if (w < 2) touch(w ? mkrs(b1 + ct * 96, 192) : mkrs(wqkv + 94 * 3 * D_, 546 * 3 * D_ * 2), w ? lane * 4 : (b * 64 + lane) * 128, lds0 + (w ? L_ST + 1536 : L_PF));
    u32x4 xr[3]; u64 e0;
    auto stats_ld = [&]() {
      if (w < 2) asm volatile("s_waitcnt vmcnt(1)" ::: "memory"); else DRAIN();
#pragma unroll
      for (int i = 0; i < 3; ++i) xr[i] = *(LAS u32x4*)(lds + 5 * XS + w * 4096 + i * 1024 + lane * 16);
      e0 = *(LAS u64*)(lds + 5 * XS + w * 4096 + 3072);
    };
    auto stats = [&]() {
      float p = 0.f, q = 0.f;
#pragma unroll
      for (int i = 0; i < 12; ++i) {
        const unsigned u2 = xr[i >> 2][i & 3];
        const bf16x2 v2 = __builtin_bit_cast(bf16x2, u2);
        p = __builtin_amdgcn_fdot2_f32_bf16(v2, __builtin_bit_cast(bf16x2, 0x3f803f80u), p, false); q = __builtin_amdgcn_fdot2_f32_bf16(v2, v2, q, false);
      }
      p = dppadd<0x140>(dppadd<0x141>(dppadd<0x4E>(dppadd<0xB1>(p)))); q = dppadd<0x140>(dppadd<0x141>(dppadd<0x4E>(dppadd<0xB1>(q))));
      p = dppadd<0x142>(p); q = dppadd<0x142>(q);
      const float mu = p * (1.f / D_), rstd = __builtin_amdgcn_rsqf(fmaxf(q * (1.f / D_) - mu * mu, 0.f) + 1e-5f);
      tag = uni64(e0 + 1);
      if (threadIdx.x == 0) { *(LAS u64*)(lds + L_C) = tag; *(LAS u32x4*)(lds + L_C + 16) = u32x4{0, 0, 0, 0}; *(LAS u32x4*)(lds + L_C + 32) = u32x4{0, 0, 0, 0}; *(LAS u32x4*)(lds + L_C + 48) = u32x4{0, 0, 0, 0}; }
      st16(u32x4{__float_as_uint(rstd), (unsigned)tag, __float_as_uint(-mu * rstd), (unsigned)tag}, rw, r == 16 ? row * 16 : OOB, WS_S);
    };
    G1<2>::Frag f2; G1<2>::Ptr p2;
    const G1<2>::Off o2 = G1<2>::offs(w, lane);
    G1<2>::ptrx<0>(p2, o2, lds, 3 * XS); G1<2>::ptrx<1>(p2, o2, lds, 3 * XS); G1<2>::ptrw(p2, o2, lds, 0);
    {
      f32x4 acc[4][4];
      if (vtype) {
        f32x4 r4[4], t4[4]; float c1[4];
        G1<1>::Frag f1;
        LAS float* pr4 = st_r + 64 * wm + 8 * kq; LAS float* pc0 = st_c + 32 * G1<1>::span(w, 0) + c16; LAS float* pc1 = st_c + 32 * G1<1>::span(w, 1) + c16;
        asm volatile("" : "+v"(pr4), "+v"(pc0), "+v"(pc1));
        G1<1>::loop(acc, f1, G1<1>::offs(w, lane), lds, 0, 0, w, lane, -1, fl, 0, tag, [&](auto k) {
          constexpr int K = decltype(k)::v;
          if constexpr (K < 0) { stats_ld(); stats(); }
          else if constexpr (K < 4) { r4[K] = *(LAS f32x4*)(pr4 + 32 * (K >> 1) + 4 * (K & 1)); t4[K] = *(LAS f32x4*)(pr4 + 128 + 32 * (K >> 1) + 4 * (K & 1)); }
          else if constexpr (K < 6) { c1[2 * K - 8] = (K == 4 ? pc0 : pc1)[0]; c1[2 * K - 7] = (K == 4 ? pc0 : pc1)[16]; }
          else G1<2>::rds<2 * K - 12, 2 * K - 10, XS, WSL>(f2, p2);
        }, stv);
        STAMP(2);
#pragma unroll
        for (int i = 0; i < 4; ++i)
#pragma unroll
          for (int a = 0; a < 2; ++a)
            st16(pk8(acc[2 * a][i] * r4[2 * a] + t4[2 * a] * c1[i], acc[2 * a + 1][i] * r4[2 * a + 1] + t4[2 * a + 1] * c1[i]), rw, vo[i], a * PT);
      } else {
        float r1[4], t1[4]; f32x4 c4[4];
        G1<0>::Frag f0;
        LAS float* pr1 = st_r + 64 * wm + c16; LAS float* pc0 = st_c + 32 * G1<0>::span(w, 0) + 8 * kq; LAS float* pc1 = st_c + 32 * G1<0>::span(w, 1) + 8 * kq;
        asm volatile("" : "+v"(pr1), "+v"(pc0), "+v"(pc1));
        G1<0>::loop(acc, f0, G1<0>::offs(w, lane), lds, 0, 0, w, lane, -1, fl, 0, tag, [&](auto k) {
          constexpr int K = decltype(k)::v;
          if constexpr (K < 0) { stats_ld(); stats(); }
          else if constexpr (K < 4) { r1[K] = pr1[16 * K]; t1[K] = pr1[128 + 16 * K]; }
          else if constexpr (K < 6) { c4[2 * K - 8] = *(LAS f32x4*)(K == 4 ? pc0 : pc1); c4[2 * K - 7] = *(LAS f32x4*)((K == 4 ? pc0 : pc1) + 4); }
          else G1<2>::rds<2 * K - 12, 2 * K - 10, XS, WSL>(f2, p2);
        }, stv);
        STAMP(2);
        const float sc = ct < 5 ? QSCALE : 1.f;
#pragma unroll
        for (int pp = 0; pp < 2; ++pp)
#pragma unroll
          for (int m = 0; m < 4; ++m)
            st16(pk8(acc[m][2 * pp] * (r1[m] * sc) + c4[2 * pp] * (t1[m] * sc), acc[m][2 * pp + 1] * (r1[m] * sc) + c4[2 * pp + 1] * (t1[m] * sc)), rw,
                 vo[pp], (m & 1) * 256 + (m >> 1) * PT);
        st16(u32x4{0x3f803f80u, 0x3f803f80u, 0x3f803f80u, 0x3f803f80u}, rw, min(vo[2], vo[3]), 0);
      }
    }
    STAMP(3);
    f32x4 acc[2][6];
    float r1[2], t1[2]; f32x4 c4[6]; u32x4 bb[3];
    LAS float* pr1 = st_r + 32 * w + c16;
    G1<2>::loop2(acc, f2, o2, p2, lds, w, lane, [&]() { if (w == 0) publish(fl, F_Q + ct * 16 + rt, tag, lane); }, [&](auto k) {
      constexpr int K = decltype(k)::v;
      if constexpr (K < 2) { r1[K] = pr1[16 * K]; t1[K] = pr1[128 + 16 * K]; }
      else if constexpr (K < 5) bb[K - 2] = *(LAS u32x4*)(st_c + 128 + 16 * G1<2>::span(w, K - 2) + 4 * kq);
    });
    STAMP(4);
    __builtin_amdgcn_s_setprio(2);
    lds_meet<2>((LAS unsigned*)(lds + L_C + 20), 4, lane);
#pragma unroll
    for (int pp = 0; pp < 3; ++pp) { LAS float* pc = st_c + 32 * G1<2>::span(w, pp) + 8 * kq; c4[2 * pp] = *(LAS f32x4*)pc; c4[2 * pp + 1] = *(LAS f32x4*)(pc + 4); }
    const bool ret = w < 2 || (w == 2 && idx8 < 4);
    const int slab = w < 2 ? 2 * idx8 + w : 64 + idx8;
    u32x4 hq[6];
#pragma unroll
    for (int pp = 0; pp < 3; ++pp) {
      const f32x4 ba = {blo(bb[pp][0]), bhi(bb[pp][0]), blo(bb[pp][1]), bhi(bb[pp][1])}, bc = {blo(bb[pp][2]), bhi(bb[pp][2]), blo(bb[pp][3]), bhi(bb[pp][3])};
#pragma unroll
      for (int m = 0; m < 2; ++m) {
        f32x4 v0 = acc[m][2 * pp] * r1[m] + (c4[2 * pp] * t1[m] + ba), v1 = acc[m][2 * pp + 1] * r1[m] + (c4[2 * pp + 1] * t1[m] + bc);
#pragma unroll
        for (int j = 0; j < 4; ++j) { v0[j] = fmaxf(v0[j], 0.f); v1[j] = fmaxf(v1[j], 0.f); }
        hq[2 * pp + m] = pk8(v0 * v0, v1 * v1);
      }
    }
    asm volatile("" : "+v"(hq[0]), "+v"(hq[1]), "+v"(hq[2]), "+v"(hq[3]), "+v"(hq[4]), "+v"(hq[5]));
#pragma unroll
    for (int i = 0; i < 6; ++i) { st16(hq[i], rw, lane * 16, WS_H + ((8 * rt + 2 * w + (i & 1)) * 48 + 3 * ct + G1<2>::span(w, i >> 1)) * 1024); SB(); __builtin_amdgcn_s_sleep(4); SB(); }
    if (ret) {
      const int s2 = slab - 48;
      const rsrc_t rr = mkrs((slab < 48 ? w2 : wo) + 80 * g, ((slab < 48 ? F_ : D_) * D_ - 80 * g) * 2);
      const unsigned r0 = slab < 48 ? slab * 32 : (s2 < 16 ? s2 * 40 : (s2 - 16) * 160 + 16), rsh = s2 < 16 ? 4 : 2, rmu = slab < 48 ? 0 : (s2 < 16 ? 4 : 16);
#pragma unroll
      for (int i = 0; i < 5; ++i) {
        const unsigned c = 64 * i + lane, k = (c * 205) >> 11;
        dma16(rr, (k + (k >> rsh) * rmu) * (D_ * 2) + (c - k * 10) * 16, r0 * (D_ * 2), lds0 + L_RED + w * 5120 + i * 1024);
      }
      const unsigned tro = (8 * kq + ((lane >> 2) & 3)) * 160 + (lane & 3) * 16, tr4 = tro - (lane & 3) * 8 + 128;
      if (REARLY) asm volatile("s_waitcnt vmcnt(6)" ::: "memory"); else DRAIN();
#pragma unroll
      for (int t = 0; t < 5; ++t) {
        LAS char* p = lds + L_RED + w * 5120 + (t < 4 ? tro + (t >> 1) * 64 + (t & 1) * 8 : tr4);
        st16(__builtin_bit_cast(u32x4, cat8(trd(p), trd(p + 640))), rw, lane * 16 + WS_W + (g * 68 + slab) * 5120, t * 1024); SB();
      }
    }
    STAMP(5);
  }

  if (w >= 4) {
    const rsrc_t rk = mkrs(ws + WS_T + (pr * 64 + kh * 32) * PT, 32 * PT);
    unsigned qo[4];
#pragma unroll
    for (int t = 0; t < 4; ++t) { qo[t] = WS_T + (pr * 64 + 8 * qc + 4 * qh + (t ^ (2 * kh))) * PT + P_Q; asm volatile("" : "+s"(qo[t])); }
    asm volatile("" :: "s"(rk));
    HBAR(1);
    DRAIN();
    if (!__all(*(volatile LAS u64*)(lds + L_G + aw * 1024 + (oK1 & 1023)) == tag)) await<2>(fl, fi, tag);
    __builtin_amdgcn_fence(__ATOMIC_ACQUIRE, "wavefront");
    STAMP(6);
    bool stored = false;
    auto sweep = [&](auto pass_) -> unsigned {
      constexpr int PASS = decltype(pass_)::v;
      const int lane = fresh_lane(), r = lane & 31, h = lane >> 5, c16 = lane & 15;
      if constexpr (PASS) kv_offsets(lane);
      bf16x8 q1[4]; s16x4 q2[4];
      KV b0, b1_, b2_, b3;
      unsigned so = 0;
#define LDK(B, d) do { B.k1 = ld16(rk, oK1, so + (d) * PT); SB(); B.k2 = ld8(rk, oK2, so + (d) * PT); SB(); } while (0)
#define LDV(B, d) do { B.v0 = ld16(rk, oV0, so + (d) * PT); SB(); B.v1 = ld16(rk, oV1, so + (d) * PT); SB(); } while (0)
      SB();
      LDK(b1_, 1);
#pragma unroll
      for (int t = 0; t < 4; ++t) { q1[t] = ld16(rw, oK1, qo[t]); SB(); q2[t] = ld8(rw, oQ2, qo[t]); SB(); }
      LDK(b0, 0);
      float mx[4] = {-3.0e38f, -3.0e38f, -3.0e38f, -3.0e38f};
      if constexpr (PASS) {
#pragma unroll 1
        for (int j = 2; j < 32; ++j) {
          const bf16x8 k1 = ld16(rk, oK1, j * PT); const s16x4 k2 = ld8(rk, oK2, j * PT);
#pragma unroll
          for (int t = 0; t < 4; ++t) {
            const f32x16 sv = qk_b(k2, q2[t], qk_a(k1, q1[t]));
#pragma unroll
            for (int e = 0; e < 16; ++e) mx[t] = fmaxf(mx[t], sv[e]);
          }
        }
      }
      int idx = 0xDDDD;
      f32x16 o0 = {}, o1 = {}, o2 = {}, o3 = {}, s0, s1, s2, s3;
      b3.v0 = bf16x8{}; b3.v1 = bf16x8{};
      LAS char* sp = lds + L_MRG + aw * 6144 + lane * 16; LAS char* gp = lds + L_MRG + (aw ^ 2) * 6144 + lane * 16;
      const int tt = (8 * qc + 4 * qh + (2 ^ (2 * kh))) * 2 + (r >> 4);
      unsigned oa = WS_O + (tt * 16 + pr) * 1024 + ((2 * odd + h) * 16 + c16) * 16, ob = h ? OOB : WS_O2 + (tt * 32 + head) * 128 + c16 * 8;
      volatile LAS f32x4* mw = (volatile LAS f32x4*)(lds + L_M) + aw * 64 + lane; volatile LAS f32x4* mr = (volatile LAS f32x4*)(lds + L_M) + (aw ^ 2) * 64 + r;
      asm volatile("" : "+v"(o0), "+v"(o1), "+v"(o2), "+v"(o3), "+v"(b3.v0), "+v"(b3.v1), "+v"(idx), "+v"(sp), "+v"(gp), "+v"(oa), "+v"(ob), "+v"(mw), "+v"(mr)); SB();
      {
        f32x16 sm[8];
#define SQ(n) ((n) < 4 ? (n) : ((n) - 2) & 3)
#define SMA(n) do { sm[n] = qk_a((n) < 4 ? b1_.k1 : b0.k1, q1[SQ(n)]); if ((n) == 6) s0 = sm[n]; if ((n) == 7) s1 = sm[n]; SB(); } while (0)
#define SMB(n) do { sm[n] = qk_b((n) < 4 ? b1_.k2 : b0.k2, q2[SQ(n)], sm[n]); SB(); } while (0)
        SMA(0); SMB(0); SMA(1); SMB(1);
        FS(1);
        LDV(b0, 0); LDV(b1_, 1); LDK(b2_, 2); LDV(b2_, 2); LDK(b3, 3);
        FS(2);
#pragma unroll
        for (int n = 0; n < 8; ++n) {
          if (n < 6) SMA(n + 2);
          max8<0>(sm[n], mx[SQ(n)]); SB();
          if (n < 6) SMB(n + 2);
          max8<8>(sm[n], mx[SQ(n)]); SB();
        }
      }
      FS(3);
      *mw = f32x4{mx[0], mx[1], mx[2], mx[3]};
      if constexpr (PASS) lds_meet<0>((LAS unsigned*)(lds + L_C + 44), 4, lane);
#pragma unroll
      for (int t = 0; t < 4; ++t) {
        auto sw = __builtin_amdgcn_permlane32_swap(__float_as_uint(mx[t]), __float_as_uint(mx[t]), false, false);
        asm volatile("v_max_f32 %0, %1, %2" : "=v"(mx[t]) : "v"(sw[0]), "v"(sw[1]));
      }
      s3 = o3; asm volatile("" : "+v"(s3));
      f32x4 ma, mb;
#pragma unroll 1
      for (int n = 0; n < (1 << 22); ++n) {
        ma = mr[0]; mb = mr[32];
        if (PASS || !__any(__builtin_isunordered(ma[0], mb[0]) | __builtin_isunordered(ma[1], mb[1]) | __builtin_isunordered(ma[2], mb[2]) | __builtin_isunordered(ma[3], mb[3]))) break;
      }
      FS(4);
#pragma unroll
      for (int t = 0; t < 4; ++t) {
        float nm;
        asm("v_max3_f32 %0, %1, %2, %3\n\tv_sub_f32 %0, %4, %0" : "=&v"(nm) : "v"(mx[t]), "v"(ma[t ^ 2]), "v"(mb[t ^ 2]), "s"(PASS ? 0.f : -MARGIN));
        u32x2 pm = __builtin_bit_cast(u32x2, q2[t]);
        if constexpr (PASS) {
          const float a0 = bhi(__float_as_uint(nm)), r0 = nm - a0, a1 = bhi(__float_as_uint(r0)), a2 = r0 - a1;
          if (h) pm = u32x2{(__float_as_uint(a0) >> 16) | __float_as_uint(a1), __float_as_uint(a2) >> 16};
        } else if (h) pm[0] = __float_as_uint(nm) >> 16;
        q2[t] = __builtin_bit_cast(s16x4, pm);
        if (t == 0) s0 = qk_b(b0.k2, q2[0], s0);
        if (t == 1) s1 = qk_b(b0.k2, q2[1], s1);
        SB();
      }
      if constexpr (!PASS) { STAMP(7); SB(); }
#define KT(C, P, N, d) do { \
  STEP(s0, s3, o3, P, s2, C, q1[2], q2[2]); LDV(P, d + 3); __builtin_amdgcn_s_waitcnt(0x0F7A); SB(); \
  STEP(s1, s0, o0, C, s3, C, q1[3], q2[3]); LDK(C, d + 4); \
  STEP(s2, s1, o1, C, s0, N, q1[0], q2[0]); \
  STEP(s3, s2, o2, C, s1, N, q1[1], q2[1]); } while (0)
#pragma unroll 1
      for (int it = 0; it < 7; ++it) {
        KT(b0, b3, b1_, 0); KT(b1_, b0, b2_, 1); KT(b2_, b1_, b3, 2); KT(b3, b2_, b0, 3);
        so += 4 * PT;
      }
#define KTE(C, P, N) do { STEP(s0, s3, o3, P, s2, C, q1[2], q2[2]); STEP(s1, s0, o0, C, s3, C, q1[3], q2[3]); \
  STEP(s2, s1, o1, C, s0, N, q1[0], q2[0]); STEP(s3, s2, o2, C, s1, N, q1[1], q2[1]); } while (0)
#define SEND(O, z, e) do { *(LAS f32x4*)(sp + (z) * 3072 + (e) * 1024) = f32x4{O[4 * (e)], O[4 * (e) + 1], O[4 * (e) + 2], O[4 * (e) + 3]}; SB(); } while (0)
      STEP(s0, s3, o3, b3, s2, b0, q1[2], q2[2]); LDV(b3, 3);
      STEP(s1, s0, o0, b0, s3, b0, q1[3], q2[3]); STEP(s2, s1, o1, b0, s0, b1_, q1[0], q2[0]); STEP(s3, s2, o2, b0, s1, b1_, q1[1], q2[1]);
      KTE(b1_, b0, b2_); KTE(b2_, b1_, b3);
      STEP(s0, s3, o3, b2_, s2, b3, q1[2], q2[2]); STEP(s1, s0, o0, b3, s3, b3, q1[3], q2[3]);
      EX4(s2, 0); SB(); o1 = pv_lo(b3.v0, s1, o1, idx); SB(); EX4(s2, 4); SB(); o1 = pv_hi(b3.v1, s1, o1, idx); SB();
      EX4(s2, 8); SB(); SEND(o0, 0, 0); SEND(o0, 0, 1); EX4(s2, 12); SB(); SEND(o0, 0, 2);
      o2 = pv_lo(b3.v0, s2, o2, idx); SB(); EX4(s3, 0); SB(); SEND(o1, 1, 0); SEND(o1, 1, 1);
      o2 = pv_hi(b3.v1, s2, o2, idx); SB(); EX4(s3, 4); SB(); SEND(o1, 1, 2);
      lds_add((LAS unsigned*)(lds + L_C + 32 + 4 * qh), 1u); SB();
      EX4(s3, 8); SB(); o3 = pv_lo(b3.v0, s3, o3, idx); SB(); EX4(s3, 12); SB(); o3 = pv_hi(b3.v1, s3, o3, idx); SB();
      asm volatile("" : "+v"(o2), "+v"(o3));
      if constexpr (!PASS) { STAMP(8); SB(); }
#pragma unroll 1
      for (int n = 0; n < (1 << 22); ++n) { if (__builtin_amdgcn_readfirstlane(*(volatile LAS unsigned*)(lds + L_C + 32 + 4 * qh)) >= 1024 + 2 + 2 * PASS) break; }
      asm volatile("" ::: "memory");
      u32x4 pa[2]; u32x2 pb_[2]; float chk = 0.f, lm = 1.f;
#pragma unroll
      for (int z = 0; z < 2; ++z) {
        const f32x16 k = z ? o3 : o2;
        f32x4 v[3];
#pragma unroll
        for (int e = 0; e < 3; ++e) v[e] = f32x4{k[4 * e], k[4 * e + 1], k[4 * e + 2], k[4 * e + 3]} + *(LAS f32x4*)(gp + z * 3072 + e * 1024);
        const float l = __uint_as_float(__builtin_amdgcn_permlane32_swap(__float_as_uint(v[2][0]), __float_as_uint(v[2][0]), false, false)[1]);
        const float inv = __builtin_amdgcn_rcpf(l);
        const f32x4 u0 = v[0] * inv, u1 = v[1] * inv, u2 = v[2] * inv, us = (u0 + u1) + u2;
        pa[z] = pk8(u0, u1); pb_[z] = u32x2{pk2(u2[0], u2[1]), pk2(u2[2], u2[3])};
        chk += (us[0] + us[1]) + (us[2] + us[3]);
        if (!(l > 1.0e-30f && l < 1.0e37f)) lm = 0.f;
      }
      unsigned bad = 1u;
      if constexpr (!PASS) { stored = !__any(!(fabsf(chk) < 3.0e38f) || lm == 0.f); if (!stored) bad = 0x101u; }
      else {
#pragma unroll
        for (int z = 0; z < 2; ++z) {
#pragma unroll
          for (int j = 0; j < 4; ++j) pa[z][j] = min(pa[z][j], 0xfffffffeu);
          pb_[z][0] = min(pb_[z][0], 0xfffffffeu);
        }
      }
      if (PASS ? !stored : stored) { st16(pa[0], rw, oa, 0); st8(pb_[0], rw, ob, 0); st16(pa[1], rw, oa, 32768); st8(pb_[1], rw, ob, 8192); }
      unsigned cn = lds_add_rtn((LAS unsigned*)(lds + L_C + 24), bad) + bad;
      if constexpr (!PASS) {
        STAMP(9);
#pragma unroll 1
        for (int n = 0; n < (1 << 22); ++n) { if ((cn & 255) >= 4) break; cn = __builtin_amdgcn_readfirstlane(*(volatile LAS unsigned*)(lds + L_C + 24)); }
        asm volatile("" ::: "memory");
      }
      return cn;
    };
    if (__builtin_expect(sweep(IC<0>{}) >= 256, 0)) sweep(IC<1>{});
    const int lane = fresh_lane();
    if (b == 0) {
      await<8>(fl, F_O + aw * 64 + lane, tag);
      lds_meet<2>((LAS unsigned*)(lds + L_C + 28), 4, lane);
      if (w == 4) publish(fl, 0, tag, lane);
    }
  } else {
    const int rt2 = 4 * g + (idx8 & 3), ct2 = idx8 >> 2;
    {
      DRAIN();
      if (lds_add_rtn((LAS unsigned*)(lds + L_C + 16), 1u) == 3) publish(fl, F_H + b, tag, lane);
      const int j = lane & 15, hc = 4 * w + (j & 3), hr = rt2 >> 1;
      const int pb = (j < 4 || j > 12) ? (hr >> 2) + 4 * (hc >> 3) + 8 * ((hr & 3) + 4 * (hc & 7)) : 8 * (j < 10 ? 6 * w + j - 4 : (j < 12 ? 14 + 2 * w + j : w)) + ct2;
      const int tt = (8 * qc + 4 * (w & 1) + (2 ^ (w & 2))) * 2 + (r >> 4);
      const unsigned oa = WS_O + (tt * 16 + pr) * 1024 + ((2 * odd + h) * 16 + c16) * 16, ob = h ? OOB : WS_O2 + (tt * 32 + head) * 128 + c16 * 8;
#pragma unroll
      for (int z = 0; z < 2; ++z) {
        st16(u32x4{~0u, ~0u, ~0u, ~0u}, rw, oa, z * 32768); SB(); __builtin_amdgcn_s_sleep(6); st8(u32x2{~0u, ~0u}, rw, ob, z * 8192); SB(); __builtin_amdgcn_s_sleep(3); }
      DRAIN();
      lds_add((LAS unsigned*)(lds + L_C + 32), 256u);
      if ((lds_add_rtn((LAS unsigned*)(lds + L_C + 36), 256u) >> 8) == 3) publish(fl, F_O + qc * 32 + head, tag, lane);
      await<6>(fl, F_H + pb, tag);
    }
    STAMP(6);
    const int o = (64 * rt2 + 16 * w + c16) * D_ + 80 * ct2;
    u32x4 xv[2], bv[2]; u32x2 xv4, bv4;
    f32x4 y[5], ya[5], yb[5], yc[5];
    {
      f32x4 acc[4][5];
      bf16x8 f0[11], f1[11];
      const unsigned vo = lane * 16, wo_ = WS_W + (ct2 * 68 + 12 * w) * 5120, wq = WS_W + (ct2 * 68 + 48) * 5120;
      unsigned ao[4];
#pragma unroll
      for (int m = 0; m < 4; ++m) ao[m] = WS_H + ((4 * rt2 + (m ^ w)) * 48 + 12 * w) * 1024;
      LAS char* wl = lds + L_WO + lane * 16;
#define WOFF(s) ((s) < 10 ? w * 20480 + (s) * 2048 : 81920 + w * 5120 + ((s) - 10) * 2048)
#define WPOS(s) (((w - 1 - (g >> 1)) & 3) * 25600 + (s) * 2048)
#define GL1(F, j, s) F[j] = (j) < 4 ? ld16(rw, vo, ao[(j) & 3] + (s) * 1024) : ((j) < 9 ? ld16(rw, vo + (((j) - 4) & 3) * 1024, wo_ + ((s) * 5 + (((j) - 4) & 4)) * 1024) : ld16(rw, vo + ((j) - 9) * 1024, wq + WOFF(s)))
#define GSTEP(FC, FN, s, LD, Z) do { _Pragma("unroll") for (int n = 0; n < 20; ++n) { \
        acc[n & 3][n >> 2] = __builtin_amdgcn_mfma_f32_16x16x32_bf16(FC[4 + (n >> 2)], FC[n & 3], (Z) ? f32x4{} : acc[n & 3][n >> 2], 0, 0, 0); \
        if ((LD) && n < 2) { GL1(FN, n, (s) + 1); } else if ((LD) && !(n & 1)) { GL1(FN, (n >> 1) + 1, (s) + 1); } \
        if (n == 17 || n == 19) *(LAS bf16x8*)(wl + WPOS(s) + (n == 19 ? 1024 : 0)) = FC[n == 19 ? 10 : 9]; \
        SB(); } } while (0)
#pragma unroll
      for (int j = 0; j < 11; ++j) { GL1(f0, j, 0); }
      __builtin_amdgcn_s_setprio(0);
      SB();
      GSTEP(f0, f1, 0, true, true);
#pragma unroll 1
      for (int s = 1; s < 11; s += 2) { GSTEP(f1, f0, s, true, false); GSTEP(f0, f1, s + 1, true, false); }
      const bf16x8 wlast = ld16(rw, vo, wq + WOFF(12));
      xv[0] = *(const u32x4*)(x + o + 8 * kq); xv[1] = *(const u32x4*)(x + o + 32 + 8 * kq); xv4 = *(const u32x2*)(x + o + 64 + 4 * kq);
      bv[0] = *(const u32x4*)(b2 + 80 * ct2 + 8 * kq); bv[1] = *(const u32x4*)(b2 + 80 * ct2 + 32 + 8 * kq); bv4 = *(const u32x2*)(b2 + 80 * ct2 + 64 + 4 * kq);
      SB();
      GSTEP(f1, f0, 11, false, false);
      __builtin_amdgcn_s_setprio(2);
      STAMP(7);
      *(LAS bf16x8*)(wl + WPOS(12)) = wlast;
      const u64 mk = __hip_atomic_load(fl + 16 * (F_O + g * 32 + r), RLX_AGENT);
      LAS char* rp = lds + L_RED + lane * 16;
      volatile LAS unsigned* gc = (volatile LAS unsigned*)(lds + L_C + 48);
#define GHOP(n, from) do { asm volatile("" ::: "memory"); gc[w] = n; \
        _Pragma("unroll 1") for (int k_ = 0; k_ < (1 << 22); ++k_) { if (__builtin_amdgcn_readfirstlane(gc[from]) >= n) break; __builtin_amdgcn_s_sleep(1); } \
        asm volatile("" ::: "memory"); } while (0)
#pragma unroll
      for (int t = 0; t < 5; ++t) *(LAS f32x4*)(rp + w * 5120 + t * 1024) = acc[3][t];
      GHOP(1, w ^ 3);
#pragma unroll
      for (int t = 0; t < 5; ++t) { yc[t] = *(LAS f32x4*)(rp + (w ^ 3) * 5120 + t * 1024); *(LAS f32x4*)(rp + (w ^ 3) * 5120 + t * 1024) = acc[2][t]; }
      GHOP(2, w ^ 2);
#pragma unroll
      for (int t = 0; t < 5; ++t) { ya[t] = *(LAS f32x4*)(rp + (w ^ 1) * 5120 + t * 1024); *(LAS f32x4*)(rp + (w ^ 1) * 5120 + t * 1024) = acc[1][t]; }
      GHOP(3, w ^ 1);
#pragma unroll
      for (int t = 0; t < 5; ++t) { yb[t] = *(LAS f32x4*)(rp + w * 5120 + t * 1024); y[t] = acc[0][t]; }
      if (!__all(mk == tag)) await<2>(fl, F_O + g * 32 + r, tag);
    }
    STAMP(8);
    constexpr int NW = 34;
    bf16x8 wr[NW];
    LAS char* wp = lds + L_WO + lane * 16; LAS char* wp2 = wp + 61440;
    asm volatile("" : "+v"(wp2));
    STAMP(9);
    unsigned oo[4], oo2[4];
#pragma unroll
    for (int j = 0; j < 4; ++j) {
      const int gr = (j + 1 + (g >> 1)) & 3;
      oo[j] = WS_O + (4 * rt2 + w) * 16384 + gr * 4096; oo2[j] = WS_O2 + (4 * rt2 + w) * 4096 + gr * 1024; asm volatile("" : "+s"(oo[j]), "+s"(oo2[j]));
    }
    const unsigned v16 = lane * 16, v8 = kq * 256 + c16 * 8;
    u32x4 a[16]; u32x2 al[8];
#define OL16(p, e, C) asm volatile("buffer_load_dwordx4 %0, %1, %2, %3 offen offset:%4 sc1" : C(a[4 * (p) + (e)]) : "v"(v16), "s"(rw), "s"(oo[p]), "n"((e) * 1024) : "memory")
#define OL8(p, e, C) asm volatile("buffer_load_dwordx2 %0, %1, %2, %3 offen offset:%4 sc1" : C(al[2 * (p) + (e)]) : "v"(v8), "s"(rw), "s"(oo2[p]), "n"((e) * 128) : "memory")
#define OLG(p, C) do { OL16(p, 0, C); OL16(p, 1, C); OL16(p, 2, C); OL16(p, 3, C); OL8(p, 0, C); OL8(p, 1, C); } while (0)
#pragma unroll 1
    for (int n = 0; n < (1 << 24); ++n) { if ((__builtin_amdgcn_readfirstlane(*(volatile LAS unsigned*)(lds + L_C + 24)) & 255) >= 4) break; __builtin_amdgcn_s_sleep(1); }
    STAMP(10);
    OLG(0, "=&v"); OLG(1, "=&v");
#pragma unroll
    for (int t = 0; t < 5; ++t) {
      asm volatile("" : "+v"(y[t]), "+v"(ya[t]), "+v"(yb[t]), "+v"(yc[t]));
      unsigned x0 = t < 4 ? xv[(t >> 1) & 1][2 * (t & 1)] : xv4[0], x1 = t < 4 ? xv[(t >> 1) & 1][2 * (t & 1) + 1] : xv4[1];
      const unsigned b0 = t < 4 ? bv[(t >> 1) & 1][2 * (t & 1)] : bv4[0], b1v = t < 4 ? bv[(t >> 1) & 1][2 * (t & 1) + 1] : bv4[1];
      asm volatile("" : "+v"(x0), "+v"(x1));
      y[t] = (y[t] + ya[t]) + (yb[t] + yc[t]) + (f32x4{blo(x0) + blo(b0), bhi(x0) + bhi(b0), blo(x1) + blo(b1v), bhi(x1) + bhi(b1v)});
      asm volatile("" : "+v"(y[t]));
    }
#pragma unroll
    for (int i = 0; i < NW; ++i) wr[i] = *(LAS bf16x8*)(wp + i * 1024);
    SB();
#define OCHK(p, N) ({ unsigned m_; asm volatile("s_waitcnt vmcnt(" #N ")" : "+v"(a[4 * (p)]), "+v"(a[4 * (p) + 1]), "+v"(a[4 * (p) + 2]), "+v"(a[4 * (p) + 3]), "+v"(al[2 * (p)]), "+v"(al[2 * (p) + 1]) :: "memory"); \
      m_ = max(al[2 * (p)][0], al[2 * (p) + 1][0]); _Pragma("unroll") for (int e = 0; e < 4; ++e) m_ = max(m_, max(a[4 * (p) + e][0], a[4 * (p) + e][2])); __any(m_ == ~0u); })
#define OGRP(p, N) do { \
      if (__builtin_expect(OCHK(p, N), 0)) { \
        _Pragma("unroll 1") for (int n = 0; n < (1 << 20); ++n) { OLG(p, "+v"); if ((p) < 3) OLG(((p) + 1) & 3, "+v"); if (!OCHK(p, N)) break; } } \
      SB(); \
      _Pragma("unroll") for (int e = 0; e < 25; ++e) { const int i = 25 * (p) + e; \
        const u32x4 af = e < 20 ? a[4 * (p) + (e / 5 & 3)] : u32x4{al[2 * (p)][0], al[2 * (p)][1], al[2 * (p) + 1][0], al[2 * (p) + 1][1]}; \
        asm volatile("v_mfma_f32_16x16x32_bf16 %0, %1, %2, %0" : "+v"(y[i % 5]) : "v"(wr[i % NW]), "v"(af)); \
        if ((p) < 2 && e % 4 == 3) { if (e < 16) OL16(((p) + 2) & 3, (e / 4) & 3, "=&v"); else OL8(((p) + 2) & 3, (e / 4) & 1, "=&v"); } \
        const int j = NW + (i * 3 + 3) / 4;        \
        if (i % 4 != 3 && j < 100) wr[j % NW] = *(LAS bf16x8*)(j < 60 ? wp + j * 1024 : wp2 + (j - 60) * 1024); \
        SB(); } } while (0)
    OGRP(0, 6); FS(13); OGRP(1, 6); FS(14); OGRP(2, 6); OGRP(3, 0);
    asm volatile("s_nop 7" : "+v"(y[0]), "+v"(y[1]), "+v"(y[2]), "+v"(y[3]), "+v"(y[4]));
    const rsrc_t rout = mkrs(out, 2048 * D_ * 2);
    __builtin_amdgcn_raw_buffer_store_b128(pk8(y[0], y[1]), rout, o * 2 + 16 * kq, 0, 0);
    __builtin_amdgcn_raw_buffer_store_b128(pk8(y[2], y[3]), rout, o * 2 + 64 + 16 * kq, 0, 0);
    __builtin_amdgcn_raw_buffer_store_b64(u32x2{pk2(y[4][0], y[4][1]), pk2(y[4][2], y[4][3])}, rout, o * 2 + 128 + 8 * kq, 0, 0);
    STAMP(11);
  }
}

extern "C" void kernel_launch(void* const* d_in, const int* in_sizes, int n_in, void* d_out, int out_size, void* d_ws, size_t ws_size,
                              hipStream_t stream) {
  fused<<<256, 512, 0, stream>>>((const u16*)d_in[0], (const u16*)d_in[1], (const u16*)d_in[2], (const u16*)d_in[4], (const u16*)d_in[5],
                                 (const u16*)d_in[6], (const u16*)d_in[7], (u16*)d_out, (char*)d_ws);
}
```

```cpp
#include <hip/hip_runtime.h>
#include <hip/hip_bf16.h>

typedef __attribute__((ext_vector_type(8))) __bf16 bf16x8;
typedef __attribute__((ext_vector_type(16))) __bf16 bf16x16;
typedef __attribute__((ext_vector_type(2))) __bf16 bf16x2;
typedef __attribute__((ext_vector_type(4))) short s16x4;
typedef __attribute__((ext_vector_type(16))) float f32x16;
typedef __attribute__((ext_vector_type(8))) float f32x8;
typedef __attribute__((ext_vector_type(4))) float f32x4;
typedef __attribute__((ext_vector_type(4))) unsigned u32x4;
typedef __attribute__((ext_vector_type(2))) unsigned u32x2;
typedef unsigned short u16;
typedef unsigned long long u64;
typedef __amdgpu_buffer_rsrc_t rsrc_t;
#define LAS __attribute__((address_space(3)))
#define GAS __attribute__((address_space(1)))
#define SB() __builtin_amdgcn_sched_barrier(0)
#define RLX_AGENT __ATOMIC_RELAXED, __HIP_MEMORY_SCOPE_AGENT
#define BAR_L() do { asm volatile("" ::: "memory"); __builtin_amdgcn_s_waitcnt(0xC07F); asm volatile("s_barrier" ::: "memory"); } while (0)
#define DRAIN() asm volatile("s_waitcnt vmcnt(0)" ::: "memory")

constexpr int D_ = 640, F_ = 1536;
constexpr unsigned PT = 7808, P_V = 2560, P_Q = 5120, P_ONE = 7680;
constexpr unsigned WS_T = 0;
constexpr unsigned WS_H = 8u << 20;
constexpr unsigned WS_W = 16u << 20;
constexpr unsigned WS_O = 20u << 20;
constexpr unsigned WS_O2 = 22u << 20;
constexpr unsigned WS_S = 23u << 20;
constexpr unsigned WS_F = 24u << 20;
constexpr unsigned WS_END = WS_F;
constexpr unsigned F_Q = 256, F_H = 512, F_O = 768;
constexpr float QSCALE = 0.22360679774997896f * 1.4426950408889634f;
constexpr float MARGIN = 88.f;
constexpr unsigned OOB = 0x80000000u;
constexpr unsigned CODE_SKIP = 4608;
constexpr unsigned XS = 16448, WSL = 16640, L_W = 6 * XS;
constexpr unsigned L_WO = 0, L_RED = 102400, L_MRG = 122880;
constexpr unsigned L_ST = L_W + 3 * WSL;
constexpr unsigned L_G = L_ST + 2048;
constexpr unsigned L_M = L_G + 4096;
constexpr unsigned L_C = L_M + 4096;
constexpr unsigned L_PF = L_C + 64;
constexpr unsigned L_END = L_PF + 256;

#define STAMP(i)
#define STEPSTAMP()

#define FS(v)
__device__ __forceinline__ unsigned pk2(float a, float b) { bf16x2 r = {(__bf16)a, (__bf16)b}; return __builtin_bit_cast(unsigned, r); }
__device__ __forceinline__ u32x4 pk8(f32x4 a, f32x4 b) { return u32x4{pk2(a[0], a[1]), pk2(a[2], a[3]), pk2(b[0], b[1]), pk2(b[2], b[3])}; }
__device__ __forceinline__ float blo(unsigned u) { return __uint_as_float(u << 16); }
__device__ __forceinline__ float bhi(unsigned u) { return __uint_as_float(u & 0xffff0000u); }
__device__ __forceinline__ u64 uni64(u64 v) {
  return ((u64)(unsigned)__builtin_amdgcn_readfirstlane((unsigned)(v >> 32)) << 32) | (unsigned)__builtin_amdgcn_readfirstlane((unsigned)v);
}
__device__ __forceinline__ int fresh_lane() { int l = __builtin_amdgcn_mbcnt_hi(~0u, __builtin_amdgcn_mbcnt_lo(~0u, 0u)); asm volatile("" : "+v"(l)); return l; }
__device__ __forceinline__ rsrc_t mkrs(const void* p, unsigned bytes) { return __builtin_amdgcn_make_buffer_rsrc((void*)p, 0, bytes, 0x00020000); }
__device__ __forceinline__ void dma16(rsrc_t r, unsigned vo, unsigned so, unsigned lds_base) {
  asm volatile("s_mov_b32 m0, %0\n\ts_nop 0\n\tbuffer_load_dwordx4 %1, %2, %3 offen lds" :: "s"(lds_base), "v"(vo), "s"(r), "s"(so) : "memory");
}
__device__ __forceinline__ void touch(rsrc_t r, unsigned vo, unsigned lds_base) {
  asm volatile("s_mov_b32 m0, %0\n\ts_nop 0\n\tbuffer_load_dword %1, %2, 0 offen lds" :: "s"(lds_base), "v"(vo), "s"(r) : "memory");
}
#define STOUCH3(p) asm volatile("s_load_dword s100, %0, 0\n\ts_load_dword s100, %0, 64\n\ts_load_dword s100, %0, 128" :: "s"(p) : "s100")
#define SPFX(x) x
#define STOUCH4(p) asm volatile("s_load_dword s100, %0, 0\n\ts_load_dword s100, %0, 64\n\ts_load_dword s100, %0, 128\n\ts_load_dword s100, %0, 192" :: "s"(p) : "s100")
__device__ __forceinline__ bf16x8 ld16(rsrc_t r, unsigned vo, unsigned so) { return __builtin_bit_cast(bf16x8, __builtin_amdgcn_raw_buffer_load_b128(r, vo, so, 16)); }
__device__ __forceinline__ s16x4 ld8(rsrc_t r, unsigned vo, unsigned so) { return __builtin_bit_cast(s16x4, __builtin_amdgcn_raw_buffer_load_b64(r, vo, so, 16)); }
__device__ __forceinline__ void st16(u32x4 v, rsrc_t r, unsigned vo, unsigned so) { __builtin_amdgcn_raw_buffer_store_b128(v, r, vo + so, 0, 16); }
__device__ __forceinline__ void st8(u32x2 v, rsrc_t r, unsigned vo, unsigned so) { __builtin_amdgcn_raw_buffer_store_b64(v, r, vo + so, 0, 16); }
__device__ __forceinline__ void publish(GAS u64* f, unsigned i, u64 tag, int lane) {
  st16(u32x4{(unsigned)tag, (unsigned)(tag >> 32), (unsigned)tag, (unsigned)(tag >> 32)}, mkrs((const void*)(f + 16 * i), 128), lane * 16, 0);
}
template <int SLP> __device__ __forceinline__ void await(GAS u64* f, unsigned i, u64 tag) {
#pragma unroll 1
  for (int n = 0; n < (1 << 20); ++n) {
    if (__all(__hip_atomic_load(f + 16 * i, RLX_AGENT) == tag)) break;
    __builtin_amdgcn_s_sleep(SLP);
  }
  __builtin_amdgcn_fence(__ATOMIC_ACQUIRE, "wavefront");
}
__device__ __forceinline__ void lds_add(LAS unsigned* c, unsigned v) {
  asm volatile("s_mov_b64 exec, 1\n\tds_add_u32 %0, %1\n\ts_mov_b64 exec, -1" :: "v"(c), "v"(v) : "memory");
}
__device__ __forceinline__ unsigned lds_add_rtn(LAS unsigned* c, unsigned v) {
  unsigned r_;
  asm volatile("s_mov_b64 exec, 1\n\tds_add_rtn_u32 %0, %1, %2\n\ts_mov_b64 exec, -1\n\ts_waitcnt lgkmcnt(0)" : "=&v"(r_) : "v"(c), "v"(v) : "memory");
  return __builtin_amdgcn_readfirstlane(r_);
}
template <int SLP> __device__ __forceinline__ void lds_meet(LAS unsigned* c, unsigned n, int lane) {
  lds_add(c, 1u);
#pragma unroll 1
  for (int k = 0; k < (1 << 22); ++k) {
    if (__builtin_amdgcn_readfirstlane(*(volatile LAS unsigned*)c) >= n) break;
    if (SLP) __builtin_amdgcn_s_sleep(SLP);
  }
  asm volatile("" ::: "memory");
}
__device__ __forceinline__ s16x4 trd(LAS char* p) { return __builtin_amdgcn_ds_read_tr16_b64_v4i16((LAS s16x4*)p); }
__device__ __forceinline__ bf16x8 cat8(s16x4 a, s16x4 b) { return __builtin_bit_cast(bf16x8, __builtin_shufflevector(a, b, 0, 1, 2, 3, 4, 5, 6, 7)); }
template <int C> __device__ __forceinline__ float dppadd(float a) {
  return a + __int_as_float(__builtin_amdgcn_update_dpp(0, __float_as_int(a), C, 0xf, 0xf, true));
}

__device__ __forceinline__ u32x2 sqrelu4(f32x4 a, f32x4 c, f32x4 bb, float r, float t) {
  float v0, v1, v2, v3;
  asm("v_fma_f32 %0, %8, %17, %12\n\tv_fma_f32 %1, %9, %17, %13\n\tv_fma_f32 %2, %10, %17, %14\n\tv_fma_f32 %3, %11, %17, %15\n\t"
      "v_fmac_f32 %0, %4, %16\n\tv_fmac_f32 %1, %5, %16\n\tv_fmac_f32 %2, %6, %16\n\tv_fmac_f32 %3, %7, %16\n\t"
      "v_mul_f32 %0, %0, |%0|\n\tv_mul_f32 %1, %1, |%1|\n\tv_mul_f32 %2, %2, |%2|\n\tv_mul_f32 %3, %3, |%3|\n\t"
      "v_cvt_pk_bf16_f32 %0, %0, %1\n\tv_cvt_pk_bf16_f32 %2, %2, %3\n\tv_pk_max_i16 %0, %0, 0\n\tv_pk_max_i16 %2, %2, 0"
      : "=&v"(v0), "=&v"(v1), "=&v"(v2), "=&v"(v3)
      : "v"(a[0]), "v"(a[1]), "v"(a[2]), "v"(a[3]), "v"(c[0]), "v"(c[1]), "v"(c[2]), "v"(c[3]), "v"(bb[0]), "v"(bb[1]), "v"(bb[2]), "v"(bb[3]), "v"(r), "v"(t));
  return u32x2{__float_as_uint(v0), __float_as_uint(v2)};
}

template <int N> struct IC { static constexpr int v = N; };
template <int MODE> struct G1 {
  static constexpr int NM = MODE == 2 ? 2 : 4, NI = MODE == 2 ? 6 : 4, PAD = MODE == 1 ? 32 : 8, NP = NI / 2;
  static constexpr int NMMA = 2 * NM * NI, NRD = 2 * (NM + 2 * NI), HEAD = MODE == 2 ? 6 : 12, R2H = 3, R2S = 18;
  struct Frag { bf16x8 x[2][NM]; s16x4 w[2][NI][2]; };
  struct Ptr { LAS char* x[2][2]; LAS char* w[NP]; };
  template <int R, int XO = 0, int WO = 0> static __device__ __forceinline__ void rd(Frag& f, const Ptr& p) {
    constexpr int u = R / (NRD / 2), r = R % (NRD / 2);
    if constexpr (r >= 2 && r < 2 + NM) f.x[u][r - 2] = *(LAS bf16x8*)(p.x[u][r & 1] + XO + ((r - 2) >> 1) * 4096 + (r & 1) * (MODE == 1 ? 512 : 2048));
    else { constexpr int q = r < 2 ? r : r - NM, i = q >> 1;
      f.w[u][i][q & 1] = trd(p.w[i >> 1] + WO + (i & 1) * PAD + u * (8192 + 4 * PAD) + (q & 1) * 1024); }
  }
  template <int N> static __device__ __forceinline__ void mma(f32x4 (&acc)[NM][NI], const Frag& f) {
    constexpr int u = N / (NM * NI), i = (N / NM) % NI, m = N % NM;
    const bf16x8 wf = cat8(f.w[u][i][0], f.w[u][i][1]);
    if constexpr (MODE == 1) asm volatile("v_mfma_f32_16x16x32_bf16 %0, %1, %2, %0" : "+v"(acc[m][i]) : "v"(f.x[u][m]), "v"(wf));
    else asm volatile("v_mfma_f32_16x16x32_bf16 %0, %1, %2, %0" : "+v"(acc[m][i]) : "v"(wf), "v"(f.x[u][m]));
  }
  template <int U> static __device__ __forceinline__ void csum(f32x4& cs, const Frag& f, bf16x8 sel, int idx) {
    const bf16x16 wf = __builtin_shufflevector(cat8(f.w[U][0][0], f.w[U][0][1]), cat8(f.w[U][1][0], f.w[U][1][1]), 0, 1, 2, 3, 4, 5, 6, 7, 8, 9, 10, 11, 12, 13, 14, 15);
    asm volatile("v_smfmac_f32_16x16x64_bf16 %0, %1, %2, %3" : "+v"(cs) : "v"(sel), "v"(wf), "v"(idx));
  }
  static __device__ __forceinline__ int span(int w, int j) { return MODE == 2 ? (w + j >= 3 ? w + j - 3 : w + j) : 2 * (w >> 1) + (j ^ (w & 1)); }
  template <int R0, int R1, int XO = 0, int WO = 0> static __device__ __forceinline__ void rds(Frag& f, const Ptr& p) {
    if constexpr (R0 < R1) { rd<R0, XO, WO>(f, p); rds<R0 + 1, R1, XO, WO>(f, p); } }
  template <int N0, int N1> static __device__ __forceinline__ void mmas(f32x4 (&acc)[NM][NI], const Frag& f) { if constexpr (N0 < N1) { mma<N0>(acc, f); mmas<N0 + 1, N1>(acc, f); } }
  template <int N> static __device__ __forceinline__ void mix(f32x4 (&acc)[NM][NI], const Frag& fc, Frag& fn, const Ptr& p) {
    if constexpr (N < NMMA) {
      constexpr int SPAN = NMMA - HEAD - 3, k = N - HEAD;
      constexpr int r0 = k >= SPAN ? NRD : (k * NRD + SPAN - 1) / SPAN, r1 = k + 1 >= SPAN ? NRD : ((k + 1) * NRD + SPAN - 1) / SPAN;
      mma<N>(acc, fc); rds<r0, r1>(fn, p); SB();
      mix<N + 1>(acc, fc, fn, p);
    }
  }
  struct Off { unsigned x, w[NP]; };
  static __device__ __forceinline__ Off offs(int w, int lane) {
    const int kq = lane >> 4, c16 = lane & 15, a = (lane >> 2) & 3;
    Off o;
    o.x = MODE == 1 ? (64 * (w & 1) + 8 * (c16 >> 2) + (c16 & 3)) * 128 + ((kq ^ ((c16 >> 1) & 1) ^ ((c16 >> 3) << 1) ^ (c16 & 4)) << 4)
                    : ((MODE == 2 ? 32 * w : 64 * (w & 1)) + c16) * 128 + ((kq ^ (c16 >> 1)) << 4);
    const unsigned wb = L_W + kq * (2048 + PAD) + a * 256 + (lane & 3) * (MODE == 1 ? 8 : 16);
#pragma unroll
    for (int j = 0; j < NP; ++j) o.w[j] = wb + (span(w, j) ^ a) * 64;
    return o;
  }
  template <int U> static __device__ __forceinline__ void ptrx(Ptr& p, const Off& o, LAS char* lds, unsigned sx) {
    p.x[U][0] = lds + sx + (o.x ^ (64 * U)); p.x[U][1] = lds + sx + (o.x ^ (64 * U + 32)); asm volatile("" : "+v"(p.x[U][0]), "+v"(p.x[U][1])); }
  static __device__ __forceinline__ void ptrw(Ptr& p, const Off& o, LAS char* lds, unsigned sw) {
#pragma unroll
    for (int j = 0; j < NP; ++j) { p.w[j] = lds + (sw + o.w[j]); asm volatile("" : "+v"(p.w[j])); } }
  template <int N, class T> static __device__ __forceinline__ void fin(f32x4 (&acc)[NM][NI], const Frag& fc, T& tail) {
    if constexpr (N < NMMA) { mma<N>(acc, fc); tail(IC<N - HEAD>{}); SB(); fin<N + 1>(acc, fc, tail); }
  }
  template <class T>
  static __device__ __forceinline__ void loop(f32x4 (&acc)[NM][NI], Frag& fa, const Off& o, LAS char* lds, unsigned sx, unsigned sw, int w, int lane, int pub,
                                              GAS u64* fl, unsigned fi, u64 tag, T tail, unsigned& stv) {
    const int kq = lane >> 4, c16 = lane & 15;
    Frag fb; Ptr p;
#pragma unroll
    for (int n = 0; n < NM * NI; ++n) { acc[n % NM][n / NM] = f32x4{}; asm volatile("" : "+v"(acc[n % NM][n / NM])); }
    const unsigned one = (kq >> 1) == ((c16 >> 1) & 1) ? 0x3f803f80u : 0u;
    bf16x8 sel = __builtin_bit_cast(bf16x8, u32x4{one, one, one, one});
    int sidx = (lane & 1) ? 0xEEEE : 0x4444;
    f32x4 cs = {};
    LAS float* cp = (LAS float*)(lds + L_ST + 1024) + 32 * span(w, 0) + (MODE == 1 ? c16 : 8 * (c16 >> 2) + (c16 & 3));
    asm volatile("s_nop 1" : "+v"(sel), "+v"(sidx), "+v"(cs), "+v"(cp));
    if constexpr (MODE != 2) { ptrx<0>(p, o, lds, sx); ptrx<1>(p, o, lds, sx); ptrw(p, o, lds, sw); SB(); tail(IC<-2>{}); STAMP(1); BAR_L(); STAMP(12); rds<0, NRD>(fa, p); }
#define G1_HEAD(FC, PUB, LAST, BAR) do { \
      mma<0>(acc, FC); sx = (sx == 5 * XS) ? 0 : sx + XS; asm volatile("" : "+s"(sx)); SB(); \
      csum<0>(cs, FC, sel, sidx); SB(); \
      mma<1>(acc, FC); sw = (sw == 2 * WSL) ? 0 : sw + WSL; asm volatile("" : "+s"(sw)); SB(); \
      mma<2>(acc, FC); ptrx<0>(p, o, lds, sx); SB(); \
      mma<3>(acc, FC); ptrw(p, o, lds, sw); SB(); \
      mmas<4, HEAD - 2>(acc, FC); SB(); \
      csum<1>(cs, FC, sel, sidx); SB(); \
      mma<HEAD - 2>(acc, FC); ptrx<1>(p, o, lds, sx); SB(); \
      mma<HEAD - 1>(acc, FC); SB(); \
      if (LAST) { asm volatile("s_nop 5" : "+v"(cs)); cp[0] = cs[0] + cs[1]; cp[MODE == 1 ? 16 : 4] = cs[2] + cs[3]; } \
      if (PUB) DRAIN(); \
      if (BAR) BAR_L(); \
      if ((PUB) && w == 0) publish(fl, fi, tag, lane); \
      SB(); } while (0)
    SB();
    G1_HEAD(fa, false, false, true);
#pragma unroll 1
    for (int s = 0; s < 4; ++s) {
      mix<HEAD>(acc, fa, fb, p);
      G1_HEAD(fb, false, false, true);
      mix<HEAD>(acc, fb, fa, p);
      G1_HEAD(fa, false, false, true);
    }
    mix<HEAD>(acc, fa, fb, p);
    G1_HEAD(fb, false, true, true);
    fin<HEAD>(acc, fb, tail);
    asm volatile("s_nop 7");
    SB();
  }
  template <int J, int N> static __device__ __forceinline__ void r2mix(f32x4 (&acc)[NM][NI], f32x4& cs, bf16x8 sel, int sidx, const Frag& fc, Frag& fn, const Ptr& pl, const Ptr& ph) {
    if constexpr (N < NMMA) {
      constexpr int H2 = R2H, SPAN = R2S, k = N - H2, xs = (5 + J) % 6;
      constexpr int r0 = k >= SPAN ? NRD : (k * NRD + SPAN - 1) / SPAN, r1 = k + 1 >= SPAN ? NRD : ((k + 1) * NRD + SPAN - 1) / SPAN;
      mma<N>(acc, fc);
      Ptr p = xs >= 3 ? ph : pl;
#pragma unroll
      for (int j = 0; j < NP; ++j) p.w[j] = J == 8 ? ph.w[j] : pl.w[j];
      rds<r0, r1, (xs % 3) * XS, J == 8 ? 0 : ((2 + J) % 3) * WSL>(fn, p); SB();
      if constexpr (N == 13) { csum<1>(cs, fc, sel, sidx); SB(); }
      r2mix<J, N + 1>(acc, cs, sel, sidx, fc, fn, pl, ph);
    }
  }
  template <int J, class PUB> static __device__ __forceinline__ void r2steps(f32x4 (&acc)[NM][NI], f32x4& cs, bf16x8 sel, int sidx, Frag& fc, Frag& fn, const Ptr& pl, const Ptr& ph, PUB& pub) {
    if constexpr (J < 9) {
      mmas<0, 2>(acc, fc); csum<0>(cs, fc, sel, sidx); mmas<2, R2H>(acc, fc); SB();
      if constexpr (J == 2) DRAIN();
      if constexpr (J < 8) BAR_L();
      if constexpr (J == 2) pub();
      SB();
      r2mix<J, R2H>(acc, cs, sel, sidx, fc, fn, pl, ph);
      r2steps<J + 1>(acc, cs, sel, sidx, fn, fc, pl, ph, pub);
    }
  }
  template <class PUB, class T>
  static __device__ __forceinline__ void loop2(f32x4 (&acc)[NM][NI], Frag& fa, const Off& o, const Ptr& ph, LAS char* lds, int w, int lane, PUB pub, T tail) {
    const int kq = lane >> 4, c16 = lane & 15;
    Frag fb; Ptr pl, ph2 = ph;
    ptrx<0>(pl, o, lds, 0); ptrx<1>(pl, o, lds, 0);
#pragma unroll
    for (int j = 0; j < NP; ++j) pl.w[j] = ph.w[j];
    ptrw(ph2, o, lds, 3 * XS - L_W);
#pragma unroll
    for (int n = 0; n < NM * NI; ++n) { acc[n % NM][n / NM] = f32x4{}; asm volatile("" : "+v"(acc[n % NM][n / NM])); }
    const unsigned one = (kq >> 1) == ((c16 >> 1) & 1) ? 0x3f803f80u : 0u;
    bf16x8 sel = __builtin_bit_cast(bf16x8, u32x4{one, one, one, one});
    int sidx = (lane & 1) ? 0xEEEE : 0x4444;
    f32x4 cs = {};
    LAS float* cp = (LAS float*)(lds + L_ST + 1024) + 32 * span(w, 0) + 8 * (c16 >> 2) + (c16 & 3);
    asm volatile("s_nop 1" : "+v"(sel), "+v"(sidx), "+v"(cs), "+v"(cp));
    SB();
    r2steps<0>(acc, cs, sel, sidx, fa, fb, pl, ph2, pub);
    mmas<0, 2>(acc, fb); csum<0>(cs, fb, sel, sidx); mmas<2, 4>(acc, fb); csum<1>(cs, fb, sel, sidx); mmas<4, HEAD>(acc, fb); SB();
    fin<HEAD>(acc, fb, tail);
    asm volatile("s_nop 7" : "+v"(cs));
    cp[0] = cs[0] + cs[1]; cp[4] = cs[2] + cs[3];
    SB();
  }
};

struct KV { bf16x8 k1; s16x4 k2; bf16x8 v0, v1; };
__device__ __forceinline__ f32x16 qk_a(bf16x8 k, bf16x8 q) { f32x16 z = {}; return __builtin_amdgcn_mfma_f32_32x32x16_bf16(k, q, z, 0, 0, 0); }
__device__ __forceinline__ f32x16 qk_b(s16x4 k, s16x4 q, f32x16 c) { return __builtin_amdgcn_mfma_f32_32x32x8bf16_1k(k, q, c, 0, 0, 0); }
__device__ __forceinline__ f32x16 pv_lo(bf16x8 v, f32x16 p, f32x16 o, int idx) {
  f32x8 h = __builtin_shufflevector(p, p, 0, 1, 2, 3, 4, 5, 6, 7);
  o = __builtin_amdgcn_smfmac_f32_32x32x32_bf16(v, __builtin_bit_cast(bf16x16, h), o, idx, 0, 0);
  asm volatile("" : "+v"(o));
  return o; }
__device__ __forceinline__ f32x16 pv_hi(bf16x8 v, f32x16 p, f32x16 o, int idx) {
  f32x8 h = __builtin_shufflevector(p, p, 8, 9, 10, 11, 12, 13, 14, 15);
  o = __builtin_amdgcn_smfmac_f32_32x32x32_bf16(v, __builtin_bit_cast(bf16x16, h), o, idx, 0, 0);
  asm volatile("" : "+v"(o));
  return o; }
template <int E> __device__ __forceinline__ void max8(const f32x16& s, float& m) {
  asm volatile("v_max3_f32 %0, %0, %1, %2\n\tv_max3_f32 %0, %0, %3, %4\n\tv_max3_f32 %0, %0, %5, %6\n\tv_max3_f32 %0, %0, %7, %8"
               : "+v"(m) : "v"(s[E]), "v"(s[E + 1]), "v"(s[E + 2]), "v"(s[E + 3]), "v"(s[E + 4]), "v"(s[E + 5]), "v"(s[E + 6]), "v"(s[E + 7]));
}
struct F4 { float v[4]; };
__device__ __forceinline__ F4 add4(f32x4 g, float a, float b, float c, float d, float& chk) {
  F4 r;
  asm volatile("v_add_f32 %0, %9, %5\n\tv_add_f32 %1, %10, %6\n\tv_add_f32 %2, %11, %7\n\tv_add_f32 %3, %12, %8\n\t"
               "v_maximum3_f32 %4, %4, |%0|, |%1|\n\tv_maximum3_f32 %4, %4, |%2|, |%3|"
               : "=&v"(r.v[0]), "=&v"(r.v[1]), "=&v"(r.v[2]), "=&v"(r.v[3]), "+v"(chk) : "v"(a), "v"(b), "v"(c), "v"(d), "v"(g[0]), "v"(g[1]), "v"(g[2]), "v"(g[3]));
  return r;
}
__device__ __forceinline__ u32x2 scale4(F4 g, float s) {
  unsigned r0, r1;
  asm volatile("v_mul_f32 %2, %2, %6\n\tv_mul_f32 %3, %3, %6\n\tv_mul_f32 %4, %4, %6\n\tv_mul_f32 %5, %5, %6\n\tv_cvt_pk_bf16_f32 %0, %2, %3\n\tv_cvt_pk_bf16_f32 %1, %4, %5"
               : "=&v"(r0), "=&v"(r1), "+v"(g.v[0]), "+v"(g.v[1]), "+v"(g.v[2]), "+v"(g.v[3]) : "v"(s));
  return u32x2{r0, r1};
}
#define EX4(s, b) do { s[b] = __builtin_amdgcn_exp2f(s[b]); s[b+1] = __builtin_amdgcn_exp2f(s[b+1]); s[b+2] = __builtin_amdgcn_exp2f(s[b+2]); s[b+3] = __builtin_amdgcn_exp2f(s[b+3]); } while (0)
#define STEPX(SC, SP, OP, VP, SN, KN, Q1, Q2, X0, X1) do { \
  EX4(SC, 0); SB(); OP = pv_lo(VP.v0, SP, OP, idx); SB(); X0; SB(); \
  EX4(SC, 4); SB(); SN = qk_a(KN.k1, Q1); SB(); \
  EX4(SC, 8); SB(); OP = pv_hi(VP.v1, SP, OP, idx); SB(); X1; SB(); \
  EX4(SC, 12); SB(); SN = qk_b(KN.k2, Q2, SN); SB(); } while (0)
#define STEP(SC, SP, OP, VP, SN, KN, Q1, Q2) STEPX(SC, SP, OP, VP, SN, KN, Q1, Q2, , )

__global__ void __launch_bounds__(512, 2)
fused(const u16* __restrict__ x, const u16* __restrict__ wqkv, const u16* __restrict__ wo, const u16* __restrict__ w1,
      const u16* __restrict__ b1, const u16* __restrict__ w2, const u16* __restrict__ b2, u16* __restrict__ out, char* __restrict__ ws) {
  __shared__ __attribute__((aligned(16))) char smem[L_END + 256];
  LAS char* lds = (LAS char*)smem;
  const unsigned lds0 = (unsigned)(size_t)lds;
  const int lane = threadIdx.x & 63, w = __builtin_amdgcn_readfirstlane(threadIdx.x >> 6);
  const int b = blockIdx.x, g = b & 7, idx8 = b >> 3;
  asm volatile("" :: "s"(x), "s"(wqkv), "s"(wo), "s"(w1), "s"(b1), "s"(w2), "s"(b2), "s"(out), "s"(ws));
  const int rt = (g & 3) * 4 + (idx8 & 3), ct = (g >> 2) * 8 + (idx8 >> 2);
  const bool vtype = ct >= 10;
  const int kq = lane >> 4, c16 = lane & 15;
  LAS float* st_r = (LAS float*)(lds + L_ST);
  LAS float* st_t = st_r + 128;
  LAS float* st_c = st_r + 256;
  GAS u64* fl = (GAS u64*)(ws + WS_F);
  const rsrc_t rw = mkrs(ws, WS_END), rx = mkrs(x, 2048 * D_ * 2);
  u64 tag = 0;
  const int head = 4 * g + (idx8 >> 3), qc = idx8 & 7;
  const int aw = w - 4, qh = aw & 1, kh = aw >> 1, r = lane & 31, h = lane >> 5, odd = head & 1, pr = head >> 1;
  int hstep = 0; unsigned stv = 0;
  unsigned oK1 = 0, oK2 = 0, oV0 = 0, oV1 = 0, oQ2 = 0;
  auto kv_offsets = [&](int ln) {
    const int r = ln & 31, h = ln >> 5;
    oK1 = odd * 1536 + ln * 16; oK2 = h ? P_ONE : 1024 + r * 16 + odd * 8; oQ2 = h ? OOB : oK2;
    const int dv = r < 16 ? (r & 3) + 4 * (r >> 3) + 8 * ((r >> 2) & 1) : r;
    oV0 = r < 20 ? P_V + odd * 1280 + h * 320 + dv * 16 : (r == 20 ? P_ONE : OOB); oV1 = r < 20 ? oV0 + 640 : oV0;
  };
  unsigned fi = 0;
  STAMP(0);

  if (w >= 4) {
    const int hw = w - 4;
    rsrc_t rwt = mkrs(wqkv, D_ * 3 * D_ * 2);
    unsigned rs = 3 * D_ * 2, pad = vtype ? 32 : 8;
    unsigned wso = ct * 256 + 16 * hw * rs, xso = (rt * 128 + hw * 32) * (D_ * 2);
    const unsigned xv = (lane >> 3) * (D_ * 2) + (((lane & 7) ^ kq) << 4), xv1 = xv ^ 64, xv2 = xv ^ 32, xv3 = xv ^ 96;
    const int wc = c16 ^ (kq << 2);
    unsigned wv = ct < 15 ? kq * rs + (wc << 4) : OOB;
    unsigned nx = lds0 + hw * 4096, nw = lds0 + L_W + hw * 4096;
#define DX(i) dma16(rx, i == 0 ? xv : (i == 1 ? xv1 : (i == 2 ? xv2 : xv3)), xso + i * 8 * (D_ * 2), nx + i * 1024)
#define DW(i) dma16(rwt, wv, wso + 4 * i * rs, nw + i * 1024 + (2 * hw + (i >> 1)) * pad)
#define NEXTX() do { xso += 128; nx = (nx == lds0 + hw * 4096 + 5 * XS) ? nx - 5 * XS : nx + XS; } while (0)
#define NEXTW() do { wso += 64 * rs; nw = (nw == lds0 + L_W + hw * 4096 + 2 * WSL) ? nw - 2 * WSL : nw + WSL; } while (0)
#define HBAR(n) asm volatile("s_waitcnt vmcnt(" #n ")\n\ts_barrier" ::: "memory")
    __builtin_amdgcn_s_setprio(1);
#define DW4() do { DW(0); DW(1); DW(2); DW(3); NEXTW(); } while (0)
#define DX4() do { DX(0); DX(1); DX(2); DX(3); NEXTX(); } while (0)
    const char* pw = (const char*)w1 + 256 * F_ * 2 + (b * 4 + hw) * 192;
    {
      DX(0); DW(0); DX(1); DW(1); DX(2); DW(2); DX(3); DW(3); NEXTW(); NEXTX();
      DX4();
#pragma unroll 1
      for (int s = -1; s < 7; ++s) { DW4(); DX4(); SPFX(STOUCH3(pw)); pw += (s == 4 ? -576 : 64) * F_ * 2; HBAR(12); }
    }
    const unsigned go = WS_S + (rt * 128 + (hw & 1) * 64 + lane) * 16;
    asm volatile("s_mov_b32 m0, %0\n\ts_nop 0\n\tbuffer_load_dwordx4 %1, %2, 0 offen sc1 lds" :: "s"(lds0 + L_G + hw * 1024), "v"(go), "s"(rw) : "memory");
    DW4(); SPFX(STOUCH3(pw)); pw += 64 * F_ * 2; HBAR(9);
    rwt = mkrs(w1, D_ * F_ * 2); rs = F_ * 2; pad = 8; wso = ct * 192 + (256 + 16 * hw) * rs; wv = wc < 12 ? kq * rs + (wc << 4) : OOB;
    xso -= 1280; nx = lds0 + hw * 4096 + 4 * XS; nw = lds0 + L_W + hw * 4096 + WSL;
    DW4();
    tag = uni64(*(LAS u64*)(lds + L_C));
    {
      asm volatile("s_waitcnt vmcnt(8)" ::: "memory");
      u32x4 gr = *(LAS u32x4*)(lds + L_G + hw * 1024 + lane * 16);
#pragma unroll 1
      for (int n = 0; n < (1 << 20); ++n) {
        if (__all(gr[1] == (unsigned)tag && gr[3] == (unsigned)tag)) break;
        asm volatile("buffer_load_dwordx4 %0, %1, %2, 0 offen sc1\n\ts_waitcnt vmcnt(0)" : "=&v"(gr) : "v"(go), "s"(rw) : "memory");
      }
      st_r[hw * 64 + lane] = __uint_as_float(hw < 2 ? gr[0] : gr[2]);
    }
    HBAR(4);
    DW4(); asm volatile("s_waitcnt lgkmcnt(0)" ::: "memory"); SPFX(STOUCH3(pw)); HBAR(4);
    DW4();
    {
      const int c0 = pr * 5 + 2 * odd, e = (lane >> 3) & 1;
      const int tqk = (c0 + 2 * e) >> 4, tv = (head * 20 + 19 * e) >> 7;
      fi = F_Q + (lane < 16 ? 5 + tqk : 10 + tv) * 16 + 8 * kh + (lane & 7);
      if (lane >= 32) fi = F_Q + tqk * 16 + 2 * qc + qh;
      kv_offsets(lane);
      asm volatile("" : "+v"(fi), "+v"(oK1), "+v"(oK2), "+v"(oV0), "+v"(oV1), "+v"(oQ2));
    }
    HBAR(4);
    DW4(); DX4(); HBAR(8);
    DW4(); DX4(); HBAR(12);
    DW4(); DX4(); wso -= 640 * rs; HBAR(12);
    DW4(); DX4(); HBAR(12);
    DW4(); HBAR(8);
    DW4(); nw = lds0 + 3 * XS + hw * 4096; DW4(); HBAR(8);
    asm volatile("s_mov_b32 m0, %0\n\ts_nop 0\n\tbuffer_load_dwordx4 %1, %2, 0 offen sc1 lds" :: "s"(lds0 + L_G + hw * 1024), "v"(fi * 128), "s"(mkrs(ws + WS_F, 1u << 17)) : "memory");
  } else {
    const int wm = w & 1, wn = w >> 1;
    const int row = rt * 128 + ct * 8 + 2 * w + (lane >> 5);
    {
      const unsigned lb = lds0 + 5 * XS + w * 4096, xo = row * (D_ * 2) + r * 16;
      dma16(rx, xo, 0, lb); dma16(rx, xo, 512, lb + 1024); dma16(rx, r < 16 ? xo : OOB, 1024, lb + 2048);
      asm volatile("s_mov_b32 m0, %0\n\ts_nop 0\n\tbuffer_load_dword %1, %2, 0 offen sc1 lds" :: "s"(lb + 3072), "v"(lane * 4), "s"(mkrs(ws + WS_F, 8)) : "memory");
      u64 pc; asm volatile("s_getpc_b64 %0" : "=s"(pc));
      STOUCH4((pc & ~(u64)255) + CODE_SKIP + (idx8 * 4 + w) * 256u);
      *(LAS u32x4*)(lds + L_M + threadIdx.x * 16) = u32x4{0x7fc00000u, 0x7fc00000u, 0x7fc00000u, 0x7fc00000u};
    }
    unsigned vo[4];
    if (vtype) {
#pragma unroll
      for (int i = 0; i < 4; ++i) {
        const int fv = (ct - 10) * 128 + 32 * G1<1>::span(w, i >> 1) + 16 * (i & 1) + c16, hd = fv / 20, d = fv - hd * 20;
        vo[i] = (WS_T + ((hd >> 1) * 64 + 4 * rt + 2 * wm) * PT + P_V + (hd & 1) * 1280 + kq * 320 + d * 16) | (ct < 15 ? 0u : OOB);
      }
    } else {
#pragma unroll
      for (int pp = 0; pp < 2; ++pp) {
        const int c = (ct < 5 ? ct : ct - 5) * 16 + 4 * G1<0>::span(w, pp) + kq, p5 = c / 5, pos = c - p5 * 5;
        const unsigned tb = WS_T + (p5 * 64 + 4 * rt + 2 * wm) * PT;
        vo[pp] = tb + pos * 512 + (ct < 5 ? P_Q : 0u) + c16 * 16;
        vo[2 + pp] = (ct >= 5 && pos == 2) ? tb + P_ONE + (c16 >> 3) * PT + (c16 & 7) * 16 : OOB;
      }
    }
    asm volatile("" : "+v"(vo[0]), "+v"(vo[1]), "+v"(vo[2]), "+v"(vo[3]));
    if (w < 2) touch(w ? mkrs(b1 + ct * 96, 192) : mkrs(wqkv + 94 * 3 * D_, 546 * 3 * D_ * 2), w ? lane * 4 : (b * 64 + lane) * 128, lds0 + (w ? L_ST + 1536 : L_PF));
    u32x4 xr[3]; u64 e0;
    auto stats_ld = [&]() {
      if (w < 2) asm volatile("s_waitcnt vmcnt(1)" ::: "memory"); else DRAIN();
#pragma unroll
      for (int i = 0; i < 3; ++i) xr[i] = *(LAS u32x4*)(lds + 5 * XS + w * 4096 + i * 1024 + lane * 16);
      e0 = *(LAS u64*)(lds + 5 * XS + w * 4096 + 3072);
    };
    auto stats = [&]() {
      float p = 0.f, q = 0.f;
#pragma unroll
      for (int i = 0; i < 12; ++i) {
        const unsigned u2 = xr[i >> 2][i & 3];
        const bf16x2 v2 = __builtin_bit_cast(bf16x2, u2);
        p = __builtin_amdgcn_fdot2_f32_bf16(v2, __builtin_bit_cast(bf16x2, 0x3f803f80u), p, false); q = __builtin_amdgcn_fdot2_f32_bf16(v2, v2, q, false);
      }
      p = dppadd<0x140>(dppadd<0x141>(dppadd<0x4E>(dppadd<0xB1>(p)))); q = dppadd<0x140>(dppadd<0x141>(dppadd<0x4E>(dppadd<0xB1>(q))));
      p = dppadd<0x142>(p); q = dppadd<0x142>(q);
      const float mu = p * (1.f / D_), rstd = __builtin_amdgcn_rsqf(fmaxf(q * (1.f / D_) - mu * mu, 0.f) + 1e-5f);
      tag = uni64(e0 + 1);
      if (threadIdx.x == 0) { *(LAS u64*)(lds + L_C) = tag; *(LAS u32x4*)(lds + L_C + 16) = u32x4{0, 0, 0, 0}; *(LAS u32x4*)(lds + L_C + 32) = u32x4{0, 0, 0, 0}; *(LAS u32x4*)(lds + L_C + 48) = u32x4{0, 0, 0, 0}; }
      st16(u32x4{__float_as_uint(rstd), (unsigned)tag, __float_as_uint(-mu * rstd), (unsigned)tag}, rw, r == 16 ? row * 16 : OOB, WS_S);
    };
    G1<2>::Frag f2; G1<2>::Ptr p2;
    const G1<2>::Off o2 = G1<2>::offs(w, lane);
    G1<2>::ptrx<0>(p2, o2, lds, 3 * XS); G1<2>::ptrx<1>(p2, o2, lds, 3 * XS); G1<2>::ptrw(p2, o2, lds, 0);
    {
      f32x4 acc[4][4];
      if (vtype) {
        f32x4 r4[4], t4[4]; float c1[4];
        G1<1>::Frag f1;
        LAS float* pr4 = st_r + 64 * wm + 8 * kq; LAS float* pc0 = st_c + 32 * G1<1>::span(w, 0) + c16; LAS float* pc1 = st_c + 32 * G1<1>::span(w, 1) + c16;
        asm volatile("" : "+v"(pr4), "+v"(pc0), "+v"(pc1));
        G1<1>::loop(acc, f1, G1<1>::offs(w, lane), lds, 0, 0, w, lane, -1, fl, 0, tag, [&](auto k) {
          constexpr int K = decltype(k)::v;
          if constexpr (K < 0) { stats_ld(); stats(); }
          else if constexpr (K < 4) { r4[K] = *(LAS f32x4*)(pr4 + 32 * (K >> 1) + 4 * (K & 1)); t4[K] = *(LAS f32x4*)(pr4 + 128 + 32 * (K >> 1) + 4 * (K & 1)); }
          else if constexpr (K < 6) { c1[2 * K - 8] = (K == 4 ? pc0 : pc1)[0]; c1[2 * K - 7] = (K == 4 ? pc0 : pc1)[16]; }
          else G1<2>::rds<2 * K - 12, 2 * K - 10, XS, WSL>(f2, p2);
        }, stv);
        STAMP(2);
#pragma unroll
        for (int i = 0; i < 4; ++i)
#pragma unroll
          for (int a = 0; a < 2; ++a)
            st16(pk8(acc[2 * a][i] * r4[2 * a] + t4[2 * a] * c1[i], acc[2 * a + 1][i] * r4[2 * a + 1] + t4[2 * a + 1] * c1[i]), rw, vo[i], a * PT);
      } else {
        float r1[4], t1[4]; f32x4 c4[4];
        G1<0>::Frag f0;
        LAS float* pr1 = st_r + 64 * wm + c16; LAS float* pc0 = st_c + 32 * G1<0>::span(w, 0) + 8 * kq; LAS float* pc1 = st_c + 32 * G1<0>::span(w, 1) + 8 * kq;
        asm volatile("" : "+v"(pr1), "+v"(pc0), "+v"(pc1));
        G1<0>::loop(acc, f0, G1<0>::offs(w, lane), lds, 0, 0, w, lane, -1, fl, 0, tag, [&](auto k) {
          constexpr int K = decltype(k)::v;
          if constexpr (K < 0) { stats_ld(); stats(); }
          else if constexpr (K < 4) { r1[K] = pr1[16 * K]; t1[K] = pr1[128 + 16 * K]; }
          else if constexpr (K < 6) { c4[2 * K - 8] = *(LAS f32x4*)(K == 4 ? pc0 : pc1); c4[2 * K - 7] = *(LAS f32x4*)((K == 4 ? pc0 : pc1) + 4); }
          else G1<2>::rds<2 * K - 12, 2 * K - 10, XS, WSL>(f2, p2);
        }, stv);
        STAMP(2);
        const float sc = ct < 5 ? QSCALE : 1.f;
#pragma unroll
        for (int pp = 0; pp < 2; ++pp)
#pragma unroll
          for (int m = 0; m < 4; ++m)
            st16(pk8(acc[m][2 * pp] * (r1[m] * sc) + c4[2 * pp] * (t1[m] * sc), acc[m][2 * pp + 1] * (r1[m] * sc) + c4[2 * pp + 1] * (t1[m] * sc)), rw,
                 vo[pp], (m & 1) * 256 + (m >> 1) * PT);
        st16(u32x4{0x3f803f80u, 0x3f803f80u, 0x3f803f80u, 0x3f803f80u}, rw, min(vo[2], vo[3]), 0);
      }
    }
    STAMP(3);
    f32x4 acc[2][6];
    float r1[2], t1[2]; f32x4 c4[6]; u32x4 bb[3];
    LAS float* pr1 = st_r + 32 * w + c16;
    G1<2>::loop2(acc, f2, o2, p2, lds, w, lane, [&]() { if (w == 0) publish(fl, F_Q + ct * 16 + rt, tag, lane); }, [&](auto k) {
      constexpr int K = decltype(k)::v;
      if constexpr (K < 2) { r1[K] = pr1[16 * K]; t1[K] = pr1[128 + 16 * K]; }
      else if constexpr (K < 5) bb[K - 2] = *(LAS u32x4*)(st_c + 128 + 16 * G1<2>::span(w, K - 2) + 4 * kq);
    });
    STAMP(4);
    __builtin_amdgcn_s_setprio(2);
    lds_meet<2>((LAS unsigned*)(lds + L_C + 20), 4, lane);
#pragma unroll
    for (int pp = 0; pp < 3; ++pp) { LAS float* pc = st_c + 32 * G1<2>::span(w, pp) + 8 * kq; c4[2 * pp] = *(LAS f32x4*)pc; c4[2 * pp + 1] = *(LAS f32x4*)(pc + 4); }
    const bool ret = w < 2 || (w == 2 && idx8 < 4);
    const int slab = w < 2 ? 2 * idx8 + w : 64 + idx8;
    u32x4 hq[6];
#pragma unroll
    for (int pp = 0; pp < 3; ++pp) {
      const f32x4 ba = {blo(bb[pp][0]), bhi(bb[pp][0]), blo(bb[pp][1]), bhi(bb[pp][1])}, bc = {blo(bb[pp][2]), bhi(bb[pp][2]), blo(bb[pp][3]), bhi(bb[pp][3])};
#pragma unroll
      for (int m = 0; m < 2; ++m) {
        const u32x2 lo = sqrelu4(acc[m][2 * pp], c4[2 * pp], ba, r1[m], t1[m]), hi = sqrelu4(acc[m][2 * pp + 1], c4[2 * pp + 1], bc, r1[m], t1[m]);
        hq[2 * pp + m] = u32x4{lo[0], lo[1], hi[0], hi[1]};
      }
    }
    asm volatile("" : "+v"(hq[0]), "+v"(hq[1]), "+v"(hq[2]), "+v"(hq[3]), "+v"(hq[4]), "+v"(hq[5]));
#define HST(i0, i1) do { _Pragma("unroll") for (int i = i0; i < i1; ++i) { st16(hq[i], rw, lane * 16, WS_H + ((8 * rt + 2 * w + (i & 1)) * 48 + 3 * ct + G1<2>::span(w, i >> 1)) * 1024); SB(); __builtin_amdgcn_s_sleep(4); SB(); } } while (0)
    FS(13); HST(0, 6); FS(14);
    if (ret) {
      const int s2 = slab - 48;
      const rsrc_t rr = mkrs((slab < 48 ? w2 : wo) + 80 * g, ((slab < 48 ? F_ : D_) * D_ - 80 * g) * 2);
      const unsigned r0 = slab < 48 ? slab * 32 : (s2 < 16 ? s2 * 40 : (s2 - 16) * 160 + 16), rsh = s2 < 16 ? 4 : 2, rmu = slab < 48 ? 0 : (s2 < 16 ? 4 : 16);
#pragma unroll
      for (int i = 0; i < 5; ++i) {
        const unsigned c = 64 * i + lane, k = (c * 205) >> 11;
        dma16(rr, (k + (k >> rsh) * rmu) * (D_ * 2) + (c - k * 10) * 16, r0 * (D_ * 2), lds0 + L_RED + w * 5120 + i * 1024);
      }
      const unsigned tro = (8 * kq + ((lane >> 2) & 3)) * 160 + (lane & 3) * 16, tr4 = tro - (lane & 3) * 8 + 128;
      DRAIN(); FS(15);
#pragma unroll
      for (int t = 0; t < 5; ++t) {
        LAS char* p = lds + L_RED + w * 5120 + (t < 4 ? tro + (t >> 1) * 64 + (t & 1) * 8 : tr4);
        st16(__builtin_bit_cast(u32x4, cat8(trd(p), trd(p + 640))), rw, lane * 16 + WS_W + (g * 68 + slab) * 5120, t * 1024); SB();
      }
    }
    STAMP(5);
  }

  if (w >= 4) {
    const rsrc_t rk = mkrs(ws + WS_T + (pr * 64 + kh * 32) * PT, 32 * PT);
    unsigned qo[4];
#pragma unroll
    for (int t = 0; t < 4; ++t) { qo[t] = WS_T + (pr * 64 + 8 * qc + 4 * qh + (t ^ (2 * kh))) * PT + P_Q; asm volatile("" : "+s"(qo[t])); }
    asm volatile("" :: "s"(rk));
    HBAR(1);
    DRAIN();
    if (!__all(*(volatile LAS u64*)(lds + L_G + aw * 1024 + (oK1 & 1023)) == tag)) await<2>(fl, fi, tag);
    __builtin_amdgcn_fence(__ATOMIC_ACQUIRE, "wavefront");
    STAMP(6);
    bool stored = false;
    auto sweep = [&](auto pass_) -> unsigned {
      constexpr int PASS = decltype(pass_)::v;
      const int lane = fresh_lane(), r = lane & 31, h = lane >> 5, c16 = lane & 15;
      if constexpr (PASS) kv_offsets(lane);
      bf16x8 q1[4]; s16x4 q2[4];
      KV b0, b1_, b2_, b3;
      unsigned so = 0;
#define LDK(B, d) do { B.k1 = ld16(rk, oK1, so + (d) * PT); SB(); B.k2 = ld8(rk, oK2, so + (d) * PT); SB(); } while (0)
#define LDV(B, d) do { B.v0 = ld16(rk, oV0, so + (d) * PT); SB(); B.v1 = ld16(rk, oV1, so + (d) * PT); SB(); } while (0)
      SB();
      LDK(b1_, 1);
#pragma unroll
      for (int t = 0; t < 4; ++t) { q1[t] = ld16(rw, oK1, qo[t]); SB(); q2[t] = ld8(rw, oQ2, qo[t]); SB(); }
      LDK(b0, 0);
      float mx[4] = {-3.0e38f, -3.0e38f, -3.0e38f, -3.0e38f};
      if constexpr (PASS) {
#pragma unroll 1
        for (int j = 2; j < 32; ++j) {
          const bf16x8 k1 = ld16(rk, oK1, j * PT); const s16x4 k2 = ld8(rk, oK2, j * PT);
#pragma unroll
          for (int t = 0; t < 4; ++t) {
            const f32x16 sv = qk_b(k2, q2[t], qk_a(k1, q1[t]));
#pragma unroll
            for (int e = 0; e < 16; ++e) mx[t] = fmaxf(mx[t], sv[e]);
          }
        }
      }
      int idx = 0xDDDD;
      f32x16 o0 = {}, o1 = {}, o2 = {}, o3 = {}, s0, s1, s2, s3;
      b3.v0 = bf16x8{}; b3.v1 = bf16x8{};
      LAS char* sp = lds + L_MRG + aw * 6144 + lane * 16; LAS char* gp = lds + L_MRG + (aw ^ 2) * 6144 + lane * 16;
      const int tt = (8 * qc + 4 * qh + (2 ^ (2 * kh))) * 2 + (r >> 4);
      unsigned oa = WS_O + (tt * 16 + pr) * 1024 + ((2 * odd + h) * 16 + c16) * 16, ob = h ? OOB : WS_O2 + (tt * 32 + head) * 128 + c16 * 8;
      volatile LAS f32x4* mw = (volatile LAS f32x4*)(lds + L_M) + aw * 64 + lane; volatile LAS f32x4* mr = (volatile LAS f32x4*)(lds + L_M) + (aw ^ 2) * 64 + r;
      asm volatile("" : "+v"(o0), "+v"(o1), "+v"(o2), "+v"(o3), "+v"(b3.v0), "+v"(b3.v1), "+v"(idx), "+v"(sp), "+v"(gp), "+v"(oa), "+v"(ob), "+v"(mw), "+v"(mr)); SB();
      {
        f32x16 sm[8];
#define SQ(n) ((n) < 4 ? (n) : ((n) - 2) & 3)
#define SMA(n) do { sm[n] = qk_a((n) < 4 ? b1_.k1 : b0.k1, q1[SQ(n)]); if ((n) == 6) s0 = sm[n]; if ((n) == 7) s1 = sm[n]; SB(); } while (0)
#define SMB(n) do { sm[n] = qk_b((n) < 4 ? b1_.k2 : b0.k2, q2[SQ(n)], sm[n]); SB(); } while (0)
        SMA(0); SMB(0); SMA(1); SMB(1);
        FS(1);
        LDV(b0, 0); LDV(b1_, 1); LDK(b2_, 2); LDV(b2_, 2); LDK(b3, 3);
        FS(2);
#pragma unroll
        for (int n = 0; n < 8; ++n) {
          if (n < 6) SMA(n + 2);
          max8<0>(sm[n], mx[SQ(n)]); SB();
          if (n < 6) SMB(n + 2);
          max8<8>(sm[n], mx[SQ(n)]); SB();
        }
      }
      FS(3);
      *mw = f32x4{mx[0], mx[1], mx[2], mx[3]};
      if constexpr (PASS) lds_meet<0>((LAS unsigned*)(lds + L_C + 44), 4, lane);
#pragma unroll
      for (int t = 0; t < 4; ++t) {
        auto sw = __builtin_amdgcn_permlane32_swap(__float_as_uint(mx[t]), __float_as_uint(mx[t]), false, false);
        asm volatile("v_max_f32 %0, %1, %2" : "=v"(mx[t]) : "v"(sw[0]), "v"(sw[1]));
      }
      s3 = o3; asm volatile("" : "+v"(s3));
      f32x4 ma, mb;
#pragma unroll 1
      for (int n = 0; n < (1 << 22); ++n) {
        ma = mr[0]; mb = mr[32];
        if (PASS || !__any(__builtin_isunordered(ma[0], mb[0]) | __builtin_isunordered(ma[1], mb[1]) | __builtin_isunordered(ma[2], mb[2]) | __builtin_isunordered(ma[3], mb[3]))) break;
      }
      FS(4);
#pragma unroll
      for (int t = 0; t < 4; ++t) {
        float nm;
        asm("v_max3_f32 %0, %1, %2, %3\n\tv_sub_f32 %0, %4, %0" : "=&v"(nm) : "v"(mx[t]), "v"(ma[t ^ 2]), "v"(mb[t ^ 2]), "s"(PASS ? 0.f : -MARGIN));
        u32x2 pm = __builtin_bit_cast(u32x2, q2[t]);
        if constexpr (PASS) {
          const float a0 = bhi(__float_as_uint(nm)), r0 = nm - a0, a1 = bhi(__float_as_uint(r0)), a2 = r0 - a1;
          if (h) pm = u32x2{(__float_as_uint(a0) >> 16) | __float_as_uint(a1), __float_as_uint(a2) >> 16};
        } else if (h) pm[0] = __float_as_uint(nm) >> 16;
        q2[t] = __builtin_bit_cast(s16x4, pm);
        if (t == 0) s0 = qk_b(b0.k2, q2[0], s0);
        if (t == 1) s1 = qk_b(b0.k2, q2[1], s1);
        SB();
      }
      if constexpr (!PASS) { STAMP(7); SB(); }
#define KT(C, P, N) do { \
  STEPX(s0, s3, o3, P, s2, C, q1[2], q2[2], P.v0 = ld16(rk, oV0, so), __builtin_amdgcn_s_waitcnt(0x0F79)); \
  STEPX(s1, s0, o0, C, s3, C, q1[3], q2[3], P.v1 = ld16(rk, oV1, so), so += PT; asm volatile("" : "+s"(so))); \
  STEPX(s2, s1, o1, C, s0, N, q1[0], q2[0], C.k1 = ld16(rk, oK1, so), ); \
  STEPX(s3, s2, o2, C, s1, N, q1[1], q2[1], C.k2 = ld8(rk, oK2, so), ); } while (0)
      so = 3 * PT;
#pragma unroll 1
      do { KT(b0, b3, b1_); KT(b1_, b0, b2_); KT(b2_, b1_, b3); KT(b3, b2_, b0); } while (so != 31 * PT);
#define KTE(C, P, N, W) do { STEPX(s0, s3, o3, P, s2, C, q1[2], q2[2], , __builtin_amdgcn_s_waitcnt(W)); STEP(s1, s0, o0, C, s3, C, q1[3], q2[3]); \
  STEP(s2, s1, o1, C, s0, N, q1[0], q2[0]); STEP(s3, s2, o2, C, s1, N, q1[1], q2[1]); } while (0)
#define SEND(O, z, e) do { *(LAS f32x4*)(sp + (z) * 3072 + (e) * 1024) = f32x4{O[4 * (e)], O[4 * (e) + 1], O[4 * (e) + 2], O[4 * (e) + 3]}; SB(); } while (0)
      STEPX(s0, s3, o3, b3, s2, b0, q1[2], q2[2], b3.v0 = ld16(rk, oV0, so), __builtin_amdgcn_s_waitcnt(0x0F79));
      STEPX(s1, s0, o0, b0, s3, b0, q1[3], q2[3], b3.v1 = ld16(rk, oV1, so), ); STEP(s2, s1, o1, b0, s0, b1_, q1[0], q2[0]); STEP(s3, s2, o2, b0, s1, b1_, q1[1], q2[1]);
      KTE(b1_, b0, b2_, 0x0F76); KTE(b2_, b1_, b3, 0x0F72);
      STEPX(s0, s3, o3, b2_, s2, b3, q1[2], q2[2], , __builtin_amdgcn_s_waitcnt(0x0F70)); STEP(s1, s0, o0, b3, s3, b3, q1[3], q2[3]);
      LAS unsigned* mc = (LAS unsigned*)(lds + L_C + 32 + 4 * qh); unsigned one = 1u;
      EX4(s2, 0); SB(); o1 = pv_lo(b3.v0, s1, o1, idx); SB(); asm volatile("" : "+v"(mc), "+v"(one)); EX4(s2, 4); SB(); o1 = pv_hi(b3.v1, s1, o1, idx); SB(); SEND(o0, 0, 0);
      EX4(s2, 8); SB(); o2 = pv_lo(b3.v0, s2, o2, idx); SB(); SEND(o0, 0, 1); EX4(s2, 12); SB(); SEND(o0, 0, 2);
      EX4(s3, 0); SB(); o2 = pv_hi(b3.v1, s2, o2, idx); SB(); SEND(o1, 1, 0); EX4(s3, 4); SB(); SEND(o1, 1, 1); SEND(o1, 1, 2);
      lds_add(mc, one); SB();
      unsigned cv; f32x4 gv[6];
#define GET() do { cv = *(volatile LAS unsigned*)mc; _Pragma("unroll") for (int e = 0; e < 6; ++e) gv[e / 3 * 3 + (e + 2) % 3] = *(volatile LAS f32x4*)(gp + (e / 3 * 3 + (e + 2) % 3) * 1024); } while (0)
      o3 = pv_lo(b3.v0, s3, o3, idx); SB(); EX4(s3, 8); SB(); EX4(s3, 12); SB(); GET(); SB(); o3 = pv_hi(b3.v1, s3, o3, idx); SB();
      asm volatile("" : "+v"(o2), "+v"(o3) : "v"(o0), "v"(o1));
      if constexpr (!PASS) { STAMP(8); SB(); }
      if (__builtin_amdgcn_readfirstlane(cv) < 1024 + 2 + 2 * PASS) {
#pragma unroll 1
        for (int n = 0; n < (1 << 22); ++n) { if (__builtin_amdgcn_readfirstlane(*(volatile LAS unsigned*)mc) >= 1024 + 2 + 2 * PASS) break; }
        GET();
      }
      u32x4 pa[2]; u32x2 pb_[2]; float chk = 0.f, l[2];
#pragma unroll
      for (int z = 0; z < 2; ++z) {
        const f32x16 k = z ? o3 : o2;
        const F4 u2 = add4(gv[3 * z + 2], k[8], k[9], k[10], k[11], chk);
        l[z] = __uint_as_float(__builtin_amdgcn_permlane32_swap(__float_as_uint(u2.v[0]), __float_as_uint(u2.v[0]), false, false)[1]);
        float inv = __builtin_amdgcn_rcpf(l[z]);
        asm volatile("" : "+v"(inv));
        const F4 u0 = add4(gv[3 * z], k[0], k[1], k[2], k[3], chk), u1 = add4(gv[3 * z + 1], k[4], k[5], k[6], k[7], chk);
        const u32x2 x0 = scale4(u0, inv), x1 = scale4(u1, inv);
        pa[z] = u32x4{x0[0], x0[1], x1[0], x1[1]}; pb_[z] = scale4(u2, inv);
      }
      asm volatile("" :: "v"(o3));
      unsigned bad = 1u;
      if constexpr (!PASS) { stored = __all(chk < 1.0e37f && l[0] > 1.0e-30f && l[1] > 1.0e-30f); if (!stored) bad = 0x101u; }
      else {
#pragma unroll
        for (int z = 0; z < 2; ++z) {
#pragma unroll
          for (int j = 0; j < 4; ++j) pa[z][j] = min(pa[z][j], 0xfffffffeu);
          pb_[z][0] = min(pb_[z][0], 0xfffffffeu);
        }
      }
      if (PASS ? !stored : stored) { st16(pa[0], rw, oa, 0); st8(pb_[0], rw, ob, 0); st16(pa[1], rw, oa, 32768); st8(pb_[1], rw, ob, 8192); }
      unsigned cn = lds_add_rtn((LAS unsigned*)(lds + L_C + 24), bad) + bad;
      if constexpr (!PASS) {
        STAMP(9);
#pragma unroll 1
        for (int n = 0; n < (1 << 22); ++n) { if ((cn & 255) >= 4) break; cn = __builtin_amdgcn_readfirstlane(*(volatile LAS unsigned*)(lds + L_C + 24)); }
        asm volatile("" ::: "memory");
      }
      return cn;
    };
    if (__builtin_expect(sweep(IC<0>{}) >= 256, 0)) sweep(IC<1>{});
    const int lane = fresh_lane();
    if (b == 0) {
      await<8>(fl, F_O + aw * 64 + lane, tag);
      lds_meet<2>((LAS unsigned*)(lds + L_C + 28), 4, lane);
      if (w == 4) publish(fl, 0, tag, lane);
    }
  } else {
    const int rt2 = 4 * g + (idx8 & 3), ct2 = idx8 >> 2;
    {
      DRAIN();
      if (lds_add_rtn((LAS unsigned*)(lds + L_C + 16), 1u) == 3) publish(fl, F_H + b, tag, lane);
      const int j = lane & 15, hc = 4 * w + (j & 3), hr = rt2 >> 1;
      const int pb = (j < 4 || j > 12) ? (hr >> 2) + 4 * (hc >> 3) + 8 * ((hr & 3) + 4 * (hc & 7)) : 8 * (j < 10 ? 6 * w + j - 4 : (j < 12 ? 14 + 2 * w + j : w)) + ct2;
#pragma unroll 1
      for (int n = 0; n < (1 << 22); ++n) { if (__builtin_amdgcn_readfirstlane(*(volatile LAS unsigned*)(lds + L_C + 16)) >= 4) break; __builtin_amdgcn_s_sleep(4); }
      await<10>(fl, F_H + pb, tag);
    }
    STAMP(6);
    const int o = (64 * rt2 + 16 * w + c16) * D_ + 80 * ct2;
    u32x4 xv[2], bv[2]; u32x2 xv4, bv4;
    {
      f32x4 acc[4][5];
      bf16x8 f0[11], f1[11];
      unsigned vo = lane * 16, vo4 = vo + 4096, va[4];
#pragma unroll
      for (int m = 0; m < 4; ++m) va[m] = vo + (m ^ w) * 49152;
      unsigned sa = WS_H + (4 * rt2 * 48 + 12 * w) * 1024, sw = WS_W + (ct2 * 68 + 12 * w) * 5120, sq = WS_W + (ct2 * 68 + 48 + 4 * w) * 5120;
      const unsigned sq0 = sq + 20480, sq1 = WS_W + (ct2 * 68 + 64 + w) * 5120;
      LAS char* wl = lds + L_WO + lane * 16; LAS char* wst = wl + ((w - 1 - (g >> 1)) & 3) * 25600;
      asm volatile("" : "+v"(vo), "+v"(vo4), "+v"(va[0]), "+v"(va[1]), "+v"(va[2]), "+v"(va[3]), "+v"(wl), "+v"(wst));
#define GL1(F, j) F[j] = (j) < 4 ? ld16(rw, va[(j) & 3], sa) : ((j) < 8 ? ld16(rw, vo + (((j) - 4) & 3) * 1024, sw) : ((j) < 9 ? ld16(rw, vo4, sw) : ld16(rw, vo + ((j) - 9) * 1024, sq)))
#define GSTEP(FC, FN, WO, LD, Z) do { _Pragma("unroll") for (int n = 0; n < 20; ++n) { \
        acc[n & 3][n >> 2] = __builtin_amdgcn_mfma_f32_16x16x32_bf16(FC[4 + (n >> 2)], FC[n & 3], (Z) ? f32x4{} : acc[n & 3][n >> 2], 0, 0, 0); \
        SB(); \
        if ((LD) && n > 2 && (n & 1) && n < 17) { asm volatile("s_nop 4"); SB(); }         \
        if ((LD) && n < 2) { GL1(FN, n); } else if ((LD) && !(n & 1)) { GL1(FN, (n >> 1) + 1); } \
        if ((LD) && n == 5) { sa += 1024; asm volatile("" : "+s"(sa)); } \
        if ((LD) && n == 15) { sw += 5120; asm volatile("" : "+s"(sw)); } \
        if ((LD) && n == 3) { sq += 2048; if (sq == sq0) sq = sq1; asm volatile("" : "+s"(sq)); } \
        if (n == 17 || n == 19) *(LAS bf16x8*)(wst + (WO) + (n == 19 ? 1024 : 0)) = FC[n == 19 ? 10 : 9]; \
        SB(); } } while (0)
#pragma unroll
      for (int j = 0; j < 11; ++j) { GL1(f0, j); }
      const bf16x8 wlast = ld16(rw, vo4, sq1);
      sa += 1024; sw += 5120;
      SB();
      {
        const int tt = (8 * qc + 4 * (w & 1) + (w & 2)) * 2 + (r >> 4);
        st16(u32x4{~0u, ~0u, ~0u, ~0u}, rw, WS_O + (tt * 16 + pr) * 1024 + ((2 * odd + h) * 16 + c16) * 16, 0);
        st16(u32x4{~0u, ~0u, ~0u, ~0u}, rw, WS_O + (tt * 16 + pr) * 1024 + ((2 * odd + h) * 16 + c16) * 16, 32768);
        st8(u32x2{~0u, ~0u}, rw, WS_O2 + ((tt + 2 * h) * 32 + head) * 128 + c16 * 8, 0);
        __builtin_amdgcn_s_setprio(0); SB();
      }
      GSTEP(f0, f1, 0, true, true);
      __builtin_amdgcn_s_setprio(2);
      DRAIN();
      *(LAS bf16x8*)(wst + 24576) = wlast;
      unsigned mo = (F_O + g * 32 + r) * 128 + h * 4, xo = o * 2 + 16 * kq, xo4 = o * 2 + 128 + 8 * kq;
      LAS char* r0 = lds + L_RED + w * 5120 + lane * 16;
      asm volatile("" : "+v"(mo), "+v"(xo), "+v"(xo4), "+v"(r0));
      lds_add((LAS unsigned*)(lds + L_C + 32), 256u);
      if ((lds_add_rtn((LAS unsigned*)(lds + L_C + 36), 256u) >> 8) == 3) publish(fl, F_O + qc * 32 + head, tag, lane);
      __builtin_amdgcn_s_setprio(0); SB();
#pragma unroll 1
      for (int s = 1; s < 11; s += 2) {
        if (s == 7) {
          const unsigned lb = lds0 + L_RED + w * 5120;
          asm volatile("s_mov_b32 m0, %0\n\ts_nop 0\n\tbuffer_load_dword %1, %2, 0 offen sc1 lds" :: "s"(lb + 4096), "v"(mo), "s"(mkrs(ws + WS_F, 1u << 17)) : "memory");
          dma16(rx, xo, 0, lb); dma16(rx, xo, 64, lb + 1024); dma16(rx, xo4, 0, lb + 2048); dma16(mkrs(b2 + 80 * ct2, 160), vo, 0, lb + 3072);
        }
        GSTEP(f1, f0, 2048, true, false); GSTEP(f0, f1, 4096, true, false);
        __builtin_amdgcn_s_setprio(2); SB(); wst += 4096; asm volatile("" : "+v"(wst)); SB(); __builtin_amdgcn_s_setprio(0);
      }
      unsigned oo[4], oo2[4];
#pragma unroll
      for (int j = 0; j < 4; ++j) {
        const int gr = (j + 1 + (g >> 1)) & 3;
        oo[j] = WS_O + (4 * rt2 + w) * 16384 + gr * 4096; oo2[j] = WS_O2 + (4 * rt2 + w) * 4096 + gr * 1024; asm volatile("" : "+s"(oo[j]), "+s"(oo2[j]));
      }
      xv[0] = *(volatile LAS u32x4*)r0; xv[1] = *(volatile LAS u32x4*)(r0 + 1024); xv4 = *(volatile LAS u32x2*)(r0 + 2048);
      SB();
      GSTEP(f1, f0, 2048, false, false);
      __builtin_amdgcn_s_setprio(2);
      STAMP(7);
      const int ln = fresh_lane();
      const unsigned gate = *(volatile LAS unsigned*)(lds + L_C + 24), mk = *(volatile LAS unsigned*)(lds + L_RED + w * 5120 + 4096 + ln * 4);
      LAS char* bp = lds + L_RED + w * 5120 + 3072 + (ln & 48);
      bv[0] = *(volatile LAS u32x4*)bp; bv[1] = *(volatile LAS u32x4*)(bp + 64); bv4 = *(volatile LAS u32x2*)(bp + 128 - ((ln & 48) >> 1));
      const unsigned v8 = (ln >> 4) * 256 + (ln & 15) * 8;
      volatile LAS unsigned* gc = (volatile LAS unsigned*)(lds + L_C + 48);
      asm volatile("" ::: "memory");
#pragma unroll
      for (int t = 0; t < 5; ++t) *(LAS f32x4*)(r0 + t * 1024) = acc[3][t];
      asm volatile("" ::: "memory"); gc[w] = 1;
      if (!__all(mk == (unsigned)(ln >= 32 ? tag >> 32 : tag))) await<2>(fl, F_O + g * 32 + (ln & 31), tag);
#define SPIN(COND) do { _Pragma("unroll 1") for (int n_ = 0; n_ < (1 << 24); ++n_) { if (COND) break; __builtin_amdgcn_s_sleep(1); } asm volatile("" ::: "memory"); } while (0)
      if ((__builtin_amdgcn_readfirstlane(gate) & 255) < 4) SPIN((__builtin_amdgcn_readfirstlane(*(volatile LAS unsigned*)(lds + L_C + 24)) & 255) >= 4);
      STAMP(10);
      u32x4 a[16], al[4];
#define OL16(p, e, C) asm volatile("buffer_load_dwordx4 %0, %1, %2, %3 offen offset:%4 sc1" : C(a[4 * (p) + (e)]) : "v"(vo), "s"(rw), "s"(oo[p]), "n"((e) * 1024) : "memory")
#define OL8_(p, C, R0, R1, R2, R3) asm volatile("buffer_load_dwordx2 v[" #R0 ":" #R1 "], %1, %2, %3 offen sc1\n\tbuffer_load_dwordx2 v[" #R2 ":" #R3 "], %1, %2, %3 offen offset:128 sc1" \
        : C "{v[" #R0 ":" #R3 "]}"(al[p]) : "v"(v8), "s"(rw), "s"(oo2[p]) : "memory")
#define OL8(p, C) do { if ((p) == 0) OL8_(0, C, 224, 225, 226, 227); else if ((p) == 1) OL8_(1, C, 228, 229, 230, 231); \
        else if ((p) == 2) OL8_(2, C, 232, 233, 234, 235); else OL8_(3, C, 236, 237, 238, 239); } while (0)
#define OLG(p, C) do { OL16(p, 0, C "v"); OL16(p, 1, C "v"); OL16(p, 2, C "v"); OL16(p, 3, C "v"); OL8(p, C); } while (0)
      unsigned pg = gc[ln & 3];
      OLG(0, "=&"); OLG(1, "=&");
      LAS char* wp2 = lds + L_WO + 61440 + ln * 16; LAS char* r1_ = lds + L_RED + ln * 16 + (w ^ 1) * 5120; LAS char* r3 = lds + L_RED + ln * 16 + (w ^ 3) * 5120;
      asm volatile("" : "+v"(wp2));
      if (!__all(pg >= 1)) SPIN(__all(gc[ln & 3] >= 1));
      f32x4 y[5], z[5];
#pragma unroll
      for (int t = 0; t < 5; ++t) { z[t] = *(LAS f32x4*)(r3 + t * 1024); *(LAS f32x4*)(r3 + t * 1024) = acc[2][t]; }
      asm volatile("" ::: "memory"); gc[w] = 2; SB();
      constexpr int NW = 28;
      bf16x8 wr[NW];
#pragma unroll
      for (int t = 0; t < 5; ++t) {
        const unsigned x0 = t < 4 ? xv[(t >> 1) & 1][2 * (t & 1)] : xv4[0], x1 = t < 4 ? xv[(t >> 1) & 1][2 * (t & 1) + 1] : xv4[1];
        const unsigned b0 = t < 4 ? bv[(t >> 1) & 1][2 * (t & 1)] : bv4[0], b1v = t < 4 ? bv[(t >> 1) & 1][2 * (t & 1) + 1] : bv4[1];
        y[t] = (acc[0][t] + f32x4{blo(x0) + blo(b0), bhi(x0) + bhi(b0), blo(x1) + blo(b1v), bhi(x1) + bhi(b1v)}) + z[t];
        asm volatile("" : "+v"(y[t]));
      }
      pg = gc[w ^ 2];
#pragma unroll
      for (int i = 0; i < 12; ++i) wr[i] = *(LAS bf16x8*)(wl + i * 1024);
      if (__builtin_amdgcn_readfirstlane(pg) < 2) SPIN(__builtin_amdgcn_readfirstlane(gc[w ^ 2]) >= 2);
      asm volatile("" ::: "memory");
#pragma unroll
      for (int t = 0; t < 5; ++t) { z[t] = *(LAS f32x4*)(r1_ + t * 1024); *(LAS f32x4*)(r1_ + t * 1024) = acc[1][t]; }
      asm volatile("" ::: "memory"); gc[w] = 3; SB();
#pragma unroll
      for (int i = 12; i < NW; ++i) wr[i] = *(LAS bf16x8*)(wl + i * 1024);
      SB();
#define OCHK(p, N) ({ unsigned m_; asm volatile("s_waitcnt vmcnt(" #N ")" ::: "memory"); SB(); \
        asm volatile("" : "+v"(a[4 * (p)]), "+v"(a[4 * (p) + 1]), "+v"(a[4 * (p) + 2]), "+v"(a[4 * (p) + 3]), "+v"(al[p]) :: "memory"); \
        m_ = max(al[p][0], al[p][2]); _Pragma("unroll") for (int e = 0; e < 4; ++e) m_ = max(m_, max(a[4 * (p) + e][0], a[4 * (p) + e][2])); __any(m_ == ~0u); })
#define OGRP(p, N) do { \
        if (OCHK(p, N)) { \
          _Pragma("unroll 1") for (int n = 0; n < (1 << 20); ++n) { OLG(p, "+"); if ((p) < 3) OLG(((p) + 1) & 3, "+"); if (!OCHK(p, N)) break; } } \
        SB(); \
        _Pragma("unroll") for (int e = 0; e < 25; ++e) { const int i = 25 * (p) + e; \
          asm volatile("v_mfma_f32_16x16x32_bf16 %0, %1, %2, %0" : "+v"(y[i % 5]) : "v"(wr[i % NW]), "v"(e < 20 ? a[4 * (p) + (e / 5 & 3)] : al[p])); \
          if ((p) < 2 && e % 4 == 3) { if (e < 16) OL16(((p) + 2) & 3, (e / 4) & 3, "=&v"); else if (e == 19) OL8(((p) + 2) & 3, "=&"); } \
          const int j = NW + (i * 4 + 4) / 5;        \
          if (i % 5 != 4 && j < 100) wr[j % NW] = *(LAS bf16x8*)(j < 60 ? wl + j * 1024 : wp2 + (j - 60) * 1024); \
          if (i == 70) pg = gc[w ^ 1]; \
          if (i >= 90 && i < 95) zz[i - 90] = *(volatile LAS f32x4*)(r0 + (i - 90) * 1024); \
          SB(); } } while (0)
      f32x4 zz[5];
      OGRP(0, 6); OGRP(1, 6); OGRP(2, 6);
      if (__builtin_amdgcn_readfirstlane(pg) < 3) SPIN(__builtin_amdgcn_readfirstlane(gc[w ^ 1]) >= 3);
      OGRP(3, 0);
#pragma unroll
      for (int t = 0; t < 5; ++t) { z[t] += zz[t]; asm volatile("" : "+v"(z[t])); }
      asm volatile("s_nop 7" : "+v"(y[0]), "+v"(y[1]), "+v"(y[2]), "+v"(y[3]), "+v"(y[4]));
      const rsrc_t rout = mkrs(out, 2048 * D_ * 2);
      __builtin_amdgcn_raw_buffer_store_b128(pk8(y[0] + z[0], y[1] + z[1]), rout, xo, 0, 0);
      __builtin_amdgcn_raw_buffer_store_b128(pk8(y[2] + z[2], y[3] + z[3]), rout, xo + 64, 0, 0);
      const f32x4 y4 = y[4] + z[4];
      __builtin_amdgcn_raw_buffer_store_b64(u32x2{pk2(y4[0], y4[1]), pk2(y4[2], y4[3])}, rout, xo4, 0, 0);
    }
    STAMP(11);
  }
}

extern "C" void kernel_launch(void* const* d_in, const int* in_sizes, int n_in, void* d_out, int out_size, void* d_ws, size_t ws_size,
                              hipStream_t stream) {
  fused<<<256, 512, 0, stream>>>((const u16*)d_in[0], (const u16*)d_in[1], (const u16*)d_in[2], (const u16*)d_in[4], (const u16*)d_in[5],
                                 (const u16*)d_in[6], (const u16*)d_in[7], (u16*)d_out, (char*)d_ws);
}
```

```cpp
#define OP1 16
#define OP2 16
#include <hip/hip_runtime.h>
#include <hip/hip_bf16.h>

typedef __attribute__((ext_vector_type(8))) __bf16 bf16x8;
typedef __attribute__((ext_vector_type(16))) __bf16 bf16x16;
typedef __attribute__((ext_vector_type(2))) __bf16 bf16x2;
typedef __attribute__((ext_vector_type(4))) short s16x4;
typedef __attribute__((ext_vector_type(16))) float f32x16;
typedef __attribute__((ext_vector_type(8))) float f32x8;
typedef __attribute__((ext_vector_type(4))) float f32x4;
typedef __attribute__((ext_vector_type(4))) unsigned u32x4;
typedef __attribute__((ext_vector_type(2))) unsigned u32x2;
typedef unsigned short u16;
typedef unsigned long long u64;
typedef __amdgpu_buffer_rsrc_t rsrc_t;
#define LAS __attribute__((address_space(3)))
#define GAS __attribute__((address_space(1)))
#define SB() __builtin_amdgcn_sched_barrier(0)
#define RLX_AGENT __ATOMIC_RELAXED, __HIP_MEMORY_SCOPE_AGENT
#define BAR_L() do { asm volatile("" ::: "memory"); __builtin_amdgcn_s_waitcnt(0xC07F); asm volatile("s_barrier" ::: "memory"); } while (0)
#define DRAIN() asm volatile("s_waitcnt vmcnt(0)" ::: "memory")

constexpr int D_ = 640, F_ = 1536;
constexpr unsigned PT = 7808, P_V = 2560, P_Q = 5120, P_ONE = 7680;
constexpr unsigned WS_T = 0;
constexpr unsigned WS_H = 8u << 20;
constexpr unsigned WS_W = 16u << 20;
constexpr unsigned WS_O = 20u << 20;
constexpr unsigned WS_O2 = 22u << 20;
constexpr unsigned WS_S = 23u << 20;
constexpr unsigned WS_F = 24u << 20;
constexpr unsigned WS_END = WS_F;
constexpr unsigned F_Q = 256, F_H = 512, F_O = 768;
constexpr float QSCALE = 0.22360679774997896f * 1.4426950408889634f;
constexpr float MARGIN = 88.f;
constexpr unsigned OOB = 0x80000000u;
constexpr unsigned CODE_SKIP = 4608;
constexpr unsigned XS = 16448, WSL = 16640, L_W = 6 * XS;
constexpr unsigned L_WO = 0, L_RED = 102400, L_MRG = 122880;
constexpr unsigned L_ST = L_W + 3 * WSL;
constexpr unsigned L_G = L_ST + 2048;
constexpr unsigned L_M = L_G + 4096;
constexpr unsigned L_C = L_M + 4096;
constexpr unsigned L_PF = L_C + 64;
constexpr unsigned L_END = L_PF + 256;

#define STAMP(i)
#define STEPSTAMP()

#define FS(v)
__device__ __forceinline__ unsigned pk2(float a, float b) { bf16x2 r = {(__bf16)a, (__bf16)b}; return __builtin_bit_cast(unsigned, r); }
__device__ __forceinline__ u32x4 pk8(f32x4 a, f32x4 b) { return u32x4{pk2(a[0], a[1]), pk2(a[2], a[3]), pk2(b[0], b[1]), pk2(b[2], b[3])}; }
__device__ __forceinline__ float blo(unsigned u) { return __uint_as_float(u << 16); }
__device__ __forceinline__ float bhi(unsigned u) { return __uint_as_float(u & 0xffff0000u); }
__device__ __forceinline__ u64 uni64(u64 v) {
  return ((u64)(unsigned)__builtin_amdgcn_readfirstlane((unsigned)(v >> 32)) << 32) | (unsigned)__builtin_amdgcn_readfirstlane((unsigned)v);
}
__device__ __forceinline__ int fresh_lane() { int l = __builtin_amdgcn_mbcnt_hi(~0u, __builtin_amdgcn_mbcnt_lo(~0u, 0u)); asm volatile("" : "+v"(l)); return l; }
__device__ __forceinline__ rsrc_t mkrs(const void* p, unsigned bytes) { return __builtin_amdgcn_make_buffer_rsrc((void*)p, 0, bytes, 0x00020000); }
__device__ __forceinline__ void dma16(rsrc_t r, unsigned vo, unsigned so, unsigned lds_base) {
  asm volatile("s_mov_b32 m0, %0\n\ts_nop 0\n\tbuffer_load_dwordx4 %1, %2, %3 offen lds" :: "s"(lds_base), "v"(vo), "s"(r), "s"(so) : "memory");
}
__device__ __forceinline__ void touch(rsrc_t r, unsigned vo, unsigned lds_base) {
  asm volatile("s_mov_b32 m0, %0\n\ts_nop 0\n\tbuffer_load_dword %1, %2, 0 offen lds" :: "s"(lds_base), "v"(vo), "s"(r) : "memory");
}
#define STOUCH3(p) asm volatile("s_load_dword s100, %0, 0\n\ts_load_dword s100, %0, 64\n\ts_load_dword s100, %0, 128" :: "s"(p) : "s100")
#define SPFX(x) x
#define STOUCH4(p) asm volatile("s_load_dword s100, %0, 0\n\ts_load_dword s100, %0, 64\n\ts_load_dword s100, %0, 128\n\ts_load_dword s100, %0, 192" :: "s"(p) : "s100")
__device__ __forceinline__ bf16x8 ld16(rsrc_t r, unsigned vo, unsigned so) { return __builtin_bit_cast(bf16x8, __builtin_amdgcn_raw_buffer_load_b128(r, vo, so, 16)); }
__device__ __forceinline__ s16x4 ld8(rsrc_t r, unsigned vo, unsigned so) { return __builtin_bit_cast(s16x4, __builtin_amdgcn_raw_buffer_load_b64(r, vo, so, 16)); }
__device__ __forceinline__ void st16(u32x4 v, rsrc_t r, unsigned vo, unsigned so) { __builtin_amdgcn_raw_buffer_store_b128(v, r, vo + so, 0, 16); }
__device__ __forceinline__ void st8(u32x2 v, rsrc_t r, unsigned vo, unsigned so) { __builtin_amdgcn_raw_buffer_store_b64(v, r, vo + so, 0, 16); }
__device__ __forceinline__ void publish(GAS u64* f, unsigned i, u64 tag, int lane) {
  st16(u32x4{(unsigned)tag, (unsigned)(tag >> 32), (unsigned)tag, (unsigned)(tag >> 32)}, mkrs((const void*)(f + 16 * i), 128), lane * 16, 0);
}
template <int SLP> __device__ __forceinline__ void await(GAS u64* f, unsigned i, u64 tag) {
#pragma unroll 1
  for (int n = 0; n < (1 << 20); ++n) {
    if (__all(__hip_atomic_load(f + 16 * i, RLX_AGENT) == tag)) break;
    __builtin_amdgcn_s_sleep(SLP);
  }
  __builtin_amdgcn_fence(__ATOMIC_ACQUIRE, "wavefront");
}
__device__ __forceinline__ void lds_add(LAS unsigned* c, unsigned v) {
  asm volatile("s_mov_b64 exec, 1\n\tds_add_u32 %0, %1\n\ts_mov_b64 exec, -1" :: "v"(c), "v"(v) : "memory");
}
__device__ __forceinline__ unsigned lds_add_rtn(LAS unsigned* c, unsigned v) {
  unsigned r_;
  asm volatile("s_mov_b64 exec, 1\n\tds_add_rtn_u32 %0, %1, %2\n\ts_mov_b64 exec, -1\n\ts_waitcnt lgkmcnt(0)" : "=&v"(r_) : "v"(c), "v"(v) : "memory");
  return __builtin_amdgcn_readfirstlane(r_);
}
template <int SLP> __device__ __forceinline__ void lds_meet(LAS unsigned* c, unsigned n, int lane) {
  lds_add(c, 1u);
#pragma unroll 1
  for (int k = 0; k < (1 << 22); ++k) {
    if (__builtin_amdgcn_readfirstlane(*(volatile LAS unsigned*)c) >= n) break;
    if (SLP) __builtin_amdgcn_s_sleep(SLP);
  }
  asm volatile("" ::: "memory");
}
__device__ __forceinline__ s16x4 trd(LAS char* p) { return __builtin_amdgcn_ds_read_tr16_b64_v4i16((LAS s16x4*)p); }
__device__ __forceinline__ bf16x8 cat8(s16x4 a, s16x4 b) { return __builtin_bit_cast(bf16x8, __builtin_shufflevector(a, b, 0, 1, 2, 3, 4, 5, 6, 7)); }
template <int C> __device__ __forceinline__ float dppadd(float a) {
  return a + __int_as_float(__builtin_amdgcn_update_dpp(0, __float_as_int(a), C, 0xf, 0xf, true));
}

__device__ __forceinline__ u32x2 sqrelu4(f32x4 a, f32x4 c, f32x4 bb, float r, float t) {
  float v0, v1, v2, v3;
  asm("v_fma_f32 %0, %8, %17, %12\n\tv_fma_f32 %1, %9, %17, %13\n\tv_fma_f32 %2, %10, %17, %14\n\tv_fma_f32 %3, %11, %17, %15\n\t"
      "v_fmac_f32 %0, %4, %16\n\tv_fmac_f32 %1, %5, %16\n\tv_fmac_f32 %2, %6, %16\n\tv_fmac_f32 %3, %7, %16\n\t"
      "v_mul_f32 %0, %0, |%0|\n\tv_mul_f32 %1, %1, |%1|\n\tv_mul_f32 %2, %2, |%2|\n\tv_mul_f32 %3, %3, |%3|\n\t"
      "v_cvt_pk_bf16_f32 %0, %0, %1\n\tv_cvt_pk_bf16_f32 %2, %2, %3\n\tv_pk_max_i16 %0, %0, 0\n\tv_pk_max_i16 %2, %2, 0"
      : "=&v"(v0), "=&v"(v1), "=&v"(v2), "=&v"(v3)
      : "v"(a[0]), "v"(a[1]), "v"(a[2]), "v"(a[3]), "v"(c[0]), "v"(c[1]), "v"(c[2]), "v"(c[3]), "v"(bb[0]), "v"(bb[1]), "v"(bb[2]), "v"(bb[3]), "v"(r), "v"(t));
  return u32x2{__float_as_uint(v0), __float_as_uint(v2)};
}

template <int N> struct IC { static constexpr int v = N; };
template <int MODE> struct G1 {
  static constexpr int NM = MODE == 2 ? 2 : 4, NI = MODE == 2 ? 6 : 4, PAD = MODE == 1 ? 32 : 8, NP = NI / 2;
  static constexpr int NMMA = 2 * NM * NI, NRD = 2 * (NM + 2 * NI), HEAD = MODE == 2 ? 6 : 12, R2H = 3, R2S = 18;
  struct Frag { bf16x8 x[2][NM]; s16x4 w[2][NI][2]; };
  struct Ptr { LAS char* x[2][2]; LAS char* w[NP]; };
  template <int R, int XO = 0, int WO = 0> static __device__ __forceinline__ void rd(Frag& f, const Ptr& p) {
    constexpr int u = R / (NRD / 2), r = R % (NRD / 2);
    if constexpr (r >= 2 && r < 2 + NM) f.x[u][r - 2] = *(LAS bf16x8*)(p.x[u][r & 1] + XO + ((r - 2) >> 1) * 4096 + (r & 1) * (MODE == 1 ? 512 : 2048));
    else { constexpr int q = r < 2 ? r : r - NM, i = q >> 1;
      f.w[u][i][q & 1] = trd(p.w[i >> 1] + WO + (i & 1) * PAD + u * (8192 + 4 * PAD) + (q & 1) * 1024); }
  }
  template <int N> static __device__ __forceinline__ void mma(f32x4 (&acc)[NM][NI], const Frag& f) {
    constexpr int u = N / (NM * NI), i = (N / NM) % NI, m = N % NM;
    const bf16x8 wf = cat8(f.w[u][i][0], f.w[u][i][1]);
    if constexpr (MODE == 1) asm volatile("v_mfma_f32_16x16x32_bf16 %0, %1, %2, %0" : "+v"(acc[m][i]) : "v"(f.x[u][m]), "v"(wf));
    else asm volatile("v_mfma_f32_16x16x32_bf16 %0, %1, %2, %0" : "+v"(acc[m][i]) : "v"(wf), "v"(f.x[u][m]));
  }
  template <int U> static __device__ __forceinline__ void csum(f32x4& cs, const Frag& f, bf16x8 sel, int idx) {
    const bf16x16 wf = __builtin_shufflevector(cat8(f.w[U][0][0], f.w[U][0][1]), cat8(f.w[U][1][0], f.w[U][1][1]), 0, 1, 2, 3, 4, 5, 6, 7, 8, 9, 10, 11, 12, 13, 14, 15);
    asm volatile("v_smfmac_f32_16x16x64_bf16 %0, %1, %2, %3" : "+v"(cs) : "v"(sel), "v"(wf), "v"(idx));
  }
  static __device__ __forceinline__ int span(int w, int j) { return MODE == 2 ? (w + j >= 3 ? w + j - 3 : w + j) : 2 * (w >> 1) + (j ^ (w & 1)); }
  template <int R0, int R1, int XO = 0, int WO = 0> static __device__ __forceinline__ void rds(Frag& f, const Ptr& p) {
    if constexpr (R0 < R1) { rd<R0, XO, WO>(f, p); rds<R0 + 1, R1, XO, WO>(f, p); } }
  template <int N0, int N1> static __device__ __forceinline__ void mmas(f32x4 (&acc)[NM][NI], const Frag& f) { if constexpr (N0 < N1) { mma<N0>(acc, f); mmas<N0 + 1, N1>(acc, f); } }
  template <int N> static __device__ __forceinline__ void mix(f32x4 (&acc)[NM][NI], const Frag& fc, Frag& fn, const Ptr& p) {
    if constexpr (N < NMMA) {
      constexpr int SPAN = NMMA - HEAD - 3, k = N - HEAD;
      constexpr int r0 = k >= SPAN ? NRD : (k * NRD + SPAN - 1) / SPAN, r1 = k + 1 >= SPAN ? NRD : ((k + 1) * NRD + SPAN - 1) / SPAN;
      mma<N>(acc, fc); rds<r0, r1>(fn, p); SB();
      mix<N + 1>(acc, fc, fn, p);
    }
  }
  struct Off { unsigned x, w[NP]; };
  static __device__ __forceinline__ Off offs(int w, int lane) {
    const int kq = lane >> 4, c16 = lane & 15, a = (lane >> 2) & 3;
    Off o;
    o.x = MODE == 1 ? (64 * (w & 1) + 8 * (c16 >> 2) + (c16 & 3)) * 128 + ((kq ^ ((c16 >> 1) & 1) ^ ((c16 >> 3) << 1) ^ (c16 & 4)) << 4)
                    : ((MODE == 2 ? 32 * w : 64 * (w & 1)) + c16) * 128 + ((kq ^ (c16 >> 1)) << 4);
    const unsigned wb = L_W + kq * (2048 + PAD) + a * 256 + (lane & 3) * (MODE == 1 ? 8 : 16);
#pragma unroll
    for (int j = 0; j < NP; ++j) o.w[j] = wb + (span(w, j) ^ a) * 64;
    return o;
  }
  template <int U> static __device__ __forceinline__ void ptrx(Ptr& p, const Off& o, LAS char* lds, unsigned sx) {
    p.x[U][0] = lds + sx + (o.x ^ (64 * U)); p.x[U][1] = lds + sx + (o.x ^ (64 * U + 32)); asm volatile("" : "+v"(p.x[U][0]), "+v"(p.x[U][1])); }
  static __device__ __forceinline__ void ptrw(Ptr& p, const Off& o, LAS char* lds, unsigned sw) {
#pragma unroll
    for (int j = 0; j < NP; ++j) { p.w[j] = lds + (sw + o.w[j]); asm volatile("" : "+v"(p.w[j])); } }
  template <int N, class T> static __device__ __forceinline__ void fin(f32x4 (&acc)[NM][NI], const Frag& fc, T& tail) {
    if constexpr (N < NMMA) { mma<N>(acc, fc); tail(IC<N - HEAD>{}); SB(); fin<N + 1>(acc, fc, tail); }
  }
  template <class T>
  static __device__ __forceinline__ void loop(f32x4 (&acc)[NM][NI], Frag& fa, const Off& o, LAS char* lds, unsigned sx, unsigned sw, int w, int lane, int pub,
                                              GAS u64* fl, unsigned fi, u64 tag, T tail, unsigned& stv) {
    const int kq = lane >> 4, c16 = lane & 15;
    Frag fb; Ptr p;
#pragma unroll
    for (int n = 0; n < NM * NI; ++n) { acc[n % NM][n / NM] = f32x4{}; asm volatile("" : "+v"(acc[n % NM][n / NM])); }
    const unsigned one = (kq >> 1) == ((c16 >> 1) & 1) ? 0x3f803f80u : 0u;
    bf16x8 sel = __builtin_bit_cast(bf16x8, u32x4{one, one, one, one});
    int sidx = (lane & 1) ? 0xEEEE : 0x4444;
    f32x4 cs = {};
    LAS float* cp = (LAS float*)(lds + L_ST + 1024) + 32 * span(w, 0) + (MODE == 1 ? c16 : 8 * (c16 >> 2) + (c16 & 3));
    asm volatile("s_nop 1" : "+v"(sel), "+v"(sidx), "+v"(cs), "+v"(cp));
    if constexpr (MODE != 2) { ptrx<0>(p, o, lds, sx); ptrx<1>(p, o, lds, sx); ptrw(p, o, lds, sw); SB(); tail(IC<-2>{}); STAMP(1); BAR_L(); STAMP(12); rds<0, NRD>(fa, p); }
#define G1_HEAD(FC, PUB, LAST, BAR) do { \
      mma<0>(acc, FC); sx = (sx == 5 * XS) ? 0 : sx + XS; asm volatile("" : "+s"(sx)); SB(); \
      csum<0>(cs, FC, sel, sidx); SB(); \
      mma<1>(acc, FC); sw = (sw == 2 * WSL) ? 0 : sw + WSL; asm volatile("" : "+s"(sw)); SB(); \
      mma<2>(acc, FC); ptrx<0>(p, o, lds, sx); SB(); \
      mma<3>(acc, FC); ptrw(p, o, lds, sw); SB(); \
      mmas<4, HEAD - 2>(acc, FC); SB(); \
      csum<1>(cs, FC, sel, sidx); SB(); \
      mma<HEAD - 2>(acc, FC); ptrx<1>(p, o, lds, sx); SB(); \
      mma<HEAD - 1>(acc, FC); SB(); \
      if (LAST) { asm volatile("s_nop 5" : "+v"(cs)); cp[0] = cs[0] + cs[1]; cp[MODE == 1 ? 16 : 4] = cs[2] + cs[3]; } \
      if (PUB) DRAIN(); \
      if (BAR) BAR_L(); \
      if ((PUB) && w == 0) publish(fl, fi, tag, lane); \
      SB(); } while (0)
    SB();
    G1_HEAD(fa, false, false, true);
#pragma unroll 1
    for (int s = 0; s < 4; ++s) {
      mix<HEAD>(acc, fa, fb, p);
      G1_HEAD(fb, false, false, true);
      mix<HEAD>(acc, fb, fa, p);
      G1_HEAD(fa, false, false, true);
    }
    mix<HEAD>(acc, fa, fb, p);
    G1_HEAD(fb, false, true, true);
    fin<HEAD>(acc, fb, tail);
    asm volatile("s_nop 7");
    SB();
  }
  template <int J, int N> static __device__ __forceinline__ void r2mix(f32x4 (&acc)[NM][NI], f32x4& cs, bf16x8 sel, int sidx, const Frag& fc, Frag& fn, const Ptr& pl, const Ptr& ph) {
    if constexpr (N < NMMA) {
      constexpr int H2 = R2H, SPAN = R2S, k = N - H2, xs = (5 + J) % 6;
      constexpr int r0 = k >= SPAN ? NRD : (k * NRD + SPAN - 1) / SPAN, r1 = k + 1 >= SPAN ? NRD : ((k + 1) * NRD + SPAN - 1) / SPAN;
      mma<N>(acc, fc);
      Ptr p = xs >= 3 ? ph : pl;
#pragma unroll
      for (int j = 0; j < NP; ++j) p.w[j] = J == 8 ? ph.w[j] : pl.w[j];
      rds<r0, r1, (xs % 3) * XS, J == 8 ? 0 : ((2 + J) % 3) * WSL>(fn, p); SB();
      if constexpr (N == 13) { csum<1>(cs, fc, sel, sidx); SB(); }
      r2mix<J, N + 1>(acc, cs, sel, sidx, fc, fn, pl, ph);
    }
  }
  template <int J, class PUB> static __device__ __forceinline__ void r2steps(f32x4 (&acc)[NM][NI], f32x4& cs, bf16x8 sel, int sidx, Frag& fc, Frag& fn, const Ptr& pl, const Ptr& ph, PUB& pub) {
    if constexpr (J < 9) {
      mmas<0, 2>(acc, fc); csum<0>(cs, fc, sel, sidx); mmas<2, R2H>(acc, fc); SB();
      if constexpr (J == 2) DRAIN();
      if constexpr (J < 8) BAR_L();
      if constexpr (J == 2) pub();
      SB();
      r2mix<J, R2H>(acc, cs, sel, sidx, fc, fn, pl, ph);
      r2steps<J + 1>(acc, cs, sel, sidx, fn, fc, pl, ph, pub);
    }
  }
  template <class PUB, class T>
  static __device__ __forceinline__ void loop2(f32x4 (&acc)[NM][NI], Frag& fa, const Off& o, const Ptr& ph, LAS char* lds, int w, int lane, PUB pub, T tail) {
    const int kq = lane >> 4, c16 = lane & 15;
    Frag fb; Ptr pl, ph2 = ph;
    ptrx<0>(pl, o, lds, 0); ptrx<1>(pl, o, lds, 0);
#pragma unroll
    for (int j = 0; j < NP; ++j) pl.w[j] = ph.w[j];
    ptrw(ph2, o, lds, 3 * XS - L_W);
#pragma unroll
    for (int n = 0; n < NM * NI; ++n) { acc[n % NM][n / NM] = f32x4{}; asm volatile("" : "+v"(acc[n % NM][n / NM])); }
    const unsigned one = (kq >> 1) == ((c16 >> 1) & 1) ? 0x3f803f80u : 0u;
    bf16x8 sel = __builtin_bit_cast(bf16x8, u32x4{one, one, one, one});
    int sidx = (lane & 1) ? 0xEEEE : 0x4444;
    f32x4 cs = {};
    LAS float* cp = (LAS float*)(lds + L_ST + 1024) + 32 * span(w, 0) + 8 * (c16 >> 2) + (c16 & 3);
    asm volatile("s_nop 1" : "+v"(sel), "+v"(sidx), "+v"(cs), "+v"(cp));
    SB();
    r2steps<0>(acc, cs, sel, sidx, fa, fb, pl, ph2, pub);
    mmas<0, 2>(acc, fb); csum<0>(cs, fb, sel, sidx); mmas<2, 4>(acc, fb); csum<1>(cs, fb, sel, sidx); mmas<4, HEAD>(acc, fb); SB();
    fin<HEAD>(acc, fb, tail);
    asm volatile("s_nop 7" : "+v"(cs));
    cp[0] = cs[0] + cs[1]; cp[4] = cs[2] + cs[3];
    SB();
  }
};

struct KV { bf16x8 k1; s16x4 k2; bf16x8 v0, v1; };
__device__ __forceinline__ f32x16 qk_a(bf16x8 k, bf16x8 q) { f32x16 z = {}; return __builtin_amdgcn_mfma_f32_32x32x16_bf16(k, q, z, 0, 0, 0); }
__device__ __forceinline__ f32x16 qk_b(s16x4 k, s16x4 q, f32x16 c) { return __builtin_amdgcn_mfma_f32_32x32x8bf16_1k(k, q, c, 0, 0, 0); }
__device__ __forceinline__ f32x16 pv_lo(bf16x8 v, f32x16 p, f32x16 o, int idx) {
  f32x8 h = __builtin_shufflevector(p, p, 0, 1, 2, 3, 4, 5, 6, 7);
  o = __builtin_amdgcn_smfmac_f32_32x32x32_bf16(v, __builtin_bit_cast(bf16x16, h), o, idx, 0, 0);
  asm volatile("" : "+v"(o));
  return o; }
__device__ __forceinline__ f32x16 pv_hi(bf16x8 v, f32x16 p, f32x16 o, int idx) {
  f32x8 h = __builtin_shufflevector(p, p, 8, 9, 10, 11, 12, 13, 14, 15);
  o = __builtin_amdgcn_smfmac_f32_32x32x32_bf16(v, __builtin_bit_cast(bf16x16, h), o, idx, 0, 0);
  asm volatile("" : "+v"(o));
  return o; }
template <int E> __device__ __forceinline__ void max8(const f32x16& s, float& m) {
  asm volatile("v_max3_f32 %0, %0, %1, %2\n\tv_max3_f32 %0, %0, %3, %4\n\tv_max3_f32 %0, %0, %5, %6\n\tv_max3_f32 %0, %0, %7, %8"
               : "+v"(m) : "v"(s[E]), "v"(s[E + 1]), "v"(s[E + 2]), "v"(s[E + 3]), "v"(s[E + 4]), "v"(s[E + 5]), "v"(s[E + 6]), "v"(s[E + 7]));
}
struct F4 { float v[4]; };
__device__ __forceinline__ F4 add4(f32x4 g, float a, float b, float c, float d, float& chk) {
  F4 r;
  asm volatile("v_add_f32 %0, %9, %5\n\tv_add_f32 %1, %10, %6\n\tv_add_f32 %2, %11, %7\n\tv_add_f32 %3, %12, %8\n\t"
               "v_maximum3_f32 %4, %4, |%0|, |%1|\n\tv_maximum3_f32 %4, %4, |%2|, |%3|"
               : "=&v"(r.v[0]), "=&v"(r.v[1]), "=&v"(r.v[2]), "=&v"(r.v[3]), "+v"(chk) : "v"(a), "v"(b), "v"(c), "v"(d), "v"(g[0]), "v"(g[1]), "v"(g[2]), "v"(g[3]));
  return r;
}
__device__ __forceinline__ u32x2 scale4(F4 g, float s) {
  unsigned r0, r1;
  asm volatile("v_mul_f32 %2, %2, %6\n\tv_mul_f32 %3, %3, %6\n\tv_mul_f32 %4, %4, %6\n\tv_mul_f32 %5, %5, %6\n\tv_cvt_pk_bf16_f32 %0, %2, %3\n\tv_cvt_pk_bf16_f32 %1, %4, %5"
               : "=&v"(r0), "=&v"(r1), "+v"(g.v[0]), "+v"(g.v[1]), "+v"(g.v[2]), "+v"(g.v[3]) : "v"(s));
  return u32x2{r0, r1};
}
#define EX4(s, b) do { s[b] = __builtin_amdgcn_exp2f(s[b]); s[b+1] = __builtin_amdgcn_exp2f(s[b+1]); s[b+2] = __builtin_amdgcn_exp2f(s[b+2]); s[b+3] = __builtin_amdgcn_exp2f(s[b+3]); } while (0)
#define STEPX(SC, SP, OP, VP, SN, KN, Q1, Q2, X0, X1) do { \
  EX4(SC, 0); SB(); OP = pv_lo(VP.v0, SP, OP, idx); SB(); X0; SB(); \
  EX4(SC, 4); SB(); SN = qk_a(KN.k1, Q1); SB(); \
  EX4(SC, 8); SB(); OP = pv_hi(VP.v1, SP, OP, idx); SB(); X1; SB(); \
  EX4(SC, 12); SB(); SN = qk_b(KN.k2, Q2, SN); SB(); } while (0)
#define STEP(SC, SP, OP, VP, SN, KN, Q1, Q2) STEPX(SC, SP, OP, VP, SN, KN, Q1, Q2, , )

__global__ void __launch_bounds__(512, 2)
fused(const u16* __restrict__ x, const u16* __restrict__ wqkv, const u16* __restrict__ wo, const u16* __restrict__ w1,
      const u16* __restrict__ b1, const u16* __restrict__ w2, const u16* __restrict__ b2, u16* __restrict__ out, char* __restrict__ ws) {
  __shared__ __attribute__((aligned(16))) char smem[L_END + 256];
  LAS char* lds = (LAS char*)smem;
  const unsigned lds0 = (unsigned)(size_t)lds;
  const int lane = threadIdx.x & 63, w = __builtin_amdgcn_readfirstlane(threadIdx.x >> 6);
  const int b = blockIdx.x, g = b & 7, idx8 = b >> 3;
  asm volatile("" :: "s"(x), "s"(wqkv), "s"(wo), "s"(w1), "s"(b1), "s"(w2), "s"(b2), "s"(out), "s"(ws));
  const int rt = (g & 3) * 4 + (idx8 & 3), ct = (g >> 2) * 8 + (idx8 >> 2);
  const bool vtype = ct >= 10;
  const int kq = lane >> 4, c16 = lane & 15;
  LAS float* st_r = (LAS float*)(lds + L_ST);
  LAS float* st_t = st_r + 128;
  LAS float* st_c = st_r + 256;
  GAS u64* fl = (GAS u64*)(ws + WS_F);
  const rsrc_t rw = mkrs(ws, WS_END), rx = mkrs(x, 2048 * D_ * 2);
  u64 tag = 0;
  const int head = 4 * g + (idx8 >> 3), qc = idx8 & 7;
  const int aw = w - 4, qh = aw & 1, kh = aw >> 1, r = lane & 31, h = lane >> 5, odd = head & 1, pr = head >> 1;
  int hstep = 0; unsigned stv = 0;
  unsigned oK1 = 0, oK2 = 0, oV0 = 0, oV1 = 0, oQ2 = 0;
  auto kv_offsets = [&](int ln) {
    const int r = ln & 31, h = ln >> 5;
    oK1 = odd * 1536 + ln * 16; oK2 = h ? P_ONE : 1024 + r * 16 + odd * 8; oQ2 = h ? OOB : oK2;
    const int dv = r < 16 ? (r & 3) + 4 * (r >> 3) + 8 * ((r >> 2) & 1) : r;
    oV0 = r < 20 ? P_V + odd * 1280 + h * 320 + dv * 16 : (r == 20 ? P_ONE : OOB); oV1 = r < 20 ? oV0 + 640 : oV0;
  };
  unsigned fi = 0;
  STAMP(0);

  if (w >= 4) {
    const int hw = w - 4;
    rsrc_t rwt = mkrs(wqkv, D_ * 3 * D_ * 2);
    unsigned rs = 3 * D_ * 2, pad = vtype ? 32 : 8;
    unsigned wso = ct * 256 + 16 * hw * rs, xso = (rt * 128 + hw * 32) * (D_ * 2);
    const unsigned xv = (lane >> 3) * (D_ * 2) + (((lane & 7) ^ kq) << 4), xv1 = xv ^ 64, xv2 = xv ^ 32, xv3 = xv ^ 96;
    const int wc = c16 ^ (kq << 2);
    unsigned wv = ct < 15 ? kq * rs + (wc << 4) : OOB;
    unsigned nx = lds0 + hw * 4096, nw = lds0 + L_W + hw * 4096;
#define DX(i) dma16(rx, i == 0 ? xv : (i == 1 ? xv1 : (i == 2 ? xv2 : xv3)), xso + i * 8 * (D_ * 2), nx + i * 1024)
#define DW(i) dma16(rwt, wv, wso + 4 * i * rs, nw + i * 1024 + (2 * hw + (i >> 1)) * pad)
#define NEXTX() do { xso += 128; nx = (nx == lds0 + hw * 4096 + 5 * XS) ? nx - 5 * XS : nx + XS; } while (0)
#define NEXTW() do { wso += 64 * rs; nw = (nw == lds0 + L_W + hw * 4096 + 2 * WSL) ? nw - 2 * WSL : nw + WSL; } while (0)
#define HBAR(n) asm volatile("s_waitcnt vmcnt(" #n ")\n\ts_barrier" ::: "memory")
    __builtin_amdgcn_s_setprio(1);
#define DW4() do { DW(0); DW(1); DW(2); DW(3); NEXTW(); } while (0)
#define DX4() do { DX(0); DX(1); DX(2); DX(3); NEXTX(); } while (0)
    const char* pw = (const char*)w1 + 256 * F_ * 2 + (b * 4 + hw) * 192;
    {
      DX(0); DW(0); DX(1); DW(1); DX(2); DW(2); DX(3); DW(3); NEXTW(); NEXTX();
      DX4();
#pragma unroll 1
      for (int s = -1; s < 7; ++s) { DW4(); DX4(); SPFX(STOUCH3(pw)); pw += (s == 4 ? -576 : 64) * F_ * 2; HBAR(12); }
    }
    const unsigned go = WS_S + (rt * 128 + (hw & 1) * 64 + lane) * 16;
    asm volatile("s_mov_b32 m0, %0\n\ts_nop 0\n\tbuffer_load_dwordx4 %1, %2, 0 offen sc1 lds" :: "s"(lds0 + L_G + hw * 1024), "v"(go), "s"(rw) : "memory");
    DW4(); SPFX(STOUCH3(pw)); pw += 64 * F_ * 2; HBAR(9);
    rwt = mkrs(w1, D_ * F_ * 2); rs = F_ * 2; pad = 8; wso = ct * 192 + (256 + 16 * hw) * rs; wv = wc < 12 ? kq * rs + (wc << 4) : OOB;
    xso -= 1280; nx = lds0 + hw * 4096 + 4 * XS; nw = lds0 + L_W + hw * 4096 + WSL;
    DW4();
    tag = uni64(*(LAS u64*)(lds + L_C));
    {
      asm volatile("s_waitcnt vmcnt(8)" ::: "memory");
      u32x4 gr = *(LAS u32x4*)(lds + L_G + hw * 1024 + lane * 16);
#pragma unroll 1
      for (int n = 0; n < (1 << 20); ++n) {
        if (__all(gr[1] == (unsigned)tag && gr[3] == (unsigned)tag)) break;
        asm volatile("buffer_load_dwordx4 %0, %1, %2, 0 offen sc1\n\ts_waitcnt vmcnt(0)" : "=&v"(gr) : "v"(go), "s"(rw) : "memory");
      }
      st_r[hw * 64 + lane] = __uint_as_float(hw < 2 ? gr[0] : gr[2]);
    }
    HBAR(4);
    DW4(); asm volatile("s_waitcnt lgkmcnt(0)" ::: "memory"); SPFX(STOUCH3(pw)); HBAR(4);
    DW4();
    {
      const int c0 = pr * 5 + 2 * odd, e = (lane >> 3) & 1;
      const int tqk = (c0 + 2 * e) >> 4, tv = (head * 20 + 19 * e) >> 7;
      fi = F_Q + (lane < 16 ? 5 + tqk : 10 + tv) * 16 + 8 * kh + (lane & 7);
      if (lane >= 32) fi = F_Q + tqk * 16 + 2 * qc + qh;
      kv_offsets(lane);
      asm volatile("" : "+v"(fi), "+v"(oK1), "+v"(oK2), "+v"(oV0), "+v"(oV1), "+v"(oQ2));
    }
    HBAR(4);
    DW4(); DX4(); HBAR(8);
    DW4(); DX4(); HBAR(12);
    DW4(); DX4(); wso -= 640 * rs; HBAR(12);
    DW4(); DX4(); HBAR(12);
    DW4(); HBAR(8);
    DW4(); nw = lds0 + 3 * XS + hw * 4096; DW4(); HBAR(8);
    asm volatile("s_mov_b32 m0, %0\n\ts_nop 0\n\tbuffer_load_dwordx4 %1, %2, 0 offen sc1 lds" :: "s"(lds0 + L_G + hw * 1024), "v"(fi * 128), "s"(mkrs(ws + WS_F, 1u << 17)) : "memory");
  } else {
    const int wm = w & 1, wn = w >> 1;
    const int row = rt * 128 + ct * 8 + 2 * w + (lane >> 5);
    {
      const unsigned lb = lds0 + 5 * XS + w * 4096, xo = row * (D_ * 2) + r * 16;
      dma16(rx, xo, 0, lb); dma16(rx, xo, 512, lb + 1024); dma16(rx, r < 16 ? xo : OOB, 1024, lb + 2048);
      asm volatile("s_mov_b32 m0, %0\n\ts_nop 0\n\tbuffer_load_dword %1, %2, 0 offen sc1 lds" :: "s"(lb + 3072), "v"(lane * 4), "s"(mkrs(ws + WS_F, 8)) : "memory");
      u64 pc; asm volatile("s_getpc_b64 %0" : "=s"(pc));
      STOUCH4((pc & ~(u64)255) + CODE_SKIP + (idx8 * 4 + w) * 256u);
      *(LAS u32x4*)(lds + L_M + threadIdx.x * 16) = u32x4{0x7fc00000u, 0x7fc00000u, 0x7fc00000u, 0x7fc00000u};
    }
    unsigned vo[4];
    if (vtype) {
#pragma unroll
      for (int i = 0; i < 4; ++i) {
        const int fv = (ct - 10) * 128 + 32 * G1<1>::span(w, i >> 1) + 16 * (i & 1) + c16, hd = fv / 20, d = fv - hd * 20;
        vo[i] = (WS_T + ((hd >> 1) * 64 + 4 * rt + 2 * wm) * PT + P_V + (hd & 1) * 1280 + kq * 320 + d * 16) | (ct < 15 ? 0u : OOB);
      }
    } else {
#pragma unroll
      for (int pp = 0; pp < 2; ++pp) {
        const int c = (ct < 5 ? ct : ct - 5) * 16 + 4 * G1<0>::span(w, pp) + kq, p5 = c / 5, pos = c - p5 * 5;
        const unsigned tb = WS_T + (p5 * 64 + 4 * rt + 2 * wm) * PT;
        vo[pp] = tb + pos * 512 + (ct < 5 ? P_Q : 0u) + c16 * 16;
        vo[2 + pp] = (ct >= 5 && pos == 2) ? tb + P_ONE + (c16 >> 3) * PT + (c16 & 7) * 16 : OOB;
      }
    }
    asm volatile("" : "+v"(vo[0]), "+v"(vo[1]), "+v"(vo[2]), "+v"(vo[3]));
    if (w < 2) touch(w ? mkrs(b1 + ct * 96, 192) : mkrs(wqkv + 94 * 3 * D_, 546 * 3 * D_ * 2), w ? lane * 4 : (b * 64 + lane) * 128, lds0 + (w ? L_ST + 1536 : L_PF));
    u32x4 xr[3]; u64 e0;
    auto stats_ld = [&]() {
      if (w < 2) asm volatile("s_waitcnt vmcnt(1)" ::: "memory"); else DRAIN();
#pragma unroll
      for (int i = 0; i < 3; ++i) xr[i] = *(LAS u32x4*)(lds + 5 * XS + w * 4096 + i * 1024 + lane * 16);
      e0 = *(LAS u64*)(lds + 5 * XS + w * 4096 + 3072);
    };
    auto stats = [&]() {
      float p = 0.f, q = 0.f;
#pragma unroll
      for (int i = 0; i < 12; ++i) {
        const unsigned u2 = xr[i >> 2][i & 3];
        const bf16x2 v2 = __builtin_bit_cast(bf16x2, u2);
        p = __builtin_amdgcn_fdot2_f32_bf16(v2, __builtin_bit_cast(bf16x2, 0x3f803f80u), p, false); q = __builtin_amdgcn_fdot2_f32_bf16(v2, v2, q, false);
      }
      p = dppadd<0x140>(dppadd<0x141>(dppadd<0x4E>(dppadd<0xB1>(p)))); q = dppadd<0x140>(dppadd<0x141>(dppadd<0x4E>(dppadd<0xB1>(q))));
      p = dppadd<0x142>(p); q = dppadd<0x142>(q);
      const float mu = p * (1.f / D_), rstd = __builtin_amdgcn_rsqf(fmaxf(q * (1.f / D_) - mu * mu, 0.f) + 1e-5f);
      tag = uni64(e0 + 1);
      if (threadIdx.x == 0) { *(LAS u64*)(lds + L_C) = tag; *(LAS u32x4*)(lds + L_C + 16) = u32x4{0, 0, 0, 0}; *(LAS u32x4*)(lds + L_C + 32) = u32x4{0, 0, 0, 0}; *(LAS u32x4*)(lds + L_C + 48) = u32x4{0, 0, 0, 0}; }
      st16(u32x4{__float_as_uint(rstd), (unsigned)tag, __float_as_uint(-mu * rstd), (unsigned)tag}, rw, r == 16 ? row * 16 : OOB, WS_S);
    };
    G1<2>::Frag f2; G1<2>::Ptr p2;
    const G1<2>::Off o2 = G1<2>::offs(w, lane);
    G1<2>::ptrx<0>(p2, o2, lds, 3 * XS); G1<2>::ptrx<1>(p2, o2, lds, 3 * XS); G1<2>::ptrw(p2, o2, lds, 0);
    {
      f32x4 acc[4][4];
      if (vtype) {
        f32x4 r4[4], t4[4]; float c1[4];
        G1<1>::Frag f1;
        LAS float* pr4 = st_r + 64 * wm + 8 * kq; LAS float* pc0 = st_c + 32 * G1<1>::span(w, 0) + c16; LAS float* pc1 = st_c + 32 * G1<1>::span(w, 1) + c16;
        asm volatile("" : "+v"(pr4), "+v"(pc0), "+v"(pc1));
        G1<1>::loop(acc, f1, G1<1>::offs(w, lane), lds, 0, 0, w, lane, -1, fl, 0, tag, [&](auto k) {
          constexpr int K = decltype(k)::v;
          if constexpr (K < 0) { stats_ld(); stats(); }
          else if constexpr (K < 4) { r4[K] = *(LAS f32x4*)(pr4 + 32 * (K >> 1) + 4 * (K & 1)); t4[K] = *(LAS f32x4*)(pr4 + 128 + 32 * (K >> 1) + 4 * (K & 1)); }
          else if constexpr (K < 6) { c1[2 * K - 8] = (K == 4 ? pc0 : pc1)[0]; c1[2 * K - 7] = (K == 4 ? pc0 : pc1)[16]; }
          else G1<2>::rds<2 * K - 12, 2 * K - 10, XS, WSL>(f2, p2);
        }, stv);
        STAMP(2);
#pragma unroll
        for (int i = 0; i < 4; ++i)
#pragma unroll
          for (int a = 0; a < 2; ++a)
            st16(pk8(acc[2 * a][i] * r4[2 * a] + t4[2 * a] * c1[i], acc[2 * a + 1][i] * r4[2 * a + 1] + t4[2 * a + 1] * c1[i]), rw, vo[i], a * PT);
      } else {
        float r1[4], t1[4]; f32x4 c4[4];
        G1<0>::Frag f0;
        LAS float* pr1 = st_r + 64 * wm + c16; LAS float* pc0 = st_c + 32 * G1<0>::span(w, 0) + 8 * kq; LAS float* pc1 = st_c + 32 * G1<0>::span(w, 1) + 8 * kq;
        asm volatile("" : "+v"(pr1), "+v"(pc0), "+v"(pc1));
        G1<0>::loop(acc, f0, G1<0>::offs(w, lane), lds, 0, 0, w, lane, -1, fl, 0, tag, [&](auto k) {
          constexpr int K = decltype(k)::v;
          if constexpr (K < 0) { stats_ld(); stats(); }
          else if constexpr (K < 4) { r1[K] = pr1[16 * K]; t1[K] = pr1[128 + 16 * K]; }
          else if constexpr (K < 6) { c4[2 * K - 8] = *(LAS f32x4*)(K == 4 ? pc0 : pc1); c4[2 * K - 7] = *(LAS f32x4*)((K == 4 ? pc0 : pc1) + 4); }
          else G1<2>::rds<2 * K - 12, 2 * K - 10, XS, WSL>(f2, p2);
        }, stv);
        STAMP(2);
        const float sc = ct < 5 ? QSCALE : 1.f;
#pragma unroll
        for (int pp = 0; pp < 2; ++pp)
#pragma unroll
          for (int m = 0; m < 4; ++m)
            st16(pk8(acc[m][2 * pp] * (r1[m] * sc) + c4[2 * pp] * (t1[m] * sc), acc[m][2 * pp + 1] * (r1[m] * sc) + c4[2 * pp + 1] * (t1[m] * sc)), rw,
                 vo[pp], (m & 1) * 256 + (m >> 1) * PT);
        st16(u32x4{0x3f803f80u, 0x3f803f80u, 0x3f803f80u, 0x3f803f80u}, rw, min(vo[2], vo[3]), 0);
      }
    }
    STAMP(3);
    f32x4 acc[2][6];
    float r1[2], t1[2]; f32x4 c4[6]; u32x4 bb[3];
    LAS float* pr1 = st_r + 32 * w + c16;
    G1<2>::loop2(acc, f2, o2, p2, lds, w, lane, [&]() { if (w == 0) publish(fl, F_Q + ct * 16 + rt, tag, lane); }, [&](auto k) {
      constexpr int K = decltype(k)::v;
      if constexpr (K < 2) { r1[K] = pr1[16 * K]; t1[K] = pr1[128 + 16 * K]; }
      else if constexpr (K < 5) bb[K - 2] = *(LAS u32x4*)(st_c + 128 + 16 * G1<2>::span(w, K - 2) + 4 * kq);
    });
    STAMP(4);
    __builtin_amdgcn_s_setprio(2);
    lds_meet<2>((LAS unsigned*)(lds + L_C + 20), 4, lane);
#pragma unroll
    for (int pp = 0; pp < 3; ++pp) { LAS float* pc = st_c + 32 * G1<2>::span(w, pp) + 8 * kq; c4[2 * pp] = *(LAS f32x4*)pc; c4[2 * pp + 1] = *(LAS f32x4*)(pc + 4); }
    const bool ret = w < 2 || (w == 2 && idx8 < 4);
    const int slab = w < 2 ? 2 * idx8 + w : 64 + idx8;
    u32x4 hq[6];
#pragma unroll
    for (int pp = 0; pp < 3; ++pp) {
      const f32x4 ba = {blo(bb[pp][0]), bhi(bb[pp][0]), blo(bb[pp][1]), bhi(bb[pp][1])}, bc = {blo(bb[pp][2]), bhi(bb[pp][2]), blo(bb[pp][3]), bhi(bb[pp][3])};
#pragma unroll
      for (int m = 0; m < 2; ++m) {
        const u32x2 lo = sqrelu4(acc[m][2 * pp], c4[2 * pp], ba, r1[m], t1[m]), hi = sqrelu4(acc[m][2 * pp + 1], c4[2 * pp + 1], bc, r1[m], t1[m]);
        hq[2 * pp + m] = u32x4{lo[0], lo[1], hi[0], hi[1]};
      }
    }
    asm volatile("" : "+v"(hq[0]), "+v"(hq[1]), "+v"(hq[2]), "+v"(hq[3]), "+v"(hq[4]), "+v"(hq[5]));
#define HST(i0, i1) do { _Pragma("unroll") for (int i = i0; i < i1; ++i) { st16(hq[i], rw, lane * 16, WS_H + ((8 * rt + 2 * w + (i & 1)) * 48 + 3 * ct + G1<2>::span(w, i >> 1)) * 1024); SB(); __builtin_amdgcn_s_sleep(4); SB(); } } while (0)
    FS(13); HST(0, 6); FS(14);
    if (ret) {
      const int s2 = slab - 48;
      const rsrc_t rr = mkrs((slab < 48 ? w2 : wo) + 80 * g, ((slab < 48 ? F_ : D_) * D_ - 80 * g) * 2);
      const unsigned r0 = slab < 48 ? slab * 32 : (s2 < 16 ? s2 * 40 : (s2 - 16) * 160 + 16), rsh = s2 < 16 ? 4 : 2, rmu = slab < 48 ? 0 : (s2 < 16 ? 4 : 16);
#pragma unroll
      for (int i = 0; i < 5; ++i) {
        const unsigned c = 64 * i + lane, k = (c * 205) >> 11;
        dma16(rr, (k + (k >> rsh) * rmu) * (D_ * 2) + (c - k * 10) * 16, r0 * (D_ * 2), lds0 + L_RED + w * 5120 + i * 1024);
      }
      const unsigned tro = (8 * kq + ((lane >> 2) & 3)) * 160 + (lane & 3) * 16 + (g & 1) * 32, tr4 = tro - (lane & 3) * 8 + 128 - (g & 1) * 160;
      DRAIN(); FS(15);
#pragma unroll
      for (int t = 0; t < 5; ++t) {
        LAS char* p = lds + L_RED + w * 5120 + (t < 4 ? tro + (t >> 1) * 64 + (t & 1) * 8 : tr4);
        st16(__builtin_bit_cast(u32x4, cat8(trd(p), trd(p + 640))), rw, lane * 16 + WS_W + (g * 68 + slab) * 5120, t * 1024); SB();
      }
    }
    STAMP(5);
  }

  if (w >= 4) {
    const rsrc_t rk = mkrs(ws + WS_T + (pr * 64 + kh * 32) * PT, 32 * PT);
    unsigned qo[4];
#pragma unroll
    for (int t = 0; t < 4; ++t) { qo[t] = WS_T + (pr * 64 + 8 * qc + 4 * qh + (t ^ (2 * kh))) * PT + P_Q; asm volatile("" : "+s"(qo[t])); }
    asm volatile("" :: "s"(rk));
    HBAR(1);
    DRAIN();
    if (!__all(*(volatile LAS u64*)(lds + L_G + aw * 1024 + (oK1 & 1023)) == tag)) await<2>(fl, fi, tag);
    __builtin_amdgcn_fence(__ATOMIC_ACQUIRE, "wavefront");
    STAMP(6);
    bool stored = false;
    auto sweep = [&](auto pass_) -> unsigned {
      constexpr int PASS = decltype(pass_)::v;
      const int lane = fresh_lane(), r = lane & 31, h = lane >> 5, c16 = lane & 15;
      if constexpr (PASS) kv_offsets(lane);
      bf16x8 q1[4]; s16x4 q2[4];
      KV b0, b1_, b2_, b3;
      unsigned so = 0;
#define LDK(B, d) do { B.k1 = ld16(rk, oK1, so + (d) * PT); SB(); B.k2 = ld8(rk, oK2, so + (d) * PT); SB(); } while (0)
#define LDV(B, d) do { B.v0 = ld16(rk, oV0, so + (d) * PT); SB(); B.v1 = ld16(rk, oV1, so + (d) * PT); SB(); } while (0)
      SB();
      LDK(b1_, 1);
#pragma unroll
      for (int t = 0; t < 4; ++t) { q1[t] = ld16(rw, oK1, qo[t]); SB(); q2[t] = ld8(rw, oQ2, qo[t]); SB(); }
      LDK(b0, 0);
      float mx[4] = {-3.0e38f, -3.0e38f, -3.0e38f, -3.0e38f};
      if constexpr (PASS) {
#pragma unroll 1
        for (int j = 2; j < 32; ++j) {
          const bf16x8 k1 = ld16(rk, oK1, j * PT); const s16x4 k2 = ld8(rk, oK2, j * PT);
#pragma unroll
          for (int t = 0; t < 4; ++t) {
            const f32x16 sv = qk_b(k2, q2[t], qk_a(k1, q1[t]));
#pragma unroll
            for (int e = 0; e < 16; ++e) mx[t] = fmaxf(mx[t], sv[e]);
          }
        }
      }
      int idx = 0xDDDD;
      f32x16 o0 = {}, o1 = {}, o2 = {}, o3 = {}, s0, s1, s2, s3;
      b3.v0 = bf16x8{}; b3.v1 = bf16x8{};
      LAS char* sp = lds + L_MRG + aw * 6144 + lane * 16; LAS char* gp = lds + L_MRG + (aw ^ 2) * 6144 + lane * 16;
      const int tt = (8 * qc + 4 * qh + (2 ^ (2 * kh))) * 2 + (r >> 4);
      unsigned oa = WS_O + (tt * 16 + pr) * 1024 + ((2 * odd + h) * 16 + c16) * 16, ob = h ? OOB : WS_O2 + (tt * 32 + head) * 128 + c16 * 8;
      volatile LAS f32x4* mw = (volatile LAS f32x4*)(lds + L_M) + aw * 64 + lane; volatile LAS f32x4* mr = (volatile LAS f32x4*)(lds + L_M) + (aw ^ 2) * 64 + r;
      asm volatile("" : "+v"(o0), "+v"(o1), "+v"(o2), "+v"(o3), "+v"(b3.v0), "+v"(b3.v1), "+v"(idx), "+v"(sp), "+v"(gp), "+v"(oa), "+v"(ob), "+v"(mw), "+v"(mr)); SB();
      {
        f32x16 sm[8];
#define SQ(n) ((n) < 4 ? (n) : ((n) - 2) & 3)
#define SMA(n) do { sm[n] = qk_a((n) < 4 ? b1_.k1 : b0.k1, q1[SQ(n)]); if ((n) == 6) s0 = sm[n]; if ((n) == 7) s1 = sm[n]; SB(); } while (0)
#define SMB(n) do { sm[n] = qk_b((n) < 4 ? b1_.k2 : b0.k2, q2[SQ(n)], sm[n]); SB(); } while (0)
        SMA(0); SMB(0); SMA(1); SMB(1);
        FS(1);
        LDV(b0, 0); LDV(b1_, 1); LDK(b2_, 2); LDV(b2_, 2); LDK(b3, 3);
        FS(2);
#pragma unroll
        for (int n = 0; n < 8; ++n) {
          if (n < 6) SMA(n + 2);
          max8<0>(sm[n], mx[SQ(n)]); SB();
          if (n < 6) SMB(n + 2);
          max8<8>(sm[n], mx[SQ(n)]); SB();
        }
      }
      FS(3);
      *mw = f32x4{mx[0], mx[1], mx[2], mx[3]};
      if constexpr (PASS) lds_meet<0>((LAS unsigned*)(lds + L_C + 44), 4, lane);
#pragma unroll
      for (int t = 0; t < 4; ++t) {
        auto sw = __builtin_amdgcn_permlane32_swap(__float_as_uint(mx[t]), __float_as_uint(mx[t]), false, false);
        asm volatile("v_max_f32 %0, %1, %2" : "=v"(mx[t]) : "v"(sw[0]), "v"(sw[1]));
      }
      s3 = o3; asm volatile("" : "+v"(s3));
      f32x4 ma, mb;
#pragma unroll 1
      for (int n = 0; n < (1 << 22); ++n) {
        ma = mr[0]; mb = mr[32];
        if (PASS || !__any(__builtin_isunordered(ma[0], mb[0]) | __builtin_isunordered(ma[1], mb[1]) | __builtin_isunordered(ma[2], mb[2]) | __builtin_isunordered(ma[3], mb[3]))) break;
      }
      FS(4);
#pragma unroll
      for (int t = 0; t < 4; ++t) {
        float nm;
        asm("v_max3_f32 %0, %1, %2, %3\n\tv_sub_f32 %0, %4, %0" : "=&v"(nm) : "v"(mx[t]), "v"(ma[t ^ 2]), "v"(mb[t ^ 2]), "s"(PASS ? 0.f : -MARGIN));
        u32x2 pm = __builtin_bit_cast(u32x2, q2[t]);
        if constexpr (PASS) {
          const float a0 = bhi(__float_as_uint(nm)), r0 = nm - a0, a1 = bhi(__float_as_uint(r0)), a2 = r0 - a1;
          if (h) pm = u32x2{(__float_as_uint(a0) >> 16) | __float_as_uint(a1), __float_as_uint(a2) >> 16};
        } else if (h) pm[0] = __float_as_uint(nm) >> 16;
        q2[t] = __builtin_bit_cast(s16x4, pm);
        if (t == 0) s0 = qk_b(b0.k2, q2[0], s0);
        if (t == 1) s1 = qk_b(b0.k2, q2[1], s1);
        SB();
      }
      if constexpr (!PASS) { STAMP(7); SB(); }
#define KT(C, P, N) do { \
  STEPX(s0, s3, o3, P, s2, C, q1[2], q2[2], P.v0 = ld16(rk, oV0, so), __builtin_amdgcn_s_waitcnt(0x0F79)); \
  STEPX(s1, s0, o0, C, s3, C, q1[3], q2[3], P.v1 = ld16(rk, oV1, so), so += PT; asm volatile("" : "+s"(so))); \
  STEPX(s2, s1, o1, C, s0, N, q1[0], q2[0], C.k1 = ld16(rk, oK1, so), ); \
  STEPX(s3, s2, o2, C, s1, N, q1[1], q2[1], C.k2 = ld8(rk, oK2, so), ); } while (0)
      so = 3 * PT;
#pragma unroll 1
      do { KT(b0, b3, b1_); KT(b1_, b0, b2_); KT(b2_, b1_, b3); KT(b3, b2_, b0); } while (so != 31 * PT);
#define KTE(C, P, N, W) do { STEPX(s0, s3, o3, P, s2, C, q1[2], q2[2], , __builtin_amdgcn_s_waitcnt(W)); STEP(s1, s0, o0, C, s3, C, q1[3], q2[3]); \
  STEP(s2, s1, o1, C, s0, N, q1[0], q2[0]); STEP(s3, s2, o2, C, s1, N, q1[1], q2[1]); } while (0)
#define SEND(O, z, e) do { *(LAS f32x4*)(sp + (z) * 3072 + (e) * 1024) = f32x4{O[4 * (e)], O[4 * (e) + 1], O[4 * (e) + 2], O[4 * (e) + 3]}; SB(); } while (0)
      STEPX(s0, s3, o3, b3, s2, b0, q1[2], q2[2], b3.v0 = ld16(rk, oV0, so), __builtin_amdgcn_s_waitcnt(0x0F79));
      STEPX(s1, s0, o0, b0, s3, b0, q1[3], q2[3], b3.v1 = ld16(rk, oV1, so), ); STEP(s2, s1, o1, b0, s0, b1_, q1[0], q2[0]); STEP(s3, s2, o2, b0, s1, b1_, q1[1], q2[1]);
      KTE(b1_, b0, b2_, 0x0F76); KTE(b2_, b1_, b3, 0x0F72);
      STEPX(s0, s3, o3, b2_, s2, b3, q1[2], q2[2], , __builtin_amdgcn_s_waitcnt(0x0F70)); STEP(s1, s0, o0, b3, s3, b3, q1[3], q2[3]);
      LAS unsigned* mc = (LAS unsigned*)(lds + L_C + 32 + 4 * qh); unsigned one = 1u;
      EX4(s2, 0); SB(); o1 = pv_lo(b3.v0, s1, o1, idx); SB(); asm volatile("" : "+v"(mc), "+v"(one)); EX4(s2, 4); SB(); o1 = pv_hi(b3.v1, s1, o1, idx); SB(); SEND(o0, 0, 0);
      EX4(s2, 8); SB(); o2 = pv_lo(b3.v0, s2, o2, idx); SB(); SEND(o0, 0, 1); EX4(s2, 12); SB(); SEND(o0, 0, 2);
      EX4(s3, 0); SB(); o2 = pv_hi(b3.v1, s2, o2, idx); SB(); SEND(o1, 1, 0); EX4(s3, 4); SB(); SEND(o1, 1, 1); SEND(o1, 1, 2);
      lds_add(mc, one); SB();
      unsigned cv; f32x4 gv[6];
#define GET() do { cv = *(volatile LAS unsigned*)mc; _Pragma("unroll") for (int e = 0; e < 6; ++e) gv[e / 3 * 3 + (e + 2) % 3] = *(volatile LAS f32x4*)(gp + (e / 3 * 3 + (e + 2) % 3) * 1024); } while (0)
      o3 = pv_lo(b3.v0, s3, o3, idx); SB(); EX4(s3, 8); SB(); EX4(s3, 12); SB(); GET(); SB(); o3 = pv_hi(b3.v1, s3, o3, idx); SB();
      asm volatile("" : "+v"(o2), "+v"(o3) : "v"(o0), "v"(o1));
      if constexpr (!PASS) { STAMP(8); SB(); }
      if (__builtin_amdgcn_readfirstlane(cv) < 1024 + 2 + 2 * PASS) {
#pragma unroll 1
        for (int n = 0; n < (1 << 22); ++n) { if (__builtin_amdgcn_readfirstlane(*(volatile LAS unsigned*)mc) >= 1024 + 2 + 2 * PASS) break; }
        GET();
      }
      u32x4 pa[2]; u32x2 pb_[2]; float chk = 0.f, l[2];
#pragma unroll
      for (int z = 0; z < 2; ++z) {
        const f32x16 k = z ? o3 : o2;
        const F4 u2 = add4(gv[3 * z + 2], k[8], k[9], k[10], k[11], chk);
        l[z] = __uint_as_float(__builtin_amdgcn_permlane32_swap(__float_as_uint(u2.v[0]), __float_as_uint(u2.v[0]), false, false)[1]);
        float inv = __builtin_amdgcn_rcpf(l[z]);
        asm volatile("" : "+v"(inv));
        const F4 u0 = add4(gv[3 * z], k[0], k[1], k[2], k[3], chk), u1 = add4(gv[3 * z + 1], k[4], k[5], k[6], k[7], chk);
        const u32x2 x0 = scale4(u0, inv), x1 = scale4(u1, inv);
        pa[z] = u32x4{x0[0], x0[1], x1[0], x1[1]}; pb_[z] = scale4(u2, inv);
      }
      asm volatile("" :: "v"(o3));
      unsigned bad = 1u;
      if constexpr (!PASS) { stored = __all(chk < 1.0e37f && l[0] > 1.0e-30f && l[1] > 1.0e-30f); if (!stored) bad = 0x101u; }
      else {
#pragma unroll
        for (int z = 0; z < 2; ++z) {
#pragma unroll
          for (int j = 0; j < 4; ++j) pa[z][j] = min(pa[z][j], 0xfffffffeu);
          pb_[z][0] = min(pb_[z][0], 0xfffffffeu);
        }
      }
      if (PASS ? !stored : stored) { st16(pa[0], rw, oa, 0); st8(pb_[0], rw, ob, 0); st16(pa[1], rw, oa, 32768); st8(pb_[1], rw, ob, 8192); }
      unsigned cn = lds_add_rtn((LAS unsigned*)(lds + L_C + 24), bad) + bad;
      if constexpr (!PASS) {
        STAMP(9);
#pragma unroll 1
        for (int n = 0; n < (1 << 22); ++n) { if ((cn & 255) >= 4) break; cn = __builtin_amdgcn_readfirstlane(*(volatile LAS unsigned*)(lds + L_C + 24)); }
        asm volatile("" ::: "memory");
      }
      return cn;
    };
    if (__builtin_expect(sweep(IC<0>{}) >= 256, 0)) sweep(IC<1>{});
    const int lane = fresh_lane();
    if (b == 0) {
      await<8>(fl, F_O + aw * 64 + lane, tag);
      lds_meet<2>((LAS unsigned*)(lds + L_C + 28), 4, lane);
      if (w == 4) publish(fl, 0, tag, lane);
    }
  } else {
    const int rt2 = 4 * g + (idx8 & 3), ct2 = idx8 >> 2;
    {
      DRAIN();
      if (lds_add_rtn((LAS unsigned*)(lds + L_C + 16), 1u) == 3) publish(fl, F_H + b, tag, lane);
      const int j = lane & 15, hc = 4 * w + (j & 3), hr = rt2 >> 1;
      const int pb = (j < 4 || j > 12) ? (hr >> 2) + 4 * (hc >> 3) + 8 * ((hr & 3) + 4 * (hc & 7)) : 8 * (j < 10 ? 6 * w + j - 4 : (j < 12 ? 14 + 2 * w + j : w)) + ct2;
#pragma unroll 1
      for (int n = 0; n < (1 << 22); ++n) { if (__builtin_amdgcn_readfirstlane(*(volatile LAS unsigned*)(lds + L_C + 16)) >= 4) break; __builtin_amdgcn_s_sleep(4); }
      await<10>(fl, F_H + pb, tag);
    }
    STAMP(6);
    const int o = (64 * rt2 + 16 * w + c16) * D_ + 80 * ct2;
    u32x4 xv[2], bv[2]; u32x2 xv4, bv4;
    {
      f32x4 acc[4][5];
      bf16x8 f0[11], f1[11];
      unsigned vo = lane * 16, vo4 = vo + 4096, va[4];
#pragma unroll
      for (int m = 0; m < 4; ++m) va[m] = vo + (m ^ w) * 49152;
      unsigned sa = WS_H + (4 * rt2 * 48 + 12 * w) * 1024, sw = WS_W + (ct2 * 68 + 12 * w) * 5120, sq = WS_W + (ct2 * 68 + 48 + 4 * w) * 5120;
      const unsigned sq0 = sq + 20480, sq1 = WS_W + (ct2 * 68 + 64 + w) * 5120;
      LAS char* wl = lds + L_WO + lane * 16; LAS char* wst = wl + ((w - 1 - (g >> 1)) & 3) * 25600;
      asm volatile("" : "+v"(vo), "+v"(vo4), "+v"(va[0]), "+v"(va[1]), "+v"(va[2]), "+v"(va[3]), "+v"(wl), "+v"(wst));
#define GL1(F, j) F[j] = (j) < 4 ? ld16(rw, va[(j) & 3], sa) : ((j) < 8 ? ld16(rw, vo + (((j) - 4) & 3) * 1024, sw) : ((j) < 9 ? ld16(rw, vo4, sw) : ld16(rw, vo + ((j) - 9) * 1024, sq)))
#define GSTEP(FC, FN, WO, LD, Z) do { _Pragma("unroll") for (int n = 0; n < 20; ++n) { \
        acc[n & 3][n >> 2] = __builtin_amdgcn_mfma_f32_16x16x32_bf16(FC[4 + (n >> 2)], FC[n & 3], (Z) ? f32x4{} : acc[n & 3][n >> 2], 0, 0, 0); \
        SB(); \
        if ((LD) && n > 2 && (n & 1) && n < 17) { asm volatile("s_nop 4"); SB(); }         \
        if ((LD) && n < 2) { GL1(FN, n); } else if ((LD) && !(n & 1)) { GL1(FN, (n >> 1) + 1); } \
        if ((LD) && n == 5) { sa += 1024; asm volatile("" : "+s"(sa)); } \
        if ((LD) && n == 15) { sw += 5120; asm volatile("" : "+s"(sw)); } \
        if ((LD) && n == 3) { sq += 2048; if (sq == sq0) sq = sq1; asm volatile("" : "+s"(sq)); } \
        if (n == 17 || n == 19) *(LAS bf16x8*)(wst + (WO) + (n == 19 ? 1024 : 0)) = FC[n == 19 ? 10 : 9]; \
        SB(); } } while (0)
#pragma unroll
      for (int j = 0; j < 11; ++j) { GL1(f0, j); }
      const bf16x8 wlast = ld16(rw, vo4, sq1);
      sa += 1024; sw += 5120;
      SB();
      {
        const int tt = (8 * qc + 4 * (w & 1) + (w & 2)) * 2 + (r >> 4);
        st16(u32x4{~0u, ~0u, ~0u, ~0u}, rw, WS_O + (tt * 16 + pr) * 1024 + ((2 * odd + h) * 16 + c16) * 16, 0);
        st16(u32x4{~0u, ~0u, ~0u, ~0u}, rw, WS_O + (tt * 16 + pr) * 1024 + ((2 * odd + h) * 16 + c16) * 16, 32768);
        st8(u32x2{~0u, ~0u}, rw, WS_O2 + ((tt + 2 * h) * 32 + head) * 128 + c16 * 8, 0);
        __builtin_amdgcn_s_setprio(0); SB();
      }
      GSTEP(f0, f1, 0, true, true);
      __builtin_amdgcn_s_setprio(2);
      DRAIN();
      *(LAS bf16x8*)(wst + 24576) = wlast;
      unsigned mo = (F_O + g * 32 + r) * 128 + h * 4, xo = o * 2 + 16 * kq + (ct2 & 1) * 32, xo4 = o * 2 + 8 * kq + ((ct2 & 1) ? 0 : 128);
      LAS char* r0 = lds + L_RED + w * 5120 + lane * 16;
      asm volatile("" : "+v"(mo), "+v"(xo), "+v"(xo4), "+v"(r0));
      lds_add((LAS unsigned*)(lds + L_C + 32), 256u);
      if ((lds_add_rtn((LAS unsigned*)(lds + L_C + 36), 256u) >> 8) == 3) publish(fl, F_O + qc * 32 + head, tag, lane);
      __builtin_amdgcn_s_setprio(0); SB();
#pragma unroll 1
      for (int s = 1; s < 11; s += 2) {
        if (s == 7) {
          const unsigned lb = lds0 + L_RED + w * 5120;
          asm volatile("s_mov_b32 m0, %0\n\ts_nop 0\n\tbuffer_load_dword %1, %2, 0 offen sc1 lds" :: "s"(lb + 4096), "v"(mo), "s"(mkrs(ws + WS_F, 1u << 17)) : "memory");
          dma16(rx, xo, 0, lb); dma16(rx, xo, 64, lb + 1024); dma16(rx, xo4, 0, lb + 2048); dma16(mkrs(b2 + 80 * ct2, 160), vo, 0, lb + 3072);
        }
        GSTEP(f1, f0, 2048, true, false); GSTEP(f0, f1, 4096, true, false);
        __builtin_amdgcn_s_setprio(2); SB(); wst += 4096; asm volatile("" : "+v"(wst)); SB(); __builtin_amdgcn_s_setprio(0);
      }
      unsigned oo[4], oo2[4];
#pragma unroll
      for (int j = 0; j < 4; ++j) {
        const int gr = (j + 1 + (g >> 1)) & 3;
        oo[j] = WS_O + (4 * rt2 + w) * 16384 + gr * 4096; oo2[j] = WS_O2 + (4 * rt2 + w) * 4096 + gr * 1024; asm volatile("" : "+s"(oo[j]), "+s"(oo2[j]));
      }
      xv[0] = *(volatile LAS u32x4*)r0; xv[1] = *(volatile LAS u32x4*)(r0 + 1024); xv4 = *(volatile LAS u32x2*)(r0 + 2048);
      SB();
      GSTEP(f1, f0, 2048, false, false);
      __builtin_amdgcn_s_setprio(2);
      STAMP(7);
      const int ln = fresh_lane();
      const unsigned gate = *(volatile LAS unsigned*)(lds + L_C + 24), mk = *(volatile LAS unsigned*)(lds + L_RED + w * 5120 + 4096 + ln * 4);
      LAS char* bp = lds + L_RED + w * 5120 + 3072 + (ct2 & 1) * 32 + (ln & 48);
      bv[0] = *(volatile LAS u32x4*)bp; bv[1] = *(volatile LAS u32x4*)(bp + 64); bv4 = *(volatile LAS u32x2*)(bp + 128 - (ct2 & 1) * 160 - ((ln & 48) >> 1));
      const unsigned v8 = (ln >> 4) * 256 + (ln & 15) * 8;
      volatile LAS unsigned* gc = (volatile LAS unsigned*)(lds + L_C + 48);
      asm volatile("" ::: "memory");
#pragma unroll
      for (int t = 0; t < 5; ++t) *(LAS f32x4*)(r0 + t * 1024) = acc[3][t];
      asm volatile("" ::: "memory"); gc[w] = 1;
      if (!__all(mk == (unsigned)(ln >= 32 ? tag >> 32 : tag))) await<2>(fl, F_O + g * 32 + (ln & 31), tag);
#define SPIN(COND) do { _Pragma("unroll 1") for (int n_ = 0; n_ < (1 << 24); ++n_) { if (COND) break; __builtin_amdgcn_s_sleep(1); } asm volatile("" ::: "memory"); } while (0)
      if ((__builtin_amdgcn_readfirstlane(gate) & 255) < 4) SPIN((__builtin_amdgcn_readfirstlane(*(volatile LAS unsigned*)(lds + L_C + 24)) & 255) >= 4);
      STAMP(10);
      u32x4 a[16], al[4];
#define OL16(p, e, C) asm volatile("buffer_load_dwordx4 %0, %1, %2, %3 offen offset:%4 sc1" : C(a[4 * (p) + (e)]) : "v"(vo), "s"(rw), "s"(oo[p]), "n"((e) * 1024) : "memory")
#define OL8_(p, C, R0, R1, R2, R3) asm volatile("buffer_load_dwordx2 v[" #R0 ":" #R1 "], %1, %2, %3 offen sc1\n\tbuffer_load_dwordx2 v[" #R2 ":" #R3 "], %1, %2, %3 offen offset:128 sc1" \
        : C "{v[" #R0 ":" #R3 "]}"(al[p]) : "v"(v8), "s"(rw), "s"(oo2[p]) : "memory")
#define OL8(p, C) do { if ((p) == 0) OL8_(0, C, 224, 225, 226, 227); else if ((p) == 1) OL8_(1, C, 228, 229, 230, 231); \
        else if ((p) == 2) OL8_(2, C, 232, 233, 234, 235); else OL8_(3, C, 236, 237, 238, 239); } while (0)
#define OLG(p, C) do { OL16(p, 0, C "v"); OL16(p, 1, C "v"); OL16(p, 2, C "v"); OL16(p, 3, C "v"); OL8(p, C); } while (0)
      unsigned pg = gc[ln & 3];
      OLG(0, "=&"); OLG(1, "=&");
      LAS char* wp2 = lds + L_WO + 61440 + ln * 16; LAS char* r1_ = lds + L_RED + ln * 16 + (w ^ 1) * 5120; LAS char* r3 = lds + L_RED + ln * 16 + (w ^ 3) * 5120;
      asm volatile("" : "+v"(wp2));
      if (!__all(pg >= 1)) SPIN(__all(gc[ln & 3] >= 1));
      f32x4 y[5], z[5];
#pragma unroll
      for (int t = 0; t < 5; ++t) { z[t] = *(LAS f32x4*)(r3 + t * 1024); *(LAS f32x4*)(r3 + t * 1024) = acc[2][t]; }
      asm volatile("" ::: "memory"); gc[w] = 2; SB();
      constexpr int NW = 28;
      bf16x8 wr[NW];
#pragma unroll
      for (int t = 0; t < 5; ++t) {
        const unsigned x0 = t < 4 ? xv[(t >> 1) & 1][2 * (t & 1)] : xv4[0], x1 = t < 4 ? xv[(t >> 1) & 1][2 * (t & 1) + 1] : xv4[1];
        const unsigned b0 = t < 4 ? bv[(t >> 1) & 1][2 * (t & 1)] : bv4[0], b1v = t < 4 ? bv[(t >> 1) & 1][2 * (t & 1) + 1] : bv4[1];
        y[t] = (acc[0][t] + f32x4{blo(x0) + blo(b0), bhi(x0) + bhi(b0), blo(x1) + blo(b1v), bhi(x1) + bhi(b1v)}) + z[t];
        asm volatile("" : "+v"(y[t]));
      }
      pg = gc[w ^ 2];
#pragma unroll
      for (int i = 0; i < 12; ++i) wr[i] = *(LAS bf16x8*)(wl + i * 1024);
      if (__builtin_amdgcn_readfirstlane(pg) < 2) SPIN(__builtin_amdgcn_readfirstlane(gc[w ^ 2]) >= 2);
      asm volatile("" ::: "memory");
#pragma unroll
      for (int t = 0; t < 5; ++t) { z[t] = *(LAS f32x4*)(r1_ + t * 1024); *(LAS f32x4*)(r1_ + t * 1024) = acc[1][t]; }
      asm volatile("" ::: "memory"); gc[w] = 3; SB();
#pragma unroll
      for (int i = 12; i < NW; ++i) wr[i] = *(LAS bf16x8*)(wl + i * 1024);
      SB();
#define OCHK(p, N) ({ unsigned m_; asm volatile("s_waitcnt vmcnt(" #N ")" ::: "memory"); SB(); \
        asm volatile("" : "+v"(a[4 * (p)]), "+v"(a[4 * (p) + 1]), "+v"(a[4 * (p) + 2]), "+v"(a[4 * (p) + 3]), "+v"(al[p]) :: "memory"); \
        m_ = max(al[p][0], al[p][2]); _Pragma("unroll") for (int e = 0; e < 4; ++e) m_ = max(m_, max(a[4 * (p) + e][0], a[4 * (p) + e][2])); __any(m_ == ~0u); })
#define OGRP(p, N) do { \
        if (OCHK(p, N)) { \
          _Pragma("unroll 1") for (int n = 0; n < (1 << 20); ++n) { OLG(p, "+"); if ((p) < 3) OLG(((p) + 1) & 3, "+"); if (!OCHK(p, N)) break; } } \
        SB(); \
        _Pragma("unroll") for (int e = 0; e < 25; ++e) { const int i = 25 * (p) + e; \
          asm volatile("v_mfma_f32_16x16x32_bf16 %0, %1, %2, %0" : "+v"(y[i % 5]) : "v"(wr[i % NW]), "v"(e < 20 ? a[4 * (p) + (e / 5 & 3)] : al[p])); \
          if ((p) < 2 && e % 4 == 3) { if (e < 16) OL16(((p) + 2) & 3, (e / 4) & 3, "=&v"); else if (e == 19) OL8(((p) + 2) & 3, "=&"); } \
          const int j = NW + (i * 4 + 4) / 5;        \
          if (i % 5 != 4 && j < 100) wr[j % NW] = *(LAS bf16x8*)(j < 60 ? wl + j * 1024 : wp2 + (j - 60) * 1024); \
          if (i == 70) pg = gc[w ^ 1]; \
          if (i >= 90 && i < 95) zz[i - 90] = *(volatile LAS f32x4*)(r0 + (i - 90) * 1024); \
          SB(); } } while (0)
      f32x4 zz[5];
      OGRP(0, 6); OGRP(1, 6); OGRP(2, 6);
      if (__builtin_amdgcn_readfirstlane(pg) < 3) SPIN(__builtin_amdgcn_readfirstlane(gc[w ^ 1]) >= 3);
      OGRP(3, 0);
#pragma unroll
      for (int t = 0; t < 5; ++t) { z[t] += zz[t]; asm volatile("" : "+v"(z[t])); }
      asm volatile("s_nop 7" : "+v"(y[0]), "+v"(y[1]), "+v"(y[2]), "+v"(y[3]), "+v"(y[4]));
      const rsrc_t rout = mkrs(out, 2048 * D_ * 2);
      __builtin_amdgcn_raw_buffer_store_b128(pk8(y[0] + z[0], y[1] + z[1]), rout, xo, 0, OP1);
      __builtin_amdgcn_raw_buffer_store_b128(pk8(y[2] + z[2], y[3] + z[3]), rout, xo + 64, 0, OP1);
      const f32x4 y4 = y[4] + z[4];
      __builtin_amdgcn_raw_buffer_store_b64(u32x2{pk2(y4[0], y4[1]), pk2(y4[2], y4[3])}, rout, xo4, 0, OP2);
    }
    STAMP(11);
  }
}

extern "C" void kernel_launch(void* const* d_in, const int* in_sizes, int n_in, void* d_out, int out_size, void* d_ws, size_t ws_size,
                              hipStream_t stream) {
  fused<<<256, 512, 0, stream>>>((const u16*)d_in[0], (const u16*)d_in[1], (const u16*)d_in[2], (const u16*)d_in[4], (const u16*)d_in[5],
                                 (const u16*)d_in[6], (const u16*)d_in[7], (u16*)d_out, (char*)d_ws);
}
```

```cpp
#include <hip/hip_runtime.h>
#include <hip/hip_bf16.h>

typedef __attribute__((ext_vector_type(8))) __bf16 bf16x8;
typedef __attribute__((ext_vector_type(16))) __bf16 bf16x16;
typedef __attribute__((ext_vector_type(2))) __bf16 bf16x2;
typedef __attribute__((ext_vector_type(4))) short s16x4;
typedef __attribute__((ext_vector_type(16))) float f32x16;
typedef __attribute__((ext_vector_type(8))) float f32x8;
typedef __attribute__((ext_vector_type(4))) float f32x4;
typedef __attribute__((ext_vector_type(4))) unsigned u32x4;
typedef __attribute__((ext_vector_type(2))) unsigned u32x2;
typedef unsigned short u16;
typedef unsigned long long u64;
typedef __amdgpu_buffer_rsrc_t rsrc_t;
#define LAS __attribute__((address_space(3)))
#define GAS __attribute__((address_space(1)))
#define SB() __builtin_amdgcn_sched_barrier(0)
#define RLX_AGENT __ATOMIC_RELAXED, __HIP_MEMORY_SCOPE_AGENT
#define BAR_L() do { asm volatile("" ::: "memory"); __builtin_amdgcn_s_waitcnt(0xC07F); asm volatile("s_barrier" ::: "memory"); } while (0)
#define DRAIN() asm volatile("s_waitcnt vmcnt(0)" ::: "memory")

constexpr int D_ = 640, F_ = 1536;
constexpr unsigned PT = 7808, P_V = 2560, P_Q = 5120, P_ONE = 7680;
constexpr unsigned WS_T = 0;
constexpr unsigned WS_H = 8u << 20;
constexpr unsigned WS_W = 16u << 20;
constexpr unsigned WS_O = 20u << 20;
constexpr unsigned WS_O2 = 22u << 20;
constexpr unsigned WS_S = 23u << 20;
constexpr unsigned WS_F = 24u << 20;
constexpr unsigned WS_END = WS_F;
constexpr unsigned F_Q = 256, F_H = 512, F_O = 768;
constexpr float QSCALE = 0.22360679774997896f * 1.4426950408889634f;
constexpr float MARGIN = 88.f;
constexpr unsigned OOB = 0x80000000u;
constexpr unsigned CODE_SKIP = 4608;
constexpr unsigned XS = 16448, WSL = 16640, L_W = 6 * XS;
constexpr unsigned L_WO = 0, L_RED = 102400, L_MRG = 122880;
constexpr unsigned L_ST = L_W + 3 * WSL;
constexpr unsigned L_G = L_ST + 2048;
constexpr unsigned L_M = L_G + 4096;
constexpr unsigned L_C = L_M + 4096;
constexpr unsigned L_PF = L_C + 64;
constexpr unsigned L_END = L_PF + 256;

#define STAMP(i)
#define STEPSTAMP()

#define FS(v)
__device__ __forceinline__ unsigned pk2(float a, float b) { bf16x2 r = {(__bf16)a, (__bf16)b}; return __builtin_bit_cast(unsigned, r); }
__device__ __forceinline__ u32x4 pk8(f32x4 a, f32x4 b) { return u32x4{pk2(a[0], a[1]), pk2(a[2], a[3]), pk2(b[0], b[1]), pk2(b[2], b[3])}; }
__device__ __forceinline__ float blo(unsigned u) { return __uint_as_float(u << 16); }
__device__ __forceinline__ float bhi(unsigned u) { return __uint_as_float(u & 0xffff0000u); }
__device__ __forceinline__ u64 uni64(u64 v) {
  return ((u64)(unsigned)__builtin_amdgcn_readfirstlane((unsigned)(v >> 32)) << 32) | (unsigned)__builtin_amdgcn_readfirstlane((unsigned)v);
}
__device__ __forceinline__ int fresh_lane() { int l = __builtin_amdgcn_mbcnt_hi(~0u, __builtin_amdgcn_mbcnt_lo(~0u, 0u)); asm volatile("" : "+v"(l)); return l; }
__device__ __forceinline__ rsrc_t mkrs(const void* p, unsigned bytes) { return __builtin_amdgcn_make_buffer_rsrc((void*)p, 0, bytes, 0x00020000); }
__device__ __forceinline__ void dma16(rsrc_t r, unsigned vo, unsigned so, unsigned lds_base) {
  asm volatile("s_mov_b32 m0, %0\n\ts_nop 0\n\tbuffer_load_dwordx4 %1, %2, %3 offen lds" :: "s"(lds_base), "v"(vo), "s"(r), "s"(so) : "memory");
}
__device__ __forceinline__ void touch(rsrc_t r, unsigned vo, unsigned lds_base) {
  asm volatile("s_mov_b32 m0, %0\n\ts_nop 0\n\tbuffer_load_dword %1, %2, 0 offen lds" :: "s"(lds_base), "v"(vo), "s"(r) : "memory");
}
#define STOUCH3(p) asm volatile("s_load_dword s100, %0, 0\n\ts_load_dword s100, %0, 64\n\ts_load_dword s100, %0, 128" :: "s"(p) : "s100")
#define SPFX(x) x
#define STOUCH4(p) asm volatile("s_load_dword s100, %0, 0\n\ts_load_dword s100, %0, 64\n\ts_load_dword s100, %0, 128\n\ts_load_dword s100, %0, 192" :: "s"(p) : "s100")
__device__ __forceinline__ bf16x8 ld16(rsrc_t r, unsigned vo, unsigned so) { return __builtin_bit_cast(bf16x8, __builtin_amdgcn_raw_buffer_load_b128(r, vo, so, 16)); }
__device__ __forceinline__ s16x4 ld8(rsrc_t r, unsigned vo, unsigned so) { return __builtin_bit_cast(s16x4, __builtin_amdgcn_raw_buffer_load_b64(r, vo, so, 16)); }
__device__ __forceinline__ void st16(u32x4 v, rsrc_t r, unsigned vo, unsigned so) { __builtin_amdgcn_raw_buffer_store_b128(v, r, vo + so, 0, 16); }
__device__ __forceinline__ void st8(u32x2 v, rsrc_t r, unsigned vo, unsigned so) { __builtin_amdgcn_raw_buffer_store_b64(v, r, vo + so, 0, 16); }
__device__ __forceinline__ void publish(GAS u64* f, unsigned i, u64 tag, int lane) {
  st16(u32x4{(unsigned)tag, (unsigned)(tag >> 32), (unsigned)tag, (unsigned)(tag >> 32)}, mkrs((const void*)(f + 16 * i), 128), lane * 16, 0);
}
template <int SLP> __device__ __forceinline__ void await(GAS u64* f, unsigned i, u64 tag) {
#pragma unroll 1
  for (int n = 0; n < (1 << 20); ++n) {
    if (__all(__hip_atomic_load(f + 16 * i, RLX_AGENT) == tag)) break;
    __builtin_amdgcn_s_sleep(SLP);
  }
  __builtin_amdgcn_fence(__ATOMIC_ACQUIRE, "wavefront");
}
__device__ __forceinline__ void lds_add(LAS unsigned* c, unsigned v) {
  asm volatile("s_mov_b64 exec, 1\n\tds_add_u32 %0, %1\n\ts_mov_b64 exec, -1" :: "v"(c), "v"(v) : "memory");
}
__device__ __forceinline__ unsigned lds_add_rtn(LAS unsigned* c, unsigned v) {
  unsigned r_;
  asm volatile("s_mov_b64 exec, 1\n\tds_add_rtn_u32 %0, %1, %2\n\ts_mov_b64 exec, -1\n\ts_waitcnt lgkmcnt(0)" : "=&v"(r_) : "v"(c), "v"(v) : "memory");
  return __builtin_amdgcn_readfirstlane(r_);
}
template <int SLP> __device__ __forceinline__ void lds_meet(LAS unsigned* c, unsigned n, int lane) {
  lds_add(c, 1u);
#pragma unroll 1
  for (int k = 0; k < (1 << 22); ++k) {
    if (__builtin_amdgcn_readfirstlane(*(volatile LAS unsigned*)c) >= n) break;
    if (SLP) __builtin_amdgcn_s_sleep(SLP);
  }
  asm volatile("" ::: "memory");
}
__device__ __forceinline__ s16x4 trd(LAS char* p) { return __builtin_amdgcn_ds_read_tr16_b64_v4i16((LAS s16x4*)p); }
__device__ __forceinline__ bf16x8 cat8(s16x4 a, s16x4 b) { return __builtin_bit_cast(bf16x8, __builtin_shufflevector(a, b, 0, 1, 2, 3, 4, 5, 6, 7)); }
template <int C> __device__ __forceinline__ float dppadd(float a) {
  return a + __int_as_float(__builtin_amdgcn_update_dpp(0, __float_as_int(a), C, 0xf, 0xf, true));
}

__device__ __forceinline__ u32x2 sqrelu4(f32x4 a, f32x4 c, f32x4 bb, float r, float t) {
  float v0, v1, v2, v3;
  asm("v_fma_f32 %0, %8, %17, %12\n\tv_fma_f32 %1, %9, %17, %13\n\tv_fma_f32 %2, %10, %17, %14\n\tv_fma_f32 %3, %11, %17, %15\n\t"
      "v_fmac_f32 %0, %4, %16\n\tv_fmac_f32 %1, %5, %16\n\tv_fmac_f32 %2, %6, %16\n\tv_fmac_f32 %3, %7, %16\n\t"
      "v_mul_f32 %0, %0, |%0|\n\tv_mul_f32 %1, %1, |%1|\n\tv_mul_f32 %2, %2, |%2|\n\tv_mul_f32 %3, %3, |%3|\n\t"
      "v_cvt_pk_bf16_f32 %0, %0, %1\n\tv_cvt_pk_bf16_f32 %2, %2, %3\n\tv_pk_max_i16 %0, %0, 0\n\tv_pk_max_i16 %2, %2, 0"
      : "=&v"(v0), "=&v"(v1), "=&v"(v2), "=&v"(v3)
      : "v"(a[0]), "v"(a[1]), "v"(a[2]), "v"(a[3]), "v"(c[0]), "v"(c[1]), "v"(c[2]), "v"(c[3]), "v"(bb[0]), "v"(bb[1]), "v"(bb[2]), "v"(bb[3]), "v"(r), "v"(t));
  return u32x2{__float_as_uint(v0), __float_as_uint(v2)};
}

template <int N> struct IC { static constexpr int v = N; };
template <int MODE> struct G1 {
  static constexpr int NM = MODE == 2 ? 2 : 4, NI = MODE == 2 ? 6 : 4, PAD = MODE == 1 ? 32 : 8, NP = NI / 2;
  static constexpr int NMMA = 2 * NM * NI, NRD = 2 * (NM + 2 * NI), HEAD = MODE == 2 ? 6 : 12, R2H = 3, R2S = 18;
  struct Frag { bf16x8 x[2][NM]; s16x4 w[2][NI][2]; };
  struct Ptr { LAS char* x[2][2]; LAS char* w[NP]; };
  template <int R, int XO = 0, int WO = 0> static __device__ __forceinline__ void rd(Frag& f, const Ptr& p) {
    constexpr int u = R / (NRD / 2), r = R % (NRD / 2);
    if constexpr (r >= 2 && r < 2 + NM) f.x[u][r - 2] = *(LAS bf16x8*)(p.x[u][r & 1] + XO + ((r - 2) >> 1) * 4096 + (r & 1) * (MODE == 1 ? 512 : 2048));
    else { constexpr int q = r < 2 ? r : r - NM, i = q >> 1;
      f.w[u][i][q & 1] = trd(p.w[i >> 1] + WO + (i & 1) * PAD + u * (8192 + 4 * PAD) + (q & 1) * 1024); }
  }
  template <int N> static __device__ __forceinline__ void mma(f32x4 (&acc)[NM][NI], const Frag& f) {
    constexpr int u = N / (NM * NI), i = (N / NM) % NI, m = N % NM;
    const bf16x8 wf = cat8(f.w[u][i][0], f.w[u][i][1]);
    if constexpr (MODE == 1) asm volatile("v_mfma_f32_16x16x32_bf16 %0, %1, %2, %0" : "+v"(acc[m][i]) : "v"(f.x[u][m]), "v"(wf));
    else asm volatile("v_mfma_f32_16x16x32_bf16 %0, %1, %2, %0" : "+v"(acc[m][i]) : "v"(wf), "v"(f.x[u][m]));
  }
  template <int U> static __device__ __forceinline__ void csum(f32x4& cs, const Frag& f, bf16x8 sel, int idx) {
    const bf16x16 wf = __builtin_shufflevector(cat8(f.w[U][0][0], f.w[U][0][1]), cat8(f.w[U][1][0], f.w[U][1][1]), 0, 1, 2, 3, 4, 5, 6, 7, 8, 9, 10, 11, 12, 13, 14, 15);
    asm volatile("v_smfmac_f32_16x16x64_bf16 %0, %1, %2, %3" : "+v"(cs) : "v"(sel), "v"(wf), "v"(idx));
  }
  static __device__ __forceinline__ int span(int w, int j) { return MODE == 2 ? (w + j >= 3 ? w + j - 3 : w + j) : 2 * (w >> 1) + (j ^ (w & 1)); }
  template <int R0, int R1, int XO = 0, int WO = 0> static __device__ __forceinline__ void rds(Frag& f, const Ptr& p) {
    if constexpr (R0 < R1) { rd<R0, XO, WO>(f, p); rds<R0 + 1, R1, XO, WO>(f, p); } }
  template <int N0, int N1> static __device__ __forceinline__ void mmas(f32x4 (&acc)[NM][NI], const Frag& f) { if constexpr (N0 < N1) { mma<N0>(acc, f); mmas<N0 + 1, N1>(acc, f); } }
  template <int N> static __device__ __forceinline__ void mix(f32x4 (&acc)[NM][NI], const Frag& fc, Frag& fn, const Ptr& p) {
    if constexpr (N < NMMA) {
      constexpr int SPAN = NMMA - HEAD - 3, k = N - HEAD;
      constexpr int r0 = k >= SPAN ? NRD : (k * NRD + SPAN - 1) / SPAN, r1 = k + 1 >= SPAN ? NRD : ((k + 1) * NRD + SPAN - 1) / SPAN;
      mma<N>(acc, fc); rds<r0, r1>(fn, p); SB();
      mix<N + 1>(acc, fc, fn, p);
    }
  }
  struct Off { unsigned x, w[NP]; };
  static __device__ __forceinline__ Off offs(int w, int lane) {
    const int kq = lane >> 4, c16 = lane & 15, a = (lane >> 2) & 3;
    Off o;
    o.x = MODE == 1 ? (64 * (w & 1) + 8 * (c16 >> 2) + (c16 & 3)) * 128 + ((kq ^ ((c16 >> 1) & 1) ^ ((c16 >> 3) << 1) ^ (c16 & 4)) << 4)
                    : ((MODE == 2 ? 32 * w : 64 * (w & 1)) + c16) * 128 + ((kq ^ (c16 >> 1)) << 4);
    const unsigned wb = L_W + kq * (2048 + PAD) + a * 256 + (lane & 3) * (MODE == 1 ? 8 : 16);
#pragma unroll
    for (int j = 0; j < NP; ++j) o.w[j] = wb + (span(w, j) ^ a) * 64;
    return o;
  }
  template <int U> static __device__ __forceinline__ void ptrx(Ptr& p, const Off& o, LAS char* lds, unsigned sx) {
    p.x[U][0] = lds + sx + (o.x ^ (64 * U)); p.x[U][1] = lds + sx + (o.x ^ (64 * U + 32)); asm volatile("" : "+v"(p.x[U][0]), "+v"(p.x[U][1])); }
  static __device__ __forceinline__ void ptrw(Ptr& p, const Off& o, LAS char* lds, unsigned sw) {
#pragma unroll
    for (int j = 0; j < NP; ++j) { p.w[j] = lds + (sw + o.w[j]); asm volatile("" : "+v"(p.w[j])); } }
  template <int N, class T> static __device__ __forceinline__ void fin(f32x4 (&acc)[NM][NI], const Frag& fc, T& tail) {
    if constexpr (N < NMMA) { mma<N>(acc, fc); tail(IC<N - HEAD>{}); SB(); fin<N + 1>(acc, fc, tail); }
  }
  template <class T>
  static __device__ __forceinline__ void loop(f32x4 (&acc)[NM][NI], Frag& fa, const Off& o, LAS char* lds, unsigned sx, unsigned sw, int w, int lane, int pub,
                                              GAS u64* fl, unsigned fi, u64 tag, T tail, unsigned& stv) {
    const int kq = lane >> 4, c16 = lane & 15;
    Frag fb; Ptr p;
#pragma unroll
    for (int n = 0; n < NM * NI; ++n) { acc[n % NM][n / NM] = f32x4{}; asm volatile("" : "+v"(acc[n % NM][n / NM])); }
    const unsigned one = (kq >> 1) == ((c16 >> 1) & 1) ? 0x3f803f80u : 0u;
    bf16x8 sel = __builtin_bit_cast(bf16x8, u32x4{one, one, one, one});
    int sidx = (lane & 1) ? 0xEEEE : 0x4444;
    f32x4 cs = {};
    LAS float* cp = (LAS float*)(lds + L_ST + 1024) + 32 * span(w, 0) + (MODE == 1 ? c16 : 8 * (c16 >> 2) + (c16 & 3));
    asm volatile("s_nop 1" : "+v"(sel), "+v"(sidx), "+v"(cs), "+v"(cp));
    if constexpr (MODE != 2) { ptrx<0>(p, o, lds, sx); ptrx<1>(p, o, lds, sx); ptrw(p, o, lds, sw); SB(); tail(IC<-2>{}); STAMP(1); BAR_L(); STAMP(12); rds<0, NRD>(fa, p); }
#define G1_HEAD(FC, PUB, LAST, BAR) do { \
      mma<0>(acc, FC); sx = (sx == 5 * XS) ? 0 : sx + XS; asm volatile("" : "+s"(sx)); SB(); \
      csum<0>(cs, FC, sel, sidx); SB(); \
      mma<1>(acc, FC); sw = (sw == 2 * WSL) ? 0 : sw + WSL; asm volatile("" : "+s"(sw)); SB(); \
      mma<2>(acc, FC); ptrx<0>(p, o, lds, sx); SB(); \
      mma<3>(acc, FC); ptrw(p, o, lds, sw); SB(); \
      mmas<4, HEAD - 2>(acc, FC); SB(); \
      csum<1>(cs, FC, sel, sidx); SB(); \
      mma<HEAD - 2>(acc, FC); ptrx<1>(p, o, lds, sx); SB(); \
      mma<HEAD - 1>(acc, FC); SB(); \
      if (LAST) { asm volatile("s_nop 5" : "+v"(cs)); cp[0] = cs[0] + cs[1]; cp[MODE == 1 ? 16 : 4] = cs[2] + cs[3]; } \
      if (PUB) DRAIN(); \
      if (BAR) BAR_L(); \
      if ((PUB) && w == 0) publish(fl, fi, tag, lane); \
      SB(); } while (0)
    SB();
    G1_HEAD(fa, false, false, true);
#pragma unroll 1
    for (int s = 0; s < 4; ++s) {
      mix<HEAD>(acc, fa, fb, p);
      G1_HEAD(fb, false, false, true);
      mix<HEAD>(acc, fb, fa, p);
      G1_HEAD(fa, false, false, true);
    }
    mix<HEAD>(acc, fa, fb, p);
    G1_HEAD(fb, false, true, true);
    fin<HEAD>(acc, fb, tail);
    asm volatile("s_nop 7");
    SB();
  }
  template <int J, int N> static __device__ __forceinline__ void r2mix(f32x4 (&acc)[NM][NI], f32x4& cs, bf16x8 sel, int sidx, const Frag& fc, Frag& fn, const Ptr& pl, const Ptr& ph) {
    if constexpr (N < NMMA) {
      constexpr int H2 = R2H, SPAN = R2S, k = N - H2, xs = (5 + J) % 6;
      constexpr int r0 = k >= SPAN ? NRD : (k * NRD + SPAN - 1) / SPAN, r1 = k + 1 >= SPAN ? NRD : ((k + 1) * NRD + SPAN - 1) / SPAN;
      mma<N>(acc, fc);
      Ptr p = xs >= 3 ? ph : pl;
#pragma unroll
      for (int j = 0; j < NP; ++j) p.w[j] = J == 8 ? ph.w[j] : pl.w[j];
      rds<r0, r1, (xs % 3) * XS, J == 8 ? 0 : ((2 + J) % 3) * WSL>(fn, p); SB();
      if constexpr (N == 13) { csum<1>(cs, fc, sel, sidx); SB(); }
      r2mix<J, N + 1>(acc, cs, sel, sidx, fc, fn, pl, ph);
    }
  }
  template <int J, class PUB> static __device__ __forceinline__ void r2steps(f32x4 (&acc)[NM][NI], f32x4& cs, bf16x8 sel, int sidx, Frag& fc, Frag& fn, const Ptr& pl, const Ptr& ph, PUB& pub) {
    if constexpr (J < 9) {
      mmas<0, 2>(acc, fc); csum<0>(cs, fc, sel, sidx); mmas<2, R2H>(acc, fc); SB();
      if constexpr (J == 2) DRAIN();
      if constexpr (J < 8) BAR_L();
      if constexpr (J == 2) pub();
      SB();
      r2mix<J, R2H>(acc, cs, sel, sidx, fc, fn, pl, ph);
      r2steps<J + 1>(acc, cs, sel, sidx, fn, fc, pl, ph, pub);
    }
  }
  template <class PUB, class T>
  static __device__ __forceinline__ void loop2(f32x4 (&acc)[NM][NI], Frag& fa, const Off& o, const Ptr& ph, LAS char* lds, int w, int lane, PUB pub, T tail) {
    const int kq = lane >> 4, c16 = lane & 15;
    Frag fb; Ptr pl, ph2 = ph;
    ptrx<0>(pl, o, lds, 0); ptrx<1>(pl, o, lds, 0);
#pragma unroll
    for (int j = 0; j < NP; ++j) pl.w[j] = ph.w[j];
    ptrw(ph2, o, lds, 3 * XS - L_W);
#pragma unroll
    for (int n = 0; n < NM * NI; ++n) { acc[n % NM][n / NM] = f32x4{}; asm volatile("" : "+v"(acc[n % NM][n / NM])); }
    const unsigned one = (kq >> 1) == ((c16 >> 1) & 1) ? 0x3f803f80u : 0u;
    bf16x8 sel = __builtin_bit_cast(bf16x8, u32x4{one, one, one, one});
    int sidx = (lane & 1) ? 0xEEEE : 0x4444;
    f32x4 cs = {};
    LAS float* cp = (LAS float*)(lds + L_ST + 1024) + 32 * span(w, 0) + 8 * (c16 >> 2) + (c16 & 3);
    asm volatile("s_nop 1" : "+v"(sel), "+v"(sidx), "+v"(cs), "+v"(cp));
    SB();
    r2steps<0>(acc, cs, sel, sidx, fa, fb, pl, ph2, pub);
    mmas<0, 2>(acc, fb); csum<0>(cs, fb, sel, sidx); mmas<2, 4>(acc, fb); csum<1>(cs, fb, sel, sidx); mmas<4, HEAD>(acc, fb); SB();
    fin<HEAD>(acc, fb, tail);
    asm volatile("s_nop 7" : "+v"(cs));
    cp[0] = cs[0] + cs[1]; cp[4] = cs[2] + cs[3];
    SB();
  }
};

struct KV { bf16x8 k1; s16x4 k2; bf16x8 v0, v1; };
__device__ __forceinline__ f32x16 qk_a(bf16x8 k, bf16x8 q) { f32x16 z = {}; return __builtin_amdgcn_mfma_f32_32x32x16_bf16(k, q, z, 0, 0, 0); }
__device__ __forceinline__ f32x16 qk_b(s16x4 k, s16x4 q, f32x16 c) { return __builtin_amdgcn_mfma_f32_32x32x8bf16_1k(k, q, c, 0, 0, 0); }
__device__ __forceinline__ f32x16 pv_lo(bf16x8 v, f32x16 p, f32x16 o, int idx) {
  f32x8 h = __builtin_shufflevector(p, p, 0, 1, 2, 3, 4, 5, 6, 7);
  o = __builtin_amdgcn_smfmac_f32_32x32x32_bf16(v, __builtin_bit_cast(bf16x16, h), o, idx, 0, 0);
  asm volatile("" : "+v"(o));
  return o; }
__device__ __forceinline__ f32x16 pv_hi(bf16x8 v, f32x16 p, f32x16 o, int idx) {
  f32x8 h = __builtin_shufflevector(p, p, 8, 9, 10, 11, 12, 13, 14, 15);
  o = __builtin_amdgcn_smfmac_f32_32x32x32_bf16(v, __builtin_bit_cast(bf16x16, h), o, idx, 0, 0);
  asm volatile("" : "+v"(o));
  return o; }
template <int E> __device__ __forceinline__ void max8(const f32x16& s, float& m) {
  asm volatile("v_max3_f32 %0, %0, %1, %2\n\tv_max3_f32 %0, %0, %3, %4\n\tv_max3_f32 %0, %0, %5, %6\n\tv_max3_f32 %0, %0, %7, %8"
               : "+v"(m) : "v"(s[E]), "v"(s[E + 1]), "v"(s[E + 2]), "v"(s[E + 3]), "v"(s[E + 4]), "v"(s[E + 5]), "v"(s[E + 6]), "v"(s[E + 7]));
}
struct F4 { float v[4]; };
__device__ __forceinline__ F4 add4(f32x4 g, float a, float b, float c, float d, float& chk) {
  F4 r;
  asm volatile("v_add_f32 %0, %9, %5\n\tv_add_f32 %1, %10, %6\n\tv_add_f32 %2, %11, %7\n\tv_add_f32 %3, %12, %8\n\t"
               "v_maximum3_f32 %4, %4, |%0|, |%1|\n\tv_maximum3_f32 %4, %4, |%2|, |%3|"
               : "=&v"(r.v[0]), "=&v"(r.v[1]), "=&v"(r.v[2]), "=&v"(r.v[3]), "+v"(chk) : "v"(a), "v"(b), "v"(c), "v"(d), "v"(g[0]), "v"(g[1]), "v"(g[2]), "v"(g[3]));
  return r;
}
__device__ __forceinline__ u32x2 scale4(F4 g, float s) {
  unsigned r0, r1;
  asm volatile("v_mul_f32 %2, %2, %6\n\tv_mul_f32 %3, %3, %6\n\tv_mul_f32 %4, %4, %6\n\tv_mul_f32 %5, %5, %6\n\tv_cvt_pk_bf16_f32 %0, %2, %3\n\tv_cvt_pk_bf16_f32 %1, %4, %5"
               : "=&v"(r0), "=&v"(r1), "+v"(g.v[0]), "+v"(g.v[1]), "+v"(g.v[2]), "+v"(g.v[3]) : "v"(s));
  return u32x2{r0, r1};
}
#define EX4(s, b) do { s[b] = __builtin_amdgcn_exp2f(s[b]); s[b+1] = __builtin_amdgcn_exp2f(s[b+1]); s[b+2] = __builtin_amdgcn_exp2f(s[b+2]); s[b+3] = __builtin_amdgcn_exp2f(s[b+3]); } while (0)
#define STEPX(SC, SP, OP, VP, SN, KN, Q1, Q2, X0, X1) do { \
  EX4(SC, 0); SB(); OP = pv_lo(VP.v0, SP, OP, idx); SB(); X0; SB(); \
  EX4(SC, 4); SB(); SN = qk_a(KN.k1, Q1); SB(); \
  EX4(SC, 8); SB(); OP = pv_hi(VP.v1, SP, OP, idx); SB(); X1; SB(); \
  EX4(SC, 12); SB(); SN = qk_b(KN.k2, Q2, SN); SB(); } while (0)
#define STEP(SC, SP, OP, VP, SN, KN, Q1, Q2) STEPX(SC, SP, OP, VP, SN, KN, Q1, Q2, , )

__global__ void __launch_bounds__(512, 2)
fused(const u16* __restrict__ x, const u16* __restrict__ wqkv, const u16* __restrict__ wo, const u16* __restrict__ w1,
      const u16* __restrict__ b1, const u16* __restrict__ w2, const u16* __restrict__ b2, u16* __restrict__ out, char* __restrict__ ws) {
  __shared__ __attribute__((aligned(16))) char smem[L_END + 256];
  LAS char* lds = (LAS char*)smem;
  const unsigned lds0 = (unsigned)(size_t)lds;
  const int lane = threadIdx.x & 63, w = __builtin_amdgcn_readfirstlane(threadIdx.x >> 6);
  const int b = blockIdx.x, g = b & 7, idx8 = b >> 3;
  asm volatile("" :: "s"(x), "s"(wqkv), "s"(wo), "s"(w1), "s"(b1), "s"(w2), "s"(b2), "s"(out), "s"(ws));
  const int rt = (g & 3) * 4 + (idx8 & 3), ct = (g >> 2) * 8 + (idx8 >> 2);
  const bool vtype = ct >= 10;
  const int kq = lane >> 4, c16 = lane & 15;
  LAS float* st_r = (LAS float*)(lds + L_ST);
  LAS float* st_t = st_r + 128;
  LAS float* st_c = st_r + 256;
  GAS u64* fl = (GAS u64*)(ws + WS_F);
  const rsrc_t rw = mkrs(ws, WS_END), rx = mkrs(x, 2048 * D_ * 2);
  u64 tag = 0;
  const int head = 4 * g + (idx8 >> 3), qc = idx8 & 7;
  const int aw = w - 4, qh = aw & 1, kh = aw >> 1, r = lane & 31, h = lane >> 5, odd = head & 1, pr = head >> 1;
  int hstep = 0; unsigned stv = 0;
  unsigned oK1 = 0, oK2 = 0, oV0 = 0, oV1 = 0, oQ2 = 0;
  auto kv_offsets = [&](int ln) {
    const int r = ln & 31, h = ln >> 5;
    oK1 = odd * 1536 + ln * 16; oK2 = h ? P_ONE : 1024 + r * 16 + odd * 8; oQ2 = h ? OOB : oK2;
    const int dv = r < 16 ? (r & 3) + 4 * (r >> 3) + 8 * ((r >> 2) & 1) : r;
    oV0 = r < 20 ? P_V + odd * 1280 + h * 320 + dv * 16 : (r == 20 ? P_ONE : OOB); oV1 = r < 20 ? oV0 + 640 : oV0;
  };
  unsigned fi = 0;
  STAMP(0);

  if (w >= 4) {
    const int hw = w - 4;
    rsrc_t rwt = mkrs(wqkv, D_ * 3 * D_ * 2);
    unsigned rs = 3 * D_ * 2, pad = vtype ? 32 : 8;
    unsigned wso = ct * 256 + 16 * hw * rs, xso = (rt * 128 + hw * 32) * (D_ * 2);
    const unsigned xv = (lane >> 3) * (D_ * 2) + (((lane & 7) ^ kq) << 4), xv1 = xv ^ 64, xv2 = xv ^ 32, xv3 = xv ^ 96;
    const int wc = c16 ^ (kq << 2);
    unsigned wv = ct < 15 ? kq * rs + (wc << 4) : OOB;
    unsigned nx = lds0 + hw * 4096, nw = lds0 + L_W + hw * 4096;
#define DX(i) dma16(rx, i == 0 ? xv : (i == 1 ? xv1 : (i == 2 ? xv2 : xv3)), xso + i * 8 * (D_ * 2), nx + i * 1024)
#define DW(i) dma16(rwt, wv, wso + 4 * i * rs, nw + i * 1024 + (2 * hw + (i >> 1)) * pad)
#define NEXTX() do { xso += 128; nx = (nx == lds0 + hw * 4096 + 5 * XS) ? nx - 5 * XS : nx + XS; } while (0)
#define NEXTW() do { wso += 64 * rs; nw = (nw == lds0 + L_W + hw * 4096 + 2 * WSL) ? nw - 2 * WSL : nw + WSL; } while (0)
#define HBAR(n) asm volatile("s_waitcnt vmcnt(" #n ")\n\ts_barrier" ::: "memory")
    __builtin_amdgcn_s_setprio(1);
#define DW4() do { DW(0); DW(1); DW(2); DW(3); NEXTW(); } while (0)
#define DX4() do { DX(0); DX(1); DX(2); DX(3); NEXTX(); } while (0)
    const char* pw = (const char*)w1 + 256 * F_ * 2 + (b * 4 + hw) * 192;
    {
      DX(0); DW(0); DX(1); DW(1); DX(2); DW(2); DX(3); DW(3); NEXTW(); NEXTX();
      DX4();
#pragma unroll 1
      for (int s = -1; s < 7; ++s) { DW4(); DX4(); SPFX(STOUCH3(pw)); pw += (s == 4 ? -576 : 64) * F_ * 2; HBAR(12); }
    }
    const unsigned go = WS_S + (rt * 128 + (hw & 1) * 64 + lane) * 16;
    asm volatile("s_mov_b32 m0, %0\n\ts_nop 0\n\tbuffer_load_dwordx4 %1, %2, 0 offen sc1 lds" :: "s"(lds0 + L_G + hw * 1024), "v"(go), "s"(rw) : "memory");
    DW4(); SPFX(STOUCH3(pw)); pw += 64 * F_ * 2; HBAR(9);
    rwt = mkrs(w1, D_ * F_ * 2); rs = F_ * 2; pad = 8; wso = ct * 192 + (256 + 16 * hw) * rs; wv = wc < 12 ? kq * rs + (wc << 4) : OOB;
    xso -= 1280; nx = lds0 + hw * 4096 + 4 * XS; nw = lds0 + L_W + hw * 4096 + WSL;
    DW4();
    tag = uni64(*(LAS u64*)(lds + L_C));
    {
      asm volatile("s_waitcnt vmcnt(8)" ::: "memory");
      u32x4 gr = *(LAS u32x4*)(lds + L_G + hw * 1024 + lane * 16);
#pragma unroll 1
      for (int n = 0; n < (1 << 20); ++n) {
        if (__all(gr[1] == (unsigned)tag && gr[3] == (unsigned)tag)) break;
        asm volatile("buffer_load_dwordx4 %0, %1, %2, 0 offen sc1\n\ts_waitcnt vmcnt(0)" : "=&v"(gr) : "v"(go), "s"(rw) : "memory");
      }
      st_r[hw * 64 + lane] = __uint_as_float(hw < 2 ? gr[0] : gr[2]);
    }
    HBAR(4);
    DW4(); asm volatile("s_waitcnt lgkmcnt(0)" ::: "memory"); SPFX(STOUCH3(pw)); HBAR(4);
    DW4();
    {
      const int c0 = pr * 5 + 2 * odd, e = (lane >> 3) & 1;
      const int tqk = (c0 + 2 * e) >> 4, tv = (head * 20 + 19 * e) >> 7;
      fi = F_Q + (lane < 16 ? 5 + tqk : 10 + tv) * 16 + 8 * kh + (lane & 7);
      if (lane >= 32) fi = F_Q + tqk * 16 + 2 * qc + qh;
      kv_offsets(lane);
      asm volatile("" : "+v"(fi), "+v"(oK1), "+v"(oK2), "+v"(oV0), "+v"(oV1), "+v"(oQ2));
    }
    HBAR(4);
    DW4(); DX4(); HBAR(8);
    DW4(); DX4(); HBAR(12);
    DW4(); DX4(); wso -= 640 * rs; HBAR(12);
    DW4(); DX4(); HBAR(12);
    DW4(); HBAR(8);
    DW4(); nw = lds0 + 3 * XS + hw * 4096; DW4(); HBAR(8);
    asm volatile("s_mov_b32 m0, %0\n\ts_nop 0\n\tbuffer_load_dwordx4 %1, %2, 0 offen sc1 lds" :: "s"(lds0 + L_G + hw * 1024), "v"(fi * 128), "s"(mkrs(ws + WS_F, 1u << 17)) : "memory");
  } else {
    const int wm = w & 1, wn = w >> 1;
    const int row = rt * 128 + ct * 8 + 2 * w + (lane >> 5);
    {
      const unsigned lb = lds0 + 5 * XS + w * 4096, xo = row * (D_ * 2) + r * 16;
      dma16(rx, xo, 0, lb); dma16(rx, xo, 512, lb + 1024); dma16(rx, r < 16 ? xo : OOB, 1024, lb + 2048);
      asm volatile("s_mov_b32 m0, %0\n\ts_nop 0\n\tbuffer_load_dword %1, %2, 0 offen sc1 lds" :: "s"(lb + 3072), "v"(lane * 4), "s"(mkrs(ws + WS_F, 8)) : "memory");
      u64 pc; asm volatile("s_getpc_b64 %0" : "=s"(pc));
      STOUCH4((pc & ~(u64)255) + CODE_SKIP + (idx8 * 4 + w) * 256u);
      *(LAS u32x4*)(lds + L_M + threadIdx.x * 16) = u32x4{0x7fc00000u, 0x7fc00000u, 0x7fc00000u, 0x7fc00000u};
    }
    unsigned vo[4];
    if (vtype) {
#pragma unroll
      for (int i = 0; i < 4; ++i) {
        const int fv = (ct - 10) * 128 + 32 * G1<1>::span(w, i >> 1) + 16 * (i & 1) + c16, hd = fv / 20, d = fv - hd * 20;
        vo[i] = (WS_T + ((hd >> 1) * 64 + 4 * rt + 2 * wm) * PT + P_V + (hd & 1) * 1280 + kq * 320 + d * 16) | (ct < 15 ? 0u : OOB);
      }
    } else {
#pragma unroll
      for (int pp = 0; pp < 2; ++pp) {
        const int c = (ct < 5 ? ct : ct - 5) * 16 + 4 * G1<0>::span(w, pp) + kq, p5 = c / 5, pos = c - p5 * 5;
        const unsigned tb = WS_T + (p5 * 64 + 4 * rt + 2 * wm) * PT;
        vo[pp] = tb + pos * 512 + (ct < 5 ? P_Q : 0u) + c16 * 16;
        vo[2 + pp] = (ct >= 5 && pos == 2) ? tb + P_ONE + (c16 >> 3) * PT + (c16 & 7) * 16 : OOB;
      }
    }
    asm volatile("" : "+v"(vo[0]), "+v"(vo[1]), "+v"(vo[2]), "+v"(vo[3]));
    if (w < 2) touch(w ? mkrs(b1 + ct * 96, 192) : mkrs(wqkv + 94 * 3 * D_, 546 * 3 * D_ * 2), w ? lane * 4 : (b * 64 + lane) * 128, lds0 + (w ? L_ST + 1536 : L_PF));
    u32x4 xr[3]; u64 e0;
    auto stats_ld = [&]() {
      if (w < 2) asm volatile("s_waitcnt vmcnt(1)" ::: "memory"); else DRAIN();
#pragma unroll
      for (int i = 0; i < 3; ++i) xr[i] = *(LAS u32x4*)(lds + 5 * XS + w * 4096 + i * 1024 + lane * 16);
      e0 = *(LAS u64*)(lds + 5 * XS + w * 4096 + 3072);
    };
    auto stats = [&]() {
      float p = 0.f, q = 0.f;
#pragma unroll
      for (int i = 0; i < 12; ++i) {
        const unsigned u2 = xr[i >> 2][i & 3];
        const bf16x2 v2 = __builtin_bit_cast(bf16x2, u2);
        p = __builtin_amdgcn_fdot2_f32_bf16(v2, __builtin_bit_cast(bf16x2, 0x3f803f80u), p, false); q = __builtin_amdgcn_fdot2_f32_bf16(v2, v2, q, false);
      }
      p = dppadd<0x140>(dppadd<0x141>(dppadd<0x4E>(dppadd<0xB1>(p)))); q = dppadd<0x140>(dppadd<0x141>(dppadd<0x4E>(dppadd<0xB1>(q))));
      p = dppadd<0x142>(p); q = dppadd<0x142>(q);
      const float mu = p * (1.f / D_), rstd = __builtin_amdgcn_rsqf(fmaxf(q * (1.f / D_) - mu * mu, 0.f) + 1e-5f);
      tag = uni64(e0 + 1);
      if (threadIdx.x == 0) { *(LAS u64*)(lds + L_C) = tag; *(LAS u32x4*)(lds + L_C + 16) = u32x4{0, 0, 0, 0}; *(LAS u32x4*)(lds + L_C + 32) = u32x4{0, 0, 0, 0}; *(LAS u32x4*)(lds + L_C + 48) = u32x4{0, 0, 0, 0}; }
      st16(u32x4{__float_as_uint(rstd), (unsigned)tag, __float_as_uint(-mu * rstd), (unsigned)tag}, rw, r == 16 ? row * 16 : OOB, WS_S);
    };
    G1<2>::Frag f2; G1<2>::Ptr p2;
    const G1<2>::Off o2 = G1<2>::offs(w, lane);
    G1<2>::ptrx<0>(p2, o2, lds, 3 * XS); G1<2>::ptrx<1>(p2, o2, lds, 3 * XS); G1<2>::ptrw(p2, o2, lds, 0);
    {
      f32x4 acc[4][4];
      if (vtype) {
        f32x4 r4[4], t4[4]; float c1[4];
        G1<1>::Frag f1;
        LAS float* pr4 = st_r + 64 * wm + 8 * kq; LAS float* pc0 = st_c + 32 * G1<1>::span(w, 0) + c16; LAS float* pc1 = st_c + 32 * G1<1>::span(w, 1) + c16;
        asm volatile("" : "+v"(pr4), "+v"(pc0), "+v"(pc1));
        G1<1>::loop(acc, f1, G1<1>::offs(w, lane), lds, 0, 0, w, lane, -1, fl, 0, tag, [&](auto k) {
          constexpr int K = decltype(k)::v;
          if constexpr (K < 0) { stats_ld(); stats(); }
          else if constexpr (K < 4) { r4[K] = *(LAS f32x4*)(pr4 + 32 * (K >> 1) + 4 * (K & 1)); t4[K] = *(LAS f32x4*)(pr4 + 128 + 32 * (K >> 1) + 4 * (K & 1)); }
          else if constexpr (K < 6) { c1[2 * K - 8] = (K == 4 ? pc0 : pc1)[0]; c1[2 * K - 7] = (K == 4 ? pc0 : pc1)[16]; }
          else G1<2>::rds<2 * K - 12, 2 * K - 10, XS, WSL>(f2, p2);
        }, stv);
        STAMP(2);
#pragma unroll
        for (int i = 0; i < 4; ++i)
#pragma unroll
          for (int a = 0; a < 2; ++a)
            st16(pk8(acc[2 * a][i] * r4[2 * a] + t4[2 * a] * c1[i], acc[2 * a + 1][i] * r4[2 * a + 1] + t4[2 * a + 1] * c1[i]), rw, vo[i], a * PT);
      } else {
        float r1[4], t1[4]; f32x4 c4[4];
        G1<0>::Frag f0;
        LAS float* pr1 = st_r + 64 * wm + c16; LAS float* pc0 = st_c + 32 * G1<0>::span(w, 0) + 8 * kq; LAS float* pc1 = st_c + 32 * G1<0>::span(w, 1) + 8 * kq;
        asm volatile("" : "+v"(pr1), "+v"(pc0), "+v"(pc1));
        G1<0>::loop(acc, f0, G1<0>::offs(w, lane), lds, 0, 0, w, lane, -1, fl, 0, tag, [&](auto k) {
          constexpr int K = decltype(k)::v;
          if constexpr (K < 0) { stats_ld(); stats(); }
          else if constexpr (K < 4) { r1[K] = pr1[16 * K]; t1[K] = pr1[128 + 16 * K]; }
          else if constexpr (K < 6) { c4[2 * K - 8] = *(LAS f32x4*)(K == 4 ? pc0 : pc1); c4[2 * K - 7] = *(LAS f32x4*)((K == 4 ? pc0 : pc1) + 4); }
          else G1<2>::rds<2 * K - 12, 2 * K - 10, XS, WSL>(f2, p2);
        }, stv);
        STAMP(2);
        const float sc = ct < 5 ? QSCALE : 1.f;
#pragma unroll
        for (int pp = 0; pp < 2; ++pp)
#pragma unroll
          for (int m = 0; m < 4; ++m)
            st16(pk8(acc[m][2 * pp] * (r1[m] * sc) + c4[2 * pp] * (t1[m] * sc), acc[m][2 * pp + 1] * (r1[m] * sc) + c4[2 * pp + 1] * (t1[m] * sc)), rw,
                 vo[pp], (m & 1) * 256 + (m >> 1) * PT);
        st16(u32x4{0x3f803f80u, 0x3f803f80u, 0x3f803f80u, 0x3f803f80u}, rw, min(vo[2], vo[3]), 0);
      }
    }
    STAMP(3);
    f32x4 acc[2][6];
    float r1[2], t1[2]; f32x4 c4[6]; u32x4 bb[3];
    LAS float* pr1 = st_r + 32 * w + c16;
    G1<2>::loop2(acc, f2, o2, p2, lds, w, lane, [&]() { if (w == 0) publish(fl, F_Q + ct * 16 + rt, tag, lane); }, [&](auto k) {
      constexpr int K = decltype(k)::v;
      if constexpr (K < 2) { r1[K] = pr1[16 * K]; t1[K] = pr1[128 + 16 * K]; }
      else if constexpr (K < 5) bb[K - 2] = *(LAS u32x4*)(st_c + 128 + 16 * G1<2>::span(w, K - 2) + 4 * kq);
    });
    STAMP(4);
    __builtin_amdgcn_s_setprio(2);
    lds_meet<2>((LAS unsigned*)(lds + L_C + 20), 4, lane);
#pragma unroll
    for (int pp = 0; pp < 3; ++pp) { LAS float* pc = st_c + 32 * G1<2>::span(w, pp) + 8 * kq; c4[2 * pp] = *(LAS f32x4*)pc; c4[2 * pp + 1] = *(LAS f32x4*)(pc + 4); }
    const bool ret = w < 2 || (w == 2 && idx8 < 4);
    const int slab = w < 2 ? 2 * idx8 + w : 64 + idx8;
    u32x4 hq[6];
#pragma unroll
    for (int pp = 0; pp < 3; ++pp) {
      const f32x4 ba = {blo(bb[pp][0]), bhi(bb[pp][0]), blo(bb[pp][1]), bhi(bb[pp][1])}, bc = {blo(bb[pp][2]), bhi(bb[pp][2]), blo(bb[pp][3]), bhi(bb[pp][3])};
#pragma unroll
      for (int m = 0; m < 2; ++m) {
        const u32x2 lo = sqrelu4(acc[m][2 * pp], c4[2 * pp], ba, r1[m], t1[m]), hi = sqrelu4(acc[m][2 * pp + 1], c4[2 * pp + 1], bc, r1[m], t1[m]);
        hq[2 * pp + m] = u32x4{lo[0], lo[1], hi[0], hi[1]};
      }
    }
    asm volatile("" : "+v"(hq[0]), "+v"(hq[1]), "+v"(hq[2]), "+v"(hq[3]), "+v"(hq[4]), "+v"(hq[5]));
#define HST(i0, i1) do { _Pragma("unroll") for (int i = i0; i < i1; ++i) { st16(hq[i], rw, lane * 16, WS_H + ((8 * rt + 2 * w + (i & 1)) * 48 + 3 * ct + G1<2>::span(w, i >> 1)) * 1024); SB(); __builtin_amdgcn_s_sleep(4); SB(); } } while (0)
    FS(13); HST(0, 6); FS(14);
    if (ret) {
      const int s2 = slab - 48;
      const rsrc_t rr = mkrs((slab < 48 ? w2 : wo) + 80 * g, ((slab < 48 ? F_ : D_) * D_ - 80 * g) * 2);
      const unsigned r0 = slab < 48 ? slab * 32 : (s2 < 16 ? s2 * 40 : (s2 - 16) * 160 + 16), rsh = s2 < 16 ? 4 : 2, rmu = slab < 48 ? 0 : (s2 < 16 ? 4 : 16);
#pragma unroll
      for (int i = 0; i < 5; ++i) {
        const unsigned c = 64 * i + lane, k = (c * 205) >> 11;
        dma16(rr, (k + (k >> rsh) * rmu) * (D_ * 2) + (c - k * 10) * 16, r0 * (D_ * 2), lds0 + L_RED + w * 5120 + i * 1024);
      }
      const unsigned tro = (8 * kq + ((lane >> 2) & 3)) * 160 + (lane & 3) * 16 + (g & 1) * 32, tr4 = tro - (lane & 3) * 8 + 128 - (g & 1) * 160;
      DRAIN(); FS(15);
#pragma unroll
      for (int t = 0; t < 5; ++t) {
        LAS char* p = lds + L_RED + w * 5120 + (t < 4 ? tro + (t >> 1) * 64 + (t & 1) * 8 : tr4);
        st16(__builtin_bit_cast(u32x4, cat8(trd(p), trd(p + 640))), rw, lane * 16 + WS_W + (g * 68 + slab) * 5120, t * 1024); SB();
      }
    }
    STAMP(5);
  }

  if (w >= 4) {
    const rsrc_t rk = mkrs(ws + WS_T + (pr * 64 + kh * 32) * PT, 32 * PT);
    unsigned qo[4];
#pragma unroll
    for (int t = 0; t < 4; ++t) { qo[t] = WS_T + (pr * 64 + 8 * qc + 4 * qh + (t ^ (2 * kh))) * PT + P_Q; asm volatile("" : "+s"(qo[t])); }
    asm volatile("" :: "s"(rk));
    HBAR(1);
    DRAIN();
    if (!__all(*(volatile LAS u64*)(lds + L_G + aw * 1024 + (oK1 & 1023)) == tag)) await<2>(fl, fi, tag);
    __builtin_amdgcn_fence(__ATOMIC_ACQUIRE, "wavefront");
    STAMP(6);
    bool stored = false;
    auto sweep = [&](auto pass_) -> unsigned {
      constexpr int PASS = decltype(pass_)::v;
      const int lane = fresh_lane(), r = lane & 31, h = lane >> 5, c16 = lane & 15;
      if constexpr (PASS) kv_offsets(lane);
      bf16x8 q1[4]; s16x4 q2[4];
      KV b0, b1_, b2_, b3;
      unsigned so = 0;
#define LDK(B, d) do { B.k1 = ld16(rk, oK1, so + (d) * PT); SB(); B.k2 = ld8(rk, oK2, so + (d) * PT); SB(); } while (0)
#define LDV(B, d) do { B.v0 = ld16(rk, oV0, so + (d) * PT); SB(); B.v1 = ld16(rk, oV1, so + (d) * PT); SB(); } while (0)
      SB();
      LDK(b1_, 1);
#pragma unroll
      for (int t = 0; t < 4; ++t) { q1[t] = ld16(rw, oK1, qo[t]); SB(); q2[t] = ld8(rw, oQ2, qo[t]); SB(); }
      LDK(b0, 0);
      float mx[4] = {-3.0e38f, -3.0e38f, -3.0e38f, -3.0e38f};
      if constexpr (PASS) {
#pragma unroll 1
        for (int j = 2; j < 32; ++j) {
          const bf16x8 k1 = ld16(rk, oK1, j * PT); const s16x4 k2 = ld8(rk, oK2, j * PT);
#pragma unroll
          for (int t = 0; t < 4; ++t) {
            const f32x16 sv = qk_b(k2, q2[t], qk_a(k1, q1[t]));
#pragma unroll
            for (int e = 0; e < 16; ++e) mx[t] = fmaxf(mx[t], sv[e]);
          }
        }
      }
      int idx = 0xDDDD;
      f32x16 o0 = {}, o1 = {}, o2 = {}, o3 = {}, s0, s1, s2, s3;
      b3.v0 = bf16x8{}; b3.v1 = bf16x8{};
      LAS char* sp = lds + L_MRG + aw * 6144 + lane * 16; LAS char* gp = lds + L_MRG + (aw ^ 2) * 6144 + lane * 16;
      const int tt = (8 * qc + 4 * qh + (2 ^ (2 * kh))) * 2 + (r >> 4);
      unsigned oa = WS_O + (tt * 16 + pr) * 1024 + ((2 * odd + h) * 16 + c16) * 16, ob = h ? OOB : WS_O2 + (tt * 32 + head) * 128 + c16 * 8;
      volatile LAS f32x4* mw = (volatile LAS f32x4*)(lds + L_M) + aw * 64 + lane; volatile LAS f32x4* mr = (volatile LAS f32x4*)(lds + L_M) + (aw ^ 2) * 64 + r;
      asm volatile("" : "+v"(o0), "+v"(o1), "+v"(o2), "+v"(o3), "+v"(b3.v0), "+v"(b3.v1), "+v"(idx), "+v"(sp), "+v"(gp), "+v"(oa), "+v"(ob), "+v"(mw), "+v"(mr)); SB();
      {
        f32x16 sm[8];
#define SQ(n) ((n) < 4 ? (n) : ((n) - 2) & 3)
#define SMA(n) do { sm[n] = qk_a((n) < 4 ? b1_.k1 : b0.k1, q1[SQ(n)]); if ((n) == 6) s0 = sm[n]; if ((n) == 7) s1 = sm[n]; SB(); } while (0)
#define SMB(n) do { sm[n] = qk_b((n) < 4 ? b1_.k2 : b0.k2, q2[SQ(n)], sm[n]); SB(); } while (0)
        SMA(0); SMB(0); SMA(1); SMB(1);
        FS(1);
        LDV(b0, 0); LDV(b1_, 1); LDK(b2_, 2); LDV(b2_, 2); LDK(b3, 3);
        FS(2);
#pragma unroll
        for (int n = 0; n < 8; ++n) {
          if (n < 6) SMA(n + 2);
          max8<0>(sm[n], mx[SQ(n)]); SB();
          if (n < 6) SMB(n + 2);
          max8<8>(sm[n], mx[SQ(n)]); SB();
        }
      }
      FS(3);
      *mw = f32x4{mx[0], mx[1], mx[2], mx[3]};
      if constexpr (PASS) lds_meet<0>((LAS unsigned*)(lds + L_C + 44), 4, lane);
#pragma unroll
      for (int t = 0; t < 4; ++t) {
        auto sw = __builtin_amdgcn_permlane32_swap(__float_as_uint(mx[t]), __float_as_uint(mx[t]), false, false);
        asm volatile("v_max_f32 %0, %1, %2" : "=v"(mx[t]) : "v"(sw[0]), "v"(sw[1]));
      }
      s3 = o3; asm volatile("" : "+v"(s3));
      f32x4 ma, mb;
#pragma unroll 1
      for (int n = 0; n < (1 << 22); ++n) {
        ma = mr[0]; mb = mr[32];
        if (PASS || !__any(__builtin_isunordered(ma[0], mb[0]) | __builtin_isunordered(ma[1], mb[1]) | __builtin_isunordered(ma[2], mb[2]) | __builtin_isunordered(ma[3], mb[3]))) break;
      }
      FS(4);
#pragma unroll
      for (int t = 0; t < 4; ++t) {
        float nm;
        asm("v_max3_f32 %0, %1, %2, %3\n\tv_sub_f32 %0, %4, %0" : "=&v"(nm) : "v"(mx[t]), "v"(ma[t ^ 2]), "v"(mb[t ^ 2]), "s"(PASS ? 0.f : -MARGIN));
        u32x2 pm = __builtin_bit_cast(u32x2, q2[t]);
        if constexpr (PASS) {
          const float a0 = bhi(__float_as_uint(nm)), r0 = nm - a0, a1 = bhi(__float_as_uint(r0)), a2 = r0 - a1;
          if (h) pm = u32x2{(__float_as_uint(a0) >> 16) | __float_as_uint(a1), __float_as_uint(a2) >> 16};
        } else if (h) pm[0] = __float_as_uint(nm) >> 16;
        q2[t] = __builtin_bit_cast(s16x4, pm);
        if (t == 0) s0 = qk_b(b0.k2, q2[0], s0);
        if (t == 1) s1 = qk_b(b0.k2, q2[1], s1);
        SB();
      }
      if constexpr (!PASS) { STAMP(7); SB(); }
#define KT(C, P, N) do { \
  STEPX(s0, s3, o3, P, s2, C, q1[2], q2[2], P.v0 = ld16(rk, oV0, so), __builtin_amdgcn_s_waitcnt(0x0F79)); \
  STEPX(s1, s0, o0, C, s3, C, q1[3], q2[3], P.v1 = ld16(rk, oV1, so), so += PT; asm volatile("" : "+s"(so))); \
  STEPX(s2, s1, o1, C, s0, N, q1[0], q2[0], C.k1 = ld16(rk, oK1, so), ); \
  STEPX(s3, s2, o2, C, s1, N, q1[1], q2[1], C.k2 = ld8(rk, oK2, so), ); } while (0)
      so = 3 * PT;
#pragma unroll 1
      do { KT(b0, b3, b1_); KT(b1_, b0, b2_); KT(b2_, b1_, b3); KT(b3, b2_, b0); } while (so != 31 * PT);
#define KTE(C, P, N, W) do { STEPX(s0, s3, o3, P, s2, C, q1[2], q2[2], , __builtin_amdgcn_s_waitcnt(W)); STEP(s1, s0, o0, C, s3, C, q1[3], q2[3]); \
  STEP(s2, s1, o1, C, s0, N, q1[0], q2[0]); STEP(s3, s2, o2, C, s1, N, q1[1], q2[1]); } while (0)
#define SEND(O, z, e) do { *(LAS f32x4*)(sp + (z) * 3072 + (e) * 1024) = f32x4{O[4 * (e)], O[4 * (e) + 1], O[4 * (e) + 2], O[4 * (e) + 3]}; SB(); } while (0)
      STEPX(s0, s3, o3, b3, s2, b0, q1[2], q2[2], b3.v0 = ld16(rk, oV0, so), __builtin_amdgcn_s_waitcnt(0x0F79));
      STEPX(s1, s0, o0, b0, s3, b0, q1[3], q2[3], b3.v1 = ld16(rk, oV1, so), ); STEP(s2, s1, o1, b0, s0, b1_, q1[0], q2[0]); STEP(s3, s2, o2, b0, s1, b1_, q1[1], q2[1]);
      KTE(b1_, b0, b2_, 0x0F76); KTE(b2_, b1_, b3, 0x0F72);
      STEPX(s0, s3, o3, b2_, s2, b3, q1[2], q2[2], , __builtin_amdgcn_s_waitcnt(0x0F70)); STEP(s1, s0, o0, b3, s3, b3, q1[3], q2[3]);
      LAS unsigned* mc = (LAS unsigned*)(lds + L_C + 32 + 4 * qh); unsigned one = 1u;
      EX4(s2, 0); SB(); o1 = pv_lo(b3.v0, s1, o1, idx); SB(); asm volatile("" : "+v"(mc), "+v"(one)); EX4(s2, 4); SB(); o1 = pv_hi(b3.v1, s1, o1, idx); SB(); SEND(o0, 0, 0);
      EX4(s2, 8); SB(); o2 = pv_lo(b3.v0, s2, o2, idx); SB(); SEND(o0, 0, 1); EX4(s2, 12); SB(); SEND(o0, 0, 2);
      EX4(s3, 0); SB(); o2 = pv_hi(b3.v1, s2, o2, idx); SB(); SEND(o1, 1, 0); EX4(s3, 4); SB(); SEND(o1, 1, 1); SEND(o1, 1, 2);
      lds_add(mc, one); SB();
      unsigned cv; f32x4 gv[6];
#define GET() do { cv = *(volatile LAS unsigned*)mc; _Pragma("unroll") for (int e = 0; e < 6; ++e) gv[e / 3 * 3 + (e + 2) % 3] = *(volatile LAS f32x4*)(gp + (e / 3 * 3 + (e + 2) % 3) * 1024); } while (0)
      o3 = pv_lo(b3.v0, s3, o3, idx); SB(); EX4(s3, 8); SB(); EX4(s3, 12); SB(); GET(); SB(); o3 = pv_hi(b3.v1, s3, o3, idx); SB();
      asm volatile("" : "+v"(o2), "+v"(o3) : "v"(o0), "v"(o1));
      if constexpr (!PASS) { STAMP(8); SB(); }
      if (__builtin_amdgcn_readfirstlane(cv) < 1024 + 2 + 2 * PASS) {
#pragma unroll 1
        for (int n = 0; n < (1 << 22); ++n) { if (__builtin_amdgcn_readfirstlane(*(volatile LAS unsigned*)mc) >= 1024 + 2 + 2 * PASS) break; }
        GET();
      }
      u32x4 pa[2]; u32x2 pb_[2]; float chk = 0.f, l[2];
#pragma unroll
      for (int z = 0; z < 2; ++z) {
        const f32x16 k = z ? o3 : o2;
        const F4 u2 = add4(gv[3 * z + 2], k[8], k[9], k[10], k[11], chk);
        l[z] = __uint_as_float(__builtin_amdgcn_permlane32_swap(__float_as_uint(u2.v[0]), __float_as_uint(u2.v[0]), false, false)[1]);
        float inv = __builtin_amdgcn_rcpf(l[z]);
        asm volatile("" : "+v"(inv));
        const F4 u0 = add4(gv[3 * z], k[0], k[1], k[2], k[3], chk), u1 = add4(gv[3 * z + 1], k[4], k[5], k[6], k[7], chk);
        const u32x2 x0 = scale4(u0, inv), x1 = scale4(u1, inv);
        pa[z] = u32x4{x0[0], x0[1], x1[0], x1[1]}; pb_[z] = scale4(u2, inv);
      }
      asm volatile("" :: "v"(o3));
      unsigned bad = 1u;
      if constexpr (!PASS) { stored = __all(chk < 1.0e37f && l[0] > 1.0e-30f && l[1] > 1.0e-30f); if (!stored) bad = 0x101u; }
      else {
#pragma unroll
        for (int z = 0; z < 2; ++z) {
#pragma unroll
          for (int j = 0; j < 4; ++j) pa[z][j] = min(pa[z][j], 0xfffffffeu);
          pb_[z][0] = min(pb_[z][0], 0xfffffffeu);
        }
      }
      if (PASS ? !stored : stored) { st16(pa[0], rw, oa, 0); st8(pb_[0], rw, ob, 0); st16(pa[1], rw, oa, 32768); st8(pb_[1], rw, ob, 8192); }
      unsigned cn = lds_add_rtn((LAS unsigned*)(lds + L_C + 24), bad) + bad;
      if constexpr (!PASS) {
        STAMP(9);
#pragma unroll 1
        for (int n = 0; n < (1 << 22); ++n) { if ((cn & 255) >= 4) break; cn = __builtin_amdgcn_readfirstlane(*(volatile LAS unsigned*)(lds + L_C + 24)); }
        asm volatile("" ::: "memory");
      }
      return cn;
    };
    if (__builtin_expect(sweep(IC<0>{}) >= 256, 0)) sweep(IC<1>{});
    const int lane = fresh_lane();
    if (b == 0) {
      await<8>(fl, F_O + aw * 64 + lane, tag);
      lds_meet<2>((LAS unsigned*)(lds + L_C + 28), 4, lane);
      if (w == 4) publish(fl, 0, tag, lane);
    }
  } else {
    const int rt2 = 4 * g + (idx8 & 3), ct2 = idx8 >> 2;
    {
      DRAIN();
      if (lds_add_rtn((LAS unsigned*)(lds + L_C + 16), 1u) == 3) publish(fl, F_H + b, tag, lane);
      const int j = lane & 15, hc = 4 * w + (j & 3), hr = rt2 >> 1;
      const int pb = (j < 4 || j > 12) ? (hr >> 2) + 4 * (hc >> 3) + 8 * ((hr & 3) + 4 * (hc & 7)) : 8 * (j < 10 ? 6 * w + j - 4 : (j < 12 ? 14 + 2 * w + j : w)) + ct2;
#pragma unroll 1
      for (int n = 0; n < (1 << 22); ++n) { if (__builtin_amdgcn_readfirstlane(*(volatile LAS unsigned*)(lds + L_C + 16)) >= 4) break; __builtin_amdgcn_s_sleep(4); }
      await<10>(fl, F_H + pb, tag);
    }
    STAMP(6);
    const int o = (64 * rt2 + 16 * w + c16) * D_ + 80 * ct2;
    u32x4 xv[2], bv[2]; u32x2 xv4, bv4;
    {
      f32x4 acc[4][5];
      bf16x8 f0[11], f1[11];
      unsigned vo = lane * 16, vo4 = vo + 4096, va[4];
#pragma unroll
      for (int m = 0; m < 4; ++m) va[m] = vo + (m ^ w) * 49152;
      unsigned sa = WS_H + (4 * rt2 * 48 + 12 * w) * 1024, sw = WS_W + (ct2 * 68 + 12 * w) * 5120, sq = WS_W + (ct2 * 68 + 48 + 4 * w) * 5120;
      const unsigned sq0 = sq + 20480, sq1 = WS_W + (ct2 * 68 + 64 + w) * 5120;
      LAS char* wl = lds + L_WO + lane * 16; LAS char* wst = wl + ((w - 1 - (g >> 1)) & 3) * 25600;
      asm volatile("" : "+v"(vo), "+v"(vo4), "+v"(va[0]), "+v"(va[1]), "+v"(va[2]), "+v"(va[3]), "+v"(wl), "+v"(wst));
#define GL1(F, j) F[j] = (j) < 4 ? ld16(rw, va[(j) & 3], sa) : ((j) < 8 ? ld16(rw, vo + (((j) - 4) & 3) * 1024, sw) : ((j) < 9 ? ld16(rw, vo4, sw) : ld16(rw, vo + ((j) - 9) * 1024, sq)))
#define GSTEP(FC, FN, WO, LD, Z) do { _Pragma("unroll") for (int n = 0; n < 20; ++n) { \
        acc[n & 3][n >> 2] = __builtin_amdgcn_mfma_f32_16x16x32_bf16(FC[4 + (n >> 2)], FC[n & 3], (Z) ? f32x4{} : acc[n & 3][n >> 2], 0, 0, 0); \
        SB(); \
        if ((LD) && n > 2 && (n & 1) && n < 17) { asm volatile("s_nop 4"); SB(); }         \
        if ((LD) && n < 2) { GL1(FN, n); } else if ((LD) && !(n & 1)) { GL1(FN, (n >> 1) + 1); } \
        if ((LD) && n == 5) { sa += 1024; asm volatile("" : "+s"(sa)); } \
        if ((LD) && n == 15) { sw += 5120; asm volatile("" : "+s"(sw)); } \
        if ((LD) && n == 3) { sq += 2048; if (sq == sq0) sq = sq1; asm volatile("" : "+s"(sq)); } \
        if (n == 17 || n == 19) *(LAS bf16x8*)(wst + (WO) + (n == 19 ? 1024 : 0)) = FC[n == 19 ? 10 : 9]; \
        SB(); } } while (0)
#pragma unroll
      for (int j = 0; j < 11; ++j) { GL1(f0, j); }
      const bf16x8 wlast = ld16(rw, vo4, sq1);
      sa += 1024; sw += 5120;
      SB();
      {
        const int tt = (8 * qc + 4 * (w & 1) + (w & 2)) * 2 + (r >> 4);
        st16(u32x4{~0u, ~0u, ~0u, ~0u}, rw, WS_O + (tt * 16 + pr) * 1024 + ((2 * odd + h) * 16 + c16) * 16, 0);
        st16(u32x4{~0u, ~0u, ~0u, ~0u}, rw, WS_O + (tt * 16 + pr) * 1024 + ((2 * odd + h) * 16 + c16) * 16, 32768);
        st8(u32x2{~0u, ~0u}, rw, WS_O2 + ((tt + 2 * h) * 32 + head) * 128 + c16 * 8, 0);
        __builtin_amdgcn_s_setprio(0); SB();
      }
      GSTEP(f0, f1, 0, true, true);
      __builtin_amdgcn_s_setprio(2);
      DRAIN();
      *(LAS bf16x8*)(wst + 24576) = wlast;
      unsigned mo = (F_O + g * 32 + r) * 128 + h * 4, xo = o * 2 + 16 * kq + (ct2 & 1) * 32, xo4 = o * 2 + 8 * kq + ((ct2 & 1) ? 0 : 128);
      LAS char* r0 = lds + L_RED + w * 5120 + lane * 16;
      asm volatile("" : "+v"(mo), "+v"(xo), "+v"(xo4), "+v"(r0));
      lds_add((LAS unsigned*)(lds + L_C + 32), 256u);
      if ((lds_add_rtn((LAS unsigned*)(lds + L_C + 36), 256u) >> 8) == 3) publish(fl, F_O + qc * 32 + head, tag, lane);
      __builtin_amdgcn_s_setprio(0); SB();
#pragma unroll 1
      for (int s = 1; s < 11; s += 2) {
        if (s == 7) {
          const unsigned lb = lds0 + L_RED + w * 5120;
          asm volatile("s_mov_b32 m0, %0\n\ts_nop 0\n\tbuffer_load_dword %1, %2, 0 offen sc1 lds" :: "s"(lb + 4096), "v"(mo), "s"(mkrs(ws + WS_F, 1u << 17)) : "memory");
          dma16(rx, xo, 0, lb); dma16(rx, xo, 64, lb + 1024); dma16(rx, xo4, 0, lb + 2048); dma16(mkrs(b2 + 80 * ct2, 160), vo, 0, lb + 3072);
        }
        GSTEP(f1, f0, 2048, true, false); GSTEP(f0, f1, 4096, true, false);
        __builtin_amdgcn_s_setprio(2); SB(); wst += 4096; asm volatile("" : "+v"(wst)); SB(); __builtin_amdgcn_s_setprio(0);
      }
      unsigned oo[4], oo2[4];
#pragma unroll
      for (int j = 0; j < 4; ++j) {
        const int gr = (j + 1 + (g >> 1)) & 3;
        oo[j] = WS_O + (4 * rt2 + w) * 16384 + gr * 4096; oo2[j] = WS_O2 + (4 * rt2 + w) * 4096 + gr * 1024; asm volatile("" : "+s"(oo[j]), "+s"(oo2[j]));
      }
      xv[0] = *(volatile LAS u32x4*)r0; xv[1] = *(volatile LAS u32x4*)(r0 + 1024); xv4 = *(volatile LAS u32x2*)(r0 + 2048);
      SB();
      GSTEP(f1, f0, 2048, false, false);
      __builtin_amdgcn_s_setprio(2);
      STAMP(7);
      const int ln = fresh_lane();
      const unsigned gate = *(volatile LAS unsigned*)(lds + L_C + 24), mk = *(volatile LAS unsigned*)(lds + L_RED + w * 5120 + 4096 + ln * 4);
      LAS char* bp = lds + L_RED + w * 5120 + 3072 + (ct2 & 1) * 32 + (ln & 48);
      bv[0] = *(volatile LAS u32x4*)bp; bv[1] = *(volatile LAS u32x4*)(bp + 64); bv4 = *(volatile LAS u32x2*)(bp + 128 - (ct2 & 1) * 160 - ((ln & 48) >> 1));
      const unsigned v8 = (ln >> 4) * 256 + (ln & 15) * 8;
      volatile LAS unsigned* gc = (volatile LAS unsigned*)(lds + L_C + 48);
      asm volatile("" ::: "memory");
#pragma unroll
      for (int t = 0; t < 5; ++t) *(LAS f32x4*)(r0 + t * 1024) = acc[3][t];
      asm volatile("" ::: "memory"); gc[w] = 1;
      if (!__all(mk == (unsigned)(ln >= 32 ? tag >> 32 : tag))) await<2>(fl, F_O + g * 32 + (ln & 31), tag);
#define SPIN(COND) do { _Pragma("unroll 1") for (int n_ = 0; n_ < (1 << 24); ++n_) { if (COND) break; __builtin_amdgcn_s_sleep(1); } asm volatile("" ::: "memory"); } while (0)
      if ((__builtin_amdgcn_readfirstlane(gate) & 255) < 4) SPIN((__builtin_amdgcn_readfirstlane(*(volatile LAS unsigned*)(lds + L_C + 24)) & 255) >= 4);
      STAMP(10);
      u32x4 a[16], al[4];
#define OL16(p, e, C) asm volatile("buffer_load_dwordx4 %0, %1, %2, %3 offen offset:%4 sc1" : C(a[4 * (p) + (e)]) : "v"(vo), "s"(rw), "s"(oo[p]), "n"((e) * 1024) : "memory")
#define OL8_(p, C, R0, R1, R2, R3) asm volatile("buffer_load_dwordx2 v[" #R0 ":" #R1 "], %1, %2, %3 offen sc1\n\tbuffer_load_dwordx2 v[" #R2 ":" #R3 "], %1, %2, %3 offen offset:128 sc1" \
        : C "{v[" #R0 ":" #R3 "]}"(al[p]) : "v"(v8), "s"(rw), "s"(oo2[p]) : "memory")
#define OL8(p, C) do { if ((p) == 0) OL8_(0, C, 224, 225, 226, 227); else if ((p) == 1) OL8_(1, C, 228, 229, 230, 231); \
        else if ((p) == 2) OL8_(2, C, 232, 233, 234, 235); else OL8_(3, C, 236, 237, 238, 239); } while (0)
#define OLG(p, C) do { OL16(p, 0, C "v"); OL16(p, 1, C "v"); OL16(p, 2, C "v"); OL16(p, 3, C "v"); OL8(p, C); } while (0)
      unsigned pg = gc[ln & 3];
      OLG(0, "=&"); OLG(1, "=&");
      LAS char* wp2 = lds + L_WO + 61440 + ln * 16; LAS char* r1_ = lds + L_RED + ln * 16 + (w ^ 1) * 5120; LAS char* r3 = lds + L_RED + ln * 16 + (w ^ 3) * 5120;
      asm volatile("" : "+v"(wp2));
      if (!__all(pg >= 1)) SPIN(__all(gc[ln & 3] >= 1));
      f32x4 y[5], z[5];
#pragma unroll
      for (int t = 0; t < 5; ++t) { z[t] = *(LAS f32x4*)(r3 + t * 1024); *(LAS f32x4*)(r3 + t * 1024) = acc[2][t]; }
      asm volatile("" ::: "memory"); gc[w] = 2; SB();
      constexpr int NW = 28;
      bf16x8 wr[NW];
#pragma unroll
      for (int t = 0; t < 5; ++t) {
        const unsigned x0 = t < 4 ? xv[(t >> 1) & 1][2 * (t & 1)] : xv4[0], x1 = t < 4 ? xv[(t >> 1) & 1][2 * (t & 1) + 1] : xv4[1];
        const unsigned b0 = t < 4 ? bv[(t >> 1) & 1][2 * (t & 1)] : bv4[0], b1v = t < 4 ? bv[(t >> 1) & 1][2 * (t & 1) + 1] : bv4[1];
        y[t] = (acc[0][t] + f32x4{blo(x0) + blo(b0), bhi(x0) + bhi(b0), blo(x1) + blo(b1v), bhi(x1) + bhi(b1v)}) + z[t];
        asm volatile("" : "+v"(y[t]));
      }
      pg = gc[w ^ 2];
#pragma unroll
      for (int i = 0; i < 12; ++i) wr[i] = *(LAS bf16x8*)(wl + i * 1024);
      if (__builtin_amdgcn_readfirstlane(pg) < 2) SPIN(__builtin_amdgcn_readfirstlane(gc[w ^ 2]) >= 2);
      asm volatile("" ::: "memory");
#pragma unroll
      for (int t = 0; t < 5; ++t) { z[t] = *(LAS f32x4*)(r1_ + t * 1024); *(LAS f32x4*)(r1_ + t * 1024) = acc[1][t]; }
      asm volatile("" ::: "memory"); gc[w] = 3; SB();
#pragma unroll
      for (int i = 12; i < NW; ++i) wr[i] = *(LAS bf16x8*)(wl + i * 1024);
      SB();
#define OCHK(p, N) ({ unsigned m_; asm volatile("s_waitcnt vmcnt(" #N ")" ::: "memory"); SB(); \
        asm volatile("" : "+v"(a[4 * (p)]), "+v"(a[4 * (p) + 1]), "+v"(a[4 * (p) + 2]), "+v"(a[4 * (p) + 3]), "+v"(al[p]) :: "memory"); \
        m_ = max(al[p][0], al[p][2]); _Pragma("unroll") for (int e = 0; e < 4; ++e) m_ = max(m_, max(a[4 * (p) + e][0], a[4 * (p) + e][2])); __any(m_ == ~0u); })
#define OGRP(p, N) do { \
        if (OCHK(p, N)) { \
          _Pragma("unroll 1") for (int n = 0; n < (1 << 20); ++n) { OLG(p, "+"); if ((p) < 3) OLG(((p) + 1) & 3, "+"); if (!OCHK(p, N)) break; } } \
        SB(); \
        _Pragma("unroll") for (int e = 0; e < 25; ++e) { const int i = 25 * (p) + e; \
          asm volatile("v_mfma_f32_16x16x32_bf16 %0, %1, %2, %0" : "+v"(y[i % 5]) : "v"(wr[i % NW]), "v"(e < 20 ? a[4 * (p) + (e / 5 & 3)] : al[p])); \
          if ((p) < 2 && e % 4 == 3) { if (e < 16) OL16(((p) + 2) & 3, (e / 4) & 3, "=&v"); else if (e == 19) OL8(((p) + 2) & 3, "=&"); } \
          const int j = NW + (i * 4 + 4) / 5;        \
          if (i % 5 != 4 && j < 100) wr[j % NW] = *(LAS bf16x8*)(j < 60 ? wl + j * 1024 : wp2 + (j - 60) * 1024); \
          if (i == 70) pg = gc[w ^ 1]; \
          if (i >= 90 && i < 95) zz[i - 90] = *(volatile LAS f32x4*)(r0 + (i - 90) * 1024); \
          SB(); } } while (0)
      f32x4 zz[5];
      OGRP(0, 6); OGRP(1, 6); OGRP(2, 6);
      if (__builtin_amdgcn_readfirstlane(pg) < 3) SPIN(__builtin_amdgcn_readfirstlane(gc[w ^ 1]) >= 3);
      OGRP(3, 0);
#pragma unroll
      for (int t = 0; t < 5; ++t) { z[t] += zz[t]; asm volatile("" : "+v"(z[t])); }
      asm volatile("s_nop 7" : "+v"(y[0]), "+v"(y[1]), "+v"(y[2]), "+v"(y[3]), "+v"(y[4]));
      const rsrc_t rout = mkrs(out, 2048 * D_ * 2);
      const f32x4 y4 = y[4] + z[4];
      __builtin_amdgcn_raw_buffer_store_b64(u32x2{pk2(y4[0], y4[1]), pk2(y4[2], y4[3])}, rout, xo4, 0, 16);
      __builtin_amdgcn_raw_buffer_store_b128(pk8(y[0] + z[0], y[1] + z[1]), rout, xo, 0, 16);
      __builtin_amdgcn_raw_buffer_store_b128(pk8(y[2] + z[2], y[3] + z[3]), rout, xo + 64, 0, 16);
    }
    STAMP(11);
  }
}

extern "C" void kernel_launch(void* const* d_in, const int* in_sizes, int n_in, void* d_out, int out_size, void* d_ws, size_t ws_size,
                              hipStream_t stream) {
  fused<<<256, 512, 0, stream>>>((const u16*)d_in[0], (const u16*)d_in[1], (const u16*)d_in[2], (const u16*)d_in[4], (const u16*)d_in[5],
                                 (const u16*)d_in[6], (const u16*)d_in[7], (u16*)d_out, (char*)d_ws);
}
```

```cpp
#define CP1 1
#define CPFO (idx8 * 8 + w)
#define SM 0
#include <hip/hip_runtime.h>
#include <hip/hip_bf16.h>

typedef __attribute__((ext_vector_type(8))) __bf16 bf16x8;
typedef __attribute__((ext_vector_type(16))) __bf16 bf16x16;
typedef __attribute__((ext_vector_type(2))) __bf16 bf16x2;
typedef __attribute__((ext_vector_type(4))) short s16x4;
typedef __attribute__((ext_vector_type(16))) float f32x16;
typedef __attribute__((ext_vector_type(8))) float f32x8;
typedef __attribute__((ext_vector_type(4))) float f32x4;
typedef __attribute__((ext_vector_type(4))) unsigned u32x4;
typedef __attribute__((ext_vector_type(2))) unsigned u32x2;
typedef unsigned short u16;
typedef unsigned long long u64;
typedef __amdgpu_buffer_rsrc_t rsrc_t;
#define LAS __attribute__((address_space(3)))
#define GAS __attribute__((address_space(1)))
#define SB() __builtin_amdgcn_sched_barrier(0)
#define RLX_AGENT __ATOMIC_RELAXED, __HIP_MEMORY_SCOPE_AGENT
#define BAR_L() do { asm volatile("" ::: "memory"); __builtin_amdgcn_s_waitcnt(0xC07F); asm volatile("s_barrier" ::: "memory"); } while (0)
#define DRAIN() asm volatile("s_waitcnt vmcnt(0)" ::: "memory")

constexpr int D_ = 640, F_ = 1536;
constexpr unsigned PT = 7808, P_V = 2560, P_Q = 5120, P_ONE = 7680;
constexpr unsigned WS_T = 0;
constexpr unsigned WS_H = 8u << 20;
constexpr unsigned WS_W = 16u << 20;
constexpr unsigned WS_O = 20u << 20;
constexpr unsigned WS_O2 = 22u << 20;
constexpr unsigned WS_S = 23u << 20;
constexpr unsigned WS_F = 24u << 20;
constexpr unsigned WS_END = WS_F;
constexpr unsigned F_Q = 256, F_H = 512, F_O = 768;
constexpr float QSCALE = 0.22360679774997896f * 1.4426950408889634f;
constexpr float MARGIN = 88.f;
constexpr unsigned OOB = 0x80000000u;
constexpr unsigned CODE_SPAN = 36864;
constexpr unsigned XS = 16448, WSL = 16640, L_W = 6 * XS;
constexpr unsigned L_WO = 0, L_RED = 102400, L_MRG = 122880;
constexpr unsigned L_ST = L_W + 3 * WSL;
constexpr unsigned L_G = L_ST + 2048;
constexpr unsigned L_M = L_G + 4096;
constexpr unsigned L_C = L_M + 4096;
constexpr unsigned L_PF = L_C + 64;
constexpr unsigned L_END = L_PF + 256;

#define STAMP(i)
#define STEPSTAMP()

#define FS(v)
__device__ __forceinline__ unsigned pk2(float a, float b) { bf16x2 r = {(__bf16)a, (__bf16)b}; return __builtin_bit_cast(unsigned, r); }
__device__ __forceinline__ u32x4 pk8(f32x4 a, f32x4 b) { return u32x4{pk2(a[0], a[1]), pk2(a[2], a[3]), pk2(b[0], b[1]), pk2(b[2], b[3])}; }
__device__ __forceinline__ float blo(unsigned u) { return __uint_as_float(u << 16); }
__device__ __forceinline__ float bhi(unsigned u) { return __uint_as_float(u & 0xffff0000u); }
__device__ __forceinline__ u64 uni64(u64 v) {
  return ((u64)(unsigned)__builtin_amdgcn_readfirstlane((unsigned)(v >> 32)) << 32) | (unsigned)__builtin_amdgcn_readfirstlane((unsigned)v);
}
__device__ __forceinline__ int fresh_lane() { int l = __builtin_amdgcn_mbcnt_hi(~0u, __builtin_amdgcn_mbcnt_lo(~0u, 0u)); asm volatile("" : "+v"(l)); return l; }
__device__ __forceinline__ rsrc_t mkrs(const void* p, unsigned bytes) { return __builtin_amdgcn_make_buffer_rsrc((void*)p, 0, bytes, 0x00020000); }
__device__ __forceinline__ void dma16(rsrc_t r, unsigned vo, unsigned so, unsigned lds_base) {
  asm volatile("s_mov_b32 m0, %0\n\ts_nop 0\n\tbuffer_load_dwordx4 %1, %2, %3 offen lds" :: "s"(lds_base), "v"(vo), "s"(r), "s"(so) : "memory");
}
__device__ __forceinline__ void touch(rsrc_t r, unsigned vo, unsigned lds_base) {
  asm volatile("s_mov_b32 m0, %0\n\ts_nop 0\n\tbuffer_load_dword %1, %2, 0 offen lds" :: "s"(lds_base), "v"(vo), "s"(r) : "memory");
}
#define STOUCH3(p) asm volatile("s_load_dword s100, %0, 0\n\ts_load_dword s100, %0, 64\n\ts_load_dword s100, %0, 128" :: "s"(p) : "s100")
#define SPFX(x) x
#define STOUCH4(p) asm volatile("s_load_dword s100, %0, 0\n\ts_load_dword s100, %0, 64\n\ts_load_dword s100, %0, 128\n\ts_load_dword s100, %0, 192" :: "s"(p) : "s100")
__device__ __forceinline__ bf16x8 ld16(rsrc_t r, unsigned vo, unsigned so) { return __builtin_bit_cast(bf16x8, __builtin_amdgcn_raw_buffer_load_b128(r, vo, so, 16)); }
__device__ __forceinline__ s16x4 ld8(rsrc_t r, unsigned vo, unsigned so) { return __builtin_bit_cast(s16x4, __builtin_amdgcn_raw_buffer_load_b64(r, vo, so, 16)); }
__device__ __forceinline__ void st16(u32x4 v, rsrc_t r, unsigned vo, unsigned so) { __builtin_amdgcn_raw_buffer_store_b128(v, r, vo + so, 0, 16); }
__device__ __forceinline__ void st8(u32x2 v, rsrc_t r, unsigned vo, unsigned so) { __builtin_amdgcn_raw_buffer_store_b64(v, r, vo + so, 0, 16); }
__device__ __forceinline__ void publish(GAS u64* f, unsigned i, u64 tag, int lane) {
  st16(u32x4{(unsigned)tag, (unsigned)(tag >> 32), (unsigned)tag, (unsigned)(tag >> 32)}, mkrs((const void*)(f + 16 * i), 128), lane * 16, 0);
}
template <int SLP> __device__ __forceinline__ void await(GAS u64* f, unsigned i, u64 tag) {
#pragma unroll 1
  for (int n = 0; n < (1 << 20); ++n) {
    if (__all(__hip_atomic_load(f + 16 * i, RLX_AGENT) == tag)) break;
    __builtin_amdgcn_s_sleep(SLP);
  }
  __builtin_amdgcn_fence(__ATOMIC_ACQUIRE, "wavefront");
}
__device__ __forceinline__ void lds_add(LAS unsigned* c, unsigned v) {
  asm volatile("s_mov_b64 exec, 1\n\tds_add_u32 %0, %1\n\ts_mov_b64 exec, -1" :: "v"(c), "v"(v) : "memory");
}
__device__ __forceinline__ unsigned lds_add_rtn(LAS unsigned* c, unsigned v) {
  unsigned r_;
  asm volatile("s_mov_b64 exec, 1\n\tds_add_rtn_u32 %0, %1, %2\n\ts_mov_b64 exec, -1\n\ts_waitcnt lgkmcnt(0)" : "=&v"(r_) : "v"(c), "v"(v) : "memory");
  return __builtin_amdgcn_readfirstlane(r_);
}
template <int SLP> __device__ __forceinline__ void lds_meet(LAS unsigned* c, unsigned n, int lane) {
  lds_add(c, 1u);
#pragma unroll 1
  for (int k = 0; k < (1 << 22); ++k) {
    if (__builtin_amdgcn_readfirstlane(*(volatile LAS unsigned*)c) >= n) break;
    if (SLP) __builtin_amdgcn_s_sleep(SLP);
  }
  asm volatile("" ::: "memory");
}
__device__ __forceinline__ s16x4 trd(LAS char* p) { return __builtin_amdgcn_ds_read_tr16_b64_v4i16((LAS s16x4*)p); }
__device__ __forceinline__ bf16x8 cat8(s16x4 a, s16x4 b) { return __builtin_bit_cast(bf16x8, __builtin_shufflevector(a, b, 0, 1, 2, 3, 4, 5, 6, 7)); }
template <int C> __device__ __forceinline__ float dppadd(float a) {
  return a + __int_as_float(__builtin_amdgcn_update_dpp(0, __float_as_int(a), C, 0xf, 0xf, true));
}

__device__ __forceinline__ u32x2 sqrelu4(f32x4 a, f32x4 c, f32x4 bb, float r, float t) {
  float v0, v1, v2, v3;
  asm("v_fma_f32 %0, %8, %17, %12\n\tv_fma_f32 %1, %9, %17, %13\n\tv_fma_f32 %2, %10, %17, %14\n\tv_fma_f32 %3, %11, %17, %15\n\t"
      "v_fmac_f32 %0, %4, %16\n\tv_fmac_f32 %1, %5, %16\n\tv_fmac_f32 %2, %6, %16\n\tv_fmac_f32 %3, %7, %16\n\t"
      "v_mul_f32 %0, %0, |%0|\n\tv_mul_f32 %1, %1, |%1|\n\tv_mul_f32 %2, %2, |%2|\n\tv_mul_f32 %3, %3, |%3|\n\t"
      "v_cvt_pk_bf16_f32 %0, %0, %1\n\tv_cvt_pk_bf16_f32 %2, %2, %3\n\tv_pk_max_i16 %0, %0, 0\n\tv_pk_max_i16 %2, %2, 0"
      : "=&v"(v0), "=&v"(v1), "=&v"(v2), "=&v"(v3)
      : "v"(a[0]), "v"(a[1]), "v"(a[2]), "v"(a[3]), "v"(c[0]), "v"(c[1]), "v"(c[2]), "v"(c[3]), "v"(bb[0]), "v"(bb[1]), "v"(bb[2]), "v"(bb[3]), "v"(r), "v"(t));
  return u32x2{__float_as_uint(v0), __float_as_uint(v2)};
}

template <int N> struct IC { static constexpr int v = N; };
template <int MODE> struct G1 {
  static constexpr int NM = MODE == 2 ? 2 : 4, NI = MODE == 2 ? 6 : 4, PAD = MODE == 1 ? 32 : 8, NP = NI / 2;
  static constexpr int NMMA = 2 * NM * NI, NRD = 2 * (NM + 2 * NI), HEAD = MODE == 2 ? 6 : 12, R2H = 3, R2S = 18;
  struct Frag { bf16x8 x[2][NM]; s16x4 w[2][NI][2]; };
  struct Ptr { LAS char* x[2][2]; LAS char* w[NP]; };
  template <int R, int XO = 0, int WO = 0> static __device__ __forceinline__ void rd(Frag& f, const Ptr& p) {
    constexpr int u = R / (NRD / 2), r = R % (NRD / 2);
    if constexpr (r >= 2 && r < 2 + NM) f.x[u][r - 2] = *(LAS bf16x8*)(p.x[u][r & 1] + XO + ((r - 2) >> 1) * 4096 + (r & 1) * (MODE == 1 ? 512 : 2048));
    else { constexpr int q = r < 2 ? r : r - NM, i = q >> 1;
      f.w[u][i][q & 1] = trd(p.w[i >> 1] + WO + (i & 1) * PAD + u * (8192 + 4 * PAD) + (q & 1) * 1024); }
  }
  template <int N> static __device__ __forceinline__ void mma(f32x4 (&acc)[NM][NI], const Frag& f) {
    constexpr int u = N / (NM * NI), i = (N / NM) % NI, m = N % NM;
    const bf16x8 wf = cat8(f.w[u][i][0], f.w[u][i][1]);
    if constexpr (MODE == 1) asm volatile("v_mfma_f32_16x16x32_bf16 %0, %1, %2, %0" : "+v"(acc[m][i]) : "v"(f.x[u][m]), "v"(wf));
    else asm volatile("v_mfma_f32_16x16x32_bf16 %0, %1, %2, %0" : "+v"(acc[m][i]) : "v"(wf), "v"(f.x[u][m]));
  }
  template <int U> static __device__ __forceinline__ void csum(f32x4& cs, const Frag& f, bf16x8 sel, int idx) {
    const bf16x16 wf = __builtin_shufflevector(cat8(f.w[U][0][0], f.w[U][0][1]), cat8(f.w[U][1][0], f.w[U][1][1]), 0, 1, 2, 3, 4, 5, 6, 7, 8, 9, 10, 11, 12, 13, 14, 15);
    asm volatile("v_smfmac_f32_16x16x64_bf16 %0, %1, %2, %3" : "+v"(cs) : "v"(sel), "v"(wf), "v"(idx));
  }
  static __device__ __forceinline__ int span(int w, int j) { return MODE == 2 ? (w + j >= 3 ? w + j - 3 : w + j) : 2 * (w >> 1) + (j ^ (w & 1)); }
  template <int R0, int R1, int XO = 0, int WO = 0> static __device__ __forceinline__ void rds(Frag& f, const Ptr& p) {
    if constexpr (R0 < R1) { rd<R0, XO, WO>(f, p); rds<R0 + 1, R1, XO, WO>(f, p); } }
  template <int N0, int N1> static __device__ __forceinline__ void mmas(f32x4 (&acc)[NM][NI], const Frag& f) { if constexpr (N0 < N1) { mma<N0>(acc, f); mmas<N0 + 1, N1>(acc, f); } }
  template <int N> static __device__ __forceinline__ void mix(f32x4 (&acc)[NM][NI], const Frag& fc, Frag& fn, const Ptr& p) {
    if constexpr (N < NMMA) {
      constexpr int SPAN = NMMA - HEAD - 3, k = N - HEAD;
      constexpr int r0 = k >= SPAN ? NRD : (k * NRD + SPAN - 1) / SPAN, r1 = k + 1 >= SPAN ? NRD : ((k + 1) * NRD + SPAN - 1) / SPAN;
      mma<N>(acc, fc); rds<r0, r1>(fn, p); SB();
      mix<N + 1>(acc, fc, fn, p);
    }
  }
  struct Off { unsigned x, w[NP]; };
  static __device__ __forceinline__ Off offs(int w, int lane) {
    const int kq = lane >> 4, c16 = lane & 15, a = (lane >> 2) & 3;
    Off o;
    o.x = MODE == 1 ? (64 * (w & 1) + 8 * (c16 >> 2) + (c16 & 3)) * 128 + ((kq ^ ((c16 >> 1) & 1) ^ ((c16 >> 3) << 1) ^ (c16 & 4)) << 4)
                    : ((MODE == 2 ? 32 * w : 64 * (w & 1)) + c16) * 128 + ((kq ^ (c16 >> 1)) << 4);
    const unsigned wb = L_W + kq * (2048 + PAD) + a * 256 + (lane & 3) * (MODE == 1 ? 8 : 16);
#pragma unroll
    for (int j = 0; j < NP; ++j) o.w[j] = wb + (span(w, j) ^ a) * 64;
    return o;
  }
  template <int U> static __device__ __forceinline__ void ptrx(Ptr& p, const Off& o, LAS char* lds, unsigned sx) {
    p.x[U][0] = lds + sx + (o.x ^ (64 * U)); p.x[U][1] = lds + sx + (o.x ^ (64 * U + 32)); asm volatile("" : "+v"(p.x[U][0]), "+v"(p.x[U][1])); }
  static __device__ __forceinline__ void ptrw(Ptr& p, const Off& o, LAS char* lds, unsigned sw) {
#pragma unroll
    for (int j = 0; j < NP; ++j) { p.w[j] = lds + (sw + o.w[j]); asm volatile("" : "+v"(p.w[j])); } }
  template <int N, class T> static __device__ __forceinline__ void fin(f32x4 (&acc)[NM][NI], const Frag& fc, T& tail) {
    if constexpr (N < NMMA) { mma<N>(acc, fc); tail(IC<N - HEAD>{}); SB(); fin<N + 1>(acc, fc, tail); }
  }
  template <class T>
  static __device__ __forceinline__ void loop(f32x4 (&acc)[NM][NI], Frag& fa, const Off& o, LAS char* lds, unsigned sx, unsigned sw, int w, int lane, int pub,
                                              GAS u64* fl, unsigned fi, u64 tag, T tail, unsigned& stv) {
    const int kq = lane >> 4, c16 = lane & 15;
    Frag fb; Ptr p;
#pragma unroll
    for (int n = 0; n < NM * NI; ++n) { acc[n % NM][n / NM] = f32x4{}; asm volatile("" : "+v"(acc[n % NM][n / NM])); }
    const unsigned one = (kq >> 1) == ((c16 >> 1) & 1) ? 0x3f803f80u : 0u;
    bf16x8 sel = __builtin_bit_cast(bf16x8, u32x4{one, one, one, one});
    int sidx = (lane & 1) ? 0xEEEE : 0x4444;
    f32x4 cs = {};
    LAS float* cp = (LAS float*)(lds + L_ST + 1024) + 32 * span(w, 0) + (MODE == 1 ? c16 : 8 * (c16 >> 2) + (c16 & 3));
    asm volatile("s_nop 1" : "+v"(sel), "+v"(sidx), "+v"(cs), "+v"(cp));
    if constexpr (MODE != 2) { ptrx<0>(p, o, lds, sx); ptrx<1>(p, o, lds, sx); ptrw(p, o, lds, sw); SB(); tail(IC<-3>{}); STAMP(1); BAR_L(); STAMP(12); rds<0, NRD>(fa, p); SB(); tail(IC<-2>{}); }
#define G1_HEAD(FC, PUB, LAST, BAR) do { \
      mma<0>(acc, FC); sx = (sx == 5 * XS) ? 0 : sx + XS; asm volatile("" : "+s"(sx)); SB(); \
      csum<0>(cs, FC, sel, sidx); SB(); \
      mma<1>(acc, FC); sw = (sw == 2 * WSL) ? 0 : sw + WSL; asm volatile("" : "+s"(sw)); SB(); \
      mma<2>(acc, FC); ptrx<0>(p, o, lds, sx); SB(); \
      mma<3>(acc, FC); ptrw(p, o, lds, sw); SB(); \
      mmas<4, HEAD - 2>(acc, FC); SB(); \
      csum<1>(cs, FC, sel, sidx); SB(); \
      mma<HEAD - 2>(acc, FC); ptrx<1>(p, o, lds, sx); SB(); \
      mma<HEAD - 1>(acc, FC); SB(); \
      if (LAST) { asm volatile("s_nop 5" : "+v"(cs)); cp[0] = cs[0] + cs[1]; cp[MODE == 1 ? 16 : 4] = cs[2] + cs[3]; } \
      if (PUB) DRAIN(); \
      if (BAR) BAR_L(); \
      if ((PUB) && w == 0) publish(fl, fi, tag, lane); \
      SB(); } while (0)
    SB();
    G1_HEAD(fa, false, false, false);
    tail(IC<-1>{}); BAR_L(); SB();
#pragma unroll 1
    for (int s = 0; s < 4; ++s) {
      mix<HEAD>(acc, fa, fb, p);
      G1_HEAD(fb, false, false, true);
      mix<HEAD>(acc, fb, fa, p);
      G1_HEAD(fa, false, false, true);
    }
    mix<HEAD>(acc, fa, fb, p);
    G1_HEAD(fb, false, true, true);
    fin<HEAD>(acc, fb, tail);
    asm volatile("s_nop 7");
    SB();
  }
  template <int J, int N> static __device__ __forceinline__ void r2mix(f32x4 (&acc)[NM][NI], f32x4& cs, bf16x8 sel, int sidx, const Frag& fc, Frag& fn, const Ptr& pl, const Ptr& ph) {
    if constexpr (N < NMMA) {
      constexpr int H2 = R2H, SPAN = R2S, k = N - H2, xs = (5 + J) % 6;
      constexpr int r0 = k >= SPAN ? NRD : (k * NRD + SPAN - 1) / SPAN, r1 = k + 1 >= SPAN ? NRD : ((k + 1) * NRD + SPAN - 1) / SPAN;
      mma<N>(acc, fc);
      Ptr p = xs >= 3 ? ph : pl;
#pragma unroll
      for (int j = 0; j < NP; ++j) p.w[j] = J == 8 ? ph.w[j] : pl.w[j];
      rds<r0, r1, (xs % 3) * XS, J == 8 ? 0 : ((2 + J) % 3) * WSL>(fn, p); SB();
      if constexpr (N == 13) { csum<1>(cs, fc, sel, sidx); SB(); }
      r2mix<J, N + 1>(acc, cs, sel, sidx, fc, fn, pl, ph);
    }
  }
  template <int J, class PUB> static __device__ __forceinline__ void r2steps(f32x4 (&acc)[NM][NI], f32x4& cs, bf16x8 sel, int sidx, Frag& fc, Frag& fn, const Ptr& pl, const Ptr& ph, PUB& pub) {
    if constexpr (J < 9) {
      mmas<0, 2>(acc, fc); csum<0>(cs, fc, sel, sidx); mmas<2, R2H>(acc, fc); SB();
      if constexpr (J == 2) DRAIN();
      if constexpr (J < 8) BAR_L();
      if constexpr (J == 2) pub();
      SB();
      r2mix<J, R2H>(acc, cs, sel, sidx, fc, fn, pl, ph);
      r2steps<J + 1>(acc, cs, sel, sidx, fn, fc, pl, ph, pub);
    }
  }
  template <class PUB, class T>
  static __device__ __forceinline__ void loop2(f32x4 (&acc)[NM][NI], Frag& fa, const Off& o, const Ptr& ph, LAS char* lds, int w, int lane, PUB pub, T tail) {
    const int kq = lane >> 4, c16 = lane & 15;
    Frag fb; Ptr pl, ph2 = ph;
    ptrx<0>(pl, o, lds, 0); ptrx<1>(pl, o, lds, 0);
#pragma unroll
    for (int j = 0; j < NP; ++j) pl.w[j] = ph.w[j];
    ptrw(ph2, o, lds, 3 * XS - L_W);
#pragma unroll
    for (int n = 0; n < NM * NI; ++n) { acc[n % NM][n / NM] = f32x4{}; asm volatile("" : "+v"(acc[n % NM][n / NM])); }
    const unsigned one = (kq >> 1) == ((c16 >> 1) & 1) ? 0x3f803f80u : 0u;
    bf16x8 sel = __builtin_bit_cast(bf16x8, u32x4{one, one, one, one});
    int sidx = (lane & 1) ? 0xEEEE : 0x4444;
    f32x4 cs = {};
    LAS float* cp = (LAS float*)(lds + L_ST + 1024) + 32 * span(w, 0) + 8 * (c16 >> 2) + (c16 & 3);
    asm volatile("s_nop 1" : "+v"(sel), "+v"(sidx), "+v"(cs), "+v"(cp));
    SB();
    r2steps<0>(acc, cs, sel, sidx, fa, fb, pl, ph2, pub);
    mmas<0, 2>(acc, fb); csum<0>(cs, fb, sel, sidx); mmas<2, 4>(acc, fb); csum<1>(cs, fb, sel, sidx); mmas<4, HEAD>(acc, fb); SB();
    fin<HEAD>(acc, fb, tail);
    asm volatile("s_nop 7" : "+v"(cs));
    cp[0] = cs[0] + cs[1]; cp[4] = cs[2] + cs[3];
    SB();
  }
};

struct KV { bf16x8 k1; s16x4 k2; bf16x8 v0, v1; };
__device__ __forceinline__ f32x16 qk_a(bf16x8 k, bf16x8 q) { f32x16 z = {}; return __builtin_amdgcn_mfma_f32_32x32x16_bf16(k, q, z, 0, 0, 0); }
__device__ __forceinline__ f32x16 qk_b(s16x4 k, s16x4 q, f32x16 c) { return __builtin_amdgcn_mfma_f32_32x32x8bf16_1k(k, q, c, 0, 0, 0); }
__device__ __forceinline__ f32x16 pv_lo(bf16x8 v, f32x16 p, f32x16 o, int idx) {
  f32x8 h = __builtin_shufflevector(p, p, 0, 1, 2, 3, 4, 5, 6, 7);
  o = __builtin_amdgcn_smfmac_f32_32x32x32_bf16(v, __builtin_bit_cast(bf16x16, h), o, idx, 0, 0);
  asm volatile("" : "+v"(o));
  return o; }
__device__ __forceinline__ f32x16 pv_hi(bf16x8 v, f32x16 p, f32x16 o, int idx) {
  f32x8 h = __builtin_shufflevector(p, p, 8, 9, 10, 11, 12, 13, 14, 15);
  o = __builtin_amdgcn_smfmac_f32_32x32x32_bf16(v, __builtin_bit_cast(bf16x16, h), o, idx, 0, 0);
  asm volatile("" : "+v"(o));
  return o; }
template <int E> __device__ __forceinline__ void max8(const f32x16& s, float& m) {
  asm volatile("v_max3_f32 %0, %0, %1, %2\n\tv_max3_f32 %0, %0, %3, %4\n\tv_max3_f32 %0, %0, %5, %6\n\tv_max3_f32 %0, %0, %7, %8"
               : "+v"(m) : "v"(s[E]), "v"(s[E + 1]), "v"(s[E + 2]), "v"(s[E + 3]), "v"(s[E + 4]), "v"(s[E + 5]), "v"(s[E + 6]), "v"(s[E + 7]));
}
struct F4 { float v[4]; };
__device__ __forceinline__ F4 add4(f32x4 g, float a, float b, float c, float d, float& chk) {
  F4 r;
  asm volatile("v_add_f32 %0, %9, %5\n\tv_add_f32 %1, %10, %6\n\tv_add_f32 %2, %11, %7\n\tv_add_f32 %3, %12, %8\n\t"
               "v_maximum3_f32 %4, %4, |%0|, |%1|\n\tv_maximum3_f32 %4, %4, |%2|, |%3|"
               : "=&v"(r.v[0]), "=&v"(r.v[1]), "=&v"(r.v[2]), "=&v"(r.v[3]), "+v"(chk) : "v"(a), "v"(b), "v"(c), "v"(d), "v"(g[0]), "v"(g[1]), "v"(g[2]), "v"(g[3]));
  return r;
}
__device__ __forceinline__ u32x2 scale4(F4 g, float s) {
  unsigned r0, r1;
  asm volatile("v_mul_f32 %2, %2, %6\n\tv_mul_f32 %3, %3, %6\n\tv_mul_f32 %4, %4, %6\n\tv_mul_f32 %5, %5, %6\n\tv_cvt_pk_bf16_f32 %0, %2, %3\n\tv_cvt_pk_bf16_f32 %1, %4, %5"
               : "=&v"(r0), "=&v"(r1), "+v"(g.v[0]), "+v"(g.v[1]), "+v"(g.v[2]), "+v"(g.v[3]) : "v"(s));
  return u32x2{r0, r1};
}
#define EX4(s, b) do { s[b] = __builtin_amdgcn_exp2f(s[b]); s[b+1] = __builtin_amdgcn_exp2f(s[b+1]); s[b+2] = __builtin_amdgcn_exp2f(s[b+2]); s[b+3] = __builtin_amdgcn_exp2f(s[b+3]); } while (0)
#define STEPX(SC, SP, OP, VP, SN, KN, Q1, Q2, X0, X1) do { \
  EX4(SC, 0); SB(); OP = pv_lo(VP.v0, SP, OP, idx); SB(); X0; SB(); \
  EX4(SC, 4); SB(); SN = qk_a(KN.k1, Q1); SB(); \
  EX4(SC, 8); SB(); OP = pv_hi(VP.v1, SP, OP, idx); SB(); X1; SB(); \
  EX4(SC, 12); SB(); SN = qk_b(KN.k2, Q2, SN); SB(); } while (0)
#define STEP(SC, SP, OP, VP, SN, KN, Q1, Q2) STEPX(SC, SP, OP, VP, SN, KN, Q1, Q2, , )

__global__ void __launch_bounds__(512, 2)
fused(const u16* __restrict__ x, const u16* __restrict__ wqkv, const u16* __restrict__ wo, const u16* __restrict__ w1,
      const u16* __restrict__ b1, const u16* __restrict__ w2, const u16* __restrict__ b2, u16* __restrict__ out, char* __restrict__ ws) {
  __shared__ __attribute__((aligned(16))) char smem[L_END + 256];
  LAS char* lds = (LAS char*)smem;
  const unsigned lds0 = (unsigned)(size_t)lds;
  const int lane = threadIdx.x & 63, w = __builtin_amdgcn_readfirstlane(threadIdx.x >> 6);
  const int b = blockIdx.x, g = b & 7, idx8 = b >> 3;
  unsigned stv = 0;
  asm volatile("s_getpc_b64 s[98:99]\n\ts_load_dword s100, s[98:99], %0" :: "s"((idx8 * 8 + w) * 64u + 192u) : "s98", "s99", "s100");
  STAMP(0);
  asm volatile("" :: "s"(x), "s"(wqkv), "s"(wo), "s"(w1), "s"(b1), "s"(w2), "s"(b2), "s"(out), "s"(ws));
  const int rt = (g & 3) * 4 + (idx8 & 3), ct = (g >> 2) * 8 + (idx8 >> 2);
  const bool vtype = ct >= 10;
  const int kq = lane >> 4, c16 = lane & 15;
  LAS float* st_r = (LAS float*)(lds + L_ST);
  LAS float* st_t = st_r + 128;
  LAS float* st_c = st_r + 256;
  GAS u64* fl = (GAS u64*)(ws + WS_F);
  const rsrc_t rw = mkrs(ws, WS_END), rx = mkrs(x, 2048 * D_ * 2);
  u64 tag = 0;
  const int head = 4 * g + (idx8 >> 3), qc = idx8 & 7;
  const int aw = w - 4, qh = aw & 1, kh = aw >> 1, r = lane & 31, h = lane >> 5, odd = head & 1, pr = head >> 1;
  int hstep = 0;
  unsigned oK1 = 0, oK2 = 0, oV0 = 0, oV1 = 0, oQ2 = 0;
  auto kv_offsets = [&](int ln) {
    const int r = ln & 31, h = ln >> 5;
    oK1 = odd * 1536 + ln * 16; oK2 = h ? P_ONE : 1024 + r * 16 + odd * 8; oQ2 = h ? OOB : oK2;
    const int dv = r < 16 ? (r & 3) + 4 * (r >> 3) + 8 * ((r >> 2) & 1) : r;
    oV0 = r < 20 ? P_V + odd * 1280 + h * 320 + dv * 16 : (r == 20 ? P_ONE : OOB); oV1 = r < 20 ? oV0 + 640 : oV0;
  };
  unsigned fi = 0;
  const int hw = w & 3, wc = c16 ^ (kq << 2);
  const unsigned xv = (lane >> 3) * (D_ * 2) + (((lane & 7) ^ kq) << 4), wv0 = ct < 15 ? kq * (3 * D_ * 2) + (wc << 4) : OOB;
  {
    const bool hi = w >= 4;
    const rsrc_t r0 = mkrs(hi ? wqkv : x, hi ? D_ * 3 * D_ * 2 : 2048 * D_ * 2);
    const unsigned v0 = hi ? wv0 : xv, m0_ = hi ? 0u : ~0u, pd = hi ? (vtype ? 32 : 8) : 0;
    const unsigned s0_ = hi ? ct * 256 + 16 * hw * (3 * D_ * 2) : (rt * 128 + hw * 32) * (D_ * 2), ds = hi ? 4 * 3 * D_ * 2 : 8 * D_ * 2;
    const unsigned l0 = lds0 + (hi ? L_W : 0u) + hw * 4096 + 2 * hw * pd;
#pragma unroll
    for (int i = 0; i < 4; ++i) dma16(r0, v0 ^ (m0_ & ((i & 1) * 64 + (i >> 1) * 32)), s0_ + i * ds, l0 + i * 1024 + (i >> 1) * pd);
    SB();
  }

  if (w < 4) {
    const int wm = w & 1, wn = w >> 1;
    const int row = rt * 128 + ct * 8 + 2 * w + (lane >> 5);
    {
      const unsigned lb = lds0 + 5 * XS + w * 4096, xo = row * (D_ * 2) + r * 16;
      dma16(rx, xo, 0, lb); dma16(rx, xo, 512, lb + 1024); dma16(rx, r < 16 ? xo : OOB, 1024, lb + 2048);
      asm volatile("s_mov_b32 m0, %0\n\ts_nop 0\n\tbuffer_load_dword %1, %2, 0 offen sc1 lds" :: "s"(lb + 3072), "v"(lane * 4), "s"(mkrs(ws + WS_F, 8)) : "memory");
      u64 pc; asm volatile("s_getpc_b64 %0" : "=s"(pc));
      STOUCH3((pc & ~(u64)255) + 11264 + min((idx8 * 4 + w) * 192u, 22528u));
      *(LAS u32x4*)(lds + L_M + threadIdx.x * 16) = u32x4{0x7fc00000u, 0x7fc00000u, 0x7fc00000u, 0x7fc00000u};
    }
    unsigned vo[4];
    if (vtype) {
#pragma unroll
      for (int i = 0; i < 4; ++i) {
        const int fv = (ct - 10) * 128 + 32 * G1<1>::span(w, i >> 1) + 16 * (i & 1) + c16, hd = fv / 20, d = fv - hd * 20;
        vo[i] = (WS_T + ((hd >> 1) * 64 + 4 * rt + 2 * wm) * PT + P_V + (hd & 1) * 1280 + kq * 320 + d * 16) | (ct < 15 ? 0u : OOB);
      }
    } else {
#pragma unroll
      for (int pp = 0; pp < 2; ++pp) {
        const int c = (ct < 5 ? ct : ct - 5) * 16 + 4 * G1<0>::span(w, pp) + kq, p5 = c / 5, pos = c - p5 * 5;
        const unsigned tb = WS_T + (p5 * 64 + 4 * rt + 2 * wm) * PT;
        vo[pp] = tb + pos * 512 + (ct < 5 ? P_Q : 0u) + c16 * 16;
        vo[2 + pp] = (ct >= 5 && pos == 2) ? tb + P_ONE + (c16 >> 3) * PT + (c16 & 7) * 16 : OOB;
      }
    }
    asm volatile("" : "+v"(vo[0]), "+v"(vo[1]), "+v"(vo[2]), "+v"(vo[3]));
    if (w < 2) touch(w ? mkrs(b1 + ct * 96, 192) : mkrs(wqkv + 94 * 3 * D_, 546 * 3 * D_ * 2), w ? lane * 4 : (b * 64 + lane) * 128, lds0 + (w ? L_ST + 1536 : L_PF));
    u32x4 xr[3]; u64 e0;
    auto stats_ld = [&]() {
      if (w < 2) asm volatile("s_waitcnt vmcnt(1)" ::: "memory"); else DRAIN();
#pragma unroll
      for (int i = 0; i < 3; ++i) xr[i] = *(LAS u32x4*)(lds + 5 * XS + w * 4096 + i * 1024 + lane * 16);
      e0 = *(LAS u64*)(lds + 5 * XS + w * 4096 + 3072);
    };
    auto stats = [&]() {
      float p = 0.f, q = 0.f;
#pragma unroll
      for (int i = 0; i < 12; ++i) {
        const unsigned u2 = xr[i >> 2][i & 3];
        const bf16x2 v2 = __builtin_bit_cast(bf16x2, u2);
        p = __builtin_amdgcn_fdot2_f32_bf16(v2, __builtin_bit_cast(bf16x2, 0x3f803f80u), p, false); q = __builtin_amdgcn_fdot2_f32_bf16(v2, v2, q, false);
      }
      p = dppadd<0x140>(dppadd<0x141>(dppadd<0x4E>(dppadd<0xB1>(p)))); q = dppadd<0x140>(dppadd<0x141>(dppadd<0x4E>(dppadd<0xB1>(q))));
      p = dppadd<0x142>(p); q = dppadd<0x142>(q);
      const float mu = p * (1.f / D_), rstd = __builtin_amdgcn_rsqf(fmaxf(q * (1.f / D_) - mu * mu, 0.f) + 1e-5f);
      tag = uni64(e0 + 1);
      if (threadIdx.x == 0) { *(LAS u64*)(lds + L_C) = tag; *(LAS u32x4*)(lds + L_C + 16) = u32x4{0, 0, 0, 0}; *(LAS u32x4*)(lds + L_C + 32) = u32x4{0, 0, 0, 0}; *(LAS u32x4*)(lds + L_C + 48) = u32x4{0, 0, 0, 0}; }
      st16(u32x4{__float_as_uint(rstd), (unsigned)tag, __float_as_uint(-mu * rstd), (unsigned)tag}, rw, r == 16 ? row * 16 : OOB, WS_S);
    };
    G1<2>::Frag f2; G1<2>::Ptr p2;
    const G1<2>::Off o2 = G1<2>::offs(w, lane);
    G1<2>::ptrx<0>(p2, o2, lds, 3 * XS); G1<2>::ptrx<1>(p2, o2, lds, 3 * XS); G1<2>::ptrw(p2, o2, lds, 0);
    {
      f32x4 acc[4][4];
      if (vtype) {
        f32x4 r4[4], t4[4]; float c1[4];
        G1<1>::Frag f1;
        LAS float* pr4 = st_r + 64 * wm + 8 * kq; LAS float* pc0 = st_c + 32 * G1<1>::span(w, 0) + c16; LAS float* pc1 = st_c + 32 * G1<1>::span(w, 1) + c16;
        asm volatile("" : "+v"(pr4), "+v"(pc0), "+v"(pc1));
        G1<1>::loop(acc, f1, G1<1>::offs(w, lane), lds, 0, 0, w, lane, -1, fl, 0, tag, [&](auto k) {
          constexpr int K = decltype(k)::v;
          if constexpr (K == -3) { if (SM <= 1) stats_ld(); if (SM == 0) stats(); }
          else if constexpr (K == -2) { if (SM == 1) stats(); }
          else if constexpr (K == -1) {}
          else if constexpr (K < 4) { r4[K] = *(LAS f32x4*)(pr4 + 32 * (K >> 1) + 4 * (K & 1)); t4[K] = *(LAS f32x4*)(pr4 + 128 + 32 * (K >> 1) + 4 * (K & 1)); }
          else if constexpr (K < 6) { c1[2 * K - 8] = (K == 4 ? pc0 : pc1)[0]; c1[2 * K - 7] = (K == 4 ? pc0 : pc1)[16]; }
          else G1<2>::rds<2 * K - 12, 2 * K - 10, XS, WSL>(f2, p2);
        }, stv);
        STAMP(2);
#pragma unroll
        for (int i = 0; i < 4; ++i)
#pragma unroll
          for (int a = 0; a < 2; ++a)
            st16(pk8(acc[2 * a][i] * r4[2 * a] + t4[2 * a] * c1[i], acc[2 * a + 1][i] * r4[2 * a + 1] + t4[2 * a + 1] * c1[i]), rw, vo[i], a * PT);
      } else {
        float r1[4], t1[4]; f32x4 c4[4];
        G1<0>::Frag f0;
        LAS float* pr1 = st_r + 64 * wm + c16; LAS float* pc0 = st_c + 32 * G1<0>::span(w, 0) + 8 * kq; LAS float* pc1 = st_c + 32 * G1<0>::span(w, 1) + 8 * kq;
        asm volatile("" : "+v"(pr1), "+v"(pc0), "+v"(pc1));
        G1<0>::loop(acc, f0, G1<0>::offs(w, lane), lds, 0, 0, w, lane, -1, fl, 0, tag, [&](auto k) {
          constexpr int K = decltype(k)::v;
          if constexpr (K == -3) { if (SM <= 1) stats_ld(); if (SM == 0) stats(); }
          else if constexpr (K == -2) { if (SM == 1) stats(); }
          else if constexpr (K == -1) {}
          else if constexpr (K < 4) { r1[K] = pr1[16 * K]; t1[K] = pr1[128 + 16 * K]; }
          else if constexpr (K < 6) { c4[2 * K - 8] = *(LAS f32x4*)(K == 4 ? pc0 : pc1); c4[2 * K - 7] = *(LAS f32x4*)((K == 4 ? pc0 : pc1) + 4); }
          else G1<2>::rds<2 * K - 12, 2 * K - 10, XS, WSL>(f2, p2);
        }, stv);
        STAMP(2);
        const float sc = ct < 5 ? QSCALE : 1.f;
#pragma unroll
        for (int pp = 0; pp < 2; ++pp)
#pragma unroll
          for (int m = 0; m < 4; ++m)
            st16(pk8(acc[m][2 * pp] * (r1[m] * sc) + c4[2 * pp] * (t1[m] * sc), acc[m][2 * pp + 1] * (r1[m] * sc) + c4[2 * pp + 1] * (t1[m] * sc)), rw,
                 vo[pp], (m & 1) * 256 + (m >> 1) * PT);
        st16(u32x4{0x3f803f80u, 0x3f803f80u, 0x3f803f80u, 0x3f803f80u}, rw, min(vo[2], vo[3]), 0);
      }
    }
    STAMP(3);
    f32x4 acc[2][6];
    float r1[2], t1[2]; f32x4 c4[6]; u32x4 bb[3];
    LAS float* pr1 = st_r + 32 * w + c16;
    G1<2>::loop2(acc, f2, o2, p2, lds, w, lane, [&]() { if (w == 0) publish(fl, F_Q + ct * 16 + rt, tag, lane); }, [&](auto k) {
      constexpr int K = decltype(k)::v;
      if constexpr (K < 2) { r1[K] = pr1[16 * K]; t1[K] = pr1[128 + 16 * K]; }
      else if constexpr (K < 5) bb[K - 2] = *(LAS u32x4*)(st_c + 128 + 16 * G1<2>::span(w, K - 2) + 4 * kq);
    });
    STAMP(4);
    __builtin_amdgcn_s_setprio(2);
    lds_meet<2>((LAS unsigned*)(lds + L_C + 20), 4, lane);
#pragma unroll
    for (int pp = 0; pp < 3; ++pp) { LAS float* pc = st_c + 32 * G1<2>::span(w, pp) + 8 * kq; c4[2 * pp] = *(LAS f32x4*)pc; c4[2 * pp + 1] = *(LAS f32x4*)(pc + 4); }
    const bool ret = w < 2 || (w == 2 && idx8 < 4);
    const int slab = w < 2 ? 2 * idx8 + w : 64 + idx8;
    u32x4 hq[6];
#pragma unroll
    for (int pp = 0; pp < 3; ++pp) {
      const f32x4 ba = {blo(bb[pp][0]), bhi(bb[pp][0]), blo(bb[pp][1]), bhi(bb[pp][1])}, bc = {blo(bb[pp][2]), bhi(bb[pp][2]), blo(bb[pp][3]), bhi(bb[pp][3])};
#pragma unroll
      for (int m = 0; m < 2; ++m) {
        const u32x2 lo = sqrelu4(acc[m][2 * pp], c4[2 * pp], ba, r1[m], t1[m]), hi = sqrelu4(acc[m][2 * pp + 1], c4[2 * pp + 1], bc, r1[m], t1[m]);
        hq[2 * pp + m] = u32x4{lo[0], lo[1], hi[0], hi[1]};
      }
    }
    asm volatile("" : "+v"(hq[0]), "+v"(hq[1]), "+v"(hq[2]), "+v"(hq[3]), "+v"(hq[4]), "+v"(hq[5]));
#define HST(i0, i1) do { _Pragma("unroll") for (int i = i0; i < i1; ++i) { st16(hq[i], rw, lane * 16, WS_H + ((8 * rt + 2 * w + (i & 1)) * 48 + 3 * ct + G1<2>::span(w, i >> 1)) * 1024); SB(); __builtin_amdgcn_s_sleep(4); SB(); } } while (0)
    FS(13); HST(0, 6); FS(14);
    if (ret) {
      const int s2 = slab - 48;
      const rsrc_t rr = mkrs((slab < 48 ? w2 : wo) + 80 * g, ((slab < 48 ? F_ : D_) * D_ - 80 * g) * 2);
      const unsigned r0 = slab < 48 ? slab * 32 : (s2 < 16 ? s2 * 40 : (s2 - 16) * 160 + 16), rsh = s2 < 16 ? 4 : 2, rmu = slab < 48 ? 0 : (s2 < 16 ? 4 : 16);
#pragma unroll
      for (int i = 0; i < 5; ++i) {
        const unsigned c = 64 * i + lane, k = (c * 205) >> 11;
        dma16(rr, (k + (k >> rsh) * rmu) * (D_ * 2) + (c - k * 10) * 16, r0 * (D_ * 2), lds0 + L_RED + w * 5120 + i * 1024);
      }
      const unsigned tro = (8 * kq + ((lane >> 2) & 3)) * 160 + (lane & 3) * 16 + (g & 1) * 32, tr4 = tro - (lane & 3) * 8 + 128 - (g & 1) * 160;
      DRAIN(); FS(15);
#pragma unroll
      for (int t = 0; t < 5; ++t) {
        LAS char* p = lds + L_RED + w * 5120 + (t < 4 ? tro + (t >> 1) * 64 + (t & 1) * 8 : tr4);
        st16(__builtin_bit_cast(u32x4, cat8(trd(p), trd(p + 640))), rw, lane * 16 + WS_W + (g * 68 + slab) * 5120, t * 1024); SB();
      }
    }
    STAMP(5);
  } else {
    FS(1);
    rsrc_t rwt = mkrs(wqkv, D_ * 3 * D_ * 2);
    unsigned rs = 3 * D_ * 2, pad = vtype ? 32 : 8;
    unsigned wso = ct * 256 + 16 * hw * rs, xso = (rt * 128 + hw * 32) * (D_ * 2);
    const unsigned xv1 = xv ^ 64, xv2 = xv ^ 32, xv3 = xv ^ 96;
    unsigned wv = wv0;
    unsigned nx = lds0 + hw * 4096, nw = lds0 + L_W + hw * 4096;
#define DX(i) dma16(rx, i == 0 ? xv : (i == 1 ? xv1 : (i == 2 ? xv2 : xv3)), xso + i * 8 * (D_ * 2), nx + i * 1024)
#define DW(i) dma16(rwt, wv, wso + 4 * i * rs, nw + i * 1024 + (2 * hw + (i >> 1)) * pad)
#define NEXTX() do { xso += 128; nx = (nx == lds0 + hw * 4096 + 5 * XS) ? nx - 5 * XS : nx + XS; } while (0)
#define NEXTW() do { wso += 64 * rs; nw = (nw == lds0 + L_W + hw * 4096 + 2 * WSL) ? nw - 2 * WSL : nw + WSL; } while (0)
#define HBAR(n) asm volatile("s_waitcnt vmcnt(" #n ")\n\ts_barrier" ::: "memory")
    __builtin_amdgcn_s_setprio(1);
#define DW4() do { DW(0); DW(1); DW(2); DW(3); NEXTW(); } while (0)
#define DX4() do { DX(0); DX(1); DX(2); DX(3); NEXTX(); } while (0)
    const char* pw = (const char*)w1 + 256 * F_ * 2 + (b * 4 + hw) * 192;
    {
      NEXTW(); NEXTX();
      DW4(); DX4(); SPFX(STOUCH3(pw)); pw += 64 * F_ * 2; HBAR(8);
      DX4();
#pragma unroll 1
      for (int s = 0; s < 7; ++s) { DW4(); DX4(); SPFX(STOUCH3(pw)); pw += (s == 4 ? -576 : 64) * F_ * 2; HBAR(12); }
    }
    const unsigned go = WS_S + (rt * 128 + (hw & 1) * 64 + lane) * 16;
    asm volatile("s_mov_b32 m0, %0\n\ts_nop 0\n\tbuffer_load_dwordx4 %1, %2, 0 offen sc1 lds" :: "s"(lds0 + L_G + hw * 1024), "v"(go), "s"(rw) : "memory");
    DW4(); SPFX(STOUCH3(pw)); pw += 64 * F_ * 2; HBAR(9);
    rwt = mkrs(w1, D_ * F_ * 2); rs = F_ * 2; pad = 8; wso = ct * 192 + (256 + 16 * hw) * rs; wv = wc < 12 ? kq * rs + (wc << 4) : OOB;
    xso -= 1280; nx = lds0 + hw * 4096 + 4 * XS; nw = lds0 + L_W + hw * 4096 + WSL;
    DW4();
    tag = uni64(*(LAS u64*)(lds + L_C));
    {
      asm volatile("s_waitcnt vmcnt(8)" ::: "memory");
      u32x4 gr = *(LAS u32x4*)(lds + L_G + hw * 1024 + lane * 16);
#pragma unroll 1
      for (int n = 0; n < (1 << 20); ++n) {
        if (__all(gr[1] == (unsigned)tag && gr[3] == (unsigned)tag)) break;
        asm volatile("buffer_load_dwordx4 %0, %1, %2, 0 offen sc1\n\ts_waitcnt vmcnt(0)" : "=&v"(gr) : "v"(go), "s"(rw) : "memory");
      }
      st_r[hw * 64 + lane] = __uint_as_float(hw < 2 ? gr[0] : gr[2]);
    }
    HBAR(4);
    DW4(); asm volatile("s_waitcnt lgkmcnt(0)" ::: "memory"); SPFX(STOUCH3(pw)); HBAR(4);
    DW4();
    {
      const int c0 = pr * 5 + 2 * odd, e = (lane >> 3) & 1;
      const int tqk = (c0 + 2 * e) >> 4, tv = (head * 20 + 19 * e) >> 7;
      fi = F_Q + (lane < 16 ? 5 + tqk : 10 + tv) * 16 + 8 * kh + (lane & 7);
      if (lane >= 32) fi = F_Q + tqk * 16 + 2 * qc + qh;
      kv_offsets(lane);
      asm volatile("" : "+v"(fi), "+v"(oK1), "+v"(oK2), "+v"(oV0), "+v"(oV1), "+v"(oQ2));
    }
    HBAR(4);
    DW4(); DX4(); HBAR(8);
    DW4(); DX4(); HBAR(12);
    DW4(); DX4(); wso -= 640 * rs; HBAR(12);
    DW4(); DX4(); HBAR(12);
    DW4(); HBAR(8);
    DW4(); nw = lds0 + 3 * XS + hw * 4096; DW4(); HBAR(8);
    asm volatile("s_mov_b32 m0, %0\n\ts_nop 0\n\tbuffer_load_dwordx4 %1, %2, 0 offen sc1 lds" :: "s"(lds0 + L_G + hw * 1024), "v"(fi * 128), "s"(mkrs(ws + WS_F, 1u << 17)) : "memory");
  }

  if (w >= 4) {
    const rsrc_t rk = mkrs(ws + WS_T + (pr * 64 + kh * 32) * PT, 32 * PT);
    unsigned qo[4];
#pragma unroll
    for (int t = 0; t < 4; ++t) { qo[t] = WS_T + (pr * 64 + 8 * qc + 4 * qh + (t ^ (2 * kh))) * PT + P_Q; asm volatile("" : "+s"(qo[t])); }
    asm volatile("" :: "s"(rk));
    HBAR(1);
    DRAIN();
    if (!__all(*(volatile LAS u64*)(lds + L_G + aw * 1024 + (oK1 & 1023)) == tag)) await<2>(fl, fi, tag);
    __builtin_amdgcn_fence(__ATOMIC_ACQUIRE, "wavefront");
    STAMP(6);
    bool stored = false;
    auto sweep = [&](auto pass_) -> unsigned {
      constexpr int PASS = decltype(pass_)::v;
      const int lane = fresh_lane(), r = lane & 31, h = lane >> 5, c16 = lane & 15;
      if constexpr (PASS) kv_offsets(lane);
      bf16x8 q1[4]; s16x4 q2[4];
      KV b0, b1_, b2_, b3;
      unsigned so = 0;
#define LDK(B, d) do { B.k1 = ld16(rk, oK1, so + (d) * PT); SB(); B.k2 = ld8(rk, oK2, so + (d) * PT); SB(); } while (0)
#define LDV(B, d) do { B.v0 = ld16(rk, oV0, so + (d) * PT); SB(); B.v1 = ld16(rk, oV1, so + (d) * PT); SB(); } while (0)
      SB();
      LDK(b1_, 1);
#pragma unroll
      for (int t = 0; t < 4; ++t) { q1[t] = ld16(rw, oK1, qo[t]); SB(); q2[t] = ld8(rw, oQ2, qo[t]); SB(); }
      LDK(b0, 0);
      float mx[4] = {-3.0e38f, -3.0e38f, -3.0e38f, -3.0e38f};
      if constexpr (PASS) {
#pragma unroll 1
        for (int j = 2; j < 32; ++j) {
          const bf16x8 k1 = ld16(rk, oK1, j * PT); const s16x4 k2 = ld8(rk, oK2, j * PT);
#pragma unroll
          for (int t = 0; t < 4; ++t) {
            const f32x16 sv = qk_b(k2, q2[t], qk_a(k1, q1[t]));
#pragma unroll
            for (int e = 0; e < 16; ++e) mx[t] = fmaxf(mx[t], sv[e]);
          }
        }
      }
      int idx = 0xDDDD;
      f32x16 o0 = {}, o1 = {}, o2 = {}, o3 = {}, s0, s1, s2, s3;
      b3.v0 = bf16x8{}; b3.v1 = bf16x8{};
      LAS char* sp = lds + L_MRG + aw * 6144 + lane * 16; LAS char* gp = lds + L_MRG + (aw ^ 2) * 6144 + lane * 16;
      const int tt = (8 * qc + 4 * qh + (2 ^ (2 * kh))) * 2 + (r >> 4);
      unsigned oa = WS_O + (tt * 16 + pr) * 1024 + ((2 * odd + h) * 16 + c16) * 16, ob = h ? OOB : WS_O2 + (tt * 32 + head) * 128 + c16 * 8;
      volatile LAS f32x4* mw = (volatile LAS f32x4*)(lds + L_M) + aw * 64 + lane; volatile LAS f32x4* mr = (volatile LAS f32x4*)(lds + L_M) + (aw ^ 2) * 64 + r;
      asm volatile("" : "+v"(o0), "+v"(o1), "+v"(o2), "+v"(o3), "+v"(b3.v0), "+v"(b3.v1), "+v"(idx), "+v"(sp), "+v"(gp), "+v"(oa), "+v"(ob), "+v"(mw), "+v"(mr)); SB();
      {
        f32x16 sm[8];
#define SQ(n) ((n) < 4 ? (n) : ((n) - 2) & 3)
#define SMA(n) do { sm[n] = qk_a((n) < 4 ? b1_.k1 : b0.k1, q1[SQ(n)]); if ((n) == 6) s0 = sm[n]; if ((n) == 7) s1 = sm[n]; SB(); } while (0)
#define SMB(n) do { sm[n] = qk_b((n) < 4 ? b1_.k2 : b0.k2, q2[SQ(n)], sm[n]); SB(); } while (0)
        SMA(0); SMB(0); SMA(1); SMB(1);
        LDV(b0, 0); LDV(b1_, 1); LDK(b2_, 2); LDV(b2_, 2); LDK(b3, 3);
        FS(2);
#pragma unroll
        for (int n = 0; n < 8; ++n) {
          if (n < 6) SMA(n + 2);
          max8<0>(sm[n], mx[SQ(n)]); SB();
          if (n < 6) SMB(n + 2);
          max8<8>(sm[n], mx[SQ(n)]); SB();
        }
      }
      FS(3);
      *mw = f32x4{mx[0], mx[1], mx[2], mx[3]};
      if constexpr (PASS) lds_meet<0>((LAS unsigned*)(lds + L_C + 44), 4, lane);
#pragma unroll
      for (int t = 0; t < 4; ++t) {
        auto sw = __builtin_amdgcn_permlane32_swap(__float_as_uint(mx[t]), __float_as_uint(mx[t]), false, false);
        asm volatile("v_max_f32 %0, %1, %2" : "=v"(mx[t]) : "v"(sw[0]), "v"(sw[1]));
      }
      s3 = o3; asm volatile("" : "+v"(s3));
      f32x4 ma, mb;
#pragma unroll 1
      for (int n = 0; n < (1 << 22); ++n) {
        ma = mr[0]; mb = mr[32];
        if (PASS || !__any(__builtin_isunordered(ma[0], mb[0]) | __builtin_isunordered(ma[1], mb[1]) | __builtin_isunordered(ma[2], mb[2]) | __builtin_isunordered(ma[3], mb[3]))) break;
      }
      FS(4);
#pragma unroll
      for (int t = 0; t < 4; ++t) {
        float nm;
        asm("v_max3_f32 %0, %1, %2, %3\n\tv_sub_f32 %0, %4, %0" : "=&v"(nm) : "v"(mx[t]), "v"(ma[t ^ 2]), "v"(mb[t ^ 2]), "s"(PASS ? 0.f : -MARGIN));
        u32x2 pm = __builtin_bit_cast(u32x2, q2[t]);
        if constexpr (PASS) {
          const float a0 = bhi(__float_as_uint(nm)), r0 = nm - a0, a1 = bhi(__float_as_uint(r0)), a2 = r0 - a1;
          if (h) pm = u32x2{(__float_as_uint(a0) >> 16) | __float_as_uint(a1), __float_as_uint(a2) >> 16};
        } else if (h) pm[0] = __float_as_uint(nm) >> 16;
        q2[t] = __builtin_bit_cast(s16x4, pm);
        if (t == 0) s0 = qk_b(b0.k2, q2[0], s0);
        if (t == 1) s1 = qk_b(b0.k2, q2[1], s1);
        SB();
      }
      if constexpr (!PASS) { STAMP(7); SB(); }
#define KT(C, P, N) do { \
  STEPX(s0, s3, o3, P, s2, C, q1[2], q2[2], P.v0 = ld16(rk, oV0, so), __builtin_amdgcn_s_waitcnt(0x0F79)); \
  STEPX(s1, s0, o0, C, s3, C, q1[3], q2[3], P.v1 = ld16(rk, oV1, so), so += PT; asm volatile("" : "+s"(so))); \
  STEPX(s2, s1, o1, C, s0, N, q1[0], q2[0], C.k1 = ld16(rk, oK1, so), ); \
  STEPX(s3, s2, o2, C, s1, N, q1[1], q2[1], C.k2 = ld8(rk, oK2, so), ); } while (0)
      so = 3 * PT;
#pragma unroll 1
      do { KT(b0, b3, b1_); KT(b1_, b0, b2_); KT(b2_, b1_, b3); KT(b3, b2_, b0); } while (so != 31 * PT);
#define KTE(C, P, N, W) do { STEPX(s0, s3, o3, P, s2, C, q1[2], q2[2], , __builtin_amdgcn_s_waitcnt(W)); STEP(s1, s0, o0, C, s3, C, q1[3], q2[3]); \
  STEP(s2, s1, o1, C, s0, N, q1[0], q2[0]); STEP(s3, s2, o2, C, s1, N, q1[1], q2[1]); } while (0)
#define SEND(O, z, e) do { *(LAS f32x4*)(sp + (z) * 3072 + (e) * 1024) = f32x4{O[4 * (e)], O[4 * (e) + 1], O[4 * (e) + 2], O[4 * (e) + 3]}; SB(); } while (0)
      STEPX(s0, s3, o3, b3, s2, b0, q1[2], q2[2], b3.v0 = ld16(rk, oV0, so), __builtin_amdgcn_s_waitcnt(0x0F79));
      STEPX(s1, s0, o0, b0, s3, b0, q1[3], q2[3], b3.v1 = ld16(rk, oV1, so), ); STEP(s2, s1, o1, b0, s0, b1_, q1[0], q2[0]); STEP(s3, s2, o2, b0, s1, b1_, q1[1], q2[1]);
      KTE(b1_, b0, b2_, 0x0F76); KTE(b2_, b1_, b3, 0x0F72);
      STEPX(s0, s3, o3, b2_, s2, b3, q1[2], q2[2], , __builtin_amdgcn_s_waitcnt(0x0F70)); STEP(s1, s0, o0, b3, s3, b3, q1[3], q2[3]);
      LAS unsigned* mc = (LAS unsigned*)(lds + L_C + 32 + 4 * qh); unsigned one = 1u;
      EX4(s2, 0); SB(); o1 = pv_lo(b3.v0, s1, o1, idx); SB(); asm volatile("" : "+v"(mc), "+v"(one)); EX4(s2, 4); SB(); o1 = pv_hi(b3.v1, s1, o1, idx); SB(); SEND(o0, 0, 0);
      EX4(s2, 8); SB(); o2 = pv_lo(b3.v0, s2, o2, idx); SB(); SEND(o0, 0, 1); EX4(s2, 12); SB(); SEND(o0, 0, 2);
      EX4(s3, 0); SB(); o2 = pv_hi(b3.v1, s2, o2, idx); SB(); SEND(o1, 1, 0); EX4(s3, 4); SB(); SEND(o1, 1, 1); SEND(o1, 1, 2);
      lds_add(mc, one); SB();
      unsigned cv; f32x4 gv[6];
#define GET() do { cv = *(volatile LAS unsigned*)mc; _Pragma("unroll") for (int e = 0; e < 6; ++e) gv[e / 3 * 3 + (e + 2) % 3] = *(volatile LAS f32x4*)(gp + (e / 3 * 3 + (e + 2) % 3) * 1024); } while (0)
      o3 = pv_lo(b3.v0, s3, o3, idx); SB(); EX4(s3, 8); SB(); EX4(s3, 12); SB(); GET(); SB(); o3 = pv_hi(b3.v1, s3, o3, idx); SB();
      asm volatile("" : "+v"(o2), "+v"(o3) : "v"(o0), "v"(o1));
      if constexpr (!PASS) { STAMP(8); SB(); }
      if (__builtin_amdgcn_readfirstlane(cv) < 1024 + 2 + 2 * PASS) {
#pragma unroll 1
        for (int n = 0; n < (1 << 22); ++n) { if (__builtin_amdgcn_readfirstlane(*(volatile LAS unsigned*)mc) >= 1024 + 2 + 2 * PASS) break; }
        GET();
      }
      u32x4 pa[2]; u32x2 pb_[2]; float chk = 0.f, l[2];
#pragma unroll
      for (int z = 0; z < 2; ++z) {
        const f32x16 k = z ? o3 : o2;
        const F4 u2 = add4(gv[3 * z + 2], k[8], k[9], k[10], k[11], chk);
        l[z] = __uint_as_float(__builtin_amdgcn_permlane32_swap(__float_as_uint(u2.v[0]), __float_as_uint(u2.v[0]), false, false)[1]);
        float inv = __builtin_amdgcn_rcpf(l[z]);
        asm volatile("" : "+v"(inv));
        const F4 u0 = add4(gv[3 * z], k[0], k[1], k[2], k[3], chk), u1 = add4(gv[3 * z + 1], k[4], k[5], k[6], k[7], chk);
        const u32x2 x0 = scale4(u0, inv), x1 = scale4(u1, inv);
        pa[z] = u32x4{x0[0], x0[1], x1[0], x1[1]}; pb_[z] = scale4(u2, inv);
      }
      asm volatile("" :: "v"(o3));
      unsigned bad = 1u;
      if constexpr (!PASS) { stored = __all(chk < 1.0e37f && l[0] > 1.0e-30f && l[1] > 1.0e-30f); if (!stored) bad = 0x101u; }
      else {
#pragma unroll
        for (int z = 0; z < 2; ++z) {
#pragma unroll
          for (int j = 0; j < 4; ++j) pa[z][j] = min(pa[z][j], 0xfffffffeu);
          pb_[z][0] = min(pb_[z][0], 0xfffffffeu);
        }
      }
      if (PASS ? !stored : stored) { st16(pa[0], rw, oa, 0); st8(pb_[0], rw, ob, 0); st16(pa[1], rw, oa, 32768); st8(pb_[1], rw, ob, 8192); }
      unsigned cn = lds_add_rtn((LAS unsigned*)(lds + L_C + 24), bad) + bad;
      if constexpr (!PASS) {
        STAMP(9);
#pragma unroll 1
        for (int n = 0; n < (1 << 22); ++n) { if ((cn & 255) >= 4) break; cn = __builtin_amdgcn_readfirstlane(*(volatile LAS unsigned*)(lds + L_C + 24)); }
        asm volatile("" ::: "memory");
      }
      return cn;
    };
    if (__builtin_expect(sweep(IC<0>{}) >= 256, 0)) sweep(IC<1>{});
    const int lane = fresh_lane();
    if (b == 0) {
      await<8>(fl, F_O + aw * 64 + lane, tag);
      lds_meet<2>((LAS unsigned*)(lds + L_C + 28), 4, lane);
      if (w == 4) publish(fl, 0, tag, lane);
    }
  } else {
    const int rt2 = 4 * g + (idx8 & 3), ct2 = idx8 >> 2;
    {
      DRAIN();
      if (lds_add_rtn((LAS unsigned*)(lds + L_C + 16), 1u) == 3) publish(fl, F_H + b, tag, lane);
      const int j = lane & 15, hc = 4 * w + (j & 3), hr = rt2 >> 1;
      const int pb = (j < 4 || j > 12) ? (hr >> 2) + 4 * (hc >> 3) + 8 * ((hr & 3) + 4 * (hc & 7)) : 8 * (j < 10 ? 6 * w + j - 4 : (j < 12 ? 14 + 2 * w + j : w)) + ct2;
#pragma unroll 1
      for (int n = 0; n < (1 << 22); ++n) { if (__builtin_amdgcn_readfirstlane(*(volatile LAS unsigned*)(lds + L_C + 16)) >= 4) break; __builtin_amdgcn_s_sleep(4); }
      await<10>(fl, F_H + pb, tag);
    }
    STAMP(6);
    const int o = (64 * rt2 + 16 * w + c16) * D_ + 80 * ct2;
    u32x4 xv[2], bv[2]; u32x2 xv4, bv4;
    {
      f32x4 acc[4][5];
      bf16x8 f0[11], f1[11];
      unsigned vo = lane * 16, vo4 = vo + 4096, va[4];
#pragma unroll
      for (int m = 0; m < 4; ++m) va[m] = vo + (m ^ w) * 49152;
      unsigned sa = WS_H + (4 * rt2 * 48 + 12 * w) * 1024, sw = WS_W + (ct2 * 68 + 12 * w) * 5120, sq = WS_W + (ct2 * 68 + 48 + 4 * w) * 5120;
      const unsigned sq0 = sq + 20480, sq1 = WS_W + (ct2 * 68 + 64 + w) * 5120;
      LAS char* wl = lds + L_WO + lane * 16; LAS char* wst = wl + ((w - 1 - (g >> 1)) & 3) * 25600;
      asm volatile("" : "+v"(vo), "+v"(vo4), "+v"(va[0]), "+v"(va[1]), "+v"(va[2]), "+v"(va[3]), "+v"(wl), "+v"(wst));
#define GL1(F, j) F[j] = (j) < 4 ? ld16(rw, va[(j) & 3], sa) : ((j) < 8 ? ld16(rw, vo + (((j) - 4) & 3) * 1024, sw) : ((j) < 9 ? ld16(rw, vo4, sw) : ld16(rw, vo + ((j) - 9) * 1024, sq)))
#define GSTEP(FC, FN, WO, LD, Z) do { _Pragma("unroll") for (int n = 0; n < 20; ++n) { \
        acc[n & 3][n >> 2] = __builtin_amdgcn_mfma_f32_16x16x32_bf16(FC[4 + (n >> 2)], FC[n & 3], (Z) ? f32x4{} : acc[n & 3][n >> 2], 0, 0, 0); \
        SB(); \
        if ((LD) && n > 2 && (n & 1) && n < 17) { asm volatile("s_nop 4"); SB(); }         \
        if ((LD) && n < 2) { GL1(FN, n); } else if ((LD) && !(n & 1)) { GL1(FN, (n >> 1) + 1); } \
        if ((LD) && n == 5) { sa += 1024; asm volatile("" : "+s"(sa)); } \
        if ((LD) && n == 15) { sw += 5120; asm volatile("" : "+s"(sw)); } \
        if ((LD) && n == 3) { sq += 2048; if (sq == sq0) sq = sq1; asm volatile("" : "+s"(sq)); } \
        if (n == 17 || n == 19) *(LAS bf16x8*)(wst + (WO) + (n == 19 ? 1024 : 0)) = FC[n == 19 ? 10 : 9]; \
        SB(); } } while (0)
#pragma unroll
      for (int j = 0; j < 11; ++j) { GL1(f0, j); }
      const bf16x8 wlast = ld16(rw, vo4, sq1);
      sa += 1024; sw += 5120;
      SB();
      {
        const int tt = (8 * qc + 4 * (w & 1) + (w & 2)) * 2 + (r >> 4);
        st16(u32x4{~0u, ~0u, ~0u, ~0u}, rw, WS_O + (tt * 16 + pr) * 1024 + ((2 * odd + h) * 16 + c16) * 16, 0);
        st16(u32x4{~0u, ~0u, ~0u, ~0u}, rw, WS_O + (tt * 16 + pr) * 1024 + ((2 * odd + h) * 16 + c16) * 16, 32768);
        st8(u32x2{~0u, ~0u}, rw, WS_O2 + ((tt + 2 * h) * 32 + head) * 128 + c16 * 8, 0);
        __builtin_amdgcn_s_setprio(0); SB();
      }
      GSTEP(f0, f1, 0, true, true);
      __builtin_amdgcn_s_setprio(2);
      DRAIN();
      *(LAS bf16x8*)(wst + 24576) = wlast;
      unsigned mo = (F_O + g * 32 + r) * 128 + h * 4, xo = o * 2 + 16 * kq + (ct2 & 1) * 32, xo4 = o * 2 + 8 * kq + ((ct2 & 1) ? 0 : 128);
      LAS char* r0 = lds + L_RED + w * 5120 + lane * 16;
      asm volatile("" : "+v"(mo), "+v"(xo), "+v"(xo4), "+v"(r0));
      lds_add((LAS unsigned*)(lds + L_C + 32), 256u);
      if ((lds_add_rtn((LAS unsigned*)(lds + L_C + 36), 256u) >> 8) == 3) publish(fl, F_O + qc * 32 + head, tag, lane);
      __builtin_amdgcn_s_setprio(0); SB();
#pragma unroll 1
      for (int s = 1; s < 11; s += 2) {
        if (s == 7) {
          const unsigned lb = lds0 + L_RED + w * 5120;
          asm volatile("s_mov_b32 m0, %0\n\ts_nop 0\n\tbuffer_load_dword %1, %2, 0 offen sc1 lds" :: "s"(lb + 4096), "v"(mo), "s"(mkrs(ws + WS_F, 1u << 17)) : "memory");
          dma16(rx, xo, 0, lb); dma16(rx, xo, 64, lb + 1024); dma16(rx, xo4, 0, lb + 2048); dma16(mkrs(b2 + 80 * ct2, 160), vo, 0, lb + 3072);
        }
        GSTEP(f1, f0, 2048, true, false); GSTEP(f0, f1, 4096, true, false);
        __builtin_amdgcn_s_setprio(2); SB(); wst += 4096; asm volatile("" : "+v"(wst)); SB(); __builtin_amdgcn_s_setprio(0);
      }
      unsigned oo[4], oo2[4];
#pragma unroll
      for (int j = 0; j < 4; ++j) {
        const int gr = (j + 1 + (g >> 1)) & 3;
        oo[j] = WS_O + (4 * rt2 + w) * 16384 + gr * 4096; oo2[j] = WS_O2 + (4 * rt2 + w) * 4096 + gr * 1024; asm volatile("" : "+s"(oo[j]), "+s"(oo2[j]));
      }
      xv[0] = *(volatile LAS u32x4*)r0; xv[1] = *(volatile LAS u32x4*)(r0 + 1024); xv4 = *(volatile LAS u32x2*)(r0 + 2048);
      SB();
      GSTEP(f1, f0, 2048, false, false);
      __builtin_amdgcn_s_setprio(2);
      STAMP(7);
      const int ln = fresh_lane();
      const unsigned gate = *(volatile LAS unsigned*)(lds + L_C + 24), mk = *(volatile LAS unsigned*)(lds + L_RED + w * 5120 + 4096 + ln * 4);
      LAS char* bp = lds + L_RED + w * 5120 + 3072 + (ct2 & 1) * 32 + (ln & 48);
      bv[0] = *(volatile LAS u32x4*)bp; bv[1] = *(volatile LAS u32x4*)(bp + 64); bv4 = *(volatile LAS u32x2*)(bp + 128 - (ct2 & 1) * 160 - ((ln & 48) >> 1));
      const unsigned v8 = (ln >> 4) * 256 + (ln & 15) * 8;
      volatile LAS unsigned* gc = (volatile LAS unsigned*)(lds + L_C + 48);
      asm volatile("" ::: "memory");
#pragma unroll
      for (int t = 0; t < 5; ++t) *(LAS f32x4*)(r0 + t * 1024) = acc[3][t];
      asm volatile("" ::: "memory"); gc[w] = 1;
      if (!__all(mk == (unsigned)(ln >= 32 ? tag >> 32 : tag))) await<2>(fl, F_O + g * 32 + (ln & 31), tag);
#define SPIN(COND) do { _Pragma("unroll 1") for (int n_ = 0; n_ < (1 << 24); ++n_) { if (COND) break; __builtin_amdgcn_s_sleep(1); } asm volatile("" ::: "memory"); } while (0)
      if ((__builtin_amdgcn_readfirstlane(gate) & 255) < 4) SPIN((__builtin_amdgcn_readfirstlane(*(volatile LAS unsigned*)(lds + L_C + 24)) & 255) >= 4);
      STAMP(10);
      u32x4 a[16], al[4];
#define OL16(p, e, C) asm volatile("buffer_load_dwordx4 %0, %1, %2, %3 offen offset:%4 sc1" : C(a[4 * (p) + (e)]) : "v"(vo), "s"(rw), "s"(oo[p]), "n"((e) * 1024) : "memory")
#define OL8_(p, C, R0, R1, R2, R3) asm volatile("buffer_load_dwordx2 v[" #R0 ":" #R1 "], %1, %2, %3 offen sc1\n\tbuffer_load_dwordx2 v[" #R2 ":" #R3 "], %1, %2, %3 offen offset:128 sc1" \
        : C "{v[" #R0 ":" #R3 "]}"(al[p]) : "v"(v8), "s"(rw), "s"(oo2[p]) : "memory")
#define OL8(p, C) do { if ((p) == 0) OL8_(0, C, 224, 225, 226, 227); else if ((p) == 1) OL8_(1, C, 228, 229, 230, 231); \
        else if ((p) == 2) OL8_(2, C, 232, 233, 234, 235); else OL8_(3, C, 236, 237, 238, 239); } while (0)
#define OLG(p, C) do { OL16(p, 0, C "v"); OL16(p, 1, C "v"); OL16(p, 2, C "v"); OL16(p, 3, C "v"); OL8(p, C); } while (0)
      unsigned pg = gc[ln & 3];
      OLG(0, "=&"); OLG(1, "=&");
      LAS char* wp2 = lds + L_WO + 61440 + ln * 16; LAS char* r1_ = lds + L_RED + ln * 16 + (w ^ 1) * 5120; LAS char* r3 = lds + L_RED + ln * 16 + (w ^ 3) * 5120;
      asm volatile("" : "+v"(wp2));
      if (!__all(pg >= 1)) SPIN(__all(gc[ln & 3] >= 1));
      f32x4 y[5], z[5];
#pragma unroll
      for (int t = 0; t < 5; ++t) { z[t] = *(LAS f32x4*)(r3 + t * 1024); *(LAS f32x4*)(r3 + t * 1024) = acc[2][t]; }
      asm volatile("" ::: "memory"); gc[w] = 2; SB();
      constexpr int NW = 28;
      bf16x8 wr[NW];
#pragma unroll
      for (int t = 0; t < 5; ++t) {
        const unsigned x0 = t < 4 ? xv[(t >> 1) & 1][2 * (t & 1)] : xv4[0], x1 = t < 4 ? xv[(t >> 1) & 1][2 * (t & 1) + 1] : xv4[1];
        const unsigned b0 = t < 4 ? bv[(t >> 1) & 1][2 * (t & 1)] : bv4[0], b1v = t < 4 ? bv[(t >> 1) & 1][2 * (t & 1) + 1] : bv4[1];
        y[t] = (acc[0][t] + f32x4{blo(x0) + blo(b0), bhi(x0) + bhi(b0), blo(x1) + blo(b1v), bhi(x1) + bhi(b1v)}) + z[t];
        asm volatile("" : "+v"(y[t]));
      }
      pg = gc[w ^ 2];
#pragma unroll
      for (int i = 0; i < 12; ++i) wr[i] = *(LAS bf16x8*)(wl + i * 1024);
      if (__builtin_amdgcn_readfirstlane(pg) < 2) SPIN(__builtin_amdgcn_readfirstlane(gc[w ^ 2]) >= 2);
      asm volatile("" ::: "memory");
#pragma unroll
      for (int t = 0; t < 5; ++t) { z[t] = *(LAS f32x4*)(r1_ + t * 1024); *(LAS f32x4*)(r1_ + t * 1024) = acc[1][t]; }
      asm volatile("" ::: "memory"); gc[w] = 3; SB();
#pragma unroll
      for (int i = 12; i < NW; ++i) wr[i] = *(LAS bf16x8*)(wl + i * 1024);
      SB();
#define OCHK(p, N) ({ unsigned m_; asm volatile("s_waitcnt vmcnt(" #N ")" ::: "memory"); SB(); \
        asm volatile("" : "+v"(a[4 * (p)]), "+v"(a[4 * (p) + 1]), "+v"(a[4 * (p) + 2]), "+v"(a[4 * (p) + 3]), "+v"(al[p]) :: "memory"); \
        m_ = max(al[p][0], al[p][2]); _Pragma("unroll") for (int e = 0; e < 4; ++e) m_ = max(m_, max(a[4 * (p) + e][0], a[4 * (p) + e][2])); __any(m_ == ~0u); })
#define OGRP(p, N) do { \
        if (OCHK(p, N)) { \
          _Pragma("unroll 1") for (int n = 0; n < (1 << 20); ++n) { OLG(p, "+"); if ((p) < 3) OLG(((p) + 1) & 3, "+"); if (!OCHK(p, N)) break; } } \
        SB(); \
        _Pragma("unroll") for (int e = 0; e < 25; ++e) { const int i = 25 * (p) + e; \
          asm volatile("v_mfma_f32_16x16x32_bf16 %0, %1, %2, %0" : "+v"(y[i % 5]) : "v"(wr[i % NW]), "v"(e < 20 ? a[4 * (p) + (e / 5 & 3)] : al[p])); \
          if ((p) < 2 && e % 4 == 3) { if (e < 16) OL16(((p) + 2) & 3, (e / 4) & 3, "=&v"); else if (e == 19) OL8(((p) + 2) & 3, "=&"); } \
          const int j = NW + (i * 4 + 4) / 5;        \
          if (i % 5 != 4 && j < 100) wr[j % NW] = *(LAS bf16x8*)(j < 60 ? wl + j * 1024 : wp2 + (j - 60) * 1024); \
          if (i == 70) pg = gc[w ^ 1]; \
          if (i >= 90 && i < 95) zz[i - 90] = *(volatile LAS f32x4*)(r0 + (i - 90) * 1024); \
          SB(); } } while (0)
      f32x4 zz[5];
      OGRP(0, 6); OGRP(1, 6); OGRP(2, 6);
      if (__builtin_amdgcn_readfirstlane(pg) < 3) SPIN(__builtin_amdgcn_readfirstlane(gc[w ^ 1]) >= 3);
      OGRP(3, 0);
#pragma unroll
      for (int t = 0; t < 5; ++t) { z[t] += zz[t]; asm volatile("" : "+v"(z[t])); }
      asm volatile("s_nop 7" : "+v"(y[0]), "+v"(y[1]), "+v"(y[2]), "+v"(y[3]), "+v"(y[4]));
      const rsrc_t rout = mkrs(out, 2048 * D_ * 2);
      const f32x4 y4 = y[4] + z[4];
      __builtin_amdgcn_raw_buffer_store_b64(u32x2{pk2(y4[0], y4[1]), pk2(y4[2], y4[3])}, rout, xo4, 0, 16);
      __builtin_amdgcn_raw_buffer_store_b128(pk8(y[0] + z[0], y[1] + z[1]), rout, xo, 0, 16);
      __builtin_amdgcn_raw_buffer_store_b128(pk8(y[2] + z[2], y[3] + z[3]), rout, xo + 64, 0, 16);
    }
    STAMP(11);
  }
}

extern "C" void kernel_launch(void* const* d_in, const int* in_sizes, int n_in, void* d_out, int out_size, void* d_ws, size_t ws_size,
                              hipStream_t stream) {
  fused<<<256, 512, 0, stream>>>((const u16*)d_in[0], (const u16*)d_in[1], (const u16*)d_in[2], (const u16*)d_in[4], (const u16*)d_in[5],
                                 (const u16*)d_in[6], (const u16*)d_in[7], (u16*)d_out, (char*)d_ws);
}
```

```cpp
#define WP 2
#define GP 2
#include <hip/hip_runtime.h>
#include <hip/hip_bf16.h>

typedef __attribute__((ext_vector_type(8))) __bf16 bf16x8;
typedef __attribute__((ext_vector_type(16))) __bf16 bf16x16;
typedef __attribute__((ext_vector_type(2))) __bf16 bf16x2;
typedef __attribute__((ext_vector_type(4))) short s16x4;
typedef __attribute__((ext_vector_type(16))) float f32x16;
typedef __attribute__((ext_vector_type(8))) float f32x8;
typedef __attribute__((ext_vector_type(4))) float f32x4;
typedef __attribute__((ext_vector_type(4))) unsigned u32x4;
typedef __attribute__((ext_vector_type(2))) unsigned u32x2;
typedef unsigned short u16;
typedef unsigned long long u64;
typedef __amdgpu_buffer_rsrc_t rsrc_t;
#define LAS __attribute__((address_space(3)))
#define GAS __attribute__((address_space(1)))
#define SB() __builtin_amdgcn_sched_barrier(0)
#define RLX_AGENT __ATOMIC_RELAXED, __HIP_MEMORY_SCOPE_AGENT
#define BAR_L() do { asm volatile("" ::: "memory"); __builtin_amdgcn_s_waitcnt(0xC07F); asm volatile("s_barrier" ::: "memory"); } while (0)
#define DRAIN() asm volatile("s_waitcnt vmcnt(0)" ::: "memory")

constexpr int D_ = 640, F_ = 1536;
constexpr unsigned PT = 7808, P_V = 2560, P_Q = 5120, P_ONE = 7680;
constexpr unsigned WS_T = 0;
constexpr unsigned WS_H = 8u << 20;
constexpr unsigned WS_W = 16u << 20;
constexpr unsigned WS_O = 20u << 20;
constexpr unsigned WS_O2 = 22u << 20;
constexpr unsigned WS_S = 23u << 20;
constexpr unsigned WS_F = 24u << 20;
constexpr unsigned WS_END = WS_F;
constexpr unsigned F_Q = 256, F_H = 512, F_O = 768;
constexpr float QSCALE = 0.22360679774997896f * 1.4426950408889634f;
constexpr float MARGIN = 88.f;
constexpr unsigned OOB = 0x80000000u;
constexpr unsigned CODE_SKIP = 4608;
constexpr unsigned XS = 16448, WSL = 16640, L_W = 6 * XS;
constexpr unsigned L_WO = 0, L_RED = 102400, L_MRG = 122880;
constexpr unsigned L_ST = L_W + 3 * WSL;
constexpr unsigned L_G = L_ST + 2048;
constexpr unsigned L_M = L_G + 4096;
constexpr unsigned L_C = L_M + 4096;
constexpr unsigned L_PF = L_C + 64;
constexpr unsigned L_END = L_PF + 256;

#define STAMP(i)
#define STEPSTAMP()

#define FS(v)
#define ITS()
__device__ __forceinline__ unsigned pk2(float a, float b) { bf16x2 r = {(__bf16)a, (__bf16)b}; return __builtin_bit_cast(unsigned, r); }
__device__ __forceinline__ u32x4 pk8(f32x4 a, f32x4 b) { return u32x4{pk2(a[0], a[1]), pk2(a[2], a[3]), pk2(b[0], b[1]), pk2(b[2], b[3])}; }
__device__ __forceinline__ float blo(unsigned u) { return __uint_as_float(u << 16); }
__device__ __forceinline__ float bhi(unsigned u) { return __uint_as_float(u & 0xffff0000u); }
__device__ __forceinline__ u64 uni64(u64 v) {
  return ((u64)(unsigned)__builtin_amdgcn_readfirstlane((unsigned)(v >> 32)) << 32) | (unsigned)__builtin_amdgcn_readfirstlane((unsigned)v);
}
__device__ __forceinline__ int fresh_lane() { int l; asm volatile("v_mbcnt_lo_u32_b32 %0, -1, 0\n\tv_mbcnt_hi_u32_b32 %0, -1, %0" : "=v"(l)); return l; }
__device__ __forceinline__ rsrc_t mkrs(const void* p, unsigned bytes) { return __builtin_amdgcn_make_buffer_rsrc((void*)p, 0, bytes, 0x00020000); }
__device__ __forceinline__ void dma16(rsrc_t r, unsigned vo, unsigned so, unsigned lds_base) {
  asm volatile("s_mov_b32 m0, %0\n\ts_nop 0\n\tbuffer_load_dwordx4 %1, %2, %3 offen lds" :: "s"(lds_base), "v"(vo), "s"(r), "s"(so) : "memory");
}
__device__ __forceinline__ void touch(rsrc_t r, unsigned vo, unsigned lds_base) {
  asm volatile("s_mov_b32 m0, %0\n\ts_nop 0\n\tbuffer_load_dword %1, %2, 0 offen lds" :: "s"(lds_base), "v"(vo), "s"(r) : "memory");
}
#define STOUCH3(p) asm volatile("s_load_dword s100, %0, 0\n\ts_load_dword s100, %0, 64\n\ts_load_dword s100, %0, 128" :: "s"(p) : "s100")
#define SPFX(x) x
#define STOUCH4(p) asm volatile("s_load_dword s100, %0, 0\n\ts_load_dword s100, %0, 64\n\ts_load_dword s100, %0, 128\n\ts_load_dword s100, %0, 192" :: "s"(p) : "s100")
__device__ __forceinline__ bf16x8 ld16(rsrc_t r, unsigned vo, unsigned so) { return __builtin_bit_cast(bf16x8, __builtin_amdgcn_raw_buffer_load_b128(r, vo, so, 16)); }
__device__ __forceinline__ s16x4 ld8(rsrc_t r, unsigned vo, unsigned so) { return __builtin_bit_cast(s16x4, __builtin_amdgcn_raw_buffer_load_b64(r, vo, so, 16)); }
__device__ __forceinline__ void st16(u32x4 v, rsrc_t r, unsigned vo, unsigned so) { __builtin_amdgcn_raw_buffer_store_b128(v, r, vo + so, 0, 16); }
__device__ __forceinline__ void st8(u32x2 v, rsrc_t r, unsigned vo, unsigned so) { __builtin_amdgcn_raw_buffer_store_b64(v, r, vo + so, 0, 16); }
__device__ __forceinline__ void publish(GAS u64* f, unsigned i, u64 tag, int lane) {
  st16(u32x4{(unsigned)tag, (unsigned)(tag >> 32), (unsigned)tag, (unsigned)(tag >> 32)}, mkrs((const void*)(f + 16 * i), 128), lane * 16, 0);
}
template <int SLP> __device__ __forceinline__ void await(GAS u64* f, unsigned i, u64 tag) {
#pragma unroll 1
  for (int n = 0; n < (1 << 20); ++n) {
    if (__all(__hip_atomic_load(f + 16 * i, RLX_AGENT) == tag)) break;
    __builtin_amdgcn_s_sleep(SLP);
  }
  __builtin_amdgcn_fence(__ATOMIC_ACQUIRE, "wavefront");
}
__device__ __forceinline__ void lds_add(LAS unsigned* c, unsigned v) {
  asm volatile("s_mov_b64 exec, 1\n\tds_add_u32 %0, %1\n\ts_mov_b64 exec, -1" :: "v"(c), "v"(v) : "memory");
}
__device__ __forceinline__ unsigned lds_add_rtn(LAS unsigned* c, unsigned v) {
  unsigned r_;
  asm volatile("s_mov_b64 exec, 1\n\tds_add_rtn_u32 %0, %1, %2\n\ts_mov_b64 exec, -1\n\ts_waitcnt lgkmcnt(0)" : "=&v"(r_) : "v"(c), "v"(v) : "memory");
  return __builtin_amdgcn_readfirstlane(r_);
}
template <int SLP> __device__ __forceinline__ void lds_meet(LAS unsigned* c, unsigned n, int lane) {
  lds_add(c, 1u);
#pragma unroll 1
  for (int k = 0; k < (1 << 22); ++k) {
    if (__builtin_amdgcn_readfirstlane(*(volatile LAS unsigned*)c) >= n) break;
    if (SLP) __builtin_amdgcn_s_sleep(SLP);
  }
  asm volatile("" ::: "memory");
}
__device__ __forceinline__ s16x4 trd(LAS char* p) { return __builtin_amdgcn_ds_read_tr16_b64_v4i16((LAS s16x4*)p); }
__device__ __forceinline__ bf16x8 cat8(s16x4 a, s16x4 b) { return __builtin_bit_cast(bf16x8, __builtin_shufflevector(a, b, 0, 1, 2, 3, 4, 5, 6, 7)); }
template <int C> __device__ __forceinline__ float dppadd(float a) {
  return a + __int_as_float(__builtin_amdgcn_update_dpp(0, __float_as_int(a), C, 0xf, 0xf, true));
}

__device__ __forceinline__ u32x2 sqrelu4(f32x4 a, f32x4 c, f32x4 bb, float r, float t) {
  float v0, v1, v2, v3;
  asm("v_fma_f32 %0, %8, %17, %12\n\tv_fma_f32 %1, %9, %17, %13\n\tv_fma_f32 %2, %10, %17, %14\n\tv_fma_f32 %3, %11, %17, %15\n\t"
      "v_fmac_f32 %0, %4, %16\n\tv_fmac_f32 %1, %5, %16\n\tv_fmac_f32 %2, %6, %16\n\tv_fmac_f32 %3, %7, %16\n\t"
      "v_mul_f32 %0, %0, |%0|\n\tv_mul_f32 %1, %1, |%1|\n\tv_mul_f32 %2, %2, |%2|\n\tv_mul_f32 %3, %3, |%3|\n\t"
      "v_cvt_pk_bf16_f32 %0, %0, %1\n\tv_cvt_pk_bf16_f32 %2, %2, %3\n\tv_pk_max_i16 %0, %0, 0\n\tv_pk_max_i16 %2, %2, 0"
      : "=&v"(v0), "=&v"(v1), "=&v"(v2), "=&v"(v3)
      : "v"(a[0]), "v"(a[1]), "v"(a[2]), "v"(a[3]), "v"(c[0]), "v"(c[1]), "v"(c[2]), "v"(c[3]), "v"(bb[0]), "v"(bb[1]), "v"(bb[2]), "v"(bb[3]), "v"(r), "v"(t));
  return u32x2{__float_as_uint(v0), __float_as_uint(v2)};
}

template <int N> struct IC { static constexpr int v = N; };
template <int MODE> struct G1 {
  static constexpr int NM = MODE == 2 ? 2 : 4, NI = MODE == 2 ? 6 : 4, PAD = MODE == 1 ? 32 : 8, NP = NI / 2;
  static constexpr int NMMA = 2 * NM * NI, NRD = 2 * (NM + 2 * NI), HEAD = MODE == 2 ? 6 : 12, R2H = 2, R2S = 18;
  struct Frag { bf16x8 x[2][NM]; s16x4 w[2][NI][2]; };
  struct Ptr { LAS char* x[2][2]; LAS char* w[NP]; };
  template <int R, int XO = 0, int WO = 0> static __device__ __forceinline__ void rd(Frag& f, const Ptr& p) {
    constexpr bool isx = R == 2 || (R >= 5 && R < 4 + 2 * NM);
    if constexpr (isx) { constexpr int k = R == 2 ? 0 : R - 4, u = k & 1, m = k >> 1;
      f.x[u][m] = *(LAS bf16x8*)(p.x[u][m & 1] + XO + (m >> 1) * 4096 + (m & 1) * (MODE == 1 ? 512 : 2048)); }
    else { constexpr int q = R < 2 ? R : (R < 5 ? R - 1 : R - 2 * NM), i = q >> 2, u = (q >> 1) & 1;
      f.w[u][i][q & 1] = trd(p.w[i >> 1] + WO + (i & 1) * PAD + u * (8192 + 4 * PAD) + (q & 1) * 1024); }
  }
  template <int N> static __device__ __forceinline__ void mma(f32x4 (&acc)[NM][NI], const Frag& f) {
    constexpr int u = N & 1, i = N / (2 * NM), m0 = (N >> 1) % NM, m = (i & 1) ? NM - 1 - m0 : m0;
    const bf16x8 wf = cat8(f.w[u][i][0], f.w[u][i][1]);
    if constexpr (MODE == 1) asm volatile("v_mfma_f32_16x16x32_bf16 %0, %1, %2, %0" : "+v"(acc[m][i]) : "v"(f.x[u][m]), "v"(wf));
    else asm volatile("v_mfma_f32_16x16x32_bf16 %0, %1, %2, %0" : "+v"(acc[m][i]) : "v"(wf), "v"(f.x[u][m]));
  }
  template <int U> static __device__ __forceinline__ void csum(f32x4& cs, const Frag& f, bf16x8 sel, int idx) {
    const bf16x16 wf = __builtin_shufflevector(cat8(f.w[U][0][0], f.w[U][0][1]), cat8(f.w[U][1][0], f.w[U][1][1]), 0, 1, 2, 3, 4, 5, 6, 7, 8, 9, 10, 11, 12, 13, 14, 15);
    asm volatile("v_smfmac_f32_16x16x64_bf16 %0, %1, %2, %3" : "+v"(cs) : "v"(sel), "v"(wf), "v"(idx));
  }
  static __device__ __forceinline__ int span(int w, int j) { return MODE == 2 ? (w + j >= 3 ? w + j - 3 : w + j) : 2 * (w >> 1) + (j ^ (w & 1)); }
  template <int R0, int R1, int XO = 0, int WO = 0> static __device__ __forceinline__ void rds(Frag& f, const Ptr& p) {
    if constexpr (R0 < R1) { rd<R0, XO, WO>(f, p); rds<R0 + 1, R1, XO, WO>(f, p); } }
  template <int N0, int N1> static __device__ __forceinline__ void mmas(f32x4 (&acc)[NM][NI], const Frag& f) { if constexpr (N0 < N1) { mma<N0>(acc, f); mmas<N0 + 1, N1>(acc, f); } }
  template <int N> static __device__ __forceinline__ void mix(f32x4 (&acc)[NM][NI], const Frag& fc, Frag& fn, const Ptr& p) {
    if constexpr (N < NMMA) {
      constexpr int SPAN = NMMA - HEAD - 3, k = N - HEAD;
      constexpr int r0 = k >= SPAN ? NRD : (k * NRD + SPAN - 1) / SPAN, r1 = k + 1 >= SPAN ? NRD : ((k + 1) * NRD + SPAN - 1) / SPAN;
      mma<N>(acc, fc); rds<r0, r1>(fn, p); SB();
      mix<N + 1>(acc, fc, fn, p);
    }
  }
  struct Off { unsigned x, w[NP]; };
  static __device__ __forceinline__ Off offs(int w, int lane) {
    const int kq = lane >> 4, c16 = lane & 15, a = (lane >> 2) & 3;
    Off o;
    o.x = MODE == 1 ? (64 * (w & 1) + 8 * (c16 >> 2) + (c16 & 3)) * 128 + ((kq ^ ((c16 >> 1) & 1) ^ ((c16 >> 3) << 1) ^ (c16 & 4)) << 4)
                    : ((MODE == 2 ? 32 * w : 64 * (w & 1)) + c16) * 128 + ((kq ^ (c16 >> 1)) << 4);
    const unsigned wb = L_W + kq * (2048 + PAD) + a * 256 + (lane & 3) * (MODE == 1 ? 8 : 16);
#pragma unroll
    for (int j = 0; j < NP; ++j) o.w[j] = wb + (span(w, j) ^ a) * 64;
    return o;
  }
  template <int U> static __device__ __forceinline__ void ptrx(Ptr& p, const Off& o, LAS char* lds, unsigned sx) {
    p.x[U][0] = lds + sx + (o.x ^ (64 * U)); p.x[U][1] = lds + sx + (o.x ^ (64 * U + 32)); asm volatile("" : "+v"(p.x[U][0]), "+v"(p.x[U][1])); }
  static __device__ __forceinline__ void ptrw(Ptr& p, const Off& o, LAS char* lds, unsigned sw) {
#pragma unroll
    for (int j = 0; j < NP; ++j) { p.w[j] = lds + (sw + o.w[j]); asm volatile("" : "+v"(p.w[j])); } }
  template <int N, class T> static __device__ __forceinline__ void fin(f32x4 (&acc)[NM][NI], const Frag& fc, T& tail) {
    if constexpr (N < NMMA) { mma<N>(acc, fc); tail(IC<N - HEAD>{}); SB(); fin<N + 1>(acc, fc, tail); }
  }
  template <class T>
  static __device__ __forceinline__ void loop(f32x4 (&acc)[NM][NI], Frag& fa, const Off& o, LAS char* lds, unsigned sx, unsigned sw, int w, int lane, int pub,
                                              GAS u64* fl, unsigned fi, u64 tag, T tail, unsigned& stv) {
    const int kq = lane >> 4, c16 = lane & 15;
    Frag fb; Ptr p;
#pragma unroll
    for (int n = 0; n < NM * NI; ++n) { acc[n % NM][n / NM] = f32x4{}; asm volatile("" : "+v"(acc[n % NM][n / NM])); }
    const unsigned one = (kq >> 1) == ((c16 >> 1) & 1) ? 0x3f803f80u : 0u;
    bf16x8 sel = __builtin_bit_cast(bf16x8, u32x4{one, one, one, one});
    int sidx = (lane & 1) ? 0xEEEE : 0x4444;
    f32x4 cs = {};
    LAS float* cp = (LAS float*)(lds + L_ST + 1024) + 32 * span(w, 0) + (MODE == 1 ? c16 : 8 * (c16 >> 2) + (c16 & 3));
    asm volatile("s_nop 1" : "+v"(sel), "+v"(sidx), "+v"(cs), "+v"(cp));
    if constexpr (MODE != 2) { ptrx<0>(p, o, lds, sx); ptrx<1>(p, o, lds, sx); ptrw(p, o, lds, sw); SB(); tail(IC<-1>{}); STAMP(1); BAR_L(); STAMP(12); rds<0, NRD>(fa, p); }
#define G1_HEAD(FC, PUB, LAST, BAR) do { \
      mma<0>(acc, FC); sx = (sx == 5 * XS) ? 0 : sx + XS; asm volatile("" : "+s"(sx)); SB(); \
      mma<1>(acc, FC); sw = (sw == 2 * WSL) ? 0 : sw + WSL; asm volatile("" : "+s"(sw)); SB(); \
      mma<2>(acc, FC); ptrx<0>(p, o, lds, sx); SB(); \
      mma<3>(acc, FC); ptrw(p, o, lds, sw); SB(); \
      mmas<4, HEAD - 2>(acc, FC); SB(); \
      csum<0>(cs, FC, sel, sidx); csum<1>(cs, FC, sel, sidx); SB(); \
      mma<HEAD - 2>(acc, FC); ptrx<1>(p, o, lds, sx); SB(); \
      mma<HEAD - 1>(acc, FC); SB(); \
      if (LAST) { asm volatile("s_nop 5" : "+v"(cs)); cp[0] = cs[0] + cs[1]; cp[MODE == 1 ? 16 : 4] = cs[2] + cs[3]; } \
      if (PUB) DRAIN(); \
      if (BAR) BAR_L(); \
      if ((PUB) && w == 0) publish(fl, fi, tag, lane); \
      SB(); } while (0)
    SB();
    G1_HEAD(fa, false, false, true);
#pragma unroll 1
    for (int s = 0; s < 4; ++s) {
      mix<HEAD>(acc, fa, fb, p);
      G1_HEAD(fb, false, false, true);
      mix<HEAD>(acc, fb, fa, p);
      G1_HEAD(fa, false, false, true);
    }
    mix<HEAD>(acc, fa, fb, p);
    G1_HEAD(fb, false, true, true);
    fin<HEAD>(acc, fb, tail);
    asm volatile("s_nop 7");
    SB();
  }
  template <int J, int N> static __device__ __forceinline__ void r2mix(f32x4 (&acc)[NM][NI], f32x4& cs, bf16x8 sel, int sidx, const Frag& fc, Frag& fn, const Ptr& pl, const Ptr& ph) {
    if constexpr (N < NMMA) {
      constexpr int H2 = R2H, SPAN = R2S, k = N - H2, xs = (5 + J) % 6;
      constexpr int r0 = k >= SPAN ? NRD : (k * NRD + SPAN - 1) / SPAN, r1 = k + 1 >= SPAN ? NRD : ((k + 1) * NRD + SPAN - 1) / SPAN;
      mma<N>(acc, fc);
      Ptr p = xs >= 3 ? ph : pl;
#pragma unroll
      for (int j = 0; j < NP; ++j) p.w[j] = J == 8 ? ph.w[j] : pl.w[j];
      rds<r0, r1, (xs % 3) * XS, J == 8 ? 0 : ((2 + J) % 3) * WSL>(fn, p); SB();
      r2mix<J, N + 1>(acc, cs, sel, sidx, fc, fn, pl, ph);
    }
  }
  template <int J, class PUB> static __device__ __forceinline__ void r2steps(f32x4 (&acc)[NM][NI], f32x4& cs, bf16x8 sel, int sidx, Frag& fc, Frag& fn, const Ptr& pl, const Ptr& ph, PUB& pub) {
    if constexpr (J < 9) {
      mmas<0, 2>(acc, fc); csum<0>(cs, fc, sel, sidx); csum<1>(cs, fc, sel, sidx); SB();
      if constexpr (J == 2) DRAIN();
      if constexpr (J < 8) BAR_L();
      if constexpr (J == 2) pub();
      SB();
      r2mix<J, R2H>(acc, cs, sel, sidx, fc, fn, pl, ph);
      r2steps<J + 1>(acc, cs, sel, sidx, fn, fc, pl, ph, pub);
    }
  }
  template <class PUB, class T>
  static __device__ __forceinline__ void loop2(f32x4 (&acc)[NM][NI], Frag& fa, const Off& o, const Ptr& ph, LAS char* lds, int w, int lane, PUB pub, T tail) {
    const int kq = lane >> 4, c16 = lane & 15;
    Frag fb; Ptr pl, ph2 = ph;
    ptrx<0>(pl, o, lds, 0); ptrx<1>(pl, o, lds, 0);
#pragma unroll
    for (int j = 0; j < NP; ++j) pl.w[j] = ph.w[j];
    ptrw(ph2, o, lds, 3 * XS - L_W);
#pragma unroll
    for (int n = 0; n < NM * NI; ++n) { acc[n % NM][n / NM] = f32x4{}; asm volatile("" : "+v"(acc[n % NM][n / NM])); }
    const unsigned one = (kq >> 1) == ((c16 >> 1) & 1) ? 0x3f803f80u : 0u;
    bf16x8 sel = __builtin_bit_cast(bf16x8, u32x4{one, one, one, one});
    int sidx = (lane & 1) ? 0xEEEE : 0x4444;
    f32x4 cs = {};
    LAS float* cp = (LAS float*)(lds + L_ST + 1024) + 32 * span(w, 0) + 8 * (c16 >> 2) + (c16 & 3);
    asm volatile("s_nop 1" : "+v"(sel), "+v"(sidx), "+v"(cs), "+v"(cp));
    SB();
    r2steps<0>(acc, cs, sel, sidx, fa, fb, pl, ph2, pub);
    mmas<0, 2>(acc, fb); csum<0>(cs, fb, sel, sidx); mmas<2, 4>(acc, fb); csum<1>(cs, fb, sel, sidx); mmas<4, HEAD>(acc, fb); SB();
    fin<HEAD>(acc, fb, tail);
    asm volatile("s_nop 7" : "+v"(cs));
    cp[0] = cs[0] + cs[1]; cp[4] = cs[2] + cs[3];
    SB();
  }
};

struct KV { bf16x8 k1; s16x4 k2; bf16x8 v0, v1; };
__device__ __forceinline__ f32x16 qk_a(bf16x8 k, bf16x8 q) { f32x16 z = {}; return __builtin_amdgcn_mfma_f32_32x32x16_bf16(k, q, z, 0, 0, 0); }
__device__ __forceinline__ f32x16 qk_b(s16x4 k, s16x4 q, f32x16 c) { return __builtin_amdgcn_mfma_f32_32x32x8bf16_1k(k, q, c, 0, 0, 0); }
__device__ __forceinline__ f32x16 pv_lo(bf16x8 v, f32x16 p, f32x16 o, int idx) {
  f32x8 h = __builtin_shufflevector(p, p, 0, 1, 2, 3, 4, 5, 6, 7);
  o = __builtin_amdgcn_smfmac_f32_32x32x32_bf16(v, __builtin_bit_cast(bf16x16, h), o, idx, 0, 0);
  asm volatile("" : "+v"(o));
  return o; }
__device__ __forceinline__ f32x16 pv_hi(bf16x8 v, f32x16 p, f32x16 o, int idx) {
  f32x8 h = __builtin_shufflevector(p, p, 8, 9, 10, 11, 12, 13, 14, 15);
  o = __builtin_amdgcn_smfmac_f32_32x32x32_bf16(v, __builtin_bit_cast(bf16x16, h), o, idx, 0, 0);
  asm volatile("" : "+v"(o));
  return o; }
template <int E> __device__ __forceinline__ void max8(const f32x16& s, float& m) {
  asm volatile("v_max3_f32 %0, %0, %1, %2\n\tv_max3_f32 %0, %0, %3, %4\n\tv_max3_f32 %0, %0, %5, %6\n\tv_max3_f32 %0, %0, %7, %8"
               : "+v"(m) : "v"(s[E]), "v"(s[E + 1]), "v"(s[E + 2]), "v"(s[E + 3]), "v"(s[E + 4]), "v"(s[E + 5]), "v"(s[E + 6]), "v"(s[E + 7]));
}
struct F4 { float v[4]; };
__device__ __forceinline__ F4 add4(f32x4 g, float a, float b, float c, float d, float& chk) {
  F4 r;
  asm volatile("v_add_f32 %0, %9, %5\n\tv_add_f32 %1, %10, %6\n\tv_add_f32 %2, %11, %7\n\tv_add_f32 %3, %12, %8\n\t"
               "v_maximum3_f32 %4, %4, |%0|, |%1|\n\tv_maximum3_f32 %4, %4, |%2|, |%3|"
               : "=&v"(r.v[0]), "=&v"(r.v[1]), "=&v"(r.v[2]), "=&v"(r.v[3]), "+v"(chk) : "v"(a), "v"(b), "v"(c), "v"(d), "v"(g[0]), "v"(g[1]), "v"(g[2]), "v"(g[3]));
  return r;
}
__device__ __forceinline__ u32x2 scale4(F4 g, float s) {
  unsigned r0, r1;
  asm volatile("v_mul_f32 %2, %2, %6\n\tv_mul_f32 %3, %3, %6\n\tv_mul_f32 %4, %4, %6\n\tv_mul_f32 %5, %5, %6\n\tv_cvt_pk_bf16_f32 %0, %2, %3\n\tv_cvt_pk_bf16_f32 %1, %4, %5"
               : "=&v"(r0), "=&v"(r1), "+v"(g.v[0]), "+v"(g.v[1]), "+v"(g.v[2]), "+v"(g.v[3]) : "v"(s));
  return u32x2{r0, r1};
}
#define EX4(s, b) do { s[b] = __builtin_amdgcn_exp2f(s[b]); s[b+1] = __builtin_amdgcn_exp2f(s[b+1]); s[b+2] = __builtin_amdgcn_exp2f(s[b+2]); s[b+3] = __builtin_amdgcn_exp2f(s[b+3]); } while (0)
#define STEPX(SC, SP, OP, VP, SN, KN, Q1, Q2, X0, X1) do { \
  EX4(SC, 0); SB(); OP = pv_lo(VP.v0, SP, OP, idx); SB(); X0; SB(); \
  EX4(SC, 4); SB(); SN = qk_a(KN.k1, Q1); SB(); \
  EX4(SC, 8); SB(); OP = pv_hi(VP.v1, SP, OP, idx); SB(); X1; SB(); \
  EX4(SC, 12); SB(); SN = qk_b(KN.k2, Q2, SN); SB(); } while (0)
#define STEP(SC, SP, OP, VP, SN, KN, Q1, Q2) STEPX(SC, SP, OP, VP, SN, KN, Q1, Q2, , )

__global__ void __launch_bounds__(512, 2)
fused(const u16* x_, const u16* wqkv_, const u16* wo_, const u16* w1_, const u16* b1_, const u16* w2_, const u16* b2_, u16* out_, char* ws_) {
  __shared__ __attribute__((aligned(16))) char smem[L_END + 256];
  LAS char* lds = (LAS char*)smem;
  const unsigned lds0 = (unsigned)(size_t)lds;
  const int lane = threadIdx.x & 63, w = __builtin_amdgcn_readfirstlane(threadIdx.x >> 6);
  const int b = blockIdx.x, g = b & 7, idx8 = b >> 3;
  unsigned stv = 0;
  typedef __attribute__((ext_vector_type(8))) u64 u64x8;
  u64x8 ka; u64 kw;
  asm volatile("s_load_dwordx16 %0, %2, 0\n\ts_load_dwordx2 %1, %2, 64\n\ts_getpc_b64 s[98:99]\n\ts_and_b32 s98, s98, 0xffffff00\n\ts_add_u32 s98, s98, %3\n\ts_addc_u32 s99, s99, 0\n\ts_load_dword s100, s[98:99], 0\n\ts_load_dword s100, s[98:99], 64\n\ts_waitcnt lgkmcnt(0)"
               : "=&s"(ka), "=&s"(kw) : "s"(__builtin_amdgcn_kernarg_segment_ptr()), "s"((idx8 * 8 + w) * 128u + 256u) : "s98", "s99", "s100", "scc");
  const u16* x = (const u16*)ka[0]; const u16* wqkv = (const u16*)ka[1]; const u16* wo = (const u16*)ka[2]; const u16* w1 = (const u16*)ka[3];
  const u16* b1 = (const u16*)ka[4]; const u16* w2 = (const u16*)ka[5]; const u16* b2 = (const u16*)ka[6]; u16* out = (u16*)ka[7]; char* ws = (char*)kw;
  const int rt = (g & 3) * 4 + (idx8 & 3), ct = (g >> 2) * 8 + (idx8 >> 2);
  const bool vtype = ct >= 10;
  const int kq = lane >> 4, c16 = lane & 15;
  LAS float* st_r = (LAS float*)(lds + L_ST);
  LAS float* st_t = st_r + 128;
  LAS float* st_c = st_r + 256;
  GAS u64* fl = (GAS u64*)(ws + WS_F);
  const rsrc_t rw = mkrs(ws, WS_END), rx = mkrs(x, 2048 * D_ * 2);
  u64 tag = 0;
  const int head = 4 * g + (idx8 >> 3), qc = idx8 & 7;
  const int aw = w - 4, qh = aw & 1, kh = aw >> 1, r = lane & 31, h = lane >> 5, odd = head & 1, pr = head >> 1;
  int hstep = 0;
  unsigned oK1 = 0, oK2 = 0, oV0 = 0, oV1 = 0, oQ2 = 0;
  auto kv_offsets = [&](int ln) {
    const int r = ln & 31, h = ln >> 5;
    oK1 = odd * 1536 + ln * 16; oK2 = h ? P_ONE : 1024 + r * 16 + odd * 8; oQ2 = h ? OOB : oK2;
    const int dv = r < 16 ? (r & 3) + 4 * (r >> 3) + 8 * ((r >> 2) & 1) : r;
    oV0 = r < 20 ? P_V + odd * 1280 + h * 320 + dv * 16 : (r == 20 ? P_ONE : OOB); oV1 = r < 20 ? oV0 + 640 : oV0;
  };
  unsigned fi = 0;

  if (w >= 4) {
    const int hw = w - 4;
    rsrc_t rwt = mkrs(wqkv, D_ * 3 * D_ * 2);
    unsigned rs = 3 * D_ * 2, pad = vtype ? 32 : 8;
    unsigned wso = ct * 256 + 16 * hw * rs, xso = (rt * 128 + hw * 32) * (D_ * 2);
    const unsigned xv = (lane >> 3) * (D_ * 2) + (((lane & 7) ^ kq) << 4), xv1 = xv ^ 64, xv2 = xv ^ 32, xv3 = xv ^ 96;
    const int wc = c16 ^ (kq << 2);
    unsigned wv = ct < 15 ? kq * rs + (wc << 4) : OOB;
    unsigned nx = lds0 + hw * 4096, nw = lds0 + L_W + hw * 4096;
#define DX(i) dma16(rx, i == 0 ? xv : (i == 1 ? xv1 : (i == 2 ? xv2 : xv3)), xso + i * 8 * (D_ * 2), nx + i * 1024)
#define DW(i) dma16(rwt, wv, wso + 4 * i * rs, nw + i * 1024 + (2 * hw + (i >> 1)) * pad)
#define NEXTX() do { xso += 128; nx = (nx == lds0 + hw * 4096 + 5 * XS) ? nx - 5 * XS : nx + XS; } while (0)
#define NEXTW() do { wso += 64 * rs; nw = (nw == lds0 + L_W + hw * 4096 + 2 * WSL) ? nw - 2 * WSL : nw + WSL; } while (0)
#define HBAR(n) asm volatile("s_waitcnt vmcnt(" #n ")\n\ts_barrier" ::: "memory")
    __builtin_amdgcn_s_setprio(1);
#define DW4() do { DW(0); DW(1); DW(2); DW(3); NEXTW(); } while (0)
#define DX4() do { DX(0); DX(1); DX(2); DX(3); NEXTX(); } while (0)
    const char* pw = (const char*)w1 + 256 * F_ * 2 + (b * 4 + hw) * 192;
    {
      DX(0); DW(0); DX(1); DW(1); DX(2); DW(2); DX(3); DW(3); NEXTW(); NEXTX();
      DW4(); DX4(); SPFX(STOUCH3(pw)); pw += 64 * F_ * 2; HBAR(8);
      DX4();
#pragma unroll 1
      for (int s = 0; s < 7; ++s) { DW4(); DX4(); SPFX(STOUCH3(pw)); pw += (s == 4 ? -576 : 64) * F_ * 2; HBAR(12); }
    }
    const unsigned go = WS_S + (rt * 128 + (hw & 1) * 64 + lane) * 16;
    asm volatile("s_mov_b32 m0, %0\n\ts_nop 0\n\tbuffer_load_dwordx4 %1, %2, 0 offen sc1 lds" :: "s"(lds0 + L_G + hw * 1024), "v"(go), "s"(rw) : "memory");
    DW4(); SPFX(STOUCH3(pw)); pw += 64 * F_ * 2; HBAR(9);
    rwt = mkrs(w1, D_ * F_ * 2); rs = F_ * 2; pad = 8; wso = ct * 192 + (256 + 16 * hw) * rs; wv = wc < 12 ? kq * rs + (wc << 4) : OOB;
    xso -= 1280; nx = lds0 + hw * 4096 + 4 * XS; nw = lds0 + L_W + hw * 4096 + WSL;
    DW4();
    tag = uni64(*(LAS u64*)(lds + L_C));
    {
      asm volatile("s_waitcnt vmcnt(8)" ::: "memory");
      u32x4 gr = *(LAS u32x4*)(lds + L_G + hw * 1024 + lane * 16);
#pragma unroll 1
      for (int n = 0; n < (1 << 20); ++n) {
        if (__all(gr[1] == (unsigned)tag && gr[3] == (unsigned)tag)) break;
        asm volatile("buffer_load_dwordx4 %0, %1, %2, 0 offen sc1\n\ts_waitcnt vmcnt(0)" : "=&v"(gr) : "v"(go), "s"(rw) : "memory");
      }
      st_r[hw * 64 + lane] = __uint_as_float(hw < 2 ? gr[0] : gr[2]);
    }
    HBAR(4);
    DW4(); asm volatile("s_waitcnt lgkmcnt(0)" ::: "memory"); SPFX(STOUCH3(pw)); HBAR(4);
    DW4();
    {
      const int c0 = pr * 5 + 2 * odd, e = (lane >> 3) & 1;
      const int tqk = (c0 + 2 * e) >> 4, tv = (head * 20 + 19 * e) >> 7;
      fi = F_Q + (lane < 16 ? 5 + tqk : 10 + tv) * 16 + 8 * kh + (lane & 7);
      if (lane >= 32) fi = F_Q + tqk * 16 + 2 * qc + qh;
      kv_offsets(lane);
      asm volatile("" : "+v"(fi), "+v"(oK1), "+v"(oK2), "+v"(oV0), "+v"(oV1), "+v"(oQ2));
    }
    HBAR(4);
    DW4(); DX4(); HBAR(8);
    DW4(); DX4(); HBAR(12);
    DW4(); DX4(); wso -= 640 * rs; HBAR(12);
    DW4(); DX4(); HBAR(12);
    DW4(); HBAR(8);
    DW4(); nw = lds0 + 3 * XS + hw * 4096; DW4(); HBAR(8);
    asm volatile("s_mov_b32 m0, %0\n\ts_nop 0\n\tbuffer_load_dwordx4 %1, %2, 0 offen sc1 lds" :: "s"(lds0 + L_G + hw * 1024), "v"(fi * 128), "s"(mkrs(ws + WS_F, 1u << 17)) : "memory");
  } else {
    const int wm = w & 1, wn = w >> 1;
    const int row = rt * 128 + ct * 8 + 2 * w + (lane >> 5);
    {
      const unsigned lb = lds0 + 5 * XS + w * 4096, xo = row * (D_ * 2) + r * 16;
      dma16(rx, xo, 0, lb); dma16(rx, xo, 512, lb + 1024); dma16(rx, r < 16 ? xo : OOB, 1024, lb + 2048);
      asm volatile("s_mov_b32 m0, %0\n\ts_nop 0\n\tbuffer_load_dword %1, %2, 0 offen sc1 lds" :: "s"(lb + 3072), "v"(lane * 4), "s"(mkrs(ws + WS_F, 8)) : "memory");
      u64 pc; asm volatile("s_getpc_b64 %0" : "=s"(pc));
      asm volatile("s_load_dword s100, %0, 0" :: "s"((pc & ~(u64)255) + 32512 + min((idx8 * 4 + w) * 64u, 5120u)) : "s100");
      *(LAS u32x4*)(lds + L_M + threadIdx.x * 16) = u32x4{0x7fc00000u, 0x7fc00000u, 0x7fc00000u, 0x7fc00000u};
    }
    unsigned vo[4];
    if (vtype) {
#pragma unroll
      for (int i = 0; i < 4; ++i) {
        const int fv = (ct - 10) * 128 + 32 * G1<1>::span(w, i >> 1) + 16 * (i & 1) + c16, hd = fv / 20, d = fv - hd * 20;
        vo[i] = (WS_T + ((hd >> 1) * 64 + 4 * rt + 2 * wm) * PT + P_V + (hd & 1) * 1280 + kq * 320 + d * 16) | (ct < 15 ? 0u : OOB);
      }
    } else {
#pragma unroll
      for (int pp = 0; pp < 2; ++pp) {
        const int c = (ct < 5 ? ct : ct - 5) * 16 + 4 * G1<0>::span(w, pp) + kq, p5 = c / 5, pos = c - p5 * 5;
        const unsigned tb = WS_T + (p5 * 64 + 4 * rt + 2 * wm) * PT;
        vo[pp] = tb + pos * 512 + (ct < 5 ? P_Q : 0u) + c16 * 16;
        vo[2 + pp] = (ct >= 5 && pos == 2) ? tb + P_ONE + (c16 >> 3) * PT + (c16 & 7) * 16 : OOB;
      }
    }
    asm volatile("" : "+v"(vo[0]), "+v"(vo[1]), "+v"(vo[2]), "+v"(vo[3]));
    if (w < 2) touch(w ? mkrs(b1 + ct * 96, 192) : mkrs(wqkv + 94 * 3 * D_, 546 * 3 * D_ * 2), w ? lane * 4 : (b * 64 + lane) * 128, lds0 + (w ? L_ST + 1536 : L_PF));
    u32x4 xr[3]; u64 e0;
    auto stats_ld = [&]() {
      if (w < 2) asm volatile("s_waitcnt vmcnt(1)" ::: "memory"); else DRAIN();
#pragma unroll
      for (int i = 0; i < 3; ++i) xr[i] = *(LAS u32x4*)(lds + 5 * XS + w * 4096 + i * 1024 + lane * 16);
      e0 = *(LAS u64*)(lds + 5 * XS + w * 4096 + 3072);
    };
    auto stats = [&]() {
      float p = 0.f, q = 0.f;
#pragma unroll
      for (int i = 0; i < 12; ++i) {
        const unsigned u2 = xr[i >> 2][i & 3];
        const bf16x2 v2 = __builtin_bit_cast(bf16x2, u2);
        p = __builtin_amdgcn_fdot2_f32_bf16(v2, __builtin_bit_cast(bf16x2, 0x3f803f80u), p, false); q = __builtin_amdgcn_fdot2_f32_bf16(v2, v2, q, false);
      }
      p = dppadd<0x140>(dppadd<0x141>(dppadd<0x4E>(dppadd<0xB1>(p)))); q = dppadd<0x140>(dppadd<0x141>(dppadd<0x4E>(dppadd<0xB1>(q))));
      p = dppadd<0x142>(p); q = dppadd<0x142>(q);
      const float mu = p * (1.f / D_), rstd = __builtin_amdgcn_rsqf(fmaxf(q * (1.f / D_) - mu * mu, 0.f) + 1e-5f);
      tag = uni64(e0 + 1);
      if (threadIdx.x == 0) { *(LAS u64*)(lds + L_C) = tag; *(LAS u32x4*)(lds + L_C + 16) = u32x4{0, 0, 0, 0}; *(LAS u32x4*)(lds + L_C + 32) = u32x4{0, 0, 0, 0}; *(LAS u32x4*)(lds + L_C + 48) = u32x4{0, 0, 0, 0}; }
      st16(u32x4{__float_as_uint(rstd), (unsigned)tag, __float_as_uint(-mu * rstd), (unsigned)tag}, rw, r == 16 ? row * 16 : OOB, WS_S);
    };
    G1<2>::Frag f2; G1<2>::Ptr p2;
    const G1<2>::Off o2 = G1<2>::offs(w, lane);
    G1<2>::ptrx<0>(p2, o2, lds, 3 * XS); G1<2>::ptrx<1>(p2, o2, lds, 3 * XS); G1<2>::ptrw(p2, o2, lds, 0);
    {
      f32x4 acc[4][4];
      if (vtype) {
        f32x4 r4[4], t4[4]; float c1[4];
        G1<1>::Frag f1;
        LAS float* pr4 = st_r + 64 * wm + 8 * kq; LAS float* pc0 = st_c + 32 * G1<1>::span(w, 0) + c16; LAS float* pc1 = st_c + 32 * G1<1>::span(w, 1) + c16;
        asm volatile("" : "+v"(pr4), "+v"(pc0), "+v"(pc1));
        G1<1>::loop(acc, f1, G1<1>::offs(w, lane), lds, 0, 0, w, lane, -1, fl, 0, tag, [&](auto k) {
          constexpr int K = decltype(k)::v;
          if constexpr (K < 0) { stats_ld(); stats(); }
          else if constexpr (K < 4) { r4[K] = *(LAS f32x4*)(pr4 + 32 * (K >> 1) + 4 * (K & 1)); t4[K] = *(LAS f32x4*)(pr4 + 128 + 32 * (K >> 1) + 4 * (K & 1)); }
          else if constexpr (K < 6) { c1[2 * K - 8] = (K == 4 ? pc0 : pc1)[0]; c1[2 * K - 7] = (K == 4 ? pc0 : pc1)[16]; }
          else G1<2>::rds<2 * K - 12, 2 * K - 10, XS, WSL>(f2, p2);
        }, stv);
        STAMP(2);
#pragma unroll
        for (int i = 0; i < 4; ++i)
#pragma unroll
          for (int a = 0; a < 2; ++a)
            st16(pk8(acc[2 * a][i] * r4[2 * a] + t4[2 * a] * c1[i], acc[2 * a + 1][i] * r4[2 * a + 1] + t4[2 * a + 1] * c1[i]), rw, vo[i], a * PT);
      } else {
        float r1[4], t1[4]; f32x4 c4[4];
        G1<0>::Frag f0;
        LAS float* pr1 = st_r + 64 * wm + c16; LAS float* pc0 = st_c + 32 * G1<0>::span(w, 0) + 8 * kq; LAS float* pc1 = st_c + 32 * G1<0>::span(w, 1) + 8 * kq;
        asm volatile("" : "+v"(pr1), "+v"(pc0), "+v"(pc1));
        G1<0>::loop(acc, f0, G1<0>::offs(w, lane), lds, 0, 0, w, lane, -1, fl, 0, tag, [&](auto k) {
          constexpr int K = decltype(k)::v;
          if constexpr (K < 0) { stats_ld(); stats(); }
          else if constexpr (K < 4) { r1[K] = pr1[16 * K]; t1[K] = pr1[128 + 16 * K]; }
          else if constexpr (K < 6) { c4[2 * K - 8] = *(LAS f32x4*)(K == 4 ? pc0 : pc1); c4[2 * K - 7] = *(LAS f32x4*)((K == 4 ? pc0 : pc1) + 4); }
          else G1<2>::rds<2 * K - 12, 2 * K - 10, XS, WSL>(f2, p2);
        }, stv);
        STAMP(2);
        const float sc = ct < 5 ? QSCALE : 1.f;
#pragma unroll
        for (int pp = 0; pp < 2; ++pp)
#pragma unroll
          for (int m = 0; m < 4; ++m)
            st16(pk8(acc[m][2 * pp] * (r1[m] * sc) + c4[2 * pp] * (t1[m] * sc), acc[m][2 * pp + 1] * (r1[m] * sc) + c4[2 * pp + 1] * (t1[m] * sc)), rw,
                 vo[pp], (m & 1) * 256 + (m >> 1) * PT);
        st16(u32x4{0x3f803f80u, 0x3f803f80u, 0x3f803f80u, 0x3f803f80u}, rw, min(vo[2], vo[3]), 0);
      }
    }
    STAMP(3);
    f32x4 acc[2][6];
    float r1[2], t1[2]; f32x4 c4[6]; u32x4 bb[3];
    LAS float* pr1 = st_r + 32 * w + c16;
    G1<2>::loop2(acc, f2, o2, p2, lds, w, lane, [&]() { if (w == 0) publish(fl, F_Q + ct * 16 + rt, tag, lane); }, [&](auto k) {
      constexpr int K = decltype(k)::v;
      if constexpr (K < 2) { r1[K] = pr1[16 * K]; t1[K] = pr1[128 + 16 * K]; }
      else if constexpr (K < 5) bb[K - 2] = *(LAS u32x4*)(st_c + 128 + 16 * G1<2>::span(w, K - 2) + 4 * kq);
    });
    STAMP(4);
    __builtin_amdgcn_s_setprio(2);
    lds_meet<2>((LAS unsigned*)(lds + L_C + 20), 4, lane);
#pragma unroll
    for (int pp = 0; pp < 3; ++pp) { LAS float* pc = st_c + 32 * G1<2>::span(w, pp) + 8 * kq; c4[2 * pp] = *(LAS f32x4*)pc; c4[2 * pp + 1] = *(LAS f32x4*)(pc + 4); }
    const bool ret = w < 2 || (w == 2 && idx8 < 4);
    const int slab = w < 2 ? 2 * idx8 + w : 64 + idx8;
    u32x4 hq[6];
#pragma unroll
    for (int pp = 0; pp < 3; ++pp) {
      const f32x4 ba = {blo(bb[pp][0]), bhi(bb[pp][0]), blo(bb[pp][1]), bhi(bb[pp][1])}, bc = {blo(bb[pp][2]), bhi(bb[pp][2]), blo(bb[pp][3]), bhi(bb[pp][3])};
#pragma unroll
      for (int m = 0; m < 2; ++m) {
        const u32x2 lo = sqrelu4(acc[m][2 * pp], c4[2 * pp], ba, r1[m], t1[m]), hi = sqrelu4(acc[m][2 * pp + 1], c4[2 * pp + 1], bc, r1[m], t1[m]);
        hq[2 * pp + m] = u32x4{lo[0], lo[1], hi[0], hi[1]};
      }
    }
    asm volatile("" : "+v"(hq[0]), "+v"(hq[1]), "+v"(hq[2]), "+v"(hq[3]), "+v"(hq[4]), "+v"(hq[5]));
#define HST(i0, i1) do { _Pragma("unroll") for (int i = i0; i < i1; ++i) { st16(hq[i], rw, lane * 16, WS_H + ((8 * rt + 2 * w + (i & 1)) * 48 + 3 * ct + G1<2>::span(w, i >> 1)) * 1024); SB(); __builtin_amdgcn_s_sleep(4); SB(); } } while (0)
    FS(13); HST(0, 6); FS(14);
    if (ret) {
      const int s2 = slab - 48;
      const rsrc_t rr = mkrs((slab < 48 ? w2 : wo) + 80 * g, ((slab < 48 ? F_ : D_) * D_ - 80 * g) * 2);
      const unsigned r0 = slab < 48 ? slab * 32 : (s2 < 16 ? s2 * 40 : (s2 - 16) * 160 + 16), rsh = s2 < 16 ? 4 : 2, rmu = slab < 48 ? 0 : (s2 < 16 ? 4 : 16);
#pragma unroll
      for (int i = 0; i < 5; ++i) {
        const unsigned c = 64 * i + lane, k = (c * 205) >> 11;
        dma16(rr, (k + (k >> rsh) * rmu) * (D_ * 2) + (c - k * 10) * 16, r0 * (D_ * 2), lds0 + L_RED + w * 5120 + i * 1024);
      }
      const unsigned tro = (8 * kq + ((lane >> 2) & 3)) * 160 + (lane & 3) * 16 + (g & 1) * 32, tr4 = tro - (lane & 3) * 8 + 128 - (g & 1) * 160;
      DRAIN();
#pragma unroll
      for (int t = 0; t < 5; ++t) {
        LAS char* p = lds + L_RED + w * 5120 + (t < 4 ? tro + (t >> 1) * 64 + (t & 1) * 8 : tr4);
        st16(__builtin_bit_cast(u32x4, cat8(trd(p), trd(p + 640))), rw, lane * 16 + WS_W + (g * 68 + slab) * 5120, t * 1024); SB(); if (WP) __builtin_amdgcn_s_sleep(WP); SB();
      }
    }
    STAMP(5);
  }

  if (w >= 4) {
    const rsrc_t rk = mkrs(ws + WS_T + (pr * 64 + kh * 32) * PT, 32 * PT);
    unsigned qo[4];
#pragma unroll
    for (int t = 0; t < 4; ++t) { qo[t] = WS_T + (pr * 64 + 8 * qc + 4 * qh + (t ^ (2 * kh))) * PT + P_Q; asm volatile("" : "+s"(qo[t])); }
    asm volatile("" :: "s"(rk));
    HBAR(1);
    DRAIN();
    if (!__all(*(volatile LAS u64*)(lds + L_G + aw * 1024 + (oK1 & 1023)) == tag)) await<2>(fl, fi, tag);
    __builtin_amdgcn_fence(__ATOMIC_ACQUIRE, "wavefront");
    STAMP(6);
    bool stored = false;
    auto sweep = [&](auto pass_) -> unsigned {
      constexpr int PASS = decltype(pass_)::v;
      const int lane = fresh_lane(), r = lane & 31, h = lane >> 5, c16 = lane & 15;
      if constexpr (PASS) kv_offsets(lane);
      bf16x8 q1[4]; s16x4 q2[4];
      KV b0, b1_, b2_, b3;
      unsigned so = 0;
#define LDK(B, d) do { B.k1 = ld16(rk, oK1, so + (d) * PT); SB(); B.k2 = ld8(rk, oK2, so + (d) * PT); SB(); } while (0)
#define LDV(B, d) do { B.v0 = ld16(rk, oV0, so + (d) * PT); SB(); B.v1 = ld16(rk, oV1, so + (d) * PT); SB(); } while (0)
      SB();
      LDK(b1_, 1);
#pragma unroll
      for (int t = 0; t < 4; ++t) { q1[t] = ld16(rw, oK1, qo[t]); SB(); q2[t] = ld8(rw, oQ2, qo[t]); SB(); }
      LDK(b0, 0);
      float mx[4] = {-3.0e38f, -3.0e38f, -3.0e38f, -3.0e38f};
      if constexpr (PASS) {
#pragma unroll 1
        for (int j = 2; j < 32; ++j) {
          const bf16x8 k1 = ld16(rk, oK1, j * PT); const s16x4 k2 = ld8(rk, oK2, j * PT);
#pragma unroll
          for (int t = 0; t < 4; ++t) {
            const f32x16 sv = qk_b(k2, q2[t], qk_a(k1, q1[t]));
#pragma unroll
            for (int e = 0; e < 16; ++e) mx[t] = fmaxf(mx[t], sv[e]);
          }
        }
      }
      int idx = 0xDDDD;
      f32x16 o0 = {}, o1 = {}, o2 = {}, o3 = {}, s0, s1, s2, s3;
      b3.v0 = bf16x8{}; b3.v1 = bf16x8{};
      LAS char* sp = lds + L_MRG + aw * 6144 + lane * 16; LAS char* gp = lds + L_MRG + (aw ^ 2) * 6144 + lane * 16;
      const int tt = (8 * qc + 4 * qh + (2 ^ (2 * kh))) * 2 + (r >> 4);
      unsigned oa = WS_O + (tt * 16 + pr) * 1024 + ((2 * odd + h) * 16 + c16) * 16, ob = h ? OOB : WS_O2 + (tt * 32 + head) * 128 + c16 * 8;
      volatile LAS f32x4* mw = (volatile LAS f32x4*)(lds + L_M) + aw * 64 + lane; volatile LAS f32x4* mr = (volatile LAS f32x4*)(lds + L_M) + (aw ^ 2) * 64 + r;
      asm volatile("" : "+v"(o0), "+v"(o1), "+v"(o2), "+v"(o3), "+v"(b3.v0), "+v"(b3.v1), "+v"(idx), "+v"(sp), "+v"(gp), "+v"(oa), "+v"(ob), "+v"(mw), "+v"(mr)); SB();
      {
        f32x16 sm[8];
#define SQ(n) ((n) < 4 ? (n) : ((n) - 2) & 3)
#define SMA(n) do { sm[n] = qk_a((n) < 4 ? b1_.k1 : b0.k1, q1[SQ(n)]); if ((n) == 6) s0 = sm[n]; if ((n) == 7) s1 = sm[n]; SB(); } while (0)
#define SMB(n) do { sm[n] = qk_b((n) < 4 ? b1_.k2 : b0.k2, q2[SQ(n)], sm[n]); SB(); } while (0)
        SMA(0); SMB(0); SMA(1); SMB(1);
        FS(1);
        LDV(b0, 0); LDV(b1_, 1); LDK(b2_, 2); LDV(b2_, 2); LDK(b3, 3);
        FS(2);
#pragma unroll
        for (int n = 0; n < 8; ++n) {
          if (n < 6) SMA(n + 2);
          max8<0>(sm[n], mx[SQ(n)]); SB();
          if (n < 6) SMB(n + 2);
          max8<8>(sm[n], mx[SQ(n)]); SB();
        }
      }
      FS(3);
      *mw = f32x4{mx[0], mx[1], mx[2], mx[3]};
      if constexpr (PASS) lds_meet<0>((LAS unsigned*)(lds + L_C + 44), 4, lane);
#pragma unroll
      for (int t = 0; t < 4; ++t) {
        auto sw = __builtin_amdgcn_permlane32_swap(__float_as_uint(mx[t]), __float_as_uint(mx[t]), false, false);
        asm volatile("v_max_f32 %0, %1, %2" : "=v"(mx[t]) : "v"(sw[0]), "v"(sw[1]));
      }
      s3 = o3; asm volatile("" : "+v"(s3));
      f32x4 ma, mb;
#pragma unroll 1
      for (int n = 0; n < (1 << 22); ++n) {
        ma = mr[0]; mb = mr[32];
        if (PASS || !__any(__builtin_isunordered(ma[0], mb[0]) | __builtin_isunordered(ma[1], mb[1]) | __builtin_isunordered(ma[2], mb[2]) | __builtin_isunordered(ma[3], mb[3]))) break;
      }
      FS(4);
#pragma unroll
      for (int t = 0; t < 4; ++t) {
        float nm;
        asm("v_max3_f32 %0, %1, %2, %3\n\tv_sub_f32 %0, %4, %0" : "=&v"(nm) : "v"(mx[t]), "v"(ma[t ^ 2]), "v"(mb[t ^ 2]), "s"(PASS ? 0.f : -MARGIN));
        u32x2 pm = __builtin_bit_cast(u32x2, q2[t]);
        if constexpr (PASS) {
          const float a0 = bhi(__float_as_uint(nm)), r0 = nm - a0, a1 = bhi(__float_as_uint(r0)), a2 = r0 - a1;
          if (h) pm = u32x2{(__float_as_uint(a0) >> 16) | __float_as_uint(a1), __float_as_uint(a2) >> 16};
        } else if (h) pm[0] = __float_as_uint(nm) >> 16;
        q2[t] = __builtin_bit_cast(s16x4, pm);
        if (t == 0) s0 = qk_b(b0.k2, q2[0], s0);
        if (t == 1) s1 = qk_b(b0.k2, q2[1], s1);
        SB();
      }
      if constexpr (!PASS) { STAMP(7); SB(); }
#define KT(C, P, N) do { \
  STEPX(s0, s3, o3, P, s2, C, q1[2], q2[2], P.v0 = ld16(rk, oV0, so), __builtin_amdgcn_s_waitcnt(0x0F79)); \
  STEPX(s1, s0, o0, C, s3, C, q1[3], q2[3], P.v1 = ld16(rk, oV1, so), so += PT; asm volatile("" : "+s"(so))); \
  STEPX(s2, s1, o1, C, s0, N, q1[0], q2[0], C.k1 = ld16(rk, oK1, so), ); \
  STEPX(s3, s2, o2, C, s1, N, q1[1], q2[1], C.k2 = ld8(rk, oK2, so), ); } while (0)
      so = 3 * PT;
      int itn = 32;
#pragma unroll 1
      do { KT(b0, b3, b1_); KT(b1_, b0, b2_);
        ITS();
        KT(b2_, b1_, b3); KT(b3, b2_, b0);
        ITS();
      } while (so != 31 * PT);
#define KTE(C, P, N, W) do { STEPX(s0, s3, o3, P, s2, C, q1[2], q2[2], , __builtin_amdgcn_s_waitcnt(W)); STEP(s1, s0, o0, C, s3, C, q1[3], q2[3]); \
  STEP(s2, s1, o1, C, s0, N, q1[0], q2[0]); STEP(s3, s2, o2, C, s1, N, q1[1], q2[1]); } while (0)
#define SEND(O, z, e) do { *(LAS f32x4*)(sp + (z) * 3072 + (e) * 1024) = f32x4{O[4 * (e)], O[4 * (e) + 1], O[4 * (e) + 2], O[4 * (e) + 3]}; SB(); } while (0)
      STEPX(s0, s3, o3, b3, s2, b0, q1[2], q2[2], b3.v0 = ld16(rk, oV0, so), __builtin_amdgcn_s_waitcnt(0x0F79));
      STEPX(s1, s0, o0, b0, s3, b0, q1[3], q2[3], b3.v1 = ld16(rk, oV1, so), ); STEP(s2, s1, o1, b0, s0, b1_, q1[0], q2[0]); STEP(s3, s2, o2, b0, s1, b1_, q1[1], q2[1]);
      KTE(b1_, b0, b2_, 0x0F76); KTE(b2_, b1_, b3, 0x0F72);
      STEPX(s0, s3, o3, b2_, s2, b3, q1[2], q2[2], , __builtin_amdgcn_s_waitcnt(0x0F70)); STEP(s1, s0, o0, b3, s3, b3, q1[3], q2[3]);
      LAS unsigned* mc = (LAS unsigned*)(lds + L_C + 32 + 4 * qh); unsigned one = 1u;
      EX4(s2, 0); SB(); o1 = pv_lo(b3.v0, s1, o1, idx); SB(); asm volatile("" : "+v"(mc), "+v"(one)); EX4(s2, 4); SB(); o1 = pv_hi(b3.v1, s1, o1, idx); SB(); SEND(o0, 0, 0);
      EX4(s2, 8); SB(); o2 = pv_lo(b3.v0, s2, o2, idx); SB(); SEND(o0, 0, 1); EX4(s2, 12); SB(); SEND(o0, 0, 2);
      EX4(s3, 0); SB(); o2 = pv_hi(b3.v1, s2, o2, idx); SB(); SEND(o1, 1, 0); EX4(s3, 4); SB(); SEND(o1, 1, 1); SEND(o1, 1, 2);
      lds_add(mc, one); SB();
      unsigned cv; f32x4 gv[6];
#define GET() do { cv = *(volatile LAS unsigned*)mc; _Pragma("unroll") for (int e = 0; e < 6; ++e) gv[e / 3 * 3 + (e + 2) % 3] = *(volatile LAS f32x4*)(gp + (e / 3 * 3 + (e + 2) % 3) * 1024); } while (0)
      o3 = pv_lo(b3.v0, s3, o3, idx); SB(); EX4(s3, 8); SB(); EX4(s3, 12); SB(); GET(); SB(); o3 = pv_hi(b3.v1, s3, o3, idx); SB();
      asm volatile("" : "+v"(o2), "+v"(o3) : "v"(o0), "v"(o1));
      if constexpr (!PASS) { STAMP(8); SB(); }
      if (__builtin_amdgcn_readfirstlane(cv) < 1024 + 2 + 2 * PASS) {
#pragma unroll 1
        for (int n = 0; n < (1 << 22); ++n) { if (__builtin_amdgcn_readfirstlane(*(volatile LAS unsigned*)mc) >= 1024 + 2 + 2 * PASS) break; }
        GET();
      }
      u32x4 pa[2]; u32x2 pb_[2]; float chk = 0.f, l[2];
#pragma unroll
      for (int z = 0; z < 2; ++z) {
        const f32x16 k = z ? o3 : o2;
        const F4 u2 = add4(gv[3 * z + 2], k[8], k[9], k[10], k[11], chk);
        l[z] = __uint_as_float(__builtin_amdgcn_permlane32_swap(__float_as_uint(u2.v[0]), __float_as_uint(u2.v[0]), false, false)[1]);
        float inv = __builtin_amdgcn_rcpf(l[z]);
        asm volatile("" : "+v"(inv));
        const F4 u0 = add4(gv[3 * z], k[0], k[1], k[2], k[3], chk), u1 = add4(gv[3 * z + 1], k[4], k[5], k[6], k[7], chk);
        const u32x2 x0 = scale4(u0, inv), x1 = scale4(u1, inv);
        pa[z] = u32x4{x0[0], x0[1], x1[0], x1[1]}; pb_[z] = scale4(u2, inv);
      }
      asm volatile("" :: "v"(o3));
      unsigned bad = 1u;
      if constexpr (!PASS) { stored = __all(chk < 1.0e37f && l[0] > 1.0e-30f && l[1] > 1.0e-30f); if (!stored) bad = 0x101u; }
      else {
#pragma unroll
        for (int z = 0; z < 2; ++z) {
#pragma unroll
          for (int j = 0; j < 4; ++j) pa[z][j] = min(pa[z][j], 0xfffffffeu);
          pb_[z][0] = min(pb_[z][0], 0xfffffffeu);
        }
      }
      if (PASS ? !stored : stored) { st16(pa[0], rw, oa, 0); st8(pb_[0], rw, ob, 0); st16(pa[1], rw, oa, 32768); st8(pb_[1], rw, ob, 8192); }
      unsigned cn = lds_add_rtn((LAS unsigned*)(lds + L_C + 24), bad) + bad;
      if constexpr (!PASS) {
        STAMP(9);
#pragma unroll 1
        for (int n = 0; n < (1 << 22); ++n) { if ((cn & 255) >= 4) break; cn = __builtin_amdgcn_readfirstlane(*(volatile LAS unsigned*)(lds + L_C + 24)); }
        asm volatile("" ::: "memory");
      }
      return cn;
    };
    if (__builtin_expect(sweep(IC<0>{}) >= 256, 0)) sweep(IC<1>{});
    const int lane = fresh_lane();
    if (b == 0) {
      await<8>(fl, F_O + aw * 64 + lane, tag);
      lds_meet<2>((LAS unsigned*)(lds + L_C + 28), 4, lane);
      if (w == 4) publish(fl, 0, tag, lane);
    }
  } else {
    const int rt2 = 4 * g + (idx8 & 3), ct2 = idx8 >> 2;
    {
      DRAIN();
      if (lds_add_rtn((LAS unsigned*)(lds + L_C + 16), 1u) == 3) publish(fl, F_H + b, tag, lane);
      const int j = lane & 15, hc = 4 * w + (j & 3), hr = rt2 >> 1;
      const int pb = (j < 4 || j > 12) ? (hr >> 2) + 4 * (hc >> 3) + 8 * ((hr & 3) + 4 * (hc & 7)) : 8 * (j < 10 ? 6 * w + j - 4 : (j < 12 ? 14 + 2 * w + j : w)) + ct2;
#pragma unroll 1
      for (int n = 0; n < (1 << 22); ++n) { if (__builtin_amdgcn_readfirstlane(*(volatile LAS unsigned*)(lds + L_C + 16)) >= 4) break; __builtin_amdgcn_s_sleep(4); }
      await<10>(fl, F_H + pb, tag);
    }
    STAMP(6);
    const int o = (64 * rt2 + 16 * w + c16) * D_ + 80 * ct2;
    u32x4 xv[2], bv[2]; u32x2 xv4, bv4;
    {
      f32x4 acc[4][5];
      bf16x8 f0[11], f1[11];
      unsigned vo = lane * 16, vo4 = vo + 4096, va[4];
#pragma unroll
      for (int m = 0; m < 4; ++m) va[m] = vo + (m ^ w) * 49152;
      unsigned sa = WS_H + (4 * rt2 * 48 + 12 * w) * 1024, sw = WS_W + (ct2 * 68 + 12 * w) * 5120, sq = WS_W + (ct2 * 68 + 48 + 4 * w) * 5120;
      const unsigned sq0 = sq + 20480, sq1 = WS_W + (ct2 * 68 + 64 + w) * 5120;
      LAS char* wl = lds + L_WO + lane * 16; LAS char* wst = wl + ((w - 1 - (g >> 1)) & 3) * 25600;
      asm volatile("" : "+v"(vo), "+v"(vo4), "+v"(va[0]), "+v"(va[1]), "+v"(va[2]), "+v"(va[3]), "+v"(wl), "+v"(wst));
#define GL1(F, j) F[j] = (j) < 4 ? ld16(rw, va[(j) & 3], sa) : ((j) < 8 ? ld16(rw, vo + (((j) - 4) & 3) * 1024, sw) : ((j) < 9 ? ld16(rw, vo4, sw) : ld16(rw, vo + ((j) - 9) * 1024, sq)))
#define GSTEP(FC, FN, WO, LD, Z) do { _Pragma("unroll") for (int n = 0; n < 20; ++n) { \
        acc[n & 3][n >> 2] = __builtin_amdgcn_mfma_f32_16x16x32_bf16(FC[4 + (n >> 2)], FC[n & 3], (Z) ? f32x4{} : acc[n & 3][n >> 2], 0, 0, 0); \
        SB(); \
        if ((Z) && n == 3) wlast = ld16(rw, vo4, sq1); \
        else if ((Z) && (n == 7 || n == 11)) st16(mk4, rw, n == 7 ? ma : ma2, 0); \
        else if ((Z) && (n == 5 || n == 9)) dma16(rx, xo, n == 5 ? 0 : 64, lb + (n == 5 ? 0 : 1024)); \
        else if ((Z) && n == 13) dma16(rx, xo4, 0, lb + 2048); \
        else if ((Z) && n == 19) dma16(mkrs(b2 + 80 * ct2, 160), vo, 0, lb + 3072); \
        else if ((Z) && n == 15) st8(u32x2{mk4[0], mk4[1]}, rw, mb, 0); \
        else if ((LD) && n > 2 && (n & 1) && n < 17) { asm volatile("s_nop 4"); SB(); }         \
        if ((LD) && n < 2) { GL1(FN, n); } else if ((LD) && !(n & 1)) { GL1(FN, (n >> 1) + 1); } \
        if ((LD) && n == 5) { sa += 1024; asm volatile("" : "+s"(sa)); } \
        if ((LD) && n == 15) { sw += 5120; asm volatile("" : "+s"(sw)); } \
        if ((LD) && n == 3) { sq += 2048; if (sq == sq0) sq = sq1; asm volatile("" : "+s"(sq)); } \
        if (n == 17 || n == 19) *(LAS bf16x8*)(wst + (WO) + (n == 19 ? 1024 : 0)) = FC[n == 19 ? 10 : 9]; \
        SB(); } } while (0)
      const int tt = (8 * qc + 4 * (w & 1) + (w & 2)) * 2 + (r >> 4);
      unsigned ma = WS_O + (tt * 16 + pr) * 1024 + ((2 * odd + h) * 16 + c16) * 16, ma2 = ma + 32768, mb = WS_O2 + ((tt + 2 * h) * 32 + head) * 128 + c16 * 8;
      unsigned mo = (F_O + g * 32 + r) * 128 + h * 4, xo = o * 2 + 16 * kq + (ct2 & 1) * 32, xo4 = o * 2 + 8 * kq + ((ct2 & 1) ? 0 : 128);
      u32x4 mk4 = {~0u, ~0u, ~0u, ~0u};
      LAS char* r0 = lds + L_RED + w * 5120 + lane * 16;
      asm volatile("" : "+v"(ma), "+v"(ma2), "+v"(mb), "+v"(mk4), "+v"(mo), "+v"(xo), "+v"(xo4), "+v"(r0));
#pragma unroll
      for (int j = 0; j < 11; ++j) { GL1(f0, j); SB(); if (j < 9) __builtin_amdgcn_s_sleep(GP); SB(); }
      bf16x8 wlast;
      const unsigned lb = lds0 + L_RED + w * 5120;
      LAS unsigned* c32 = (LAS unsigned*)(lds + L_C + 32); LAS unsigned* c36 = c32 + 1; unsigned k256 = 256u;
      asm volatile("" : "+v"(c32), "+v"(c36), "+v"(k256));
      sa += 1024; sw += 5120;
      __builtin_amdgcn_s_setprio(0); SB();
      GSTEP(f0, f1, 0, true, true);
      *(LAS bf16x8*)(wst + 24576) = wlast;
#pragma unroll 1
      for (int s = 1; s < 11; s += 2) {
        GSTEP(f1, f0, 2048, true, false); GSTEP(f0, f1, 4096, true, false);
        __builtin_amdgcn_s_setprio(2); SB(); asm volatile("v_add_u32 %0, 0x1000, %0" : "+v"(wst));
        if (s == 1) {
          asm volatile("s_waitcnt vmcnt(11)" ::: "memory");
          lds_add(c32, k256);
          if ((lds_add_rtn(c36, k256) >> 8) == 3) { u64 tg = tag; asm volatile("" : "+s"(tg)); publish(fl, F_O + qc * 32 + head, tg, fresh_lane()); }
        }
        if (s == 7)
          asm volatile("s_mov_b32 m0, %0\n\ts_nop 0\n\tbuffer_load_dword %1, %2, 0 offen sc1 lds" :: "s"(lb + 4096), "v"(mo), "s"(mkrs(ws + WS_F, 1u << 17)) : "memory");
        SB(); __builtin_amdgcn_s_setprio(0);
      }
      unsigned oo[4], oo2[4];
#pragma unroll
      for (int j = 0; j < 4; ++j) {
        const int gr = (j + 1 + (g >> 1)) & 3;
        oo[j] = WS_O + (4 * rt2 + w) * 16384 + gr * 4096; oo2[j] = WS_O2 + (4 * rt2 + w) * 4096 + gr * 1024; asm volatile("" : "+s"(oo[j]), "+s"(oo2[j]));
      }
      xv[0] = *(volatile LAS u32x4*)r0; xv[1] = *(volatile LAS u32x4*)(r0 + 1024); xv4 = *(volatile LAS u32x2*)(r0 + 2048);
      SB();
      GSTEP(f1, f0, 2048, false, false);
      __builtin_amdgcn_s_setprio(2);
      STAMP(7);
      const int ln = fresh_lane();
      const unsigned gate = *(volatile LAS unsigned*)(lds + L_C + 24), mk = *(volatile LAS unsigned*)(lds + L_RED + w * 5120 + 4096 + ln * 4);
      LAS char* bp = lds + L_RED + w * 5120 + 3072 + (ct2 & 1) * 32 + (ln & 48);
      bv[0] = *(volatile LAS u32x4*)bp; bv[1] = *(volatile LAS u32x4*)(bp + 64); bv4 = *(volatile LAS u32x2*)(bp + 128 - (ct2 & 1) * 160 - ((ln & 48) >> 1));
      const unsigned v8 = (ln >> 4) * 256 + (ln & 15) * 8;
      volatile LAS unsigned* gc = (volatile LAS unsigned*)(lds + L_C + 48);
      asm volatile("" ::: "memory");
#pragma unroll
      for (int t = 0; t < 5; ++t) *(LAS f32x4*)(r0 + t * 1024) = acc[3][t];
      asm volatile("" ::: "memory"); gc[w] = 1;
      if (!__all(mk == (unsigned)(ln >= 32 ? tag >> 32 : tag))) await<2>(fl, F_O + g * 32 + (ln & 31), tag);
#define SPIN(COND) do { _Pragma("unroll 1") for (int n_ = 0; n_ < (1 << 24); ++n_) { if (COND) break; __builtin_amdgcn_s_sleep(1); } asm volatile("" ::: "memory"); } while (0)
      if ((__builtin_amdgcn_readfirstlane(gate) & 255) < 4) SPIN((__builtin_amdgcn_readfirstlane(*(volatile LAS unsigned*)(lds + L_C + 24)) & 255) >= 4);
      STAMP(10);
      u32x4 a[16], al[4];
#define OL16(p, e, C) asm volatile("buffer_load_dwordx4 %0, %1, %2, %3 offen offset:%4 sc1" : C(a[4 * (p) + (e)]) : "v"(vo), "s"(rw), "s"(oo[p]), "n"((e) * 1024) : "memory")
#define OL8_(p, C, R0, R1, R2, R3) asm volatile("buffer_load_dwordx2 v[" #R0 ":" #R1 "], %1, %2, %3 offen sc1\n\tbuffer_load_dwordx2 v[" #R2 ":" #R3 "], %1, %2, %3 offen offset:128 sc1" \
        : C "{v[" #R0 ":" #R3 "]}"(al[p]) : "v"(v8), "s"(rw), "s"(oo2[p]) : "memory")
#define OL8(p, C) do { if ((p) == 0) OL8_(0, C, 224, 225, 226, 227); else if ((p) == 1) OL8_(1, C, 228, 229, 230, 231); \
        else if ((p) == 2) OL8_(2, C, 232, 233, 234, 235); else OL8_(3, C, 236, 237, 238, 239); } while (0)
#define OLG(p, C) do { OL16(p, 0, C "v"); OL16(p, 1, C "v"); OL16(p, 2, C "v"); OL16(p, 3, C "v"); OL8(p, C); } while (0)
      unsigned pg = gc[ln & 3];
      OLG(0, "=&"); OLG(1, "=&");
      LAS char* wp2 = lds + L_WO + 61440 + ln * 16; LAS char* r1_ = lds + L_RED + ln * 16 + (w ^ 1) * 5120; LAS char* r3 = lds + L_RED + ln * 16 + (w ^ 3) * 5120;
      asm volatile("" : "+v"(wp2));
      if (!__all(pg >= 1)) SPIN(__all(gc[ln & 3] >= 1));
      f32x4 y[5], z[5];
#pragma unroll
      for (int t = 0; t < 5; ++t) { z[t] = *(LAS f32x4*)(r3 + t * 1024); *(LAS f32x4*)(r3 + t * 1024) = acc[2][t]; }
      asm volatile("" ::: "memory"); gc[w] = 2; SB();
#define WJ(j) ((j) / 25 * 25 + (j) % 5 * 5 + (j) % 25 / 5)
      constexpr int NW = 28;
      bf16x8 wr[NW];
#pragma unroll
      for (int t = 0; t < 5; ++t) {
        const unsigned x0 = t < 4 ? xv[(t >> 1) & 1][2 * (t & 1)] : xv4[0], x1 = t < 4 ? xv[(t >> 1) & 1][2 * (t & 1) + 1] : xv4[1];
        const unsigned b0 = t < 4 ? bv[(t >> 1) & 1][2 * (t & 1)] : bv4[0], b1v = t < 4 ? bv[(t >> 1) & 1][2 * (t & 1) + 1] : bv4[1];
        y[t] = (acc[0][t] + f32x4{blo(x0) + blo(b0), bhi(x0) + bhi(b0), blo(x1) + blo(b1v), bhi(x1) + bhi(b1v)}) + z[t];
        asm volatile("" : "+v"(y[t]));
      }
      pg = gc[w ^ 2];
#pragma unroll
      for (int i = 0; i < 12; ++i) wr[i] = *(LAS bf16x8*)(wl + WJ(i) * 1024);
      if (__builtin_amdgcn_readfirstlane(pg) < 2) SPIN(__builtin_amdgcn_readfirstlane(gc[w ^ 2]) >= 2);
      asm volatile("" ::: "memory");
#pragma unroll
      for (int t = 0; t < 5; ++t) { z[t] = *(LAS f32x4*)(r1_ + t * 1024); *(LAS f32x4*)(r1_ + t * 1024) = acc[1][t]; }
      asm volatile("" ::: "memory"); gc[w] = 3; SB();
#pragma unroll
      for (int i = 12; i < NW; ++i) wr[i] = *(LAS bf16x8*)(wl + WJ(i) * 1024);
      SB();
#define OCHK(p, N) ({ unsigned m_; asm volatile("s_waitcnt vmcnt(" #N ")" ::: "memory"); SB(); \
        asm volatile("" : "+v"(a[4 * (p)]), "+v"(a[4 * (p) + 1]), "+v"(a[4 * (p) + 2]), "+v"(a[4 * (p) + 3]), "+v"(al[p]) :: "memory"); \
        m_ = max(al[p][0], al[p][2]); _Pragma("unroll") for (int e = 0; e < 4; ++e) m_ = max(m_, max(a[4 * (p) + e][0], a[4 * (p) + e][2])); __any(m_ == ~0u); })
#define OGRP(p, N) do { \
        if (OCHK(p, N)) { \
          _Pragma("unroll 1") for (int n = 0; n < (1 << 20); ++n) { OLG(p, "+"); if ((p) < 3) OLG(((p) + 1) & 3, "+"); if (!OCHK(p, N)) break; } } \
        SB(); \
        _Pragma("unroll") for (int e = 0; e < 25; ++e) { const int i = 25 * (p) + e; \
          asm volatile("v_mfma_f32_16x16x32_bf16 %0, %1, %2, %0" : "+v"(y[e / 5]) : "v"(wr[i % NW]), "v"(e % 5 < 4 ? a[4 * (p) + (e % 5 & 3)] : al[p])); \
          if ((p) < 2 && e % 4 == 3) { if (e < 16) OL16(((p) + 2) & 3, (e / 4) & 3, "=&v"); else if (e == 19) OL8(((p) + 2) & 3, "=&"); } \
          const int j = NW + (i * 4 + 4) / 5;        \
          if (i % 5 != 4 && j < 100) wr[j % NW] = *(LAS bf16x8*)(WJ(j) < 60 ? wl + WJ(j) * 1024 : wp2 + (WJ(j) - 60) * 1024); \
          if (i == 70) pg = gc[w ^ 1]; \
          if (i >= 90 && i < 95) zz[i - 90] = *(volatile LAS f32x4*)(r0 + (i - 90) * 1024); \
          SB(); } } while (0)
      f32x4 zz[5];
      OGRP(0, 6); OGRP(1, 6); OGRP(2, 6);
      if (__builtin_amdgcn_readfirstlane(pg) < 3) SPIN(__builtin_amdgcn_readfirstlane(gc[w ^ 1]) >= 3);
      OGRP(3, 0);
#pragma unroll
      for (int t = 0; t < 5; ++t) { z[t] += zz[t]; asm volatile("" : "+v"(z[t])); }
      asm volatile("s_nop 7" : "+v"(y[0]), "+v"(y[1]), "+v"(y[2]), "+v"(y[3]), "+v"(y[4]));
      const rsrc_t rout = mkrs(out, 2048 * D_ * 2);
      const f32x4 y4 = y[4] + z[4];
      __builtin_amdgcn_raw_buffer_store_b64(u32x2{pk2(y4[0], y4[1]), pk2(y4[2], y4[3])}, rout, xo4, 0, 16);
      __builtin_amdgcn_raw_buffer_store_b128(pk8(y[0] + z[0], y[1] + z[1]), rout, xo, 0, 16);
      __builtin_amdgcn_raw_buffer_store_b128(pk8(y[2] + z[2], y[3] + z[3]), rout, xo + 64, 0, 16);
    }
    STAMP(11);
  }
}

extern "C" void kernel_launch(void* const* d_in, const int* in_sizes, int n_in, void* d_out, int out_size, void* d_ws, size_t ws_size,
                              hipStream_t stream) {
  fused<<<256, 512, 0, stream>>>((const u16*)d_in[0], (const u16*)d_in[1], (const u16*)d_in[2], (const u16*)d_in[4], (const u16*)d_in[5],
                                 (const u16*)d_in[6], (const u16*)d_in[7], (u16*)d_out, (char*)d_ws);
}
```

```cpp
#define F2N 16
#define NA 4
#define NB 6
#define WP 2
#define GP 2
#include <hip/hip_runtime.h>
#include <hip/hip_bf16.h>

typedef __attribute__((ext_vector_type(8))) __bf16 bf16x8;
typedef __attribute__((ext_vector_type(16))) __bf16 bf16x16;
typedef __attribute__((ext_vector_type(2))) __bf16 bf16x2;
typedef __attribute__((ext_vector_type(4))) short s16x4;
typedef __attribute__((ext_vector_type(16))) float f32x16;
typedef __attribute__((ext_vector_type(8))) float f32x8;
typedef __attribute__((ext_vector_type(4))) float f32x4;
typedef __attribute__((ext_vector_type(4))) unsigned u32x4;
typedef __attribute__((ext_vector_type(2))) unsigned u32x2;
typedef unsigned short u16;
typedef unsigned long long u64;
typedef __amdgpu_buffer_rsrc_t rsrc_t;
#define LAS __attribute__((address_space(3)))
#define GAS __attribute__((address_space(1)))
#define SB() __builtin_amdgcn_sched_barrier(0)
#define RLX_AGENT __ATOMIC_RELAXED, __HIP_MEMORY_SCOPE_AGENT
#define BAR_L() do { asm volatile("" ::: "memory"); __builtin_amdgcn_s_waitcnt(0xC07F); asm volatile("s_barrier" ::: "memory"); } while (0)
#define DRAIN() asm volatile("s_waitcnt vmcnt(0)" ::: "memory")

constexpr int D_ = 640, F_ = 1536;
constexpr unsigned PT = 7808, P_V = 2560, P_Q = 5120, P_ONE = 7680;
constexpr unsigned WS_T = 0;
constexpr unsigned WS_H = 8u << 20;
constexpr unsigned WS_W = 16u << 20;
constexpr unsigned WS_O = 20u << 20;
constexpr unsigned WS_O2 = 22u << 20;
constexpr unsigned WS_S = 23u << 20;
constexpr unsigned WS_F = 24u << 20;
constexpr unsigned WS_END = WS_F;
constexpr unsigned F_Q = 256, F_H = 512, F_O = 768;
constexpr float QSCALE = 0.22360679774997896f * 1.4426950408889634f;
constexpr float MARGIN = 88.f;
constexpr unsigned OOB = 0x80000000u;
constexpr unsigned CODE_SKIP = 4608;
constexpr unsigned XS = 16448, WSL = 16640, L_W = 6 * XS;
constexpr unsigned L_WO = 0, L_RED = 102400, L_MRG = 122880;
constexpr unsigned L_ST = L_W + 3 * WSL;
constexpr unsigned L_G = L_ST + 2048;
constexpr unsigned L_M = L_G + 4096;
constexpr unsigned L_C = L_M + 4096;
constexpr unsigned L_PF = L_C + 64;
constexpr unsigned L_END = L_PF + 256;

#define STAMP(i)
#define STEPSTAMP()

#define FS(v)
#define ITS()
__device__ __forceinline__ unsigned pk2(float a, float b) { bf16x2 r = {(__bf16)a, (__bf16)b}; return __builtin_bit_cast(unsigned, r); }
__device__ __forceinline__ u32x4 pk8(f32x4 a, f32x4 b) { return u32x4{pk2(a[0], a[1]), pk2(a[2], a[3]), pk2(b[0], b[1]), pk2(b[2], b[3])}; }
__device__ __forceinline__ float blo(unsigned u) { return __uint_as_float(u << 16); }
__device__ __forceinline__ float bhi(unsigned u) { return __uint_as_float(u & 0xffff0000u); }
__device__ __forceinline__ u64 uni64(u64 v) {
  return ((u64)(unsigned)__builtin_amdgcn_readfirstlane((unsigned)(v >> 32)) << 32) | (unsigned)__builtin_amdgcn_readfirstlane((unsigned)v);
}
__device__ __forceinline__ int fresh_lane() { int l; asm volatile("v_mbcnt_lo_u32_b32 %0, -1, 0\n\tv_mbcnt_hi_u32_b32 %0, -1, %0" : "=v"(l)); return l; }
__device__ __forceinline__ rsrc_t mkrs(const void* p, unsigned bytes) { return __builtin_amdgcn_make_buffer_rsrc((void*)p, 0, bytes, 0x00020000); }
__device__ __forceinline__ void dma16(rsrc_t r, unsigned vo, unsigned so, unsigned lds_base) {
  asm volatile("s_mov_b32 m0, %0\n\ts_nop 0\n\tbuffer_load_dwordx4 %1, %2, %3 offen lds" :: "s"(lds_base), "v"(vo), "s"(r), "s"(so) : "memory");
}
__device__ __forceinline__ void touch(rsrc_t r, unsigned vo, unsigned lds_base) {
  asm volatile("s_mov_b32 m0, %0\n\ts_nop 0\n\tbuffer_load_dword %1, %2, 0 offen lds" :: "s"(lds_base), "v"(vo), "s"(r) : "memory");
}
#define STOUCH3(p) asm volatile("s_load_dword s100, %0, 0\n\ts_load_dword s100, %0, 64\n\ts_load_dword s100, %0, 128" :: "s"(p) : "s100")
#define SPFX(x) x
#define STOUCH4(p) asm volatile("s_load_dword s100, %0, 0\n\ts_load_dword s100, %0, 64\n\ts_load_dword s100, %0, 128\n\ts_load_dword s100, %0, 192" :: "s"(p) : "s100")
__device__ __forceinline__ bf16x8 ld16(rsrc_t r, unsigned vo, unsigned so) { return __builtin_bit_cast(bf16x8, __builtin_amdgcn_raw_buffer_load_b128(r, vo, so, 16)); }
__device__ __forceinline__ s16x4 ld8(rsrc_t r, unsigned vo, unsigned so) { return __builtin_bit_cast(s16x4, __builtin_amdgcn_raw_buffer_load_b64(r, vo, so, 16)); }
__device__ __forceinline__ void st16(u32x4 v, rsrc_t r, unsigned vo, unsigned so) { __builtin_amdgcn_raw_buffer_store_b128(v, r, vo + so, 0, 16); }
__device__ __forceinline__ void st8(u32x2 v, rsrc_t r, unsigned vo, unsigned so) { __builtin_amdgcn_raw_buffer_store_b64(v, r, vo + so, 0, 16); }
__device__ __forceinline__ void publish(GAS u64* f, unsigned i, u64 tag, int lane) {
  st16(u32x4{(unsigned)tag, (unsigned)(tag >> 32), (unsigned)tag, (unsigned)(tag >> 32)}, mkrs((const void*)(f + 16 * i), 128), lane * 16, 0);
}
template <int SLP> __device__ __forceinline__ void await(GAS u64* f, unsigned i, u64 tag) {
#pragma unroll 1
  for (int n = 0; n < (1 << 20); ++n) {
    if (__all(__hip_atomic_load(f + 16 * i, RLX_AGENT) == tag)) break;
    __builtin_amdgcn_s_sleep(SLP);
  }
  __builtin_amdgcn_fence(__ATOMIC_ACQUIRE, "wavefront");
}
__device__ __forceinline__ void lds_add(LAS unsigned* c, unsigned v) {
  asm volatile("s_mov_b64 exec, 1\n\tds_add_u32 %0, %1\n\ts_mov_b64 exec, -1" :: "v"(c), "v"(v) : "memory");
}
__device__ __forceinline__ unsigned lds_add_rtn(LAS unsigned* c, unsigned v) {
  unsigned r_;
  asm volatile("s_mov_b64 exec, 1\n\tds_add_rtn_u32 %0, %1, %2\n\ts_mov_b64 exec, -1\n\ts_waitcnt lgkmcnt(0)" : "=&v"(r_) : "v"(c), "v"(v) : "memory");
  return __builtin_amdgcn_readfirstlane(r_);
}
template <int SLP> __device__ __forceinline__ void lds_meet(LAS unsigned* c, unsigned n, int lane) {
  lds_add(c, 1u);
#pragma unroll 1
  for (int k = 0; k < (1 << 22); ++k) {
    if (__builtin_amdgcn_readfirstlane(*(volatile LAS unsigned*)c) >= n) break;
    if (SLP) __builtin_amdgcn_s_sleep(SLP);
  }
  asm volatile("" ::: "memory");
}
__device__ __forceinline__ s16x4 trd(LAS char* p) { return __builtin_amdgcn_ds_read_tr16_b64_v4i16((LAS s16x4*)p); }
__device__ __forceinline__ bf16x8 cat8(s16x4 a, s16x4 b) { return __builtin_bit_cast(bf16x8, __builtin_shufflevector(a, b, 0, 1, 2, 3, 4, 5, 6, 7)); }
template <int C> __device__ __forceinline__ float dppadd(float a) {
  return a + __int_as_float(__builtin_amdgcn_update_dpp(0, __float_as_int(a), C, 0xf, 0xf, true));
}

__device__ __forceinline__ u32x2 sqrelu4(f32x4 a, f32x4 c, f32x4 bb, float r, float t) {
  float v0, v1, v2, v3;
  asm("v_fma_f32 %0, %8, %17, %12\n\tv_fma_f32 %1, %9, %17, %13\n\tv_fma_f32 %2, %10, %17, %14\n\tv_fma_f32 %3, %11, %17, %15\n\t"
      "v_fmac_f32 %0, %4, %16\n\tv_fmac_f32 %1, %5, %16\n\tv_fmac_f32 %2, %6, %16\n\tv_fmac_f32 %3, %7, %16\n\t"
      "v_mul_f32 %0, %0, |%0|\n\tv_mul_f32 %1, %1, |%1|\n\tv_mul_f32 %2, %2, |%2|\n\tv_mul_f32 %3, %3, |%3|\n\t"
      "v_cvt_pk_bf16_f32 %0, %0, %1\n\tv_cvt_pk_bf16_f32 %2, %2, %3\n\tv_pk_max_i16 %0, %0, 0\n\tv_pk_max_i16 %2, %2, 0"
      : "=&v"(v0), "=&v"(v1), "=&v"(v2), "=&v"(v3)
      : "v"(a[0]), "v"(a[1]), "v"(a[2]), "v"(a[3]), "v"(c[0]), "v"(c[1]), "v"(c[2]), "v"(c[3]), "v"(bb[0]), "v"(bb[1]), "v"(bb[2]), "v"(bb[3]), "v"(r), "v"(t));
  return u32x2{__float_as_uint(v0), __float_as_uint(v2)};
}

template <int N> struct IC { static constexpr int v = N; };
template <int MODE> struct G1 {
  static constexpr int NM = MODE == 2 ? 2 : 4, NI = MODE == 2 ? 6 : 4, PAD = MODE == 1 ? 32 : 8, NP = NI / 2;
  static constexpr int NMMA = 2 * NM * NI, NRD = 2 * (NM + 2 * NI), HEAD = MODE == 2 ? 6 : 12, R2H = 2, R2S = 18;
  struct Frag { bf16x8 x[2][NM]; s16x4 w[2][NI][2]; };
  struct Ptr { LAS char* x[2][2]; LAS char* w[NP]; };
  template <int R, int XO = 0, int WO = 0> static __device__ __forceinline__ void rd(Frag& f, const Ptr& p) {
    constexpr bool isx = R == 2 || (R >= 5 && R < 4 + 2 * NM);
    if constexpr (isx) { constexpr int k = R == 2 ? 0 : R - 4, u = k & 1, m = k >> 1;
      if constexpr (XO >= 0) f.x[u][m] = *(LAS bf16x8*)(p.x[u][m & 1] + XO + (m >> 1) * 4096 + (m & 1) * (MODE == 1 ? 512 : 2048)); }
    else { constexpr int q = R < 2 ? R : (R < 5 ? R - 1 : R - 2 * NM), i = q >> 2, u = (q >> 1) & 1;
      f.w[u][i][q & 1] = trd(p.w[i >> 1] + WO + (i & 1) * PAD + u * (8192 + 4 * PAD) + (q & 1) * 1024); }
  }
  template <int N> static __device__ __forceinline__ void mma(f32x4 (&acc)[NM][NI], const Frag& f) {
    constexpr int u = N & 1, i = N / (2 * NM), m0 = (N >> 1) % NM, m = (i & 1) ? NM - 1 - m0 : m0;
    const bf16x8 wf = cat8(f.w[u][i][0], f.w[u][i][1]);
    if constexpr (MODE == 1) asm volatile("v_mfma_f32_16x16x32_bf16 %0, %1, %2, %0" : "+v"(acc[m][i]) : "v"(f.x[u][m]), "v"(wf));
    else asm volatile("v_mfma_f32_16x16x32_bf16 %0, %1, %2, %0" : "+v"(acc[m][i]) : "v"(wf), "v"(f.x[u][m]));
  }
  template <int U> static __device__ __forceinline__ void csum(f32x4& cs, const Frag& f, bf16x8 sel, int idx) {
    const bf16x16 wf = __builtin_shufflevector(cat8(f.w[U][0][0], f.w[U][0][1]), cat8(f.w[U][1][0], f.w[U][1][1]), 0, 1, 2, 3, 4, 5, 6, 7, 8, 9, 10, 11, 12, 13, 14, 15);
    asm volatile("v_smfmac_f32_16x16x64_bf16 %0, %1, %2, %3" : "+v"(cs) : "v"(sel), "v"(wf), "v"(idx));
  }
  static __device__ __forceinline__ int span(int w, int j) { return MODE == 2 ? (w + j >= 3 ? w + j - 3 : w + j) : 2 * (w >> 1) + (j ^ (w & 1)); }
  template <int R0, int R1, int XO = 0, int WO = 0> static __device__ __forceinline__ void rds(Frag& f, const Ptr& p) {
    if constexpr (R0 < R1) { rd<R0, XO, WO>(f, p); rds<R0 + 1, R1, XO, WO>(f, p); } }
  template <int N0, int N1> static __device__ __forceinline__ void mmas(f32x4 (&acc)[NM][NI], const Frag& f) { if constexpr (N0 < N1) { mma<N0>(acc, f); mmas<N0 + 1, N1>(acc, f); } }
  template <int N> static __device__ __forceinline__ void mix(f32x4 (&acc)[NM][NI], const Frag& fc, Frag& fn, const Ptr& p) {
    if constexpr (N < NMMA) {
      constexpr int SPAN = NMMA - HEAD - 3, k = N - HEAD;
      constexpr int r0 = k >= SPAN ? NRD : (k * NRD + SPAN - 1) / SPAN, r1 = k + 1 >= SPAN ? NRD : ((k + 1) * NRD + SPAN - 1) / SPAN;
      mma<N>(acc, fc); rds<r0, r1>(fn, p); SB();
      mix<N + 1>(acc, fc, fn, p);
    }
  }
  struct Off { unsigned x, w[NP]; };
  static __device__ __forceinline__ Off offs(int w, int lane) {
    const int kq = lane >> 4, c16 = lane & 15, a = (lane >> 2) & 3;
    Off o;
    o.x = MODE == 1 ? (64 * (w & 1) + 8 * (c16 >> 2) + (c16 & 3)) * 128 + ((kq ^ ((c16 >> 1) & 1) ^ ((c16 >> 3) << 1) ^ (c16 & 4)) << 4)
                    : ((MODE == 2 ? 32 * w : 64 * (w & 1)) + c16) * 128 + ((kq ^ (c16 >> 1)) << 4);
    const unsigned wb = L_W + kq * (2048 + PAD) + a * 256 + (lane & 3) * (MODE == 1 ? 8 : 16);
#pragma unroll
    for (int j = 0; j < NP; ++j) o.w[j] = wb + (span(w, j) ^ a) * 64;
    return o;
  }
  template <int U> static __device__ __forceinline__ void ptrx(Ptr& p, const Off& o, LAS char* lds, unsigned sx) {
    p.x[U][0] = lds + sx + (o.x ^ (64 * U)); p.x[U][1] = lds + sx + (o.x ^ (64 * U + 32)); asm volatile("" : "+v"(p.x[U][0]), "+v"(p.x[U][1])); }
  static __device__ __forceinline__ void ptrw(Ptr& p, const Off& o, LAS char* lds, unsigned sw) {
#pragma unroll
    for (int j = 0; j < NP; ++j) { p.w[j] = lds + (sw + o.w[j]); asm volatile("" : "+v"(p.w[j])); } }
  template <int N, class T> static __device__ __forceinline__ void fin(f32x4 (&acc)[NM][NI], const Frag& fc, T& tail) {
    if constexpr (N < NMMA) { mma<N>(acc, fc); tail(IC<N - HEAD>{}); SB(); fin<N + 1>(acc, fc, tail); }
  }
  template <class T>
  static __device__ __forceinline__ void loop(f32x4 (&acc)[NM][NI], Frag& fa, const Off& o, LAS char* lds, unsigned sx, unsigned sw, int w, int lane, int pub,
                                              GAS u64* fl, unsigned fi, u64 tag, T tail, unsigned& stv) {
    const int kq = lane >> 4, c16 = lane & 15;
    Frag fb; Ptr p;
#pragma unroll
    for (int n = 0; n < NM * NI; ++n) { acc[n % NM][n / NM] = f32x4{}; asm volatile("" : "+v"(acc[n % NM][n / NM])); }
    const unsigned one = (kq >> 1) == ((c16 >> 1) & 1) ? 0x3f803f80u : 0u;
    bf16x8 sel = __builtin_bit_cast(bf16x8, u32x4{one, one, one, one});
    int sidx = (lane & 1) ? 0xEEEE : 0x4444;
    f32x4 cs = {};
    LAS float* cp = (LAS float*)(lds + L_ST + 1024) + 32 * span(w, 0) + (MODE == 1 ? c16 : 8 * (c16 >> 2) + (c16 & 3));
    asm volatile("s_nop 1" : "+v"(sel), "+v"(sidx), "+v"(cs), "+v"(cp));
    if constexpr (MODE != 2) { ptrx<0>(p, o, lds, sx); ptrx<1>(p, o, lds, sx); ptrw(p, o, lds, sw); SB(); tail(IC<-1>{}); STAMP(1); BAR_L(); STAMP(12); rds<0, NRD>(fa, p); }
#define G1_HEAD(FC, PUB, LAST, BAR) do { \
      mma<0>(acc, FC); sx = (sx == 5 * XS) ? 0 : sx + XS; asm volatile("" : "+s"(sx)); SB(); \
      mma<1>(acc, FC); sw = (sw == 2 * WSL) ? 0 : sw + WSL; asm volatile("" : "+s"(sw)); SB(); \
      mma<2>(acc, FC); ptrx<0>(p, o, lds, sx); SB(); \
      mma<3>(acc, FC); ptrw(p, o, lds, sw); SB(); \
      mmas<4, HEAD - 2>(acc, FC); SB(); \
      csum<0>(cs, FC, sel, sidx); csum<1>(cs, FC, sel, sidx); SB(); \
      mma<HEAD - 2>(acc, FC); ptrx<1>(p, o, lds, sx); SB(); \
      mma<HEAD - 1>(acc, FC); SB(); \
      if (LAST) { asm volatile("s_nop 5" : "+v"(cs)); cp[0] = cs[0] + cs[1]; cp[MODE == 1 ? 16 : 4] = cs[2] + cs[3]; } \
      if (PUB) DRAIN(); \
      if (BAR) BAR_L(); \
      if ((PUB) && w == 0) publish(fl, fi, tag, lane); \
      SB(); } while (0)
    SB();
    G1_HEAD(fa, false, false, true);
    tail(IC<-2>{}); SB();
#pragma unroll 1
    for (int s = 0; s < 4; ++s) {
      mix<HEAD>(acc, fa, fb, p);
      G1_HEAD(fb, false, false, true);
      mix<HEAD>(acc, fb, fa, p);
      G1_HEAD(fa, false, false, true);
    }
    mix<HEAD>(acc, fa, fb, p);
    G1_HEAD(fb, false, true, false);
    tail(IC<-3>{}); BAR_L(); SB();
    fin<HEAD>(acc, fb, tail);
    asm volatile("s_nop 7");
    SB();
  }
  template <int J, int N> static __device__ __forceinline__ void r2mix(f32x4 (&acc)[NM][NI], f32x4& cs, bf16x8 sel, int sidx, const Frag& fc, Frag& fn, const Ptr& pl, const Ptr& ph) {
    if constexpr (N < NMMA) {
      constexpr int H2 = R2H, SPAN = R2S, k = N - H2, xs = (5 + J) % 6;
      constexpr int r0 = k >= SPAN ? NRD : (k * NRD + SPAN - 1) / SPAN, r1 = k + 1 >= SPAN ? NRD : ((k + 1) * NRD + SPAN - 1) / SPAN;
      mma<N>(acc, fc);
      Ptr p = xs >= 3 ? ph : pl;
#pragma unroll
      for (int j = 0; j < NP; ++j) p.w[j] = J == 8 ? ph.w[j] : pl.w[j];
      rds<r0, r1, J >= 7 ? -1 : (int)((xs % 3) * XS), J == 8 ? 0 : ((2 + J) % 3) * WSL>(fn, p); SB();
      r2mix<J, N + 1>(acc, cs, sel, sidx, fc, fn, pl, ph);
    }
  }
  template <int J, class PUB> static __device__ __forceinline__ void r2steps(f32x4 (&acc)[NM][NI], f32x4& cs, bf16x8 sel, int sidx, Frag& fc, Frag& fn, const Ptr& pl, const Ptr& ph, PUB& pub, const bf16x8 (&xk)[2][2][NM]) {
    if constexpr (J < 9) {
      mmas<0, 2>(acc, fc); csum<0>(cs, fc, sel, sidx); csum<1>(cs, fc, sel, sidx); SB();
      if constexpr (J == 2) DRAIN();
      if constexpr (J < 8) BAR_L();
      if constexpr (J == 2) pub();
      SB();
      if constexpr (J >= 7) {
#pragma unroll
        for (int n = 0; n < 2 * NM; ++n) fn.x[n & 1][n >> 1] = xk[J - 7][n & 1][n >> 1];
      }
      r2mix<J, R2H>(acc, cs, sel, sidx, fc, fn, pl, ph);
      r2steps<J + 1>(acc, cs, sel, sidx, fn, fc, pl, ph, pub, xk);
    }
  }
  template <class PUB, class T>
  static __device__ __forceinline__ void loop2(f32x4 (&acc)[NM][NI], Frag& fa, const Off& o, const Ptr& ph, LAS char* lds, int w, int lane, const bf16x8 (&xk)[2][2][NM], PUB pub, T tail) {
    const int kq = lane >> 4, c16 = lane & 15;
    Frag fb; Ptr pl, ph2 = ph;
    ptrx<0>(pl, o, lds, 0); ptrx<1>(pl, o, lds, 0);
#pragma unroll
    for (int j = 0; j < NP; ++j) pl.w[j] = ph.w[j];
    ptrw(ph2, o, lds, 3 * XS - L_W);
#pragma unroll
    for (int n = 0; n < NM * NI; ++n) { acc[n % NM][n / NM] = f32x4{}; asm volatile("" : "+v"(acc[n % NM][n / NM])); }
    const unsigned one = (kq >> 1) == ((c16 >> 1) & 1) ? 0x3f803f80u : 0u;
    bf16x8 sel = __builtin_bit_cast(bf16x8, u32x4{one, one, one, one});
    int sidx = (lane & 1) ? 0xEEEE : 0x4444;
    f32x4 cs = {};
    LAS float* cp = (LAS float*)(lds + L_ST + 1024) + 32 * span(w, 0) + 8 * (c16 >> 2) + (c16 & 3);
    asm volatile("s_nop 1" : "+v"(sel), "+v"(sidx), "+v"(cs), "+v"(cp));
    SB();
    r2steps<0>(acc, cs, sel, sidx, fa, fb, pl, ph2, pub, xk);
    mmas<0, 2>(acc, fb); csum<0>(cs, fb, sel, sidx); mmas<2, 4>(acc, fb); csum<1>(cs, fb, sel, sidx); mmas<4, HEAD>(acc, fb); SB();
    fin<HEAD>(acc, fb, tail);
    asm volatile("s_nop 7" : "+v"(cs));
    cp[0] = cs[0] + cs[1]; cp[4] = cs[2] + cs[3];
    SB();
  }
};

struct KV { bf16x8 k1; s16x4 k2; bf16x8 v0, v1; };
__device__ __forceinline__ f32x16 qk_a(bf16x8 k, bf16x8 q) { f32x16 z = {}; return __builtin_amdgcn_mfma_f32_32x32x16_bf16(k, q, z, 0, 0, 0); }
__device__ __forceinline__ f32x16 qk_b(s16x4 k, s16x4 q, f32x16 c) { return __builtin_amdgcn_mfma_f32_32x32x8bf16_1k(k, q, c, 0, 0, 0); }
__device__ __forceinline__ f32x16 pv_lo(bf16x8 v, f32x16 p, f32x16 o, int idx) {
  f32x8 h = __builtin_shufflevector(p, p, 0, 1, 2, 3, 4, 5, 6, 7);
  o = __builtin_amdgcn_smfmac_f32_32x32x32_bf16(v, __builtin_bit_cast(bf16x16, h), o, idx, 0, 0);
  asm volatile("" : "+v"(o));
  return o; }
__device__ __forceinline__ f32x16 pv_hi(bf16x8 v, f32x16 p, f32x16 o, int idx) {
  f32x8 h = __builtin_shufflevector(p, p, 8, 9, 10, 11, 12, 13, 14, 15);
  o = __builtin_amdgcn_smfmac_f32_32x32x32_bf16(v, __builtin_bit_cast(bf16x16, h), o, idx, 0, 0);
  asm volatile("" : "+v"(o));
  return o; }
template <int E> __device__ __forceinline__ void max8(const f32x16& s, float& m) {
  asm volatile("v_max3_f32 %0, %0, %1, %2\n\tv_max3_f32 %0, %0, %3, %4\n\tv_max3_f32 %0, %0, %5, %6\n\tv_max3_f32 %0, %0, %7, %8"
               : "+v"(m) : "v"(s[E]), "v"(s[E + 1]), "v"(s[E + 2]), "v"(s[E + 3]), "v"(s[E + 4]), "v"(s[E + 5]), "v"(s[E + 6]), "v"(s[E + 7]));
}
struct F4 { float v[4]; };
__device__ __forceinline__ F4 add4(f32x4 g, float a, float b, float c, float d, float& chk) {
  F4 r;
  asm volatile("v_add_f32 %0, %9, %5\n\tv_add_f32 %1, %10, %6\n\tv_add_f32 %2, %11, %7\n\tv_add_f32 %3, %12, %8\n\t"
               "v_maximum3_f32 %4, %4, |%0|, |%1|\n\tv_maximum3_f32 %4, %4, |%2|, |%3|"
               : "=&v"(r.v[0]), "=&v"(r.v[1]), "=&v"(r.v[2]), "=&v"(r.v[3]), "+v"(chk) : "v"(a), "v"(b), "v"(c), "v"(d), "v"(g[0]), "v"(g[1]), "v"(g[2]), "v"(g[3]));
  return r;
}
__device__ __forceinline__ u32x2 scale4(F4 g, float s) {
  unsigned r0, r1;
  asm volatile("v_mul_f32 %2, %2, %6\n\tv_mul_f32 %3, %3, %6\n\tv_mul_f32 %4, %4, %6\n\tv_mul_f32 %5, %5, %6\n\tv_cvt_pk_bf16_f32 %0, %2, %3\n\tv_cvt_pk_bf16_f32 %1, %4, %5"
               : "=&v"(r0), "=&v"(r1), "+v"(g.v[0]), "+v"(g.v[1]), "+v"(g.v[2]), "+v"(g.v[3]) : "v"(s));
  return u32x2{r0, r1};
}
#define EX4(s, b) do { s[b] = __builtin_amdgcn_exp2f(s[b]); s[b+1] = __builtin_amdgcn_exp2f(s[b+1]); s[b+2] = __builtin_amdgcn_exp2f(s[b+2]); s[b+3] = __builtin_amdgcn_exp2f(s[b+3]); } while (0)
#define STEPX(SC, SP, OP, VP, SN, KN, Q1, Q2, X0, X1) do { \
  EX4(SC, 0); SB(); OP = pv_lo(VP.v0, SP, OP, idx); SB(); X0; SB(); \
  EX4(SC, 4); SB(); SN = qk_a(KN.k1, Q1); SB(); \
  EX4(SC, 8); SB(); OP = pv_hi(VP.v1, SP, OP, idx); SB(); X1; SB(); \
  EX4(SC, 12); SB(); SN = qk_b(KN.k2, Q2, SN); SB(); } while (0)
#define STEP(SC, SP, OP, VP, SN, KN, Q1, Q2) STEPX(SC, SP, OP, VP, SN, KN, Q1, Q2, , )

__global__ void __launch_bounds__(512, 2)
fused(const u16* x_, const u16* wqkv_, const u16* wo_, const u16* w1_, const u16* b1_, const u16* w2_, const u16* b2_, u16* out_, char* ws_) {
  __shared__ __attribute__((aligned(16))) char smem[L_END + 256];
  LAS char* lds = (LAS char*)smem;
  const unsigned lds0 = (unsigned)(size_t)lds;
  const int lane = threadIdx.x & 63, w = __builtin_amdgcn_readfirstlane(threadIdx.x >> 6);
  const int b = blockIdx.x, g = b & 7, idx8 = b >> 3;
  unsigned stv = 0;
  typedef __attribute__((ext_vector_type(8))) u64 u64x8;
  u64x8 ka; u64 kw;
  asm volatile("s_load_dwordx16 %0, %2, 0\n\ts_load_dwordx2 %1, %2, 64\n\ts_getpc_b64 s[98:99]\n\ts_and_b32 s98, s98, 0xffffff00\n\ts_add_u32 s98, s98, %3\n\ts_addc_u32 s99, s99, 0\n\ts_load_dword s100, s[98:99], 0\n\ts_load_dword s100, s[98:99], 64\n\ts_waitcnt lgkmcnt(0)"
               : "=&s"(ka), "=&s"(kw) : "s"(__builtin_amdgcn_kernarg_segment_ptr()), "s"((idx8 * 8 + w) * 128u + 256u) : "s98", "s99", "s100", "scc");
  const u16* x = (const u16*)ka[0]; const u16* wqkv = (const u16*)ka[1]; const u16* wo = (const u16*)ka[2]; const u16* w1 = (const u16*)ka[3];
  const u16* b1 = (const u16*)ka[4]; const u16* w2 = (const u16*)ka[5]; const u16* b2 = (const u16*)ka[6]; u16* out = (u16*)ka[7]; char* ws = (char*)kw;
  const int rt = (g & 3) * 4 + (idx8 & 3), cf = (g >> 2) * 8 + (idx8 >> 2);
  const int ct = idx8 >= 30 ? cf ^ 8 : cf;
  const bool vtype = ct >= 10;
  const int kq = lane >> 4, c16 = lane & 15;
  LAS float* st_r = (LAS float*)(lds + L_ST);
  LAS float* st_t = st_r + 128;
  LAS float* st_c = st_r + 256;
  GAS u64* fl = (GAS u64*)(ws + WS_F);
  const rsrc_t rw = mkrs(ws, WS_END), rx = mkrs(x, 2048 * D_ * 2);
  u64 tag = 0;
  const int head = 4 * g + (idx8 >> 3), qc = idx8 & 7;
  const int aw = w - 4, qh = aw & 1, kh = aw >> 1, r = lane & 31, h = lane >> 5, odd = head & 1, pr = head >> 1;
  int hstep = 0;
  unsigned oK1 = 0, oK2 = 0, oV0 = 0, oV1 = 0, oQ2 = 0;
  auto kv_offsets = [&](int ln) {
    const int r = ln & 31, h = ln >> 5;
    oK1 = odd * 1536 + ln * 16; oK2 = h ? P_ONE : 1024 + r * 16 + odd * 8; oQ2 = h ? OOB : oK2;
    const int dv = r < 16 ? (r & 3) + 4 * (r >> 3) + 8 * ((r >> 2) & 1) : r;
    oV0 = r < 20 ? P_V + odd * 1280 + h * 320 + dv * 16 : (r == 20 ? P_ONE : OOB); oV1 = r < 20 ? oV0 + 640 : oV0;
  };
  unsigned fi = 0;

  if (w >= 4) {
    const int hw = w - 4;
    rsrc_t rwt = mkrs(wqkv, D_ * 3 * D_ * 2);
    unsigned rs = 3 * D_ * 2, pad = vtype ? 32 : 8;
    unsigned wso = ct * 256 + 16 * hw * rs, xso = (rt * 128 + hw * 32) * (D_ * 2);
    const unsigned xv = (lane >> 3) * (D_ * 2) + (((lane & 7) ^ kq) << 4), xv1 = xv ^ 64, xv2 = xv ^ 32, xv3 = xv ^ 96;
    const int wc = c16 ^ (kq << 2);
    unsigned wv = ct < 15 ? kq * rs + (wc << 4) : OOB;
    unsigned nx = lds0 + hw * 4096, nw = lds0 + L_W + hw * 4096;
#define DX(i) dma16(rx, i == 0 ? xv : (i == 1 ? xv1 : (i == 2 ? xv2 : xv3)), xso + i * 8 * (D_ * 2), nx + i * 1024)
#define DW(i) dma16(rwt, wv, wso + 4 * i * rs, nw + i * 1024 + (2 * hw + (i >> 1)) * pad)
#define NEXTX() do { xso += 128; nx = (nx == lds0 + hw * 4096 + 5 * XS) ? nx - 5 * XS : nx + XS; } while (0)
#define NEXTW() do { wso += 64 * rs; nw = (nw == lds0 + L_W + hw * 4096 + 2 * WSL) ? nw - 2 * WSL : nw + WSL; } while (0)
#define HBAR(n) asm volatile("s_waitcnt vmcnt(" #n ")\n\ts_barrier" ::: "memory")
    __builtin_amdgcn_s_setprio(1);
#define DW4() do { DW(0); DW(1); DW(2); DW(3); NEXTW(); } while (0)
#define DX4() do { DX(0); DX(1); DX(2); DX(3); NEXTX(); } while (0)
    const char* pw = (const char*)w1 + 256 * F_ * 2 + (b * 4 + hw) * 192;
    {
      DX(0); DW(0); DX(1); DW(1); DX(2); DW(2); DX(3); DW(3); NEXTW(); NEXTX();
      DW4(); DX4(); SPFX(STOUCH3(pw)); pw += 64 * F_ * 2; HBAR(8);
      DX4();
#pragma unroll 1
      for (int s = 0; s < 7; ++s) { DW4(); DX4(); SPFX(STOUCH3(pw)); pw += (s == 4 ? -576 : 64) * F_ * 2; HBAR(12); }
    }
    const unsigned go = WS_S + (rt * 128 + (hw & 1) * 64 + lane) * 16;
    asm volatile("s_mov_b32 m0, %0\n\ts_nop 0\n\tbuffer_load_dwordx4 %1, %2, 0 offen sc1 lds" :: "s"(lds0 + L_G + hw * 1024), "v"(go), "s"(rw) : "memory");
    DW4(); SPFX(STOUCH3(pw)); pw += 64 * F_ * 2; HBAR(9);
    rwt = mkrs(w1, D_ * F_ * 2); rs = F_ * 2; pad = 8; wso = cf * 192 + (256 + 16 * hw) * rs; wv = wc < 12 ? kq * rs + (wc << 4) : OOB;
    xso -= 1024; nx = lds0 + hw * 4096 + 4 * XS; nw = lds0 + L_W + hw * 4096 + WSL;
    DW4();
    tag = uni64(*(LAS u64*)(lds + L_C));
    {
      asm volatile("s_waitcnt vmcnt(8)" ::: "memory");
      u32x4 gr = *(LAS u32x4*)(lds + L_G + hw * 1024 + lane * 16);
#pragma unroll 1
      for (int n = 0; n < (1 << 20); ++n) {
        if (__all(gr[1] == (unsigned)tag && gr[3] == (unsigned)tag)) break;
        asm volatile("buffer_load_dwordx4 %0, %1, %2, 0 offen sc1\n\ts_waitcnt vmcnt(0)" : "=&v"(gr) : "v"(go), "s"(rw) : "memory");
      }
      st_r[hw * 64 + lane] = __uint_as_float(hw < 2 ? gr[0] : gr[2]);
    }
    HBAR(4);
    DW4(); asm volatile("s_waitcnt lgkmcnt(0)" ::: "memory"); SPFX(STOUCH3(pw)); HBAR(4);
    DW4();
    {
      const int c0 = pr * 5 + 2 * odd, e = (lane >> 3) & 1;
      const int tqk = (c0 + 2 * e) >> 4, tv = (head * 20 + 19 * e) >> 7;
      fi = F_Q + (lane < 16 ? 5 + tqk : 10 + tv) * 16 + 8 * kh + (lane & 7);
      if (lane >= 32) fi = F_Q + tqk * 16 + 2 * qc + qh;
      kv_offsets(lane);
      asm volatile("" : "+v"(fi), "+v"(oK1), "+v"(oK2), "+v"(oV0), "+v"(oV1), "+v"(oQ2));
    }
    HBAR(4);
    DW4(); HBAR(4);
    DW4(); HBAR(4);
    DW4(); DX4(); wso -= 512 * rs; HBAR(8);
    DW4(); DX4(); HBAR(12);
    DW4(); wso -= 256 * rs; HBAR(8);
    DW4(); nw = lds0 + 3 * XS + hw * 4096; DW4(); HBAR(8);
    asm volatile("s_mov_b32 m0, %0\n\ts_nop 0\n\tbuffer_load_dwordx4 %1, %2, 0 offen sc1 lds" :: "s"(lds0 + L_G + hw * 1024), "v"(fi * 128), "s"(mkrs(ws + WS_F, 1u << 17)) : "memory");
  } else {
    const int wm = w & 1, wn = w >> 1;
    const int row = rt * 128 + cf * 8 + 2 * w + (lane >> 5);
    {
      const unsigned lb = lds0 + 5 * XS + w * 4096, xo = row * (D_ * 2) + r * 16;
      dma16(rx, xo, 0, lb); dma16(rx, xo, 512, lb + 1024); dma16(rx, r < 16 ? xo : OOB, 1024, lb + 2048);
      asm volatile("s_mov_b32 m0, %0\n\ts_nop 0\n\tbuffer_load_dword %1, %2, 0 offen sc1 lds" :: "s"(lb + 3072), "v"(lane * 4), "s"(mkrs(ws + WS_F, 8)) : "memory");
      u64 pc; asm volatile("s_getpc_b64 %0" : "=s"(pc));
      asm volatile("s_load_dword s100, %0, 0" :: "s"((pc & ~(u64)255) + 32512 + min((idx8 * 4 + w) * 64u, 5120u)) : "s100");
      *(LAS u32x4*)(lds + L_M + threadIdx.x * 16) = u32x4{0x7fc00000u, 0x7fc00000u, 0x7fc00000u, 0x7fc00000u};
    }
    unsigned vo[4];
    if (vtype) {
      const unsigned vb = (WS_T + (4 * rt + 2 * wm) * PT + P_V + kq * 320) | (ct < 15 ? 0u : OOB);
#pragma unroll
      for (int i = 0; i < 4; ++i) {
        const unsigned fv = (ct - 10) * 128 + 32 * G1<1>::span(w, i >> 1) + 16 * (i & 1) + c16, hd = __umul24(fv, 3277) >> 16;
        vo[i] = vb + fv * 16 + __umul24(hd, 960) + __umul24(hd >> 1, 64 * PT - 2560);
      }
    } else {
#pragma unroll
      for (int pp = 0; pp < 2; ++pp) {
        const unsigned c = (ct < 5 ? ct : ct - 5) * 16 + 4 * G1<0>::span(w, pp) + kq, p5 = __umul24(c, 205) >> 10, pos = c - p5 * 5;
        const unsigned tb = WS_T + (4 * rt + 2 * wm) * PT + __umul24(p5, 64 * PT);
        vo[pp] = tb + pos * 512 + (ct < 5 ? P_Q : 0u) + c16 * 16;
        vo[2 + pp] = (ct >= 5 && pos == 2) ? tb + P_ONE + (c16 >> 3) * PT + (c16 & 7) * 16 : OOB;
      }
    }
    asm volatile("" : "+v"(vo[0]), "+v"(vo[1]), "+v"(vo[2]), "+v"(vo[3]));
    if (w < 2) touch(w ? mkrs(b1 + cf * 96, 192) : mkrs(wqkv + 94 * 3 * D_, 546 * 3 * D_ * 2), w ? lane * 4 : (b * 64 + lane) * 128, lds0 + (w ? L_ST + 1536 : L_PF));
    u32x4 xr[3]; u64 e0;
    auto stats_ld = [&]() {
      if (w < 2) asm volatile("s_waitcnt vmcnt(1)" ::: "memory"); else DRAIN();
#pragma unroll
      for (int i = 0; i < 3; ++i) xr[i] = *(LAS u32x4*)(lds + 5 * XS + w * 4096 + i * 1024 + lane * 16);
      e0 = *(LAS u64*)(lds + 5 * XS + w * 4096 + 3072);
    };
    auto stats = [&]() {
      float p = 0.f, q = 0.f;
#pragma unroll
      for (int i = 0; i < 12; ++i) {
        const unsigned u2 = xr[i >> 2][i & 3];
        const bf16x2 v2 = __builtin_bit_cast(bf16x2, u2);
        p = __builtin_amdgcn_fdot2_f32_bf16(v2, __builtin_bit_cast(bf16x2, 0x3f803f80u), p, false); q = __builtin_amdgcn_fdot2_f32_bf16(v2, v2, q, false);
      }
      p = dppadd<0x140>(dppadd<0x141>(dppadd<0x4E>(dppadd<0xB1>(p)))); q = dppadd<0x140>(dppadd<0x141>(dppadd<0x4E>(dppadd<0xB1>(q))));
      p = dppadd<0x142>(p); q = dppadd<0x142>(q);
      const float mu = p * (1.f / D_), rstd = __builtin_amdgcn_rsqf(fmaxf(q * (1.f / D_) - mu * mu, 0.f) + 1e-5f);
      tag = uni64(e0 + 1);
      if (threadIdx.x == 0) { *(LAS u64*)(lds + L_C) = tag; *(LAS u32x4*)(lds + L_C + 16) = u32x4{0, 0, 0, 0}; *(LAS u32x4*)(lds + L_C + 32) = u32x4{0, 0, 0, 0}; *(LAS u32x4*)(lds + L_C + 48) = u32x4{0, 0, 0, 0}; }
      st16(u32x4{__float_as_uint(rstd), (unsigned)tag, __float_as_uint(-mu * rstd), (unsigned)tag}, rw, r == 16 ? row * 16 : OOB, WS_S);
    };
    G1<2>::Frag f2; G1<2>::Ptr p2;
    G1<2>::Off o2 = G1<2>::offs(w, lane);
    asm volatile("" : "+v"(o2.x), "+v"(o2.w[0]), "+v"(o2.w[1]), "+v"(o2.w[2]));
    auto ptr2 = [&]() { G1<2>::ptrx<0>(p2, o2, lds, 3 * XS); G1<2>::ptrx<1>(p2, o2, lds, 3 * XS); G1<2>::ptrw(p2, o2, lds, 0); };
    bf16x8 xk[2][2][2];
    auto keep = [&]() {
#pragma unroll
      for (int n = 0; n < 8; ++n) { xk[n >> 2][n & 1][(n >> 1) & 1] = *(LAS bf16x8*)(lds + (o2.x ^ ((n & 1) * 64 + ((n >> 1) & 1) * 32)) + (n >> 2) * XS + ((n >> 1) & 1) * 2048); }
    };
    {
      f32x4 acc[4][4];
      if (vtype) {
        f32x4 r4[4], t4[4]; float c1[4];
        G1<1>::Frag f1;
        LAS float* pr4 = st_r + 64 * wm + 8 * kq; LAS float* pc0 = st_c + 32 * G1<1>::span(w, 0) + c16; LAS float* pc1 = st_c + 32 * G1<1>::span(w, 1) + c16;
        asm volatile("" : "+v"(pr4), "+v"(pc0), "+v"(pc1));
        G1<1>::loop(acc, f1, G1<1>::offs(w, lane), lds, 0, 0, w, lane, -1, fl, 0, tag, [&](auto k) {
          constexpr int K = decltype(k)::v;
          if constexpr (K == -1) { stats_ld(); stats(); }
          else if constexpr (K == -2) keep();
          else if constexpr (K == -3) ptr2();
          else if constexpr (K < 4) { r4[K] = *(LAS f32x4*)(pr4 + 32 * (K >> 1) + 4 * (K & 1)); t4[K] = *(LAS f32x4*)(pr4 + 128 + 32 * (K >> 1) + 4 * (K & 1)); }
          else if constexpr (K < 6) { c1[2 * K - 8] = (K == 4 ? pc0 : pc1)[0]; c1[2 * K - 7] = (K == 4 ? pc0 : pc1)[16]; }
          else if constexpr (2 * K - 12 < F2N) G1<2>::rds<2 * K - 12, 2 * K - 10, XS, WSL>(f2, p2);
        }, stv);
        STAMP(2);
#pragma unroll
        for (int i = 0; i < 4; ++i)
#pragma unroll
          for (int a = 0; a < 2; ++a)
            st16(pk8(acc[2 * a][i] * r4[2 * a] + t4[2 * a] * c1[i], acc[2 * a + 1][i] * r4[2 * a + 1] + t4[2 * a + 1] * c1[i]), rw, vo[i], a * PT);
      } else {
        float r1[4], t1[4]; f32x4 c4[4];
        G1<0>::Frag f0;
        LAS float* pr1 = st_r + 64 * wm + c16; LAS float* pc0 = st_c + 32 * G1<0>::span(w, 0) + 8 * kq; LAS float* pc1 = st_c + 32 * G1<0>::span(w, 1) + 8 * kq;
        asm volatile("" : "+v"(pr1), "+v"(pc0), "+v"(pc1));
        G1<0>::loop(acc, f0, G1<0>::offs(w, lane), lds, 0, 0, w, lane, -1, fl, 0, tag, [&](auto k) {
          constexpr int K = decltype(k)::v;
          if constexpr (K == -1) { stats_ld(); stats(); }
          else if constexpr (K == -2) keep();
          else if constexpr (K == -3) ptr2();
          else if constexpr (K < 4) { r1[K] = pr1[16 * K]; t1[K] = pr1[128 + 16 * K]; }
          else if constexpr (K < 6) { c4[2 * K - 8] = *(LAS f32x4*)(K == 4 ? pc0 : pc1); c4[2 * K - 7] = *(LAS f32x4*)((K == 4 ? pc0 : pc1) + 4); }
          else if constexpr (2 * K - 12 < F2N) G1<2>::rds<2 * K - 12, 2 * K - 10, XS, WSL>(f2, p2);
        }, stv);
        STAMP(2);
        const float sc = ct < 5 ? QSCALE : 1.f;
#pragma unroll
        for (int pp = 0; pp < 2; ++pp)
#pragma unroll
          for (int m = 0; m < 4; ++m)
            st16(pk8(acc[m][2 * pp] * (r1[m] * sc) + c4[2 * pp] * (t1[m] * sc), acc[m][2 * pp + 1] * (r1[m] * sc) + c4[2 * pp + 1] * (t1[m] * sc)), rw,
                 vo[pp], (m & 1) * 256 + (m >> 1) * PT);
        st16(u32x4{0x3f803f80u, 0x3f803f80u, 0x3f803f80u, 0x3f803f80u}, rw, min(vo[2], vo[3]), 0);
      }
    }
    STAMP(3);
    f32x4 acc[2][6];
    float r1[2], t1[2]; f32x4 c4[6]; u32x4 bb[3];
    LAS float* pr1 = st_r + 32 * w + c16;
    G1<2>::rds<F2N, 28, XS, WSL>(f2, p2);
    G1<2>::loop2(acc, f2, o2, p2, lds, w, lane, xk, [&]() { if (w == 0) publish(fl, F_Q + ct * 16 + rt, tag, lane); }, [&](auto k) {
      constexpr int K = decltype(k)::v;
      if constexpr (K < 2) { r1[K] = pr1[16 * K]; t1[K] = pr1[128 + 16 * K]; }
      else if constexpr (K < 5) bb[K - 2] = *(LAS u32x4*)(st_c + 128 + 16 * G1<2>::span(w, K - 2) + 4 * kq);
    });
    STAMP(4);
    __builtin_amdgcn_s_setprio(2);
    lds_meet<2>((LAS unsigned*)(lds + L_C + 20), 4, lane);
#pragma unroll
    for (int pp = 0; pp < 3; ++pp) { LAS float* pc = st_c + 32 * G1<2>::span(w, pp) + 8 * kq; c4[2 * pp] = *(LAS f32x4*)pc; c4[2 * pp + 1] = *(LAS f32x4*)(pc + 4); }
    const bool ret = w < 2 || ct == 15;
    const int slab = w < 2 ? 2 * idx8 + w : 62 + 2 * (idx8 & 1) + w;
    u32x4 hq[6];
#pragma unroll
    for (int pp = 0; pp < 3; ++pp) {
      const f32x4 ba = {blo(bb[pp][0]), bhi(bb[pp][0]), blo(bb[pp][1]), bhi(bb[pp][1])}, bc = {blo(bb[pp][2]), bhi(bb[pp][2]), blo(bb[pp][3]), bhi(bb[pp][3])};
#pragma unroll
      for (int m = 0; m < 2; ++m) {
        const u32x2 lo = sqrelu4(acc[m][2 * pp], c4[2 * pp], ba, r1[m], t1[m]), hi = sqrelu4(acc[m][2 * pp + 1], c4[2 * pp + 1], bc, r1[m], t1[m]);
        hq[2 * pp + m] = u32x4{lo[0], lo[1], hi[0], hi[1]};
      }
    }
    asm volatile("" : "+v"(hq[0]), "+v"(hq[1]), "+v"(hq[2]), "+v"(hq[3]), "+v"(hq[4]), "+v"(hq[5]));
#define HST(i0, i1) do { _Pragma("unroll") for (int i = i0; i < i1; ++i) { st16(hq[i], rw, lane * 16, WS_H + ((8 * rt + 2 * w + (i & 1)) * 48 + 3 * cf + G1<2>::span(w, i >> 1)) * 1024); SB(); __builtin_amdgcn_s_sleep(4); SB(); } } while (0)
    HST(0, 6);
    if (ret) {
      const int s2 = slab - 48;
      const rsrc_t rr = mkrs((slab < 48 ? w2 : wo) + 80 * g, ((slab < 48 ? F_ : D_) * D_ - 80 * g) * 2);
      const unsigned r0 = slab < 48 ? slab * 32 : (s2 < 16 ? s2 * 40 : (s2 - 16) * 160 + 16), rsh = s2 < 16 ? 4 : 2, rmu = slab < 48 ? 0 : (s2 < 16 ? 4 : 16);
#pragma unroll
      for (int i = 0; i < 5; ++i) {
        const unsigned c = 64 * i + lane, k = (c * 205) >> 11;
        dma16(rr, (k + (k >> rsh) * rmu) * (D_ * 2) + (c - k * 10) * 16, r0 * (D_ * 2), lds0 + L_RED + w * 5120 + i * 1024);
      }
      const unsigned tro = (8 * kq + ((lane >> 2) & 3)) * 160 + (lane & 3) * 16 + (g & 1) * 32, tr4 = tro - (lane & 3) * 8 + 128 - (g & 1) * 160;
      DRAIN();
#pragma unroll
      for (int t = 0; t < 5; ++t) {
        LAS char* p = lds + L_RED + w * 5120 + (t < 4 ? tro + (t >> 1) * 64 + (t & 1) * 8 : tr4);
        st16(__builtin_bit_cast(u32x4, cat8(trd(p), trd(p + 640))), rw, lane * 16 + WS_W + (g * 68 + slab) * 5120, t * 1024); SB(); if (WP) __builtin_amdgcn_s_sleep(WP); SB();
      }
    }
    STAMP(5);
  }

  if (w >= 4) {
    const rsrc_t rk = mkrs(ws + WS_T + (pr * 64 + kh * 32) * PT, 32 * PT);
    unsigned qo[4];
#pragma unroll
    for (int t = 0; t < 4; ++t) { qo[t] = WS_T + (pr * 64 + 8 * qc + 4 * qh + (t ^ (2 * kh))) * PT + P_Q; asm volatile("" : "+s"(qo[t])); }
    asm volatile("" :: "s"(rk));
    HBAR(1);
    DRAIN();
    if (!__all(*(volatile LAS u64*)(lds + L_G + aw * 1024 + (oK1 & 1023)) == tag)) await<2>(fl, fi, tag);
    __builtin_amdgcn_fence(__ATOMIC_ACQUIRE, "wavefront");
    STAMP(6);
    bool stored = false;
    auto sweep = [&](auto pass_) -> unsigned {
      constexpr int PASS = decltype(pass_)::v;
      const int lane = fresh_lane(), r = lane & 31, h = lane >> 5, c16 = lane & 15;
      if constexpr (PASS) kv_offsets(lane);
      bf16x8 q1[4]; s16x4 q2[4];
      KV b0, b1_, b2_, b3;
      unsigned so = 0;
#define LDK(B, d) do { B.k1 = ld16(rk, oK1, so + (d) * PT); SB(); B.k2 = ld8(rk, oK2, so + (d) * PT); SB(); } while (0)
#define LDV(B, d) do { B.v0 = ld16(rk, oV0, so + (d) * PT); SB(); B.v1 = ld16(rk, oV1, so + (d) * PT); SB(); } while (0)
      SB();
      LDK(b1_, 1);
#pragma unroll
      for (int t = 0; t < 4; ++t) { q1[t] = ld16(rw, oK1, qo[t]); SB(); q2[t] = ld8(rw, oQ2, qo[t]); SB(); }
      LDK(b0, 0);
      float mx[4] = {-3.0e38f, -3.0e38f, -3.0e38f, -3.0e38f};
      if constexpr (PASS) {
#pragma unroll 1
        for (int j = 2; j < 32; ++j) {
          const bf16x8 k1 = ld16(rk, oK1, j * PT); const s16x4 k2 = ld8(rk, oK2, j * PT);
#pragma unroll
          for (int t = 0; t < 4; ++t) {
            const f32x16 sv = qk_b(k2, q2[t], qk_a(k1, q1[t]));
#pragma unroll
            for (int e = 0; e < 16; ++e) mx[t] = fmaxf(mx[t], sv[e]);
          }
        }
      }
      int idx = 0xDDDD;
      f32x16 o0 = {}, o1 = {}, o2 = {}, o3 = {}, s0, s1, s2, s3;
      b3.v0 = bf16x8{}; b3.v1 = bf16x8{};
      LAS char* sp = lds + L_MRG + aw * 6144 + lane * 16; LAS char* gp = lds + L_MRG + (aw ^ 2) * 6144 + lane * 16;
      const int tt = (8 * qc + 4 * qh + (2 ^ (2 * kh))) * 2 + (r >> 4);
      unsigned oa = WS_O + (tt * 16 + pr) * 1024 + ((2 * odd + h) * 16 + c16) * 16, ob = h ? OOB : WS_O2 + (tt * 32 + head) * 128 + c16 * 8;
      volatile LAS f32x4* mw = (volatile LAS f32x4*)(lds + L_M) + aw * 64 + lane; volatile LAS f32x4* mr = (volatile LAS f32x4*)(lds + L_M) + (aw ^ 2) * 64 + r;
      asm volatile("" : "+v"(o0), "+v"(o1), "+v"(o2), "+v"(o3), "+v"(b3.v0), "+v"(b3.v1), "+v"(idx), "+v"(sp), "+v"(gp), "+v"(oa), "+v"(ob), "+v"(mw), "+v"(mr)); SB();
      {
        f32x16 sm[8];
#define SQ(n) ((n) < 4 ? (n) : ((n) - 2) & 3)
#define SMA(n) do { sm[n] = qk_a((n) < 4 ? b1_.k1 : b0.k1, q1[SQ(n)]); if ((n) == 6) s0 = sm[n]; if ((n) == 7) s1 = sm[n]; SB(); } while (0)
#define SMB(n) do { sm[n] = qk_b((n) < 4 ? b1_.k2 : b0.k2, q2[SQ(n)], sm[n]); SB(); } while (0)
        SMA(0); SMB(0); SMA(1); SMB(1);

        LDV(b0, 0); LDV(b1_, 1); LDK(b2_, 2); LDV(b2_, 2); LDK(b3, 3);

#pragma unroll
        for (int n = 0; n < 8; ++n) {
          if (n < 6) SMA(n + 2);
          max8<0>(sm[n], mx[SQ(n)]); SB();
          if (n < 6) SMB(n + 2);
          max8<8>(sm[n], mx[SQ(n)]); SB();
        }
      }

      *mw = f32x4{mx[0], mx[1], mx[2], mx[3]};
      if constexpr (PASS) lds_meet<0>((LAS unsigned*)(lds + L_C + 44), 4, lane);
#pragma unroll
      for (int t = 0; t < 4; ++t) {
        auto sw = __builtin_amdgcn_permlane32_swap(__float_as_uint(mx[t]), __float_as_uint(mx[t]), false, false);
        asm volatile("v_max_f32 %0, %1, %2" : "=v"(mx[t]) : "v"(sw[0]), "v"(sw[1]));
      }
      s3 = o3; asm volatile("" : "+v"(s3));
      f32x4 ma, mb;
#pragma unroll 1
      for (int n = 0; n < (1 << 22); ++n) {
        ma = mr[0]; mb = mr[32];
        if (PASS || !__any(__builtin_isunordered(ma[0], mb[0]) | __builtin_isunordered(ma[1], mb[1]) | __builtin_isunordered(ma[2], mb[2]) | __builtin_isunordered(ma[3], mb[3]))) break;
      }

#pragma unroll
      for (int t = 0; t < 4; ++t) {
        float nm;
        asm("v_max3_f32 %0, %1, %2, %3\n\tv_sub_f32 %0, %4, %0" : "=&v"(nm) : "v"(mx[t]), "v"(ma[t ^ 2]), "v"(mb[t ^ 2]), "s"(PASS ? 0.f : -MARGIN));
        u32x2 pm = __builtin_bit_cast(u32x2, q2[t]);
        if constexpr (PASS) {
          const float a0 = bhi(__float_as_uint(nm)), r0 = nm - a0, a1 = bhi(__float_as_uint(r0)), a2 = r0 - a1;
          if (h) pm = u32x2{(__float_as_uint(a0) >> 16) | __float_as_uint(a1), __float_as_uint(a2) >> 16};
        } else if (h) pm[0] = __float_as_uint(nm) >> 16;
        q2[t] = __builtin_bit_cast(s16x4, pm);
        if (t == 0) s0 = qk_b(b0.k2, q2[0], s0);
        if (t == 1) s1 = qk_b(b0.k2, q2[1], s1);
        SB();
      }
      if constexpr (!PASS) { STAMP(7); SB(); }
#define KT(C, P, N) do { \
  STEPX(s0, s3, o3, P, s2, C, q1[2], q2[2], P.v0 = ld16(rk, oV0, so), __builtin_amdgcn_s_waitcnt(0x0F79)); \
  STEPX(s1, s0, o0, C, s3, C, q1[3], q2[3], P.v1 = ld16(rk, oV1, so), so += PT; asm volatile("" : "+s"(so))); \
  STEPX(s2, s1, o1, C, s0, N, q1[0], q2[0], C.k1 = ld16(rk, oK1, so), ); \
  STEPX(s3, s2, o2, C, s1, N, q1[1], q2[1], C.k2 = ld8(rk, oK2, so), ); } while (0)
      so = 3 * PT;
      int itn = 32;
#pragma unroll 1
      do { KT(b0, b3, b1_); KT(b1_, b0, b2_);
        ITS();
        KT(b2_, b1_, b3); KT(b3, b2_, b0);
        ITS();
      } while (so != 31 * PT);
#define KTE(C, P, N, W) do { STEPX(s0, s3, o3, P, s2, C, q1[2], q2[2], , __builtin_amdgcn_s_waitcnt(W)); STEP(s1, s0, o0, C, s3, C, q1[3], q2[3]); \
  STEP(s2, s1, o1, C, s0, N, q1[0], q2[0]); STEP(s3, s2, o2, C, s1, N, q1[1], q2[1]); } while (0)
#define SEND(O, z, e) do { *(LAS f32x4*)(sp + (z) * 3072 + (e) * 1024) = f32x4{O[4 * (e)], O[4 * (e) + 1], O[4 * (e) + 2], O[4 * (e) + 3]}; SB(); } while (0)
      STEPX(s0, s3, o3, b3, s2, b0, q1[2], q2[2], b3.v0 = ld16(rk, oV0, so), __builtin_amdgcn_s_waitcnt(0x0F79));
      STEPX(s1, s0, o0, b0, s3, b0, q1[3], q2[3], b3.v1 = ld16(rk, oV1, so), ); STEP(s2, s1, o1, b0, s0, b1_, q1[0], q2[0]); STEP(s3, s2, o2, b0, s1, b1_, q1[1], q2[1]);
      KTE(b1_, b0, b2_, 0x0F76); KTE(b2_, b1_, b3, 0x0F72);
      STEPX(s0, s3, o3, b2_, s2, b3, q1[2], q2[2], , __builtin_amdgcn_s_waitcnt(0x0F70)); STEP(s1, s0, o0, b3, s3, b3, q1[3], q2[3]);
      LAS unsigned* mc = (LAS unsigned*)(lds + L_C + 32 + 4 * qh); unsigned one = 1u;
      EX4(s2, 0); SB(); o1 = pv_lo(b3.v0, s1, o1, idx); SB(); asm volatile("" : "+v"(mc), "+v"(one)); EX4(s2, 4); SB(); o1 = pv_hi(b3.v1, s1, o1, idx); SB(); SEND(o0, 0, 0);
      EX4(s2, 8); SB(); o2 = pv_lo(b3.v0, s2, o2, idx); SB(); SEND(o0, 0, 1); EX4(s2, 12); SB(); SEND(o0, 0, 2);
      EX4(s3, 0); SB(); o2 = pv_hi(b3.v1, s2, o2, idx); SB(); SEND(o1, 1, 0); EX4(s3, 4); SB(); SEND(o1, 1, 1); SEND(o1, 1, 2);
      lds_add(mc, one); SB();
      unsigned cv; f32x4 gv[6];
#define GET() do { cv = *(volatile LAS unsigned*)mc; _Pragma("unroll") for (int e = 0; e < 6; ++e) gv[e / 3 * 3 + (e + 2) % 3] = *(volatile LAS f32x4*)(gp + (e / 3 * 3 + (e + 2) % 3) * 1024); } while (0)
      o3 = pv_lo(b3.v0, s3, o3, idx); SB(); EX4(s3, 8); SB(); EX4(s3, 12); SB(); GET(); SB(); o3 = pv_hi(b3.v1, s3, o3, idx); SB();
      asm volatile("" : "+v"(o2), "+v"(o3) : "v"(o0), "v"(o1));
      if constexpr (!PASS) { STAMP(8); SB(); }
      if (__builtin_amdgcn_readfirstlane(cv) < 1024 + 2 + 2 * PASS) {
#pragma unroll 1
        for (int n = 0; n < (1 << 22); ++n) { if (__builtin_amdgcn_readfirstlane(*(volatile LAS unsigned*)mc) >= 1024 + 2 + 2 * PASS) break; }
        GET();
      }
      u32x4 pa[2]; u32x2 pb_[2]; float chk = 0.f, l[2];
#pragma unroll
      for (int z = 0; z < 2; ++z) {
        const f32x16 k = z ? o3 : o2;
        const F4 u2 = add4(gv[3 * z + 2], k[8], k[9], k[10], k[11], chk);
        l[z] = __uint_as_float(__builtin_amdgcn_permlane32_swap(__float_as_uint(u2.v[0]), __float_as_uint(u2.v[0]), false, false)[1]);
        float inv = __builtin_amdgcn_rcpf(l[z]);
        asm volatile("" : "+v"(inv));
        const F4 u0 = add4(gv[3 * z], k[0], k[1], k[2], k[3], chk), u1 = add4(gv[3 * z + 1], k[4], k[5], k[6], k[7], chk);
        const u32x2 x0 = scale4(u0, inv), x1 = scale4(u1, inv);
        pa[z] = u32x4{x0[0], x0[1], x1[0], x1[1]}; pb_[z] = scale4(u2, inv);
      }
      asm volatile("" :: "v"(o3));
      unsigned bad = 1u;
      if constexpr (!PASS) { stored = __all(chk < 1.0e37f && l[0] > 1.0e-30f && l[1] > 1.0e-30f); if (!stored) bad = 0x101u; }
      else {
#pragma unroll
        for (int z = 0; z < 2; ++z) {
#pragma unroll
          for (int j = 0; j < 4; ++j) pa[z][j] = min(pa[z][j], 0xfffffffeu);
          pb_[z][0] = min(pb_[z][0], 0xfffffffeu);
        }
      }
      if (PASS ? !stored : stored) { st16(pa[0], rw, oa, 0); st8(pb_[0], rw, ob, 0); st16(pa[1], rw, oa, 32768); st8(pb_[1], rw, ob, 8192); }
      unsigned cn = lds_add_rtn((LAS unsigned*)(lds + L_C + 24), bad) + bad;
      if constexpr (!PASS) {
        STAMP(9);
#pragma unroll 1
        for (int n = 0; n < (1 << 22); ++n) { if ((cn & 255) >= 4) break; cn = __builtin_amdgcn_readfirstlane(*(volatile LAS unsigned*)(lds + L_C + 24)); }
        asm volatile("" ::: "memory");
      }
      return cn;
    };
    if (__builtin_expect(sweep(IC<0>{}) >= 256, 0)) sweep(IC<1>{});
    const int lane = fresh_lane();
    if (b == 0) {
      await<8>(fl, F_O + aw * 64 + lane, tag);
      lds_meet<2>((LAS unsigned*)(lds + L_C + 28), 4, lane);
      if (w == 4) publish(fl, 0, tag, lane);
    }
  } else {
    const int rt2 = 4 * g + (idx8 & 3), ct2 = idx8 >> 2;
    {
      DRAIN();
      if (lds_add_rtn((LAS unsigned*)(lds + L_C + 16), 1u) == 3) publish(fl, F_H + b, tag, lane);
      const int j = lane & 15, hc = 4 * w + (j & 3), hr = rt2 >> 1;
      const int pb = (j < 4 || j > 12) ? (hr >> 2) + 4 * (hc >> 3) + 8 * ((hr & 3) + 4 * (hc & 7)) : 8 * (j < 10 ? 6 * w + j - 4 : (j < 12 ? 14 + 2 * w + j : (ct2 < 4 ? 30 : 28) + (w >> 1))) + ct2;
#pragma unroll 1
      for (int n = 0; n < (1 << 22); ++n) { if (__builtin_amdgcn_readfirstlane(*(volatile LAS unsigned*)(lds + L_C + 16)) >= 4) break; __builtin_amdgcn_s_sleep(4); }
      await<10>(fl, F_H + pb, tag);
    }
    STAMP(6);
    const int o = (64 * rt2 + 16 * w + c16) * D_ + 80 * ct2;
    u32x4 xv[2], bv[2]; u32x2 xv4, bv4;
    {
      f32x4 acc[4][5];
      bf16x8 f0[11], f1[11];
      unsigned vo = lane * 16, vo4 = vo + 4096, va[4];
#pragma unroll
      for (int m = 0; m < 4; ++m) va[m] = vo + (m ^ w) * 49152;
      unsigned sa = WS_H + (4 * rt2 * 48 + 12 * w) * 1024, sw = WS_W + (ct2 * 68 + 12 * w) * 5120, sq = WS_W + (ct2 * 68 + 48 + 4 * w) * 5120;
      const unsigned sq0 = sq + 20480, sq1 = WS_W + (ct2 * 68 + 64 + w) * 5120;
      LAS char* wl = lds + L_WO + lane * 16; LAS char* wst = wl + ((w - 1 - (g >> 1)) & 3) * 25600;
      asm volatile("" : "+v"(vo), "+v"(vo4), "+v"(va[0]), "+v"(va[1]), "+v"(va[2]), "+v"(va[3]), "+v"(wl), "+v"(wst));
#define GL1(F, j) F[j] = (j) < 4 ? ld16(rw, va[(j) & 3], sa) : ((j) < 8 ? ld16(rw, vo + (((j) - 4) & 3) * 1024, sw) : ((j) < 9 ? ld16(rw, vo4, sw) : ld16(rw, vo + ((j) - 9) * 1024, sq)))
#define GSTEP(FC, FN, WO, LD, Z) do { _Pragma("unroll") for (int n = 0; n < 20; ++n) { \
        acc[n & 3][n >> 2] = __builtin_amdgcn_mfma_f32_16x16x32_bf16(FC[4 + (n >> 2)], FC[n & 3], (Z) ? f32x4{} : acc[n & 3][n >> 2], 0, 0, 0); \
        SB(); \
        if ((Z) && n == 3) wlast = ld16(rw, vo4, sq1); \
        else if ((Z) && (n == 7 || n == 11)) st16(mk4, rw, n == 7 ? ma : ma2, 0); \
        else if ((Z) && (n == 5 || n == 9)) dma16(rx, xo, n == 5 ? 0 : 64, lb + (n == 5 ? 0 : 1024)); \
        else if ((Z) && n == 13) dma16(rx, xo4, 0, lb + 2048); \
        else if ((Z) && n == 19) dma16(mkrs(b2 + 80 * ct2, 160), vo, 0, lb + 3072); \
        else if ((Z) && n == 15) st8(u32x2{mk4[0], mk4[1]}, rw, mb, 0); \
        else if ((LD) && n > 2 && (n & 1) && n < 17) { asm volatile("s_nop %0" :: "n"(NB)); SB(); } \
        else if ((LD) && NA) { asm volatile("s_nop %0" :: "n"(NA - 1)); SB(); }         \
        if ((LD) && n < 2) { GL1(FN, n); } else if ((LD) && !(n & 1)) { GL1(FN, (n >> 1) + 1); } \
        if ((LD) && n == 5) { sa += 1024; asm volatile("" : "+s"(sa)); } \
        if ((LD) && n == 15) { sw += 5120; asm volatile("" : "+s"(sw)); } \
        if ((LD) && n == 3) { sq += 2048; if (sq == sq0) sq = sq1; asm volatile("" : "+s"(sq)); } \
        if (n == 17 || n == 19) *(LAS bf16x8*)(wst + (WO) + (n == 19 ? 1024 : 0)) = FC[n == 19 ? 10 : 9]; \
        SB(); } } while (0)
      const int tt = (8 * qc + 4 * (w & 1) + (w & 2)) * 2 + (r >> 4);
      unsigned ma = WS_O + (tt * 16 + pr) * 1024 + ((2 * odd + h) * 16 + c16) * 16, ma2 = ma + 32768, mb = WS_O2 + ((tt + 2 * h) * 32 + head) * 128 + c16 * 8;
      unsigned mo = (F_O + g * 32 + r) * 128 + h * 4, xo = o * 2 + 16 * kq + (ct2 & 1) * 32, xo4 = o * 2 + 8 * kq + ((ct2 & 1) ? 0 : 128);
      u32x4 mk4 = {~0u, ~0u, ~0u, ~0u};
      LAS char* r0 = lds + L_RED + w * 5120 + lane * 16;
      asm volatile("" : "+v"(ma), "+v"(ma2), "+v"(mb), "+v"(mk4), "+v"(mo), "+v"(xo), "+v"(xo4), "+v"(r0));
#pragma unroll
      for (int j = 0; j < 11; ++j) { GL1(f0, j); SB(); if (j < 9) __builtin_amdgcn_s_sleep(GP); SB(); }
      bf16x8 wlast;
      const unsigned lb = lds0 + L_RED + w * 5120;
      LAS unsigned* c32 = (LAS unsigned*)(lds + L_C + 32); LAS unsigned* c36 = c32 + 1; unsigned k256 = 256u;
      asm volatile("" : "+v"(c32), "+v"(c36), "+v"(k256));
      sa += 1024; sw += 5120;
      __builtin_amdgcn_s_setprio(0); SB();
      GSTEP(f0, f1, 0, true, true);
      *(LAS bf16x8*)(wst + 24576) = wlast;
#pragma unroll 1
      for (int s = 1; s < 11; s += 2) {
        GSTEP(f1, f0, 2048, true, false); GSTEP(f0, f1, 4096, true, false);
        __builtin_amdgcn_s_setprio(2); SB(); asm volatile("v_add_u32 %0, 0x1000, %0" : "+v"(wst));
        if (s == 1) {
          asm volatile("s_waitcnt vmcnt(11)" ::: "memory");
          lds_add(c32, k256);
          if ((lds_add_rtn(c36, k256) >> 8) == 3) { u64 tg = tag; asm volatile("" : "+s"(tg)); publish(fl, F_O + qc * 32 + head, tg, fresh_lane()); }
        }
        if (s == 7)
          asm volatile("s_mov_b32 m0, %0\n\ts_nop 0\n\tbuffer_load_dword %1, %2, 0 offen sc1 lds" :: "s"(lb + 4096), "v"(mo), "s"(mkrs(ws + WS_F, 1u << 17)) : "memory");
        SB(); __builtin_amdgcn_s_setprio(0);
      }
      unsigned oo[4], oo2[4];
#pragma unroll
      for (int j = 0; j < 4; ++j) {
        const int gr = (j + 1 + (g >> 1)) & 3;
        oo[j] = WS_O + (4 * rt2 + w) * 16384 + gr * 4096; oo2[j] = WS_O2 + (4 * rt2 + w) * 4096 + gr * 1024; asm volatile("" : "+s"(oo[j]), "+s"(oo2[j]));
      }
      xv[0] = *(volatile LAS u32x4*)r0; xv[1] = *(volatile LAS u32x4*)(r0 + 1024); xv4 = *(volatile LAS u32x2*)(r0 + 2048);
      SB();
      GSTEP(f1, f0, 2048, false, false);
      __builtin_amdgcn_s_setprio(2);
      STAMP(7);
      const int ln = fresh_lane();
      const unsigned gate = *(volatile LAS unsigned*)(lds + L_C + 24), mk = *(volatile LAS unsigned*)(lds + L_RED + w * 5120 + 4096 + ln * 4);
      LAS char* bp = lds + L_RED + w * 5120 + 3072 + (ct2 & 1) * 32 + (ln & 48);
      bv[0] = *(volatile LAS u32x4*)bp; bv[1] = *(volatile LAS u32x4*)(bp + 64); bv4 = *(volatile LAS u32x2*)(bp + 128 - (ct2 & 1) * 160 - ((ln & 48) >> 1));
      const unsigned v8 = (ln >> 4) * 256 + (ln & 15) * 8;
      volatile LAS unsigned* gc = (volatile LAS unsigned*)(lds + L_C + 48);
      asm volatile("" ::: "memory");
#pragma unroll
      for (int t = 0; t < 5; ++t) *(LAS f32x4*)(r0 + t * 1024) = acc[3][t];
      asm volatile("" ::: "memory"); gc[w] = 1;
      if (!__all(mk == (unsigned)(ln >= 32 ? tag >> 32 : tag))) await<2>(fl, F_O + g * 32 + (ln & 31), tag);
#define SPIN(COND) do { _Pragma("unroll 1") for (int n_ = 0; n_ < (1 << 24); ++n_) { if (COND) break; __builtin_amdgcn_s_sleep(1); } asm volatile("" ::: "memory"); } while (0)
      if ((__builtin_amdgcn_readfirstlane(gate) & 255) < 4) SPIN((__builtin_amdgcn_readfirstlane(*(volatile LAS unsigned*)(lds + L_C + 24)) & 255) >= 4);
      STAMP(10);
      u32x4 a[16], al[4];
#define OL16(p, e, C) asm volatile("buffer_load_dwordx4 %0, %1, %2, %3 offen offset:%4 sc1" : C(a[4 * (p) + (e)]) : "v"(vo), "s"(rw), "s"(oo[p]), "n"((e) * 1024) : "memory")
#define OL8_(p, C, R0, R1, R2, R3) asm volatile("buffer_load_dwordx2 v[" #R0 ":" #R1 "], %1, %2, %3 offen sc1\n\tbuffer_load_dwordx2 v[" #R2 ":" #R3 "], %1, %2, %3 offen offset:128 sc1" \
        : C "{v[" #R0 ":" #R3 "]}"(al[p]) : "v"(v8), "s"(rw), "s"(oo2[p]) : "memory")
#define OL8(p, C) do { if ((p) == 0) OL8_(0, C, 224, 225, 226, 227); else if ((p) == 1) OL8_(1, C, 228, 229, 230, 231); \
        else if ((p) == 2) OL8_(2, C, 232, 233, 234, 235); else OL8_(3, C, 236, 237, 238, 239); } while (0)
#define OLG(p, C) do { OL16(p, 0, C "v"); OL16(p, 1, C "v"); OL16(p, 2, C "v"); OL16(p, 3, C "v"); OL8(p, C); } while (0)
      unsigned pg = gc[ln & 3];
      OLG(0, "=&"); OLG(1, "=&");
      LAS char* wp2 = lds + L_WO + 61440 + ln * 16; LAS char* r1_ = lds + L_RED + ln * 16 + (w ^ 1) * 5120; LAS char* r3 = lds + L_RED + ln * 16 + (w ^ 3) * 5120;
      asm volatile("" : "+v"(wp2));
      if (!__all(pg >= 1)) SPIN(__all(gc[ln & 3] >= 1));
      f32x4 y[5], z[5];
#pragma unroll
      for (int t = 0; t < 5; ++t) { z[t] = *(LAS f32x4*)(r3 + t * 1024); *(LAS f32x4*)(r3 + t * 1024) = acc[2][t]; }
      asm volatile("" ::: "memory"); gc[w] = 2; SB();
#define WJ(j) ((j) / 25 * 25 + (j) % 5 * 5 + (j) % 25 / 5)
      constexpr int NW = 28;
      bf16x8 wr[NW];
#pragma unroll
      for (int t = 0; t < 5; ++t) {
        const unsigned x0 = t < 4 ? xv[(t >> 1) & 1][2 * (t & 1)] : xv4[0], x1 = t < 4 ? xv[(t >> 1) & 1][2 * (t & 1) + 1] : xv4[1];
        const unsigned b0 = t < 4 ? bv[(t >> 1) & 1][2 * (t & 1)] : bv4[0], b1v = t < 4 ? bv[(t >> 1) & 1][2 * (t & 1) + 1] : bv4[1];
        y[t] = (acc[0][t] + f32x4{blo(x0) + blo(b0), bhi(x0) + bhi(b0), blo(x1) + blo(b1v), bhi(x1) + bhi(b1v)}) + z[t];
        asm volatile("" : "+v"(y[t]));
      }
      pg = gc[w ^ 2];
#pragma unroll
      for (int i = 0; i < 12; ++i) wr[i] = *(LAS bf16x8*)(wl + WJ(i) * 1024);
      if (__builtin_amdgcn_readfirstlane(pg) < 2) SPIN(__builtin_amdgcn_readfirstlane(gc[w ^ 2]) >= 2);
      asm volatile("" ::: "memory");
#pragma unroll
      for (int t = 0; t < 5; ++t) { z[t] = *(LAS f32x4*)(r1_ + t * 1024); *(LAS f32x4*)(r1_ + t * 1024) = acc[1][t]; }
      asm volatile("" ::: "memory"); gc[w] = 3; SB();
#pragma unroll
      for (int i = 12; i < NW; ++i) wr[i] = *(LAS bf16x8*)(wl + WJ(i) * 1024);
      SB();
#define OCHK(p, N) ({ unsigned m_; asm volatile("s_waitcnt vmcnt(" #N ")" ::: "memory"); SB(); \
        asm volatile("" : "+v"(a[4 * (p)]), "+v"(a[4 * (p) + 1]), "+v"(a[4 * (p) + 2]), "+v"(a[4 * (p) + 3]), "+v"(al[p]) :: "memory"); \
        m_ = max(al[p][0], al[p][2]); _Pragma("unroll") for (int e = 0; e < 4; ++e) m_ = max(m_, max(a[4 * (p) + e][0], a[4 * (p) + e][2])); __any(m_ == ~0u); })
#define OGRP(p, N) do { \
        if (OCHK(p, N)) { \
          _Pragma("unroll 1") for (int n = 0; n < (1 << 20); ++n) { OLG(p, "+"); if ((p) < 3) OLG(((p) + 1) & 3, "+"); if (!OCHK(p, N)) break; } } \
        SB(); \
        _Pragma("unroll") for (int e = 0; e < 25; ++e) { const int i = 25 * (p) + e; \
          asm volatile("v_mfma_f32_16x16x32_bf16 %0, %1, %2, %0" : "+v"(y[e / 5]) : "v"(wr[i % NW]), "v"(e % 5 < 4 ? a[4 * (p) + (e % 5 & 3)] : al[p])); \
          if ((p) < 2 && e % 4 == 3) { if (e < 16) OL16(((p) + 2) & 3, (e / 4) & 3, "=&v"); else if (e == 19) OL8(((p) + 2) & 3, "=&"); } \
          const int j = NW + (i * 4 + 4) / 5;        \
          if (i % 5 != 4 && j < 100) wr[j % NW] = *(LAS bf16x8*)(WJ(j) < 60 ? wl + WJ(j) * 1024 : wp2 + (WJ(j) - 60) * 1024); \
          if (i == 70) pg = gc[w ^ 1]; \
          if (i >= 90 && i < 95) zz[i - 90] = *(volatile LAS f32x4*)(r0 + (i - 90) * 1024); \
          SB(); } } while (0)
      f32x4 zz[5];
      FS(8); OGRP(0, 6); FS(9); OGRP(1, 6); FS(13); OGRP(2, 6); FS(14);
      if (__builtin_amdgcn_readfirstlane(pg) < 3) SPIN(__builtin_amdgcn_readfirstlane(gc[w ^ 1]) >= 3);
      OGRP(3, 0);
#pragma unroll
      for (int t = 0; t < 5; ++t) { z[t] += zz[t]; asm volatile("" : "+v"(z[t])); }
      asm volatile("s_nop 7" : "+v"(y[0]), "+v"(y[1]), "+v"(y[2]), "+v"(y[3]), "+v"(y[4]));
      const rsrc_t rout = mkrs(out, 2048 * D_ * 2);
      const f32x4 y4 = y[4] + z[4];
      __builtin_amdgcn_raw_buffer_store_b64(u32x2{pk2(y4[0], y4[1]), pk2(y4[2], y4[3])}, rout, xo4, 0, 16);
      __builtin_amdgcn_raw_buffer_store_b128(pk8(y[0] + z[0], y[1] + z[1]), rout, xo, 0, 16);
      __builtin_amdgcn_raw_buffer_store_b128(pk8(y[2] + z[2], y[3] + z[3]), rout, xo + 64, 0, 16);
    }
    STAMP(11);
  }
}

extern "C" void kernel_launch(void* const* d_in, const int* in_sizes, int n_in, void* d_out, int out_size, void* d_ws, size_t ws_size,
                              hipStream_t stream) {
  fused<<<256, 512, 0, stream>>>((const u16*)d_in[0], (const u16*)d_in[1], (const u16*)d_in[2], (const u16*)d_in[4], (const u16*)d_in[5],
                                 (const u16*)d_in[6], (const u16*)d_in[7], (u16*)d_out, (char*)d_ws);
}
```
